```python
import torch

def _softmax(x, axis=-1):
    return torch.softmax(x, dim=axis)
def _gelu(x):
    return torch.nn.functional.gelu(x, approximate="tanh")
def _silu(x):
    return torch.nn.functional.silu(x)
def _sigmoid(x):
    return torch.sigmoid(x)
def _layernorm(x, axis=-1, eps=1e-5):
    m = x.mean(dim=axis, keepdim=True)
    v = x.var(dim=axis, keepdim=True, unbiased=False)
    return (x - m) / torch.sqrt(v + eps)
def _scatter_add(tgt, idx, src, axis=0):
    return tgt.clone().index_add_(axis, idx, src)
def _segment_sum(src, seg, n):
    out = torch.zeros((n,) + src.shape[1:], dtype=src.dtype)
    return out.index_add_(0, seg, src)
def _pad(x, pw):
    flat = tuple(v for pair in reversed(pw) for v in pair)
    return torch.nn.functional.pad(x, flat)

kernel_name = 'parallel_sum__sliding_block__dense_activation_squared_relu__prompt'

def setup_inputs():

    _w = (lambda a: (lambda t: t.to(torch.bfloat16) if t.is_floating_point() else t)(torch.as_tensor(a)))
    def _A_setup():
        B, S, D = 1, 2048, 448
        return {
            "x": _w((lambda *s: torch.randn(*s, dtype=torch.bfloat16) * 0.05)(B, S, D)),
            "wqkv": _w((lambda *s: torch.randn(*s, dtype=torch.bfloat16) * 0.1)(D, 3 * D)),
            "wo": _w((lambda *s: torch.randn(*s, dtype=torch.bfloat16) * 0.05)(D, D)),
        }
    _A_in = _A_setup()

    def _B_setup():
        B, S, D, F = 1, 2048, 448, 1792
        return {
            "x": _w((lambda *s: torch.randn(*s, dtype=torch.bfloat16) * 0.05)(B, S, D)),
            "w1": _w((lambda *s: torch.randn(*s, dtype=torch.bfloat16) * 0.05)(D, F)),
            "b1": _w((lambda *s: torch.randn(*s, dtype=torch.bfloat16) * 0.4)(F)),
            "w2": _w((lambda *s: torch.randn(*s, dtype=torch.bfloat16) * 0.05)(F, D)),
            "b2": _w((lambda *s: torch.randn(*s, dtype=torch.bfloat16) * 1.6)(D)),
        }
    _B_in = _B_setup()

    return {
        "A_x": _A_in["x"],
        "A_wqkv": _A_in["wqkv"],
        "A_wo": _A_in["wo"],
        "B_x": _B_in["x"],
        "B_w1": _B_in["w1"],
        "B_b1": _B_in["b1"],
        "B_w2": _B_in["w2"],
        "B_b2": _B_in["b2"],
    }

def reference(A_x, A_wqkv, A_wo, B_x, B_w1, B_b1, B_w2, B_b2):
    def _A_ref(x, wqkv, wo):
        B, S, D = x.shape
        H = 16; Dh = D // H; W = 256
        qkv = (lambda x, *d: x.permute(*d))((x @ wqkv).reshape(B, S, 3, H, Dh), 2, 0, 3, 1, 4)
        q, k, v = qkv[0], qkv[1], qkv[2]
        scores = torch.einsum("bhqd,bhkd->bhqk", q, k) * (Dh ** -0.5)
        idx = torch.arange(S)
        mask = (idx[:, None] - idx[None, :] >= 0) & (idx[:, None] - idx[None, :] < W)
        scores = torch.where(mask[None, None], scores, -1e9)
        p = _softmax(scores, axis=-1)
        o = torch.einsum("bhqk,bhkd->bhqd", p, v)
        return (lambda x, *d: x.permute(*d))(o, 0, 2, 1, 3).reshape(B, S, D) @ wo

    def _B_ref(x, w1, b1, w2, b2):
        h = torch.einsum("bsd,df->bsf", x, w1) + b1
        zero = torch.zeros_like(h)
        r = torch.where(h > zero, h, zero)
        h = r * r
        out = torch.einsum("bsf,fd->bsd", h, w2) + b2
        return out

    x = A_x
    xn = _layernorm(x, axis=-1)
    return x + _A_ref(xn, A_wqkv, A_wo) + _B_ref(xn, B_w1, B_b1, B_w2, B_b2)
```

```cpp
#include <hip/hip_runtime.h>
#include <hip/hip_bf16.h>
typedef unsigned short u16;
__device__ __forceinline__ float bf2f(u16 v){ return __uint_as_float(((unsigned)v)<<16); }
__device__ __forceinline__ u16 f2bf(float f){ unsigned u=__float_as_uint(f); u += 0x7fffu + ((u>>16)&1u); return (u16)(u>>16); }
__device__ __forceinline__ float rbf(float f){ return bf2f(f2bf(f)); }
constexpr int S=2048,D=448,H=16,DH=28,F=1792,W=256;

__global__ void k_ln(const u16* x, u16* xn){
  int r=blockIdx.x, t=threadIdx.x;
  float v[7]; float s=0;
  for(int i=0;i<7;i++){ v[i]=bf2f(x[r*D+t+64*i]); s+=v[i]; }
  for(int o=32;o;o>>=1) s+=__shfl_xor(s,o);
  float mean=s/D; float q=0;
  for(int i=0;i<7;i++){ float d=v[i]-mean; q+=d*d; }
  for(int o=32;o;o>>=1) q+=__shfl_xor(q,o);
  float var=rbf(q/D); float mb=rbf(mean);
  float den=rbf(sqrtf(rbf(var+1e-5f)));
  for(int i=0;i<7;i++) xn[r*D+t+64*i]=f2bf(rbf(v[i]-mb)/den);
}
template<int MODE> __global__ void k_gemm(const u16* A,const u16* B,const u16* bias,u16* C,int N,int K){
  int n=blockIdx.x*64+threadIdx.x, m=blockIdx.y;
  float acc=0;
  for(int k=0;k<K;k++) acc+=bf2f(A[m*K+k])*bf2f(B[k*N+n]);
  if(MODE==1){ float h=rbf(rbf(acc)+bf2f(bias[n])); h=h>0?h:0; acc=h*h; }
  C[m*N+n]=f2bf(acc);
}
__global__ void k_attn(const u16* qkv,u16* o){
  int q=blockIdx.x,h=blockIdx.y,t=threadIdx.x;
  __shared__ float p[256]; __shared__ float red[256];
  int key=q-t; float s=-1e30f;
  if(key>=0){ float a=0; for(int d=0;d<DH;d++) a+=bf2f(qkv[q*3*D+h*DH+d])*bf2f(qkv[key*3*D+D+h*DH+d]); s=a*0.18898223650461361f; }
  red[t]=s; __syncthreads();
  for(int of=128;of;of>>=1){ if(t<of) red[t]=fmaxf(red[t],red[t+of]); __syncthreads(); }
  float mx=red[0]; __syncthreads();
  float e=key>=0?__expf(s-mx):0.f; p[t]=e; red[t]=e; __syncthreads();
  for(int of=128;of;of>>=1){ if(t<of) red[t]+=red[t+of]; __syncthreads(); }
  float sum=red[0];
  if(t<DH){ float a=0; for(int j=0;j<256;j++){ int kk=q-j; if(kk>=0) a+=p[j]*bf2f(qkv[kk*3*D+2*D+h*DH+t]); } o[q*D+h*DH+t]=f2bf(a/sum); }
}
__global__ void k_out(const u16* x,const u16* o,const u16* wo,const u16* h,const u16* w2,const u16* b2,u16* out){
  int n=blockIdx.x*64+threadIdx.x, m=blockIdx.y;
  float a=0,b=0;
  for(int k=0;k<D;k++) a+=bf2f(o[m*D+k])*bf2f(wo[k*D+n]);
  for(int k=0;k<F;k++) b+=bf2f(h[m*F+k])*bf2f(w2[k*D+n]);
  out[m*D+n]=f2bf(bf2f(x[m*D+n])+a+b+bf2f(b2[n]));
}
extern "C" void kernel_launch(void* const* d_in, const int* in_sizes, int n_in, void* d_out, int out_size, void* d_ws, size_t ws_size, hipStream_t stream){
  const u16* x=(const u16*)d_in[0]; const u16* wqkv=(const u16*)d_in[1]; const u16* wo=(const u16*)d_in[2];
  const u16* w1=(const u16*)d_in[4]; const u16* b1=(const u16*)d_in[5]; const u16* w2=(const u16*)d_in[6]; const u16* b2=(const u16*)d_in[7];
  u16* ws=(u16*)d_ws; u16* xn=ws; u16* qkv=xn+S*D; u16* h=qkv+S*3*D; u16* o=h+S*F;
  k_ln<<<S,64,0,stream>>>(x,xn);
  k_gemm<0><<<dim3(3*D/64,S),64,0,stream>>>(xn,wqkv,nullptr,qkv,3*D,D);
  k_gemm<1><<<dim3(F/64,S),64,0,stream>>>(xn,w1,b1,h,F,D);
  k_attn<<<dim3(S,H),256,0,stream>>>(qkv,o);
  k_out<<<dim3(D/64,S),64,0,stream>>>(x,o,wo,h,w2,b2,(u16*)d_out);
}
```

```cpp
#include <hip/hip_runtime.h>
#include <hip/hip_bf16.h>
#include <stdint.h>

#define DI __device__ __forceinline__
#define LAS __attribute__((address_space(3)))
#define GAS __attribute__((address_space(1)))
typedef unsigned u32;
typedef unsigned long long u64;
typedef __bf16 bf16x8 __attribute__((ext_vector_type(8)));
typedef __bf16 bf16x2 __attribute__((ext_vector_type(2)));
typedef short s16x4 __attribute__((ext_vector_type(4)));
typedef float f32x4 __attribute__((ext_vector_type(4)));
typedef float f32x2 __attribute__((ext_vector_type(2)));
typedef unsigned u32x4 __attribute__((ext_vector_type(4)));
typedef unsigned u32x2 __attribute__((ext_vector_type(2)));
typedef __amdgpu_buffer_rsrc_t rsrc_t;

template <int N> struct IC { static constexpr int v = N; };
template <int B, int E, class F> DI void sfor(F&& f) { if constexpr (B < E) { f(IC<B>{}); sfor<B + 1, E>(f); } }

constexpr u32 WS_FH = 256, WS_FQ = WS_FH + 2048, WS_FB = WS_FQ + 2048, CTL_BYTES = 8192;
constexpr u32 WS_QKV = 8192, QKV_BYTES = 64u * 336u * 256u;
constexpr u32 WS_AC = WS_QKV + QKV_BYTES, AC_BYTES = 128u * 72u * 1024u;
constexpr u32 SLOT = 16384, RING = 7 * SLOT;
constexpr u32 L_K = 0, L_V = 21504, L_SCR = 46080;
constexpr u32 L_CSH = L_SCR, L_ST = L_SCR + 512, L_B1 = L_SCR + 1536, L_B2 = L_SCR + 1792, L_CSQ = L_SCR + 2048;
constexpr u32 L_RED = 0, RED_PITCH = 272, L_XRES = 36864;
constexpr u32 stage_addr(int s) { return s <= 19 ? RING + (s % 6) * 8192u : (s <= 23 ? (s - 20) * 8192u : 3 * SLOT + (s - 24) * 8192u); }

constexpr int T_FQPUB = 2, T_PQ = 4, T_KV = 6, TB = 9, T_FBPUB = 11, T_PB = 11, T_FBCHK = 12, T_XRES = 15;
struct Sched {
  int wA[29];
  int wFH;
  int slotA[18], slotW[18];
  int issT[18][2], issS[18][2];
  int wC[18], wB;
};
constexpr Sched make_sched() {
  Sched r{};
  int n = 0, iW[28] = {}, iX[7] = {}, iH = 0;
  auto X = [&](int k) { n += 2; iX[k] = n; };
  auto W = [&](int s) { n += 1; iW[s] = n; };
  X(0); W(0); W(1); X(1); W(2); W(3); X(2); W(4); W(5); X(3); X(4); X(5); X(6);
  r.wA[0] = n - iW[0];
  for (int s = 0; s < 28; ++s) {
    int need = 0;
    if (s + 1 < 28) { need = iW[s + 1]; if (s + 1 <= 13 && iX[(s + 1) >> 1] > need) need = iX[(s + 1) >> 1]; }
    r.wA[s + 1] = need ? n - need : 63;
    if (s <= 13) W(s + 6);
    if (s == 6) n += 2;
    if (s == 13) { for (int k = 20; k < 28; ++k) W(k); n += 4; }
    if (s == 14) { n += 4; iH = n; }
    if (s == 15 || s == 17 || s == 19) n += 2;
  }
  r.wFH = n - iH;
  n = 0;
  int iA[18] = {}, iWc[18] = {}, iQ = 0, iPQ = 0, iKV = 0, iO = 0, iPB = 0;
  r.slotA[0] = 3; r.slotA[1] = 4; r.slotA[2] = 0; r.slotA[3] = 1;
  r.slotW[0] = 5; r.slotW[1] = 6; r.slotW[2] = 8; r.slotW[3] = 9; r.slotW[4] = 7;
  for (int t = 0; t < 4; ++t) { n += 2; iA[t] = n; }
  n += 3; iQ = n;
  int nA = 4, nW = 5, fr[16] = {}, nf = 0;
  for (int t = 0; t < 18; ++t) {
    if (t == TB) { r.wB = n - iKV; n += 1; iO = n; }
    int need = iA[t] > iWc[t] ? iA[t] : iWc[t];
    if (t == T_FQPUB && iQ > need) need = iQ;
    if (t == T_KV && iPQ > need) need = iPQ;
    if (t == T_FBPUB && iO > need) need = iO;
    if (t == T_FBCHK && iPB > need) need = iPB;
    r.wC[t] = n - need;
    if (t >= 1) { if (r.slotA[t - 1] >= 3) fr[nf++] = r.slotA[t - 1]; fr[nf++] = r.slotW[t - 1]; }
    if (t == T_KV) { n += 8; iKV = n; }
    for (int e = 0; e < 2; ++e) {
      r.issT[t][e] = 0; r.issS[t][e] = 0;
      if (!nf) continue;
      bool aok = nA < 18 && (nA < 14 || t >= T_FBCHK), wok = nW < 18;
      int ty = (aok && (!wok || nA <= nW)) ? 1 : (wok ? 2 : 0);
      if (!ty) continue;
      int sl = fr[0]; for (int k = 1; k < nf; ++k) fr[k - 1] = fr[k]; --nf;
      n += 2;
      if (ty == 1) { r.slotA[nA] = sl; iA[nA] = n; r.issT[t][e] = 1; r.issS[t][e] = nA++; }
      else { r.slotW[nW] = sl; iWc[nW] = n; r.issT[t][e] = 2; r.issS[t][e] = nW++; }
    }
    if (t == T_PQ) { n += 1; iPQ = n; }
    if (t == T_PB) { n += 1; iPB = n; }
    if (t == T_XRES) n += 1;
  }
  if (nA != 18 || nW != 18) r.wB = -1;
  return r;
}
constexpr Sched SC = make_sched();
static_assert(SC.wB >= 0 && SC.wB < 64, "schedule");

DI void dma16(u32 lds, u32 voff, rsrc_t rs, u32 soff) {
  asm volatile("s_mov_b32 m0, %0\n\ts_nop 0\n\tbuffer_load_dwordx4 %1, %2, %3 offen lds" ::"s"(lds), "v"(voff), "s"(rs), "s"(soff) : "memory");
}
DI void dma16c(u32 lds, u32 voff, rsrc_t rs, u32 soff) {
  asm volatile("s_mov_b32 m0, %0\n\ts_nop 0\n\tbuffer_load_dwordx4 %1, %2, %3 offen sc1 lds" ::"s"(lds), "v"(voff), "s"(rs), "s"(soff) : "memory");
}
DI void dma4(u32 lds, u32 voff, rsrc_t rs, u32 soff) {
  asm volatile("s_mov_b32 m0, %0\n\ts_nop 0\n\tbuffer_load_dword %1, %2, %3 offen lds" ::"s"(lds), "v"(voff), "s"(rs), "s"(soff) : "memory");
}
DI u32x2 ld8c_async(u32 voff, rsrc_t rs) {
  u32x2 r; asm volatile("buffer_load_dwordx2 %0, %1, %2, 0 offen sc1" : "=v"(r) : "v"(voff), "s"(rs) : "memory"); return r;
}
template <int N> DI void wait_bar() { asm volatile("s_waitcnt vmcnt(%0) lgkmcnt(0)\n\ts_barrier" ::"n"(N) : "memory"); }
#define PIN(x) asm volatile("" : "+v"(x))
DI f32x4 mfma(bf16x8 a, bf16x8 b, f32x4 c) { return __builtin_amdgcn_mfma_f32_16x16x32_bf16(a, b, c, 0, 0, 0); }
DI u32 pk(float a, float b) { bf16x2 v = {(__bf16)a, (__bf16)b}; return __builtin_bit_cast(u32, v); }
DI float rbf(float a) { return (float)(__bf16)a; }
DI float bflo(u32 v) { return __uint_as_float(v << 16); }
DI float bfhi(u32 v) { return __uint_as_float(v & 0xffff0000u); }
DI float dot2(u32 a, u32 b, float c) { return __builtin_amdgcn_fdot2_f32_bf16(__builtin_bit_cast(bf16x2, a), __builtin_bit_cast(bf16x2, b), c, false); }
DI float xsum(float v) {
  auto a = __builtin_amdgcn_permlane16_swap(__float_as_uint(v), __float_as_uint(v), false, false);
  v = __uint_as_float(a[0]) + __uint_as_float(a[1]);
  auto b = __builtin_amdgcn_permlane32_swap(__float_as_uint(v), __float_as_uint(v), false, false);
  return __uint_as_float(b[0]) + __uint_as_float(b[1]);
}
DI float xmax(float v) {
  auto a = __builtin_amdgcn_permlane16_swap(__float_as_uint(v), __float_as_uint(v), false, false);
  v = fmaxf(__uint_as_float(a[0]), __uint_as_float(a[1]));
  auto b = __builtin_amdgcn_permlane32_swap(__float_as_uint(v), __float_as_uint(v), false, false);
  return fmaxf(__uint_as_float(b[0]), __uint_as_float(b[1]));
}
template <class T> DI T lds_ld(const LAS char* base, u32 off) { return *(const LAS T*)(base + off); }
template <class T> DI void lds_st(LAS char* base, u32 off, T v) { *(LAS T*)(base + off) = v; }
DI bf16x8 trfrag(LAS char* base, u32 off0, u32 off1) {
  s16x4 lo = __builtin_amdgcn_ds_read_tr16_b64_v4i16((LAS s16x4*)(base + off0));
  s16x4 hi = __builtin_amdgcn_ds_read_tr16_b64_v4i16((LAS s16x4*)(base + off1));
  return __builtin_bit_cast(bf16x8, __builtin_shufflevector(lo, hi, 0, 1, 2, 3, 4, 5, 6, 7));
}
DI u64 ld_flag(const char* ws, u32 off) { return __hip_atomic_load((const GAS u64*)(ws + off), __ATOMIC_RELAXED, __HIP_MEMORY_SCOPE_AGENT); }
DI void st_flag(char* ws, u32 off, u64 v) { __hip_atomic_store((GAS u64*)(ws + off), v, __ATOMIC_RELAXED, __HIP_MEMORY_SCOPE_AGENT); }
DI void spin(const char* ws, u32 off, u64 tag) {
  for (u32 n = 0; n < (1u << 18); ++n) {
    if (__all(ld_flag(ws, off) == tag)) break;
    __builtin_amdgcn_s_sleep(1);
  }
  asm volatile("" ::: "memory");
}
#define STAMP(k)

__global__ __launch_bounds__(512, 2) void fused_block(const char* __restrict__ x, const char* __restrict__ wqkv, const char* __restrict__ wo,
                                                      const char* __restrict__ w1, const char* __restrict__ b1, const char* __restrict__ w2,
                                                      const char* __restrict__ b2, char* __restrict__ out, char* ws) {
  __shared__ __attribute__((aligned(1024))) char smem[163840];
  LAS char* const L = (LAS char*)smem;
  const u32 lb = (u32)(uintptr_t)smem;
  const int tid = threadIdx.x, l = tid & 63, g = l >> 4, q4 = (l >> 2) & 3, p4 = l & 3, l15 = l & 15;
  int w = __builtin_amdgcn_readfirstlane(tid >> 6);
  asm volatile("s_nop 4" : "+s"(w));
  const int x8 = blockIdx.x & 7, rk = blockIdx.x >> 3;
  const bool busy = rk < 28;
  STAMP(0);
  const rsrc_t rX = __builtin_amdgcn_make_buffer_rsrc((void*)x, 0, 2048 * 896, 0x00020000);
  const rsrc_t rW2 = __builtin_amdgcn_make_buffer_rsrc((void*)w2, 0, 1792 * 896, 0x00020000);
  const rsrc_t rWo = __builtin_amdgcn_make_buffer_rsrc((void*)wo, 0, 448 * 896, 0x00020000);
  const rsrc_t rQKV = __builtin_amdgcn_make_buffer_rsrc(ws + WS_QKV, 0, QKV_BYTES, 0x00020000);
  const rsrc_t rAC = __builtin_amdgcn_make_buffer_rsrc(ws + WS_AC, 0, AC_BYTES, 0x00020000);
  const rsrc_t rCTL = __builtin_amdgcn_make_buffer_rsrc(ws, 0, CTL_BYTES, 0x00020000);
  const int rb = (x8 & 3) * 4 + rk / 7, cb = (x8 >> 2) * 7 + rk % 7;
  const int ai = rk & 15, ah = 2 * x8 + (rk >> 4);
  const int cR = (x8 & 3) * 8 + (x8 >> 2) * 4 + rk / 7, cc = rk % 7;
  const int ck = w >> 2, cm = (w >> 1) & 1, cn = w & 1;
  const u32 vWC = (16 * w + (l >> 3)) * 896 + cc * 128 + (((l & 7) ^ (((l >> 4) & 3) << 1)) << 4);
  auto dmaWC = [&](auto T, int slot) {
    constexpr int t = decltype(T)::v;
    const u32 d = lb + slot * SLOT + w * 2048;
    if constexpr (t < 14) { dma16(d, vWC, rW2, t * 128 * 896); dma16(d + 1024, vWC, rW2, t * 128 * 896 + 8 * 896); }
    else { dma16(d, vWC, rWo, (t - 14) * 112 * 896); dma16(d + 1024, vWC, rWo, (t - 14) * 112 * 896 + 8 * 896); }
  };
  const u32 sAC = ((cR * 4 + (w >> 1)) * 72 + 2 * (w & 1)) * 1024;
  auto dmaAC = [&](auto T, int slot) {
    constexpr int t = decltype(T)::v;
    const u32 d = lb + slot * SLOT + w * 2048;
    dma16c(d, l * 16, rAC, sAC + t * 4096); dma16c(d + 1024, l * 16, rAC, sAC + t * 4096 + 1024);
  };
  u64 tag;
  f32x4 cacc[2][2] = {};
  u32x2 qa, qb;
  const u32 vFQ = [&] {
    const int e = l & 1, k = l < 14 ? l >> 1 : 0;
    const int ty = k == 0 ? 0 : (k < 4 ? 1 : 2), ro = k == 0 ? 0 : (k < 4 ? k - 1 : k - 4);
    const int rr = ai - ro < 0 ? 0 : ai - ro;
    return WS_FQ + rr * 128 + ((ty * 448 + 28 * ah + 27 * e) / 96) * 8;
  }();
  auto loadKVQ = [&]() {
#pragma unroll
    for (int j = 0; j < 3; ++j) {
      const int seg = w * 3 + j, isv = seg >= 12, b = seg - 12 * isv;
      int tb = 4 * ai - 8 + b; tb = tb < 0 ? 0 : tb;
      const u32 so = (tb * 336 + 112 + 112 * isv + 7 * ah) * 256, d = lb + (isv ? L_V + b * 2048 : L_K + b * 1792);
      dma16c(d, l * 16, rQKV, so);
      if (l < 48) dma16c(d + 1024, l * 16, rQKV, so + 1024);
    }
    const u32 vq = ((4 * ai + (w >> 1)) * 336 + 7 * ah + g) * 256 + l15 * 16 + (w & 1) * 8;
    qa = ld8c_async(vq, rQKV);
    qb = ld8c_async(g < 3 ? vq + 1024 : 0xfffffff0u, rQKV);
    if (tid < 384) lds_st<u32x2>(L, L_V + (tid >> 5) * 2048 + 1792 + (tid & 31) * 8, u32x2{0x3f80u, 0u});
  };

  if (busy) {
    const int wm = w >> 1, wn = w & 1;
    const rsrc_t rW1 = __builtin_amdgcn_make_buffer_rsrc((void*)w1, 0, 448 * 3584, 0x00020000);
    const rsrc_t rWq = __builtin_amdgcn_make_buffer_rsrc((void*)wqkv, 0, 448 * 2688, 0x00020000);
    const u32 vX0 = (16 * w + (l >> 3)) * 896 + (((l & 7) ^ (l >> 4)) << 4);
    const u32 vX1 = (16 * w + 8 + (l >> 3)) * 896 + (((l & 7) ^ (4 + (l >> 4))) << 4);
    const u32 vWH = (4 * w + g) * 3584 + cb * 256 + ((l15 ^ (((((w >> 1) & 1) << 2) + g) << 1)) << 4);
    const u32 vWQ = (4 * w + l / 12) * 2688 + cb * 192 + (((l % 12) ^ (((w >> 1) & 1) << 1)) << 4);
    auto dmaX = [&](int kl) {
      dma16(lb + (kl * 16 + 2 * w) * 1024, vX0, rX, rb * 114688 + kl * 128);
      dma16(lb + (kl * 16 + 2 * w + 1) * 1024, vX1, rX, rb * 114688 + kl * 128);
    };
    auto dmaW = [&](auto SS) {
      constexpr int s = decltype(SS)::v;
      if constexpr (s < 14) dma16(lb + stage_addr(s) + w * 1024, vWH, rW1, s * 32 * 3584);
      else { if (l < 48) dma16(lb + stage_addr(s) + w * 1024, vWQ, rWq, (s - 14) * 32 * 2688); }
    };
    dmaX(0); dmaW(IC<0>{}); dmaW(IC<1>{}); dmaX(1); dmaW(IC<2>{}); dmaW(IC<3>{}); dmaX(2); dmaW(IC<4>{}); dmaW(IC<5>{});
    dmaX(3); dmaX(4); dmaX(5); dmaX(6);
    asm volatile("s_load_dwordx2 %0, %1, 0x0\n\ts_waitcnt lgkmcnt(0)" : "=s"(tag) : "s"(ws) : "memory");
    tag += 1;
    u32 oX[2][2], oH[5], oQ[4];
#pragma unroll
    for (int r = 0; r < 2; ++r)
#pragma unroll
      for (int p = 0; p < 2; ++p) oX[r][p] = (4 * wm + 2 * (r ^ wn) + (l15 >> 3)) * 1024 + (l & 7) * 128 + (((p * 4 + g) ^ (l15 >> 1)) << 4);
#pragma unroll
    for (int i = 0; i < 5; ++i) oH[i] = g * 2048 + q4 * 256 + (((4 * wn + (i < 4 ? i : wm)) ^ (((g & 1) << 2) + q4)) << 5) + p4 * 8;
#pragma unroll
    for (int i = 0; i < 4; ++i) oQ[i] = g * 2048 + q4 * 192 + (((3 * wn + (i < 3 ? i : (wm < 3 ? wm : 2))) ^ (g & 1)) << 5) + p4 * 8;
    const bf16x8 ones = __builtin_bit_cast(bf16x8, u32x4{0x3f803f80u, 0x3f803f80u, 0x3f803f80u, 0x3f803f80u});
    bf16x8 xf[2][14], wf[2][5];
    f32x4 aH[2][4] = {}, aQ[2][3] = {}, csH = {}, csQ = {};
    float s1 = 0.f, s2 = 0.f, mb[2], rs[2];
    auto reads = [&](auto SS) {
      constexpr int s = decltype(SS)::v;
      constexpr u32 sa = stage_addr(s);
      if constexpr (s < 14) {
#pragma unroll
        for (int r = 0; r < 2; ++r) xf[r][s] = lds_ld<bf16x8>(L, oX[r][s & 1] + (s >> 1) * 16384);
#pragma unroll
        for (int i = 0; i < 5; ++i) wf[s & 1][i] = trfrag(L, oH[i] + sa, oH[i] + sa + 1024);
      } else {
#pragma unroll
        for (int i = 0; i < 4; ++i) wf[s & 1][i] = trfrag(L, oQ[i] + sa, oQ[i] + sa + 1024);
      }
    };
    wait_bar<SC.wA[0]>();
    STAMP(1);
    reads(IC<0>{});
    sfor<0, 28>([&](auto SS) {
      constexpr int s = decltype(SS)::v;
      wait_bar<SC.wA[s + 1]>();
      if constexpr (s <= 13) dmaW(IC<s + 6>{});
      if constexpr (s == 6) {
        dma4(lb + L_B1, cb * 256 + l * 4, __builtin_amdgcn_make_buffer_rsrc((void*)b1, 0, 3584, 0x00020000), 0);
        dma4(lb + L_B2, cc * 128 + l * 4, __builtin_amdgcn_make_buffer_rsrc((void*)b2, 0, 896, 0x00020000), 0);
      }
      if constexpr (s == 13) { sfor<20, 28>(dmaW); dmaWC(IC<0>{}, SC.slotW[0]); dmaWC(IC<1>{}, SC.slotW[1]); }
      if constexpr (s == 15) dmaWC(IC<2>{}, SC.slotW[2]);
      if constexpr (s == 17) dmaWC(IC<3>{}, SC.slotW[3]);
      if constexpr (s == 19) dmaWC(IC<4>{}, SC.slotW[4]);
      if constexpr (s + 1 < 28) reads(IC<s + 1>{});
      if constexpr (s == 14) {
        STAMP(2);
        const f32x2 st = lds_ld<f32x2>(L, L_ST + ((wm * 2 + (wn ^ 1)) * 16 + l15) * 8);
        mb[1] = st[0]; rs[1] = st[1];
        u32 hp[2][4][2];
#pragma unroll
        for (int i = 0; i < 4; ++i) {
          const f32x4 cs = lds_ld<f32x4>(L, L_CSH + (wn * 4 + i) * 64 + g * 16);
          const u32x2 bb = lds_ld<u32x2>(L, L_B1 + (wn * 4 + i) * 32 + g * 8);
          const float bv[4] = {bflo(bb[0]), bfhi(bb[0]), bflo(bb[1]), bfhi(bb[1])};
#pragma unroll
          for (int r = 0; r < 2; ++r) {
            float v[4];
#pragma unroll
            for (int j = 0; j < 4; ++j) { float t = fmaxf((aH[r][i][j] - mb[r] * cs[j]) * rs[r] + bv[j], 0.f); v[j] = t * t; }
            hp[r][i][0] = pk(v[0], v[1]); hp[r][i][1] = pk(v[2], v[3]);
          }
        }
#pragma unroll
        for (int r = 0; r < 2; ++r)
#pragma unroll
          for (int k = 0; k < 2; ++k)
            __builtin_amdgcn_raw_buffer_store_b128(u32x4{hp[r][2 * k][0], hp[r][2 * k][1], hp[r][2 * k + 1][0], hp[r][2 * k + 1][1]}, rAC, l * 16,
                                                   ((rb * 8 + wm * 2 + (r ^ wn)) * 72 + cb * 4 + wn * 2 + k) * 1024, 16);
        asm volatile("" ::: "memory");
        STAMP(3);
      }
      if constexpr (s < 14) {
#pragma unroll
        for (int i = 0; i < 4; ++i)
#pragma unroll
          for (int r = 0; r < 2; ++r) aH[r][i] = mfma(wf[s & 1][i], xf[r][s], aH[r][i]);
        csH = mfma(wf[s & 1][4], ones, csH);
        const u32x4 xv = __builtin_bit_cast(u32x4, xf[0][s]);
#pragma unroll
        for (int j = 0; j < 4; ++j) { s1 = dot2(xv[j], 0x3f803f80u, s1); s2 = dot2(xv[j], xv[j], s2); }
      } else {
#pragma unroll
        for (int i = 0; i < 3; ++i)
#pragma unroll
          for (int r = 0; r < 2; ++r) aQ[r][i] = mfma(wf[s & 1][i], xf[r][s - 14], aQ[r][i]);
        csQ = mfma(wf[s & 1][3], ones, csQ);
      }
      if constexpr (s == 13) {
        const float mean = xsum(s1) * (1.f / 448.f);
        const float var = fmaxf(xsum(s2) * (1.f / 448.f) - mean * mean, 0.f);
        mb[0] = rbf(mean);
        rs[0] = 1.f / rbf(sqrtf(rbf(rbf(var) + 1e-5f)));
        if (g == 0) lds_st<f32x2>(L, L_ST + ((wm * 2 + wn) * 16 + l15) * 8, f32x2{mb[0], rs[0]});
        if (l15 == 0) lds_st<f32x4>(L, L_CSH + (wn * 4 + wm) * 64 + g * 16, csH);
      }
      __builtin_amdgcn_sched_barrier(0);
    });
    STAMP(4);
    if (l15 == 0 && wm < 3) lds_st<f32x4>(L, L_CSQ + (wn * 4 + wm) * 64 + g * 16, csQ);
    wait_bar<SC.wFH>();
    if (tid == 0) st_flag(ws, WS_FH + rb * 128 + cb * 8, tag);
    u32x4 qp[3];
    u32 qo[3];
#pragma unroll
    for (int i = 0; i < 3; ++i) {
      const int tq = 6 * cb + 3 * wn + i;
      const bool isv = tq >= 56;
      const float sc = tq < 28 ? 0.18898223650461361f * 1.4426950408889634f : 1.f;
      const f32x4 cs = lds_ld<f32x4>(L, L_CSQ + (wn * 4 + i) * 64 + g * 16);
      u32 a[2][2];
#pragma unroll
      for (int r = 0; r < 2; ++r) {
        const float m = rs[r] * sc;
        a[r][0] = pk((aQ[r][i][0] - mb[r] * cs[0]) * m, (aQ[r][i][1] - mb[r] * cs[1]) * m);
        a[r][1] = pk((aQ[r][i][2] - mb[r] * cs[2]) * m, (aQ[r][i][3] - mb[r] * cs[3]) * m);
      }
      const bool sw = (wn ^ (isv ? (g & 1) : 0)) != 0;
      qp[i] = sw ? u32x4{a[1][0], a[1][1], a[0][0], a[0][1]} : u32x4{a[0][0], a[0][1], a[1][0], a[1][1]};
      qo[i] = ((rb * 4 + wm) * 336 + 4 * tq + g) * 256 + ((l15 ^ (isv ? (g >> 1) << 3 : 0)) << 4);
    }
    STAMP(5);
    spin(ws, WS_FH + (cR >> 1) * 128 + (l < 14 ? l : 0) * 8, tag);
    STAMP(6);
    sfor<0, 4>([&](auto T) { dmaAC(T, SC.slotA[decltype(T)::v]); });
#pragma unroll
    for (int i = 0; i < 3; ++i) __builtin_amdgcn_raw_buffer_store_b128(qp[i], rQKV, qo[i], 0, 16);
    asm volatile("" ::: "memory");
  } else {
    asm volatile("s_load_dwordx2 %0, %1, 0x0\n\ts_waitcnt lgkmcnt(0)" : "=s"(tag) : "s"(ws) : "memory");
    tag += 1;
    spin(ws, vFQ, tag);
    loadKVQ();
    asm volatile("s_waitcnt vmcnt(0)" ::: "memory");
  }

  u32 oC[2][2];
#pragma unroll
  for (int o = 0; o < 2; ++o)
#pragma unroll
    for (int b = 0; b < 2; ++b) oC[o][b] = (4 * g + q4) * 128 + (((2 * cn + b) ^ ((2 * (g & 1) + (q4 >> 1)) ^ (2 * o))) << 5) + p4 * 8;
  const u32 oA = (2 * cm * 4 + 2 * ck) * 1024 + l * 16;
  const u32 oWf = ck * 64 * 128, oWo = ck * 56 * 128;
  u32x2 polled = {0u, 0u};
  auto stageC = [&](auto T) {
    constexpr int t = decltype(T)::v;
    wait_bar<SC.wC[t]>();
    if constexpr (t == T_FQPUB) { if (tid == 0) st_flag(ws, WS_FQ + rb * 128 + cb * 8, tag); }
    if constexpr (t == T_FBPUB) { if (tid == 0) st_flag(ws, WS_FB + ai * 128 + ah * 8, tag); }
    if constexpr (t == T_KV) {
      PIN(polled);
      if (!__all(polled[0] == (u32)tag && polled[1] == (u32)(tag >> 32))) spin(ws, vFQ, tag);
      loadKVQ();
    }
    if constexpr (t == T_FBCHK) {
      PIN(polled);
      if (!__all(polled[0] == (u32)tag && polled[1] == (u32)(tag >> 32))) spin(ws, WS_FB + (cR >> 1) * 128 + l15 * 8, tag);
    }
    sfor<0, 2>([&](auto E) {
      constexpr int e = decltype(E)::v, ty = SC.issT[t][e], sg = SC.issS[t][e];
      if constexpr (ty == 1) dmaAC(IC<sg>{}, SC.slotA[sg]);
      if constexpr (ty == 2) dmaWC(IC<sg>{}, SC.slotW[sg]);
    });
    if constexpr (t == T_PQ) polled = ld8c_async(vFQ, rCTL);
    if constexpr (t == T_PB) polled = ld8c_async(WS_FB + (cR >> 1) * 128 + l15 * 8, rCTL);
    if constexpr (t == T_XRES) dma16(lb + L_XRES + w * 1024, (8 * w + (l >> 3)) * 896 + (l & 7) * 16, rX, cR * 64 * 896 + cc * 128);
    constexpr u32 sa = SC.slotA[t] * SLOT, sw = SC.slotW[t] * SLOT;
    bf16x8 hf[2][2], wf[2][2];
#pragma unroll
    for (int k = 0; k < 2; ++k) {
#pragma unroll
      for (int a = 0; a < 2; ++a) hf[k][a] = lds_ld<bf16x8>(L, oA + sa + (a * 4 + k) * 1024);
#pragma unroll
      for (int b = 0; b < 2; ++b) {
        if constexpr (t < 14) wf[k][b] = trfrag(L, oC[0][b] + oWf + sw + k * 4096, oC[0][b] + oWf + sw + k * 4096 + 2048);
        else wf[k][b] = trfrag(L, oC[k][b] + oWo + sw + k * 3584, oC[k][b] + oWo + sw + k * 3584 + 2048);
      }
    }
#pragma unroll
    for (int k = 0; k < 2; ++k)
#pragma unroll
      for (int a = 0; a < 2; ++a)
#pragma unroll
        for (int b = 0; b < 2; ++b) cacc[a][b] = mfma(wf[k][b], hf[k][a], cacc[a][b]);
    __builtin_amdgcn_sched_barrier(0);
  };
  if (busy) sfor<0, TB>(stageC);
  STAMP(7);

  {
    wait_bar<SC.wB>();
    PIN(qa); PIN(qb);
    STAMP(8);
    const bf16x8 qf = __builtin_bit_cast(bf16x8, u32x4{qa[0], qa[1], qb[0], qb[1]});
    const u32 oKa = L_K + (w >> 1) * 1792 + g * 256 + l15 * 16, oKb = L_K + (w >> 1) * 1792 + (g < 3 ? 4 + g : 6) * 256 + l15 * 16;
    f32x4 sc[18];
#pragma unroll
    for (int m = 0; m < 9; ++m) {
      const u32x4 ka = lds_ld<u32x4>(L, oKa + m * 1792), kb = lds_ld<u32x4>(L, oKb + m * 1792);
      sc[2 * m] = mfma(__builtin_bit_cast(bf16x8, u32x4{ka[0], ka[1], kb[0], kb[1]}), qf, f32x4{});
      sc[2 * m + 1] = mfma(__builtin_bit_cast(bf16x8, u32x4{ka[2], ka[3], kb[2], kb[3]}), qf, f32x4{});
    }
    const int d0 = 256 + 16 * (w & 1) + l15 - 4 * g;
#pragma unroll
    for (int e = 0; e < 4; ++e) {
      const int u = e < 2 ? e : 14 + e;
#pragma unroll
      for (int j = 0; j < 4; ++j) sc[u][j] = (u32)(d0 - 16 * u - j) < 256u ? sc[u][j] : -INFINITY;
    }
    if (ai < 2) {
      const int u0 = 16 - 8 * ai - 2 * (w >> 1);
#pragma unroll
      for (int u = 0; u < 16; ++u)
        if (u < u0) sc[u] = f32x4{-INFINITY, -INFINITY, -INFINITY, -INFINITY};
    }
    float mx = fmaxf(fmaxf(sc[0][0], sc[0][1]), fmaxf(sc[0][2], sc[0][3]));
#pragma unroll
    for (int u = 1; u < 18; ++u) mx = fmaxf(fmaxf(fmaxf(mx, sc[u][0]), sc[u][1]), fmaxf(sc[u][2], sc[u][3]));
    mx = xmax(mx);
    u32 oV[2][2];
#pragma unroll
    for (int dt = 0; dt < 2; ++dt) {
      const int F = 7 * ah + 4 * dt + p4;
#pragma unroll
      for (int t = 0; t < 2; ++t) oV[dt][t] = L_V + (w >> 1) * 2048 + (4 * dt + p4) * 256 + (((4 * g + q4) ^ ((F & 2) << 2)) << 4) + ((t ^ (F & 1)) << 3);
    }
    f32x4 o0 = {}, o1 = {};
#pragma unroll
    for (int m = 0; m < 9; ++m) {
      u32x4 p;
      p[0] = pk(__builtin_amdgcn_exp2f(sc[2 * m][0] - mx), __builtin_amdgcn_exp2f(sc[2 * m][1] - mx));
      p[1] = pk(__builtin_amdgcn_exp2f(sc[2 * m][2] - mx), __builtin_amdgcn_exp2f(sc[2 * m][3] - mx));
      p[2] = pk(__builtin_amdgcn_exp2f(sc[2 * m + 1][0] - mx), __builtin_amdgcn_exp2f(sc[2 * m + 1][1] - mx));
      p[3] = pk(__builtin_amdgcn_exp2f(sc[2 * m + 1][2] - mx), __builtin_amdgcn_exp2f(sc[2 * m + 1][3] - mx));
      const bf16x8 pf = __builtin_bit_cast(bf16x8, p);
      o0 = mfma(trfrag(L, oV[0][0] + m * 2048, oV[0][1] + m * 2048), pf, o0);
      o1 = mfma(trfrag(L, oV[1][0] + m * 2048, oV[1][1] + m * 2048), pf, o1);
    }
    const float inv = 1.f / __shfl(o1[0], 48 + l15);
    const float i1 = g < 3 ? inv : 0.f;
    __builtin_amdgcn_raw_buffer_store_b128(u32x4{pk(o0[0] * inv, o0[1] * inv), pk(o0[2] * inv, o0[3] * inv), pk(o1[0] * i1, o1[1] * i1), pk(o1[2] * i1, o1[3] * i1)},
                                           rAC, l * 16, ((ai * 8 + w) * 72 + 56 + ah) * 1024, 16);
    asm volatile("" ::: "memory");
    __builtin_amdgcn_sched_barrier(0);
  }
  STAMP(9);

  if (busy) {
    sfor<TB, 18>(stageC);
    STAMP(10);
#pragma unroll
    for (int a = 0; a < 2; ++a)
#pragma unroll
      for (int b = 0; b < 2; ++b) lds_st<f32x4>(L, L_RED + (ck * 64 + cm * 32 + a * 16 + l15) * RED_PITCH + (cn * 32 + b * 16 + 4 * g) * 4, cacc[a][b]);
    wait_bar<0>();
    const int row = tid >> 3, c8 = tid & 7;
    const f32x4 r0 = lds_ld<f32x4>(L, L_RED + row * RED_PITCH + c8 * 32), r1 = lds_ld<f32x4>(L, L_RED + row * RED_PITCH + c8 * 32 + 16);
    const f32x4 r2 = lds_ld<f32x4>(L, L_RED + (64 + row) * RED_PITCH + c8 * 32), r3 = lds_ld<f32x4>(L, L_RED + (64 + row) * RED_PITCH + c8 * 32 + 16);
    const u32x4 xr = lds_ld<u32x4>(L, L_XRES + tid * 16), bb = lds_ld<u32x4>(L, L_B2 + c8 * 16);
    u32x4 res;
    res[0] = pk(r0[0] + r2[0] + bflo(xr[0]) + bflo(bb[0]), r0[1] + r2[1] + bfhi(xr[0]) + bfhi(bb[0]));
    res[1] = pk(r0[2] + r2[2] + bflo(xr[1]) + bflo(bb[1]), r0[3] + r2[3] + bfhi(xr[1]) + bfhi(bb[1]));
    res[2] = pk(r1[0] + r3[0] + bflo(xr[2]) + bflo(bb[2]), r1[1] + r3[1] + bfhi(xr[2]) + bfhi(bb[2]));
    res[3] = pk(r1[2] + r3[2] + bflo(xr[3]) + bflo(bb[3]), r1[3] + r3[3] + bfhi(xr[3]) + bfhi(bb[3]));
    *(GAS u32x4*)(out + (size_t)(cR * 64 + row) * 896 + cc * 128 + c8 * 16) = res;
  } else {
    wait_bar<0>();
    if (tid == 0) st_flag(ws, WS_FB + ai * 128 + ah * 8, tag);
    if (blockIdx.x == 255 && w == 0) {
#pragma unroll
      for (int k = 0; k < 4; ++k) spin(ws, WS_FB + (k * 64 + l) * 8, tag);
      if (l == 0) st_flag(ws, 0, tag);
    }
  }
  STAMP(11);
}

extern "C" void kernel_launch(void* const* d_in, const int* in_sizes, int n_in, void* d_out, int out_size, void* d_ws, size_t ws_size, hipStream_t stream) {
  fused_block<<<256, 512, 0, stream>>>((const char*)d_in[0], (const char*)d_in[1], (const char*)d_in[2], (const char*)d_in[4], (const char*)d_in[5],
                                       (const char*)d_in[6], (const char*)d_in[7], (char*)d_out, (char*)d_ws);
}
```

```cpp
#define NOROT
#include <hip/hip_runtime.h>
#include <hip/hip_bf16.h>
#include <stdint.h>

#define DI __device__ __forceinline__
#define LAS __attribute__((address_space(3)))
#define GAS __attribute__((address_space(1)))
typedef unsigned u32;
typedef unsigned long long u64;
typedef __bf16 bf16x8 __attribute__((ext_vector_type(8)));
typedef __bf16 bf16x2 __attribute__((ext_vector_type(2)));
typedef short s16x4 __attribute__((ext_vector_type(4)));
typedef float f32x4 __attribute__((ext_vector_type(4)));
typedef float f32x2 __attribute__((ext_vector_type(2)));
typedef float f32x16 __attribute__((ext_vector_type(16)));
typedef unsigned u32x4 __attribute__((ext_vector_type(4)));
typedef unsigned u32x2 __attribute__((ext_vector_type(2)));
typedef __amdgpu_buffer_rsrc_t rsrc_t;

template <int N> struct IC { static constexpr int v = N; };
template <int B, int E, class F> DI void sfor(F&& f) { if constexpr (B < E) { f(IC<B>{}); sfor<B + 1, E>(f); } }

constexpr u32 WS_FH = 256, WS_FQ = WS_FH + 2048, WS_FB = WS_FQ + 2048, WS_FG = WS_FB + 2048, CTL_BYTES = 16384;
constexpr u32 WS_QKV = 16384, QKV_BYTES = 64u * 336u * 256u;
constexpr u32 WS_AC = WS_QKV + QKV_BYTES, AC_BYTES = 128u * 72u * 1024u;
constexpr u32 SLOT = 16384, RING = 7 * SLOT;
constexpr u32 L_SX = 45056, L_CSQ = 45568, L_B1 = 46080, L_CSH = 46336, L_ST = 47360, L_CNT = 48384;
constexpr u32 L_K = 0, L_V = 21504;
constexpr int RW = 12;
constexpr u32 L_WC = 49152, WC_RING = RW * 2048, RED_PITCH = 272;
constexpr u32 stage_addr(int s) { return s <= 20 ? RING + (s % 6) * 8192u : (s <= 25 ? (s - 21) * 8192u : RING + (s - 22) * 8192u); }

enum { K_NONE = 0, K_X, K_WA, K_BIAS, K_WCP };
constexpr int OP(int k, int a, int e = 0) { return k * 256 + 2 * a + e; }
constexpr int opn(int op) { int k = op / 256, a = (op % 256) / 2; return k == K_NONE ? 0 : k == K_X ? 4 : k == K_WA ? (a < 14 ? 3 : 4) : k == K_WCP ? 2 : 1; }
#define CAP_1 8
#define CAP_2 8
#define CAP_E 20
#define P_DA 8
#define P_TB 11
#define P_TO 25
#define P_FQ 23
#define P_FA 27
constexpr int MAXO = 12, NPRO = 8, DA = P_DA, TB = P_TB, TO = P_TO, B_FQ = P_FQ, B_FA = P_FA;
struct Sched {
  int pro[NPRO], opA[28][MAXO];
  int wA[29];
  int wC[36], a0[36], a1[36];
  int ok;
};
constexpr int cl(int n) { return n > 63 ? 63 : n; }
constexpr int free_at(u32 b) {
  return b < RING ? 2 * (int)(b / SLOT) + 1 : (int[6]){19, 20, 21, 16, 28, 28}[(b - RING) / 8192];
}
constexpr Sched make_sched() {
  Sched r{};
  r.ok = 1;
  int n = 0, iW[28] = {}, iX[7] = {}, iB = 0;
  int lo[128] = {}, le[128] = {}, nl = 0;
  for (int s = 0; s < 28; ++s) {
    if (s < 14 && !(s & 1)) { lo[nl] = OP(K_X, s >> 1, 0); le[nl++] = -1; lo[nl] = OP(K_X, s >> 1, 1); le[nl++] = -1; }
    lo[nl] = OP(K_WA, s); le[nl++] = s <= 20 ? s - 5 : (s <= 25 ? free_at(stage_addr(s) + 8191) : s - 9);
    if (s == 17) { lo[nl] = OP(K_BIAS, 0); le[nl++] = 5; }
  }
  for (int k = 0; k < RW; ++k) for (int j = 0; j < 2; ++j) {
    int f = 0;
    for (int e = 0; e < 4; ++e) { const int t = free_at(L_WC + (2 * j + (e >> 1)) * WC_RING + k * 2048 + (e & 1) * 2047); if (t > f) f = t; }
    lo[nl] = OP(K_WCP, k, j); le[nl++] = f;
  }
  bool done[128] = {};
  auto note = [&](int op) { n += opn(op); int k = op / 256, a = (op % 256) / 2; if (k == K_X) iX[a] = (op & 1) ? n : 0; if (k == K_WA) iW[a] = n; if (k == K_BIAS) iB = n; };
  for (int k = 0; k < NPRO; ++k) { r.pro[k] = lo[k]; done[k] = true; note(lo[k]); }
  r.wA[0] = cl(n - iW[0]);
  for (int s = 0; s < 28; ++s) {
    int need = 0;
    if (s + 1 < 28) {
      need = iW[s + 1]; if (!need) r.ok = 0;
      if (s + 1 <= 13) { int v = iX[(s + 1) >> 1]; if (!v) r.ok = 0; if (v > need) need = v; }
      if (s == 21) { if (!iB) r.ok = 0; if (iB > need) need = iB; }
    }
    r.wA[s + 1] = need ? cl(n - need) : 63;
    int cap = s == 14 || s == 21 ? CAP_E : (s < 14 ? CAP_1 : CAP_2), c = 0, used = 0;
    for (int k = 0; k < nl; ++k) if (!done[k] && le[k] <= s && used + opn(lo[k]) <= cap && c < MAXO) { done[k] = true; r.opA[s][c++] = lo[k]; used += opn(lo[k]); note(lo[k]); }
  }
  for (int k = 0; k < nl; ++k) if (!done[k]) r.ok = 0;
  n = 4;
  int iA[36] = {}, iWc[36] = {}, iP = 0, nA = DA;
  for (int m = 0; m < DA; ++m) { n += 2; iA[m] = n; }
  for (int s = 0; s < 36; ++s) {
    int need = iA[s]; if (!need) r.ok = 0;
    if (s + 1 < 36 && s + 1 >= RW) { if (!iWc[s + 1]) r.ok = 0; if (iWc[s + 1] > need) need = iWc[s + 1]; }
    if ((s == TB || s == TO) && iP > need) need = iP;
    r.wC[s] = cl(n - need);
    if (s == TB - 2 || s == TO - 2) { n += 1; iP = n; }
    r.a0[s] = nA;
    for (int c = 0; c < 2 && nA < 36 && nA <= s + DA && (nA < 14 || s >= TB) && (nA < 28 || s >= TO); ++c) { n += 2; iA[nA++] = n; }
    r.a1[s] = nA;
    if (s >= 1 && s + RW - 1 < 36) { n += 2; iWc[s + RW - 1] = n; }
    if (s == 33) n += 2;
  }
  return r;
}
constexpr Sched SC = make_sched();
static_assert(SC.ok, "schedule");

template <bool C> DI void dma16(u32 lds, u32 voff, rsrc_t rs, u32 soff) {
  if constexpr (C) asm volatile("s_mov_b32 m0, %0\n\ts_nop 0\n\tbuffer_load_dwordx4 %1, %2, %3 offen sc1 lds" ::"s"(lds), "v"(voff), "s"(rs), "s"(soff) : "memory");
  else asm volatile("s_mov_b32 m0, %0\n\ts_nop 0\n\tbuffer_load_dwordx4 %1, %2, %3 offen lds" ::"s"(lds), "v"(voff), "s"(rs), "s"(soff) : "memory");
}
template <bool C> DI u32x4 ld16_async(u32 voff, rsrc_t rs, u32 soff) {
  u32x4 r;
  if constexpr (C) asm volatile("buffer_load_dwordx4 %0, %1, %2, %3 offen sc1" : "=v"(r) : "v"(voff), "s"(rs), "s"(soff) : "memory");
  else asm volatile("buffer_load_dwordx4 %0, %1, %2, %3 offen" : "=v"(r) : "v"(voff), "s"(rs), "s"(soff) : "memory");
  return r;
}
DI void dma4(u32 lds, u32 voff, rsrc_t rs, u32 soff) {
  asm volatile("s_mov_b32 m0, %0\n\ts_nop 0\n\tbuffer_load_dword %1, %2, %3 offen lds" ::"s"(lds), "v"(voff), "s"(rs), "s"(soff) : "memory");
}
DI u32x2 ld8c_async(u32 voff, rsrc_t rs) {
  u32x2 r; asm volatile("buffer_load_dwordx2 %0, %1, %2, 0 offen sc1" : "=v"(r) : "v"(voff), "s"(rs) : "memory"); return r;
}
template <int N> DI void wait_bar() { asm volatile("s_waitcnt vmcnt(%0) lgkmcnt(0)\n\ts_barrier" ::"n"(N) : "memory"); }
template <int N> DI void vm_bar() { asm volatile("s_waitcnt vmcnt(%0)\n\ts_barrier" ::"n"(N) : "memory"); }
DI void bar() { asm volatile("s_barrier" ::: "memory"); }
#define PIN(x) asm volatile("" : "+v"(x))
#define SB() __builtin_amdgcn_sched_barrier(0)
DI f32x4 mfma(bf16x8 a, bf16x8 b, f32x4 c) { return __builtin_amdgcn_mfma_f32_16x16x32_bf16(a, b, c, 0, 0, 0); }
DI f32x16 mfma32(bf16x8 a, bf16x8 b, f32x16 c) { return __builtin_amdgcn_mfma_f32_32x32x16_bf16(a, b, c, 0, 0, 0); }
DI u32 pk(float a, float b) { bf16x2 v = {(__bf16)a, (__bf16)b}; return __builtin_bit_cast(u32, v); }
DI float fmx(float a, float b) { return __builtin_elementwise_maximum(a, b); }
DI float rbf(float a) { return (float)(__bf16)a; }
DI float bflo(u32 v) { return __uint_as_float(v << 16); }
DI float bfhi(u32 v) { return __uint_as_float(v & 0xffff0000u); }
DI float dot2(u32 a, u32 b, float c) { return __builtin_amdgcn_fdot2_f32_bf16(__builtin_bit_cast(bf16x2, a), __builtin_bit_cast(bf16x2, b), c, false); }
template <class T> DI T lds_ld(const LAS char* base, u32 off) { return *(const LAS T*)(base + off); }
template <class T> DI void lds_st(LAS char* base, u32 off, T v) { *(LAS T*)(base + off) = v; }
DI bf16x8 trfrag(LAS char* base, u32 off0, u32 off1) {
  s16x4 lo = __builtin_amdgcn_ds_read_tr16_b64_v4i16((LAS s16x4*)(base + off0));
  s16x4 hi = __builtin_amdgcn_ds_read_tr16_b64_v4i16((LAS s16x4*)(base + off1));
  return __builtin_bit_cast(bf16x8, __builtin_shufflevector(lo, hi, 0, 1, 2, 3, 4, 5, 6, 7));
}
template <int N> DI void dman(u32 lds, const u32* v, rsrc_t rs, u32 soff) {
  if constexpr (N == 4) asm volatile("s_mov_b32 m0, %0\n\ts_nop 0\n\tbuffer_load_dwordx4 %1, %5, %6 offen lds\n\tbuffer_load_dwordx4 %2, %5, %6 offen offset:1024 lds\n\t"
    "buffer_load_dwordx4 %3, %5, %6 offen offset:2048 lds\n\tbuffer_load_dwordx4 %4, %5, %6 offen offset:3072 lds" ::"s"(lds), "v"(v[0]), "v"(v[1]), "v"(v[2]), "v"(v[3]), "s"(rs), "s"(soff) : "memory");
  if constexpr (N == 3) asm volatile("s_mov_b32 m0, %0\n\ts_nop 0\n\tbuffer_load_dwordx4 %1, %4, %5 offen lds\n\tbuffer_load_dwordx4 %2, %4, %5 offen offset:1024 lds\n\t"
    "buffer_load_dwordx4 %3, %4, %5 offen offset:2048 lds" ::"s"(lds), "v"(v[0]), "v"(v[1]), "v"(v[2]), "s"(rs), "s"(soff) : "memory");
  if constexpr (N == 2) asm volatile("s_mov_b32 m0, %0\n\ts_nop 0\n\tbuffer_load_dwordx4 %1, %3, %4 offen lds\n\tbuffer_load_dwordx4 %2, %3, %4 offen offset:1024 lds" ::"s"(lds), "v"(v[0]), "v"(v[1]), "s"(rs), "s"(soff) : "memory");
}
template <int AUX> DI void st16(u32x4 v, rsrc_t rs, u32 voff) { __builtin_amdgcn_raw_buffer_store_b128(v, rs, voff, 0, AUX); }
DI void st_flag(rsrc_t rc, u32 off, u64 v) { __builtin_amdgcn_raw_buffer_store_b64(u32x2{(u32)v, (u32)(v >> 32)}, rc, off, 0, 16); }
template <int SLEEP> DI void spin(rsrc_t rc, u32 off, u64 tag) {
#pragma unroll 1
  for (u32 n = 0; n < (1u << 17); ++n) {
    asm volatile("" ::: "memory");
    const u32x2 v = __builtin_amdgcn_raw_buffer_load_b64(rc, off, 0, 16);
    if (__all(v[0] == (u32)tag && v[1] == (u32)(tag >> 32))) break;
    __builtin_amdgcn_s_sleep(SLEEP);
  }
  asm volatile("" ::: "memory");
}
DI void lds_wait(LAS char* p, u32 v) {
#pragma unroll 1
  for (u32 n = 0; n < (1u << 20); ++n) {
    if (__builtin_amdgcn_readfirstlane(__hip_atomic_load((LAS u32*)p, __ATOMIC_RELAXED, __HIP_MEMORY_SCOPE_WORKGROUP)) >= v) break;
    __builtin_amdgcn_s_sleep(1);
  }
  asm volatile("" ::: "memory");
}
DI bool last_of4(LAS char* cnt) {
  u32 old = 0;
  if (__builtin_amdgcn_mbcnt_hi(~0u, __builtin_amdgcn_mbcnt_lo(~0u, 0u)) == 0) old = __hip_atomic_fetch_add((LAS u32*)cnt, 1u, __ATOMIC_RELAXED, __HIP_MEMORY_SCOPE_WORKGROUP);
  return __builtin_amdgcn_readfirstlane(old) == 3;
}
#define STAMP(k)

__global__ __launch_bounds__(512, 2) void fused_block(const char* __restrict__ x, const char* __restrict__ wqkv, const char* __restrict__ wo,
                                                      const char* __restrict__ w1, const char* __restrict__ b1, const char* __restrict__ w2,
                                                      const char* __restrict__ b2, char* __restrict__ out, char* ws) {
#define PRIO_C 0
#define PRIO_B 2
  __shared__ __attribute__((aligned(1024))) char smem[163840];
  LAS char* const L = (LAS char*)smem;
  const u32 lb = (u32)(uintptr_t)smem;
  int l = threadIdx.x & 63, g, q4, p4, l15;
  auto lanes = [&] { PIN(l); g = l >> 4; q4 = (l >> 2) & 3; p4 = l & 3; l15 = l & 15; };
  lanes();
  int w = __builtin_amdgcn_readfirstlane(threadIdx.x >> 6);
  asm volatile("s_nop 4" : "+s"(w));
  const int x8 = blockIdx.x & 7, rk = blockIdx.x >> 3;
  const bool busy = rk < 28;
  if (w == 0) STAMP(0);
  const rsrc_t rQKV = __builtin_amdgcn_make_buffer_rsrc(ws + WS_QKV, 0, QKV_BYTES, 0x00020000);
  const rsrc_t rAC = __builtin_amdgcn_make_buffer_rsrc(ws + WS_AC, 0, AC_BYTES, 0x00020000);
  const rsrc_t rX = __builtin_amdgcn_make_buffer_rsrc((void*)x, 0, 2048 * 896, 0x00020000);
  const rsrc_t rW2 = __builtin_amdgcn_make_buffer_rsrc((void*)w2, 0, 1792 * 896, 0x00020000);
  const rsrc_t rCTL = __builtin_amdgcn_make_buffer_rsrc(ws, 0, CTL_BYTES, 0x00020000);
  const int rb = (x8 & 3) * 4 + rk / 7, cb = (x8 >> 2) * 7 + rk % 7;
  const int ai = rk & 15, ah = 2 * x8 + (rk >> 4);
  const int cR = (x8 & 3) * 8 + (x8 >> 2) * 4 + rk / 7, cc = rk % 7;
  const int rot = 0;
  auto rotl = [&](int j) { const int t = j + rot; return t >= 7 ? t - 7 : t; };
  auto kstep = [&](int j, int n) {
    const int t = n % 14 + 2 * rot, u = 14 * j + (t >= 14 ? t - 14 : t);
    return u + (u & ~3) + (n >= 14 ? 4 : 0);
  };
  u64 tag;
  u32x4 xres, bres;
  auto ldres = [&] {
    const int tid = w * 64 + l;
    xres = ld16_async<false>((tid >> 3) * 896 + (tid & 7) * 16, rX, cR * 64 * 896 + cc * 128);
    bres = ld16_async<false>((l & 7) * 16, __builtin_amdgcn_make_buffer_rsrc((void*)b2, 0, 896, 0x00020000), cc * 128);
  };
  auto epoch = [&] { asm volatile("s_load_dwordx2 %0, %1, 0x0\n\ts_waitcnt lgkmcnt(0)" : "=s"(tag) : "s"(ws) : "memory"); tag += 1; };

  if (w < 4) {
    if (busy) {
      epoch();
      constexpr int PF = 4;
      u32 oX[4], oH[2], oQ[3];
#pragma unroll
      for (int p = 0; p < 4; ++p) oX[p] = (4 * w + ((l & 31) >> 3)) * 1024 + (l & 7) * 128 + (((2 * p + (g >> 1)) ^ (l15 >> 1)) << 4);
      const int rq = w == 3 ? 0 : w;
#pragma unroll
      for (int i = 0; i < 3; ++i) oQ[i] = RING + (8 * (g >> 1) + q4) * 192 + ((i + rq) % 3) * 64 + (g & 1) * 32 + p4 * 8;
      const u32 selv = l15 == (g & 1) ? 0x3f803f80u : 0u;
      const bf16x8 sel = __builtin_bit_cast(bf16x8, u32x4{selv, selv, selv, selv});
      bf16x8 xf[28], wf[8];
      f32x16 aH[2], aQ[3] = {};
      f32x4 cs = {}, sx = {};
      float rs;
      bf16x8 lnb;
      const int ks = w >> 1;
      auto rdW = [&](auto SS, auto II) {
        constexpr int s = decltype(SS)::v, i = decltype(II)::v;
        constexpr int slot = (s < 14 ? s * 6 + i : 84 + i) % 8;
        if constexpr (s < 14) { const u32 o = oQ[i % 3] + (stage_addr(s) - RING) + (i / 3) * 3072; wf[slot] = trfrag(L, o, o + 768); }
        else if constexpr (s < 28) { const u32 o = oH[i & 1] + stage_addr(s) + (i >> 1) * 2048; wf[slot] = trfrag(L, o, o + 512); }
      };
      auto rdX = [&](auto SS, auto EE) { constexpr int k = 2 * decltype(SS)::v + decltype(EE)::v; if constexpr (k < 28) xf[k] = lds_ld<bf16x8>(L, oX[k & 3] + (k >> 2) * 16384); };
      auto putcs = [&](u32 base) {
        u32x4 v;
#pragma unroll
        for (int j = 0; j < 4; ++j) { const float hi = rbf(cs[j]); v[j] = pk(hi, cs[j] - hi); }
        if (l15 < 2) lds_st<u32x4>(L, base + (l15 * 16 + 4 * g) * 4, v);
        cs = f32x4{};
      };
      auto hepi = [&](int half) {
        u32 c[2][2], bb[2];
#pragma unroll
        for (int i = 0; i < 2; ++i) {
          const int ct = i ^ (w & 1);
          c[i][0] = lds_ld<u32>(L, L_CSH + half * 512 + (ct * 32 + (l & 31)) * 4); c[i][1] = lds_ld<u32>(L, L_CSH + half * 512 + 256 + (ct * 32 + (l & 31)) * 4);
          bb[i] = lds_ld<unsigned short>(L, L_B1 + (half * 64 + ct * 32 + (l & 31)) * 2);
        }
        SB();
#pragma unroll
        for (int i = 0; i < 2; ++i) aH[i] = mfma32(__builtin_bit_cast(bf16x8, u32x4{c[i][0], c[i][1], bb[i], 0u}), lnb, aH[i]);
        SB();
        const float rs2 = rs * rs;
#pragma unroll
        for (int i = 0; i < 2; ++i)
#pragma unroll
          for (int e = 0; e < 2; ++e) {
            u32x4 hp;
#pragma unroll
            for (int j = 0; j < 4; ++j) {
              const float t0 = aH[i][8 * e + 2 * j], t1 = aH[i][8 * e + 2 * j + 1];
              hp[j] = pk(t0 * rs2 * fmx(t0, 0.f), t1 * rs2 * fmx(t1, 0.f));
            }
            st16<16>(hp, rAC, l * 16 + ((rb * 4 + w) * 144 + cb * 8 + half * 4 + (i ^ (w & 1)) * 2 + e) * 1024);
          }
        SB();
      };
      bar();
      STAMP(1);
      rdX(IC<0>{}, IC<0>{}); rdX(IC<0>{}, IC<1>{});
      sfor<0, PF>([&](auto I) { rdW(IC<0>{}, I); });
      sfor<0, 28>([&](auto SS) {
        constexpr int s = decltype(SS)::v, NT = s < 14 ? 6 : 8;
        if constexpr (s == B_FA) wait_bar<0>();
        else if constexpr (s == B_FQ) wait_bar<(B_FQ > 21 ? 4 : 0)>();
        else if constexpr (s == 13 || s == 14 || s == 21) wait_bar<63>();
        else bar();
        if constexpr (s == 13) {
          lanes();
#pragma unroll
          for (int i = 0; i < 2; ++i) oH[i] = (g >> 1) * 1024 + q4 * 128 + (((i ^ (w & 1)) ^ (q4 >> 1)) << 6) + (g & 1) * 32 + p4 * 8;
        }
        if constexpr (s == 14) {
          STAMP(2);
          const u32x2 st = lds_ld<u32x2>(L, L_ST + (w * 32 + (l & 31)) * 8);
          u32 c[3];
#pragma unroll
          for (int i = 0; i < 3; ++i) c[i] = lds_ld<u32>(L, L_CSQ + (((i + rq) % 3) * 32 + (l & 31)) * 4);
          SB();
          const u32 mm2 = l < 32 ? (st[0] & 0xffffu) * 0x10001u : 0u;
          lnb = __builtin_bit_cast(bf16x8, u32x4{mm2, mm2, l < 32 ? st[0] >> 16 : 0u, 0u});
          rs = __uint_as_float(st[1]);
#pragma unroll
          for (int i = 0; i < 3; ++i) aQ[i] = mfma32(__builtin_bit_cast(bf16x8, u32x4{c[i], 0u, 0u, 0u}), lnb, aQ[i]);
          SB();
          const int fl = ((g & 1) << 1) | (g >> 1);
#pragma unroll
          for (int i = 0; i < 3; ++i)
#pragma unroll
            for (int e = 0; e < 2; ++e) {
              const int tq = 6 * cb + 2 * ((i + rq) % 3) + e;
              const float m = tq < 28 ? rs * (0.18898223650461361f * 1.4426950408889634f) : rs;
              const auto s0 = __builtin_amdgcn_permlane16_swap(pk(aQ[i][8 * e] * m, aQ[i][8 * e + 1] * m), pk(aQ[i][8 * e + 4] * m, aQ[i][8 * e + 5] * m), false, false);
              const auto s1 = __builtin_amdgcn_permlane16_swap(pk(aQ[i][8 * e + 2] * m, aQ[i][8 * e + 3] * m), pk(aQ[i][8 * e + 6] * m, aQ[i][8 * e + 7] * m), false, false);
              st16<16>(tq >= 56 && (fl & 1) ? u32x4{s0[1], s1[1], s0[0], s1[0]} : u32x4{s0[0], s1[0], s0[1], s1[1]}, rQKV,
                       fl * 256 + ((l15 ^ (tq >= 56 ? (2 * (tq & 1) + (fl >> 1)) << 2 : 0)) << 4) + ((rb * 4 + w) * 336 + 4 * tq) * 256);
            }
          SB();
          STAMP(3);
        }
        if constexpr (s == 21) hepi(0);
        sfor<0, NT>([&](auto II) {
          constexpr int i = decltype(II)::v, slot = (s < 14 ? s * 6 + i : 84 + i) % 8;
          if constexpr (i + PF < NT) rdW(SS, IC<i + PF>{});
          else rdW(IC<s + 1>{}, IC<i + PF - NT>{});
          if constexpr (i < 2) rdX(IC<s + 1>{}, II);
          if constexpr (s < 14) {
            aQ[i % 3] = mfma32(wf[slot], xf[2 * s + i / 3], aQ[i % 3]);
            if constexpr (i % 3 == 0) cs = mfma(wf[slot], sel, cs);
            if constexpr (s == 0 && (i == 1 || i == 2)) sx = mfma(xf[i - 1], sel, sx);
            if constexpr (s < 13 && i >= 4) sx = mfma(xf[2 * s + i - 2], sel, sx);
          } else {
            constexpr int k = 4 * ((s - 14) % 7) + (i >> 1);
            aH[i & 1] = mfma32(wf[slot], xf[k], k == 0 ? f32x16{} : aH[i & 1]);
            if constexpr (!(i & 1)) { if (((i >> 1) & 1) == ks) cs = mfma(wf[slot], sel, cs); }
          }
          SB();
        });
        if constexpr (s == 12) { if (l15 < 2) lds_st<f32x4>(L, L_SX + (w * 32 + l15 * 16 + 4 * g) * 4, sx); SB(); }
        if constexpr (s == 13) { putcs(L_CSQ + w * 128); SB(); }
        if constexpr (s == 20 || s == 27) { putcs(L_CSH + (s == 27) * 512 + ks * 256 + (w & 1) * 128); SB(); }
      });
      STAMP(4);
      wait_bar<63>();
      lanes();
      STAMP(5);
      hepi(1);
      const rsrc_t rWo = __builtin_amdgcn_make_buffer_rsrc((void*)wo, 0, 448 * 896, 0x00020000);
      u32x4 hA[36][2];
      const u32 vA[2] = {(u32)l * 16, (u32)l * 16 + 144 * 1024}, ring = L_WC + w * WC_RING;
      u32 vW[2];
      vW[0] = (l >> 3) * 896 + (((l & 7) ^ ((g & 1) << 2)) << 4); vW[1] = vW[0] + 7168 - 1024;
      auto ldA = [&](auto M, int a) {
        constexpr int m = decltype(M)::v;
        hA[m][a] = ld16_async<true>(vA[a], rAC, (cR * 288 + (m < 28 ? kstep(w, m) : 112 + 8 * w + (m - 28))) * 1024);
      };
      auto ldW = [&](auto M) {
        constexpr int m = decltype(M)::v;
        if constexpr (m < 28) dman<2>(lb + ring + (m % RW) * 2048, vW, rW2, kstep(w, m) * (16 * 896) + cc * 128);
        else dman<2>(lb + ring + (m % RW) * 2048, vW, rWo, (28 * (4 * w + ((m - 28) >> 1)) + 16 * (m & 1)) * 896 + cc * 128);
      };
      lds_wait(L + L_CNT + 16, 1u);
      STAMP(6);
      __builtin_amdgcn_s_setprio(PRIO_C);
      sfor<0, DA>([&](auto M) { ldA(M, 0); ldA(M, 1); });
      u32 oW[2];
#pragma unroll
      for (int b = 0; b < 2; ++b) oW[b] = ring + (4 * (g >> 1) + q4) * 128 + ((b ^ (q4 >> 1)) << 6) + (g & 1) * 32 + p4 * 8;
      bf16x8 wc[2][2];
      auto rdC = [&](int m) {
#pragma unroll
        for (int b = 0; b < 2; ++b) wc[m & 1][b] = trfrag(L, oW[b] + (m % RW) * 2048, oW[b] + (m % RW) * 2048 + 1024);
      };
      f32x16 acc[2][2] = {};
      u32x2 polled;
      const u32 vFB = WS_FB + (cR >> 1) * 128 + (4 * w + p4) * 8, vFG = WS_FG + (cR >> 1) * 128 + ((14 * w) / 4 + p4) * 8;
      rdC(0);
      sfor<0, 36>([&](auto N) {
        constexpr int n = decltype(N)::v;
        asm volatile("s_waitcnt vmcnt(%2)" : "+v"(hA[n][0]), "+v"(hA[n][1]) : "n"(SC.wC[n]) : "memory");
        if constexpr (n == 0) { if (last_of4(L + L_CNT) && l == 0) st_flag(rCTL, WS_FG + rb * 128 + cb * 8, tag); }
        if constexpr (n == TB - 2 || n == TO - 2) polled = ld8c_async(n == TB - 2 ? vFG : vFB, rCTL);
        if constexpr (n == TB || n == TO) {
          PIN(polled);
          if (!__all(polled[0] == (u32)tag && polled[1] == (u32)(tag >> 32))) spin<1>(rCTL, n == TB ? vFG : vFB, tag);
        }
        if constexpr (n + 1 < 36) rdC(n + 1);
        SB();
        auto mm = [&](int a, int b) { acc[a][b] = mfma32(wc[n & 1][b], __builtin_bit_cast(bf16x8, hA[n][a]), acc[a][b]); SB(); };
        mm(0, 0);
        if constexpr (SC.a0[n] < SC.a1[n]) { ldA(IC<SC.a0[n] % 36>{}, 0); mm(0, 1); ldA(IC<SC.a0[n] % 36>{}, 1); } else mm(0, 1);
        if constexpr (SC.a0[n] + 1 < SC.a1[n]) { ldA(IC<(SC.a0[n] + 1) % 36>{}, 0); ldA(IC<(SC.a0[n] + 1) % 36>{}, 1); }
        mm(1, 0);
        if constexpr (n >= 1 && n + RW - 1 < 36) ldW(IC<n + RW - 1>{});
        mm(1, 1);
        if constexpr (n == 33) ldres();
      });
      STAMP(10);
#pragma unroll
      for (int a = 0; a < 2; ++a)
#pragma unroll
        for (int b = 0; b < 2; ++b)
#pragma unroll
          for (int k = 0; k < 4; ++k)
            lds_st<f32x4>(L, ring + (a * 32 + (l & 31)) * RED_PITCH + (b * 32 + k * 8 + 4 * (g >> 1)) * 4,
                          f32x4{acc[a][b][4 * k], acc[a][b][4 * k + 1], acc[a][b][4 * k + 2], acc[a][b][4 * k + 3]});
      asm volatile("s_waitcnt vmcnt(0)" : "+v"(xres), "+v"(bres)::"memory");
    } else bar();
  } else {
    const int li = w & 1;
    if (!busy) {
      epoch();
      if (w == 6 && l < 8) lds_st<u32>(L, L_CNT + l * 4, 0u);
      wait_bar<63>();
    } else if (w < 6) {
      const rsrc_t rW1 = __builtin_amdgcn_make_buffer_rsrc((void*)w1, 0, 448 * 3584, 0x00020000);
      const rsrc_t rWq = __builtin_amdgcn_make_buffer_rsrc((void*)wqkv, 0, 448 * 2688, 0x00020000);
      u32 vX[4], vH[4], vQ[3], vC[2], so7[7];
#pragma unroll
      for (int j = 0; j < 4; ++j) {
        vX[j] = (8 * j + (l >> 3)) * 896 + (((l & 7) ^ ((j & 1) * 4 + g)) << 4) - 1024 * j;
      }
#pragma unroll
      for (int j = 0; j < 3; ++j) { const int n = 64 * j + l; vQ[j] = (n / 12) * 2688 + (n % 12) * 16 - 1024 * j; }
#pragma unroll
      for (int j = 0; j < 7; ++j) so7[j] = rotl(j);
      const u32 sX = (rb * 128 + 64 * li) * 896, sH = cb * 256 + 32 * li * 3584, sQ = cb * 192 + 16 * li * 2688;
      auto issue = [&](auto O) {
        constexpr int op = decltype(O)::v, k = op / 256, a = (op % 256) / 2, e = op & 1;
        if constexpr (k == K_X) dman<4>(lb + (a * 16 + 8 * li + 4 * e) * 1024, vX, rX, sX + so7[a] * 128 + e * 28672);
        if constexpr (k == K_WA && a < 14) dman<3>(lb + stage_addr(a) + li * 3072, vQ, rWq, sQ + so7[a >> 1] * (64 * 2688) + (a & 1) * (32 * 2688));
        if constexpr (k == K_WA && a >= 14) dman<4>(lb + stage_addr(a) + li * 4096, vH, rW1, sH + so7[(a - 14) % 7] * (64 * 3584) + (a >= 21 ? 128 : 0));
        if constexpr (k == K_BIAS) dma4(lb + L_B1, cb * 256 + l * 4, __builtin_amdgcn_make_buffer_rsrc((void*)b1, 0, 3584, 0x00020000), 0);
        if constexpr (k == K_WCP) dman<2>(lb + L_WC + (2 * e + li) * WC_RING + a * 2048, vC, rW2, kstep(2 * e + li, a) * (16 * 896) + cc * 128);
      };
      sfor<0, NPRO>([&](auto K) { issue(IC<SC.pro[decltype(K)::v]>{}); });
      epoch();
#pragma unroll
      for (int j = 0; j < 4; ++j) vH[j] = (8 * j + (l >> 3)) * 3584 + (((l & 7) ^ ((g & 1) << 2)) << 4) - 1024 * j;
      vC[0] = (l >> 3) * 896 + (((l & 7) ^ ((g & 1) << 2)) << 4); vC[1] = vC[0] + 7168 - 1024;
      vm_bar<SC.wA[0]>();
      sfor<0, 28>([&](auto SS) {
        constexpr int s = decltype(SS)::v;
        vm_bar<SC.wA[s + 1]>();
        sfor<0, MAXO>([&](auto K) { issue(IC<SC.opA[s][decltype(K)::v]>{}); });
      });
      vm_bar<0>();
    } else {
      epoch();
      u32 oX[4];
#pragma unroll
      for (int p = 0; p < 4; ++p) oX[p] = (8 * li + ((l & 31) >> 3)) * 1024 + (l & 7) * 128 + (((2 * p + (g >> 1)) ^ (l15 >> 1)) << 4);
      float s2[2][2] = {};
      u32x4 xv[2][4];
      sfor<0, 15>([&](auto SS) {
        constexpr int s = decltype(SS)::v;
        wait_bar<63>();
        if constexpr (s < 14) {
#pragma unroll
          for (int k = 0; k < 4; ++k) xv[s & 1][k] = lds_ld<u32x4>(L, oX[(2 * s + (k >> 1)) & 3] + (s >> 1) * 16384 + (k & 1) * 4096);
        }
        SB();
        auto sq = [&](int p) {
#pragma unroll
          for (int k = 0; k < 4; ++k)
#pragma unroll
            for (int j = 0; j < 4; ++j) s2[k & 1][j & 1] = dot2(xv[p][k][j], xv[p][k][j], s2[k & 1][j & 1]);
          PIN(s2[0][0]); PIN(s2[0][1]); PIN(s2[1][0]); PIN(s2[1][1]);
        };
        if constexpr (s >= 1 && s < 14) sq(~s & 1);
        if constexpr (s == 13) sq(1);
        SB();
      });
#pragma unroll
      for (int r = 0; r < 2; ++r) {
        const u32 q2 = __float_as_uint(s2[r][0] + s2[r][1]);
        const auto b = __builtin_amdgcn_permlane32_swap(q2, q2, false, false);
        const float mean = lds_ld<float>(L, L_SX + ((2 * li + r) * 32 + (l & 31)) * 4) * (1.f / 448.f);
        const float var = fmaxf((__uint_as_float(b[0]) + __uint_as_float(b[1])) * (1.f / 448.f) - mean * mean, 0.f);
        const float sd = rbf(__builtin_amdgcn_sqrtf(rbf(rbf(var) + 1e-5f)));
        if (l < 32) lds_st<u32x2>(L, L_ST + ((2 * li + r) * 32 + l) * 8, u32x2{pk(-mean, sd), __float_as_uint(__builtin_amdgcn_rcpf(sd))});
      }
      if (w == 6 && l < 8) lds_st<u32>(L, L_CNT + l * 4, 0u);
      wait_bar<63>();
#pragma unroll 1
      for (int s = 15; s < 29; ++s) {
        bar();
        if (w == 7 && l == 0 && (s == B_FQ || s == B_FA)) st_flag(rCTL, (s == B_FQ ? WS_FQ : WS_FH) + rb * 128 + cb * 8, tag);
      }
      if (w == 7) {
        spin<1>(rCTL, WS_FH + (cR >> 1) * 128 + (l < 14 ? l : 0) * 8, tag);
        __hip_atomic_store((LAS u32*)(L + L_CNT + 16), 1u, __ATOMIC_RELAXED, __HIP_MEMORY_SCOPE_WORKGROUP);
      }
    }
    lanes();
    const int aw = w - 4;
    __builtin_amdgcn_s_setprio(PRIO_B);
#pragma unroll
    for (int k = 0; k < 2; ++k) if (k == 0 || l < 32) lds_st<u32x2>(L, L_V + (3 * aw + 2 * k + (l >> 5)) * 2048 + 1792 + (l & 31) * 8, u32x2{0x3f80u, 0u});
    if (w == 6) {
      const int e = l & 1, k = l < 14 ? l >> 1 : 0;
      const int ty = k == 0 ? 0 : (k < 4 ? 1 : 2), ro = k == 0 ? 0 : (k < 4 ? k - 1 : k - 4);
      const int rr = ai - ro < 0 ? 0 : ai - ro;
      spin<4>(rCTL, WS_FQ + rr * 128 + ((ty * 448 + 28 * ah + 27 * e) / 96) * 8, tag);
      __hip_atomic_store((LAS u32*)(L + L_CNT + 12), 1u, __ATOMIC_RELAXED, __HIP_MEMORY_SCOPE_WORKGROUP);
    } else lds_wait(L + L_CNT + 12, 1u);
    if (w == 4) STAMP(7);
#pragma unroll
    for (int j = 0; j < 6; ++j) {
      const int b = 3 * aw + (j >> 1), tb = 4 * ai - 8 + b < 0 ? 0 : 4 * ai - 8 + b;
      const u32 so = (tb * 336 + 112 + 112 * (j & 1) + 7 * ah) * 256, d = lb + ((j & 1) ? L_V + b * 2048 : L_K + b * 1792);
      dma16<true>(d, l * 16, rQKV, so);
      if (l < 48) dma16<true>(d + 1024, l * 16, rQKV, so + 1024);
    }
    const int hh = g >> 1;
    u32x2 qv[4];
#pragma unroll
    for (int k = 0; k < 4; ++k) qv[k] = ld8c_async(k == 3 && hh ? 0xfffffff0u : ((4 * ai + aw) * 336 + 7 * ah + 2 * k + hh) * 256 + l15 * 16 + (g & 1) * 8, rQKV);
    asm volatile("s_waitcnt vmcnt(0) lgkmcnt(0)" : "+v"(qv[0]), "+v"(qv[1]), "+v"(qv[2]), "+v"(qv[3])::"memory");
    if (!last_of4(L + L_CNT + 4)) lds_wait(L + L_CNT + 4, 4u);
    asm volatile("" ::: "memory");
    if (w == 4) STAMP(8);
    const bf16x8 qf[2] = {__builtin_bit_cast(bf16x8, u32x4{qv[0][0], qv[0][1], qv[1][0], qv[1][1]}), __builtin_bit_cast(bf16x8, u32x4{qv[2][0], qv[2][1], qv[3][0], qv[3][1]})};
    const u32 oK = L_K + aw * 1792 + hh * 256 + l15 * 16 + (g & 1) * 8, oK3 = hh ? oK + 1280 : oK + 1536;
    const int F = 7 * ah + 4 * (g & 1) + p4;
    u32 oV[2][2];
#pragma unroll
    for (int t = 0; t < 2; ++t)
#pragma unroll
      for (int e = 0; e < 2; ++e) oV[t][e] = L_V + aw * 2048 + (4 * (g & 1) + p4) * 256 + (((8 * e + 4 * hh + q4) ^ ((F & 6) << 1)) << 4) + ((t ^ (F & 1)) << 3);
    f32x16 sc[9];
    bf16x8 kf[9][2], vf[9][2];
    auto rdK = [&](int m) {
      const u32x2 a = lds_ld<u32x2>(L, oK + m * 1792), b = lds_ld<u32x2>(L, oK + m * 1792 + 512), c = lds_ld<u32x2>(L, oK + m * 1792 + 1024), d = lds_ld<u32x2>(L, oK3 + m * 1792);
      kf[m][0] = __builtin_bit_cast(bf16x8, u32x4{a[0], a[1], b[0], b[1]}); kf[m][1] = __builtin_bit_cast(bf16x8, u32x4{c[0], c[1], d[0], d[1]});
    };
    auto rdV = [&](int m) { vf[m][0] = trfrag(L, oV[0][0] + m * 2048, oV[0][1] + m * 2048); vf[m][1] = trfrag(L, oV[1][0] + m * 2048, oV[1][1] + m * 2048); };
    rdK(0); SB(); rdK(8); SB(); rdK(1); SB();
#pragma unroll
    for (int i = 0; i < 9; ++i) {
      const int m = i == 0 ? 0 : (i == 1 ? 8 : i - 1);
      if (i + 2 < 8) rdK(i + 2); else if (i >= 7) rdV(i - 6);
      sc[m] = mfma32(kf[m][1], qf[1], mfma32(kf[m][0], qf[0], f32x16{}));
      SB();
    }
    constexpr float NEG = -1e30f;
    if (ai < 2) {
      asm volatile("" ::: "memory");
      const int m0 = 8 - 4 * ai - aw;
#pragma unroll
      for (int m = 0; m < 8; ++m)
        if (m < m0) {
#pragma unroll
          for (int r = 0; r < 16; ++r) sc[m][r] = NEG;
        }
    }
    const int dq = (l & 31) - 4 * hh;
    u32 mk[8];
#pragma unroll
    for (int r = 0; r < 16; r += 2) {
      const bool c0 = (r & 3) + 8 * (r >> 2) <= dq, c1 = (r & 3) + 1 + 8 * (r >> 2) <= dq;
      sc[0][r] = c0 ? sc[8][r] : sc[0][r]; sc[0][r + 1] = c1 ? sc[8][r + 1] : sc[0][r + 1];
      mk[r >> 1] = c1 ? 0xffffffffu : (c0 ? 0xffffu : 0u);
    }
    float mx0 = fmx(sc[0][0], sc[0][1]), mx1 = fmx(sc[0][2], sc[0][3]);
#pragma unroll
    for (int k = 1; k < 32; ++k) { mx0 = fmx(fmx(mx0, sc[k >> 2][4 * (k & 3)]), sc[k >> 2][4 * (k & 3) + 1]); mx1 = fmx(fmx(mx1, sc[k >> 2][4 * (k & 3) + 2]), sc[k >> 2][4 * (k & 3) + 3]); }
    mx0 = fmx(mx0, mx1);
    const auto ms = __builtin_amdgcn_permlane32_swap(__float_as_uint(mx0), __float_as_uint(mx0), false, false);
    const float mx = fmx(__uint_as_float(ms[0]), __uint_as_float(ms[1]));
    SB();
    f32x16 o = {};
#pragma unroll
    for (int i = 0; i < 8; ++i) {
      const int m = (i + 1) & 7;
      if (i < 5) rdV(i + 3); else if (i == 5) rdV(0); else if (i == 6) rdV(8);
      u32x4 p[2];
#pragma unroll
      for (int j = 0; j < 8; ++j) {
        f32x2 d = f32x2{sc[m][2 * j], sc[m][2 * j + 1]} - f32x2{mx, mx};
        PIN(d);
        p[j >> 2][j & 3] = pk(__builtin_amdgcn_exp2f(d[0]), __builtin_amdgcn_exp2f(d[1]));
      }
#pragma unroll
      for (int s = 0; s < 2; ++s) {
        if (m) o = mfma32(vf[m][s], __builtin_bit_cast(bf16x8, p[s]), o);
        else {
          const u32x4 k4 = {mk[4 * s], mk[4 * s + 1], mk[4 * s + 2], mk[4 * s + 3]};
          o = mfma32(vf[0][s], __builtin_bit_cast(bf16x8, p[s] & ~k4), o);
          o = mfma32(vf[8][s], __builtin_bit_cast(bf16x8, p[s] & k4), o);
        }
      }
      SB();
    }
    const auto dn = __builtin_amdgcn_permlane32_swap(__float_as_uint(o[12]), __float_as_uint(o[12]), false, false);
    const float inv = __builtin_amdgcn_rcpf(__uint_as_float(dn[1]));
    const float in2 = hh ? 0.f : inv;
    const u32 so = ((ai * 4 + aw) * 144 + 112 + 2 * ah) * 1024;
    st16<16>(u32x4{pk(o[0] * inv, o[1] * inv), pk(o[2] * inv, o[3] * inv), pk(o[4] * inv, o[5] * inv), pk(o[6] * inv, o[7] * inv)}, rAC, l * 16 + so);
    st16<16>(u32x4{pk(o[8] * inv, o[9] * inv), pk(o[10] * inv, o[11] * inv), pk(o[12] * in2, o[13] * in2), pk(o[14] * in2, o[15] * in2)}, rAC, l * 16 + so + 1024);
    if (w == 4) STAMP(9);
    asm volatile("s_waitcnt vmcnt(0)" ::: "memory");
    if (last_of4(L + L_CNT + 8)) {
      if (l == 0) st_flag(rCTL, WS_FB + ai * 128 + ah * 8, tag);
      if (blockIdx.x == 255) {
#pragma unroll 1
        for (int k = 0; k < 4; ++k) spin<8>(rCTL, WS_FB + (k * 64 + l) * 8, tag);
        if (l == 0) st_flag(rCTL, 0, tag);
      }
    }
    if (busy) { ldres(); asm volatile("s_waitcnt vmcnt(0)" : "+v"(xres), "+v"(bres)::"memory"); }
  }
  if (busy) {
    wait_bar<63>();
    lanes();
    const int tid = w * 64 + l, row = tid >> 3, c8 = tid & 7;
    f32x4 r0 = {bflo(xres[0]) + bflo(bres[0]), bfhi(xres[0]) + bfhi(bres[0]), bflo(xres[1]) + bflo(bres[1]), bfhi(xres[1]) + bfhi(bres[1])};
    f32x4 r1 = {bflo(xres[2]) + bflo(bres[2]), bfhi(xres[2]) + bfhi(bres[2]), bflo(xres[3]) + bflo(bres[3]), bfhi(xres[3]) + bfhi(bres[3])};
#pragma unroll
    for (int j = 0; j < 4; ++j) {
      r0 += lds_ld<f32x4>(L, L_WC + j * WC_RING + row * RED_PITCH + c8 * 32);
      r1 += lds_ld<f32x4>(L, L_WC + j * WC_RING + row * RED_PITCH + c8 * 32 + 16);
    }
    st16<0>(u32x4{pk(r0[0], r0[1]), pk(r0[2], r0[3]), pk(r1[0], r1[1]), pk(r1[2], r1[3])},
                                           __builtin_amdgcn_make_buffer_rsrc(out, 0, 2048 * 896, 0x00020000), row * 896 + c8 * 16 + cR * 64 * 896 + cc * 128);
  }
  if (w == 0) STAMP(11);
}

extern "C" void kernel_launch(void* const* d_in, const int* in_sizes, int n_in, void* d_out, int out_size, void* d_ws, size_t ws_size, hipStream_t stream) {
  fused_block<<<256, 512, 0, stream>>>((const char*)d_in[0], (const char*)d_in[1], (const char*)d_in[2], (const char*)d_in[4], (const char*)d_in[5],
                                       (const char*)d_in[6], (const char*)d_in[7], (char*)d_out, (char*)d_ws + (ws_size - (16u  __H0__ )));
}
```

```cpp
#define NOROT
#include <hip/hip_runtime.h>
#include <hip/hip_bf16.h>
#include <stdint.h>

#define DI __device__ __forceinline__
#define LAS __attribute__((address_space(3)))
#define GAS __attribute__((address_space(1)))
typedef unsigned u32;
typedef unsigned long long u64;
typedef __bf16 bf16x8 __attribute__((ext_vector_type(8)));
typedef __bf16 bf16x2 __attribute__((ext_vector_type(2)));
typedef short s16x4 __attribute__((ext_vector_type(4)));
typedef float f32x4 __attribute__((ext_vector_type(4)));
typedef float f32x2 __attribute__((ext_vector_type(2)));
typedef float f32x16 __attribute__((ext_vector_type(16)));
typedef unsigned u32x4 __attribute__((ext_vector_type(4)));
typedef unsigned u32x2 __attribute__((ext_vector_type(2)));
typedef __amdgpu_buffer_rsrc_t rsrc_t;

template <int N> struct IC { static constexpr int v = N; };
template <int B, int E, class F> DI void sfor(F&& f) { if constexpr (B < E) { f(IC<B>{}); sfor<B + 1, E>(f); } }

constexpr u32 WS_FH = 256, WS_FQ = WS_FH + 2048, WS_FB = WS_FQ + 2048, WS_FG = WS_FB + 2048, CTL_BYTES = 16384;
constexpr u32 WS_QKV = 16384, QKV_BYTES = 64u * 336u * 256u;
constexpr u32 WS_AC = WS_QKV + QKV_BYTES, AC_BYTES = 128u * 72u * 1024u;
constexpr u32 SLOT = 16384, RING = 7 * SLOT;
constexpr u32 L_SX = 45056, L_CSQ = 45568, L_B1 = 46080, L_CSH = 46336, L_ST = 47360, L_CNT = 48384;
constexpr u32 L_K = 0, L_V = 21504;
constexpr int RW = 12;
constexpr u32 L_WC = 49152, WC_RING = RW * 2048, RED_PITCH = 272;
constexpr u32 stage_addr(int s) { return s <= 20 ? RING + (s % 6) * 8192u : (s <= 25 ? (s - 21) * 8192u : RING + (s - 22) * 8192u); }

enum { K_NONE = 0, K_X, K_WA, K_BIAS, K_WCP };
constexpr int OP(int k, int a, int e = 0) { return k * 256 + 2 * a + e; }
constexpr int opn(int op) { int k = op / 256, a = (op % 256) / 2; return k == K_NONE ? 0 : k == K_X ? 4 : k == K_WA ? (a < 14 ? 3 : 4) : k == K_WCP ? 2 : 1; }
#define CAP_1 8
#define CAP_2 8
#define CAP_E 20
#define P_DA 8
#define P_TB 11
#define P_TO 25
#define P_FQ 23
#define P_FA 27
constexpr int MAXO = 12, NPRO = 8, DA = P_DA, TB = P_TB, TO = P_TO, B_FQ = P_FQ, B_FA = P_FA;
struct Sched {
  int pro[NPRO], opA[28][MAXO];
  int wA[29];
  int wC[36], a0[36], a1[36];
  int ok;
};
constexpr int cl(int n) { return n > 63 ? 63 : n; }
constexpr int free_at(u32 b) {
  return b < RING ? 2 * (int)(b / SLOT) + 1 : (int[6]){19, 20, 21, 16, 28, 28}[(b - RING) / 8192];
}
constexpr Sched make_sched() {
  Sched r{};
  r.ok = 1;
  int n = 0, iW[28] = {}, iX[7] = {}, iB = 0;
  int lo[128] = {}, le[128] = {}, nl = 0;
  for (int s = 0; s < 28; ++s) {
    if (s < 14 && !(s & 1)) { lo[nl] = OP(K_X, s >> 1, 0); le[nl++] = -1; lo[nl] = OP(K_X, s >> 1, 1); le[nl++] = -1; }
    lo[nl] = OP(K_WA, s); le[nl++] = s <= 20 ? s - 5 : (s <= 25 ? free_at(stage_addr(s) + 8191) : s - 9);
    if (s == 17) { lo[nl] = OP(K_BIAS, 0); le[nl++] = 5; }
  }
  for (int k = 0; k < RW; ++k) for (int j = 0; j < 2; ++j) {
    int f = 0;
    for (int e = 0; e < 4; ++e) { const int t = free_at(L_WC + (2 * j + (e >> 1)) * WC_RING + k * 2048 + (e & 1) * 2047); if (t > f) f = t; }
    lo[nl] = OP(K_WCP, k, j); le[nl++] = f;
  }
  bool done[128] = {};
  auto note = [&](int op) { n += opn(op); int k = op / 256, a = (op % 256) / 2; if (k == K_X) iX[a] = (op & 1) ? n : 0; if (k == K_WA) iW[a] = n; if (k == K_BIAS) iB = n; };
  for (int k = 0; k < NPRO; ++k) { r.pro[k] = lo[k]; done[k] = true; note(lo[k]); }
  r.wA[0] = cl(n - iW[0]);
  for (int s = 0; s < 28; ++s) {
    int need = 0;
    if (s + 1 < 28) {
      need = iW[s + 1]; if (!need) r.ok = 0;
      if (s + 1 <= 13) { int v = iX[(s + 1) >> 1]; if (!v) r.ok = 0; if (v > need) need = v; }
      if (s == 21) { if (!iB) r.ok = 0; if (iB > need) need = iB; }
    }
    r.wA[s + 1] = need ? cl(n - need) : 63;
    int cap = s == 14 || s == 21 ? CAP_E : (s < 14 ? CAP_1 : CAP_2), c = 0, used = 0;
    for (int k = 0; k < nl; ++k) if (!done[k] && le[k] <= s && used + opn(lo[k]) <= cap && c < MAXO) { done[k] = true; r.opA[s][c++] = lo[k]; used += opn(lo[k]); note(lo[k]); }
  }
  for (int k = 0; k < nl; ++k) if (!done[k]) r.ok = 0;
  n = 4;
  int iA[36] = {}, iWc[36] = {}, iP = 0, nA = DA;
  for (int m = 0; m < DA; ++m) { n += 2; iA[m] = n; }
  for (int s = 0; s < 36; ++s) {
    int need = iA[s]; if (!need) r.ok = 0;
    if (s + 1 < 36 && s + 1 >= RW) { if (!iWc[s + 1]) r.ok = 0; if (iWc[s + 1] > need) need = iWc[s + 1]; }
    if ((s == TB || s == TO) && iP > need) need = iP;
    r.wC[s] = cl(n - need);
    if (s == TB - 2 || s == TO - 2) { n += 1; iP = n; }
    r.a0[s] = nA;
    for (int c = 0; c < 2 && nA < 36 && nA <= s + DA && (nA < 14 || s >= TB) && (nA < 28 || s >= TO); ++c) { n += 2; iA[nA++] = n; }
    r.a1[s] = nA;
    if (s >= 1 && s + RW - 1 < 36) { n += 2; iWc[s + RW - 1] = n; }
    if (s == 33) n += 2;
  }
  return r;
}
constexpr Sched SC = make_sched();
static_assert(SC.ok, "schedule");

template <bool C> DI void dma16(u32 lds, u32 voff, rsrc_t rs, u32 soff) {
  if constexpr (C) asm volatile("s_mov_b32 m0, %0\n\ts_nop 0\n\tbuffer_load_dwordx4 %1, %2, %3 offen sc1 lds" ::"s"(lds), "v"(voff), "s"(rs), "s"(soff) : "memory");
  else asm volatile("s_mov_b32 m0, %0\n\ts_nop 0\n\tbuffer_load_dwordx4 %1, %2, %3 offen lds" ::"s"(lds), "v"(voff), "s"(rs), "s"(soff) : "memory");
}
template <bool C> DI u32x4 ld16_async(u32 voff, rsrc_t rs, u32 soff) {
  u32x4 r;
  if constexpr (C) asm volatile("buffer_load_dwordx4 %0, %1, %2, %3 offen sc1" : "=v"(r) : "v"(voff), "s"(rs), "s"(soff) : "memory");
  else asm volatile("buffer_load_dwordx4 %0, %1, %2, %3 offen" : "=v"(r) : "v"(voff), "s"(rs), "s"(soff) : "memory");
  return r;
}
DI void dma4(u32 lds, u32 voff, rsrc_t rs, u32 soff) {
  asm volatile("s_mov_b32 m0, %0\n\ts_nop 0\n\tbuffer_load_dword %1, %2, %3 offen lds" ::"s"(lds), "v"(voff), "s"(rs), "s"(soff) : "memory");
}
DI u32x2 ld8c_async(u32 voff, rsrc_t rs) {
  u32x2 r; asm volatile("buffer_load_dwordx2 %0, %1, %2, 0 offen sc1" : "=v"(r) : "v"(voff), "s"(rs) : "memory"); return r;
}
template <int N> DI void wait_bar() { asm volatile("s_waitcnt vmcnt(%0) lgkmcnt(0)\n\ts_barrier" ::"n"(N) : "memory"); }
template <int N> DI void vm_bar() { asm volatile("s_waitcnt vmcnt(%0)\n\ts_barrier" ::"n"(N) : "memory"); }
DI void bar() { asm volatile("s_barrier" ::: "memory"); }
#define PIN(x) asm volatile("" : "+v"(x))
#define SB() __builtin_amdgcn_sched_barrier(0)
DI f32x4 mfma(bf16x8 a, bf16x8 b, f32x4 c) { return __builtin_amdgcn_mfma_f32_16x16x32_bf16(a, b, c, 0, 0, 0); }
DI f32x16 mfma32(bf16x8 a, bf16x8 b, f32x16 c) { return __builtin_amdgcn_mfma_f32_32x32x16_bf16(a, b, c, 0, 0, 0); }
DI u32 pk(float a, float b) { bf16x2 v = {(__bf16)a, (__bf16)b}; return __builtin_bit_cast(u32, v); }
DI float fmx(float a, float b) { return __builtin_elementwise_maximum(a, b); }
DI float rbf(float a) { return (float)(__bf16)a; }
DI float bflo(u32 v) { return __uint_as_float(v << 16); }
DI float bfhi(u32 v) { return __uint_as_float(v & 0xffff0000u); }
DI float dot2(u32 a, u32 b, float c) { return __builtin_amdgcn_fdot2_f32_bf16(__builtin_bit_cast(bf16x2, a), __builtin_bit_cast(bf16x2, b), c, false); }
template <class T> DI T lds_ld(const LAS char* base, u32 off) { return *(const LAS T*)(base + off); }
template <class T> DI void lds_st(LAS char* base, u32 off, T v) { *(LAS T*)(base + off) = v; }
DI bf16x8 trfrag(LAS char* base, u32 off0, u32 off1) {
  s16x4 lo = __builtin_amdgcn_ds_read_tr16_b64_v4i16((LAS s16x4*)(base + off0));
  s16x4 hi = __builtin_amdgcn_ds_read_tr16_b64_v4i16((LAS s16x4*)(base + off1));
  return __builtin_bit_cast(bf16x8, __builtin_shufflevector(lo, hi, 0, 1, 2, 3, 4, 5, 6, 7));
}
template <int N> DI void dman(u32 lds, const u32* v, rsrc_t rs, u32 soff) {
  if constexpr (N == 4) asm volatile("s_mov_b32 m0, %0\n\ts_nop 0\n\tbuffer_load_dwordx4 %1, %5, %6 offen lds\n\tbuffer_load_dwordx4 %2, %5, %6 offen offset:1024 lds\n\t"
    "buffer_load_dwordx4 %3, %5, %6 offen offset:2048 lds\n\tbuffer_load_dwordx4 %4, %5, %6 offen offset:3072 lds" ::"s"(lds), "v"(v[0]), "v"(v[1]), "v"(v[2]), "v"(v[3]), "s"(rs), "s"(soff) : "memory");
  if constexpr (N == 3) asm volatile("s_mov_b32 m0, %0\n\ts_nop 0\n\tbuffer_load_dwordx4 %1, %4, %5 offen lds\n\tbuffer_load_dwordx4 %2, %4, %5 offen offset:1024 lds\n\t"
    "buffer_load_dwordx4 %3, %4, %5 offen offset:2048 lds" ::"s"(lds), "v"(v[0]), "v"(v[1]), "v"(v[2]), "s"(rs), "s"(soff) : "memory");
  if constexpr (N == 2) asm volatile("s_mov_b32 m0, %0\n\ts_nop 0\n\tbuffer_load_dwordx4 %1, %3, %4 offen lds\n\tbuffer_load_dwordx4 %2, %3, %4 offen offset:1024 lds" ::"s"(lds), "v"(v[0]), "v"(v[1]), "s"(rs), "s"(soff) : "memory");
}
template <int AUX> DI void st16(u32x4 v, rsrc_t rs, u32 voff) { __builtin_amdgcn_raw_buffer_store_b128(v, rs, voff, 0, AUX); }
DI void st_flag(rsrc_t rc, u32 off, u64 v) { __builtin_amdgcn_raw_buffer_store_b64(u32x2{(u32)v, (u32)(v >> 32)}, rc, off, 0, 16); }
template <int SLEEP> DI void spin(rsrc_t rc, u32 off, u64 tag) {
#pragma unroll 1
  for (u32 n = 0; n < (1u << 17); ++n) {
    asm volatile("" ::: "memory");
    const u32x2 v = __builtin_amdgcn_raw_buffer_load_b64(rc, off, 0, 16);
    if (__all(v[0] == (u32)tag && v[1] == (u32)(tag >> 32))) break;
    __builtin_amdgcn_s_sleep(SLEEP);
  }
  asm volatile("" ::: "memory");
}
DI void lds_wait(LAS char* p, u32 v) {
#pragma unroll 1
  for (u32 n = 0; n < (1u << 20); ++n) {
    if (__builtin_amdgcn_readfirstlane(__hip_atomic_load((LAS u32*)p, __ATOMIC_RELAXED, __HIP_MEMORY_SCOPE_WORKGROUP)) >= v) break;
    __builtin_amdgcn_s_sleep(1);
  }
  asm volatile("" ::: "memory");
}
DI bool last_of4(LAS char* cnt) {
  u32 old = 0;
  if (__builtin_amdgcn_mbcnt_hi(~0u, __builtin_amdgcn_mbcnt_lo(~0u, 0u)) == 0) old = __hip_atomic_fetch_add((LAS u32*)cnt, 1u, __ATOMIC_RELAXED, __HIP_MEMORY_SCOPE_WORKGROUP);
  return __builtin_amdgcn_readfirstlane(old) == 3;
}
#define STAMP(k)

__global__ __launch_bounds__(512, 2) void fused_block(const char* __restrict__ x, const char* __restrict__ wqkv, const char* __restrict__ wo,
                                                      const char* __restrict__ w1, const char* __restrict__ b1, const char* __restrict__ w2,
                                                      const char* __restrict__ b2, char* __restrict__ out, char* ws) {
#define PRIO_C 0
#define PRIO_B 2
  __shared__ __attribute__((aligned(1024))) char smem[163840];
  u64 tent; asm volatile("s_memtime %0\n\ts_waitcnt lgkmcnt(0)" : "=s"(tent)::"memory");
  LAS char* const L = (LAS char*)smem;
  const u32 lb = (u32)(uintptr_t)smem;
  int l = threadIdx.x & 63, g, q4, p4, l15;
  auto lanes = [&] { PIN(l); g = l >> 4; q4 = (l >> 2) & 3; p4 = l & 3; l15 = l & 15; };
  lanes();
  int w = __builtin_amdgcn_readfirstlane(threadIdx.x >> 6);
  asm volatile("s_nop 4" : "+s"(w));
  const int x8 = blockIdx.x & 7, rk = blockIdx.x >> 3;
  const bool busy = rk < 28;
  if (w == 0) STAMP(0);
  const rsrc_t rQKV = __builtin_amdgcn_make_buffer_rsrc(ws + WS_QKV, 0, QKV_BYTES, 0x00020000);
  const rsrc_t rAC = __builtin_amdgcn_make_buffer_rsrc(ws + WS_AC, 0, AC_BYTES, 0x00020000);
  const rsrc_t rX = __builtin_amdgcn_make_buffer_rsrc((void*)x, 0, 2048 * 896, 0x00020000);
  const rsrc_t rW2 = __builtin_amdgcn_make_buffer_rsrc((void*)w2, 0, 1792 * 896, 0x00020000);
  const rsrc_t rCTL = __builtin_amdgcn_make_buffer_rsrc(ws, 0, CTL_BYTES, 0x00020000);
  const int rb = (x8 & 3) * 4 + rk / 7, cb = (x8 >> 2) * 7 + rk % 7;
  const int ai = rk & 15, ah = 2 * x8 + (rk >> 4);
  const int cR = (x8 & 3) * 8 + (x8 >> 2) * 4 + rk / 7, cc = rk % 7;
  const int rot = 0;
  auto rotl = [&](int j) { const int t = j + rot; return t >= 7 ? t - 7 : t; };
  auto kstep = [&](int j, int n) {
    const int t = n % 14 + 2 * rot, u = 14 * j + (t >= 14 ? t - 14 : t);
    return u + (u & ~3) + (n >= 14 ? 4 : 0);
  };
  u64 tag;
  u32x4 xres, bres;
  auto ldres = [&] {
    const int tid = w * 64 + l;
    xres = ld16_async<false>((tid >> 3) * 896 + (tid & 7) * 16, rX, cR * 64 * 896 + cc * 128);
    bres = ld16_async<false>((l & 7) * 16, __builtin_amdgcn_make_buffer_rsrc((void*)b2, 0, 896, 0x00020000), cc * 128);
  };
  auto epoch = [&] { asm volatile("s_load_dwordx2 %0, %1, 0x0\n\ts_waitcnt lgkmcnt(0)" : "=s"(tag) : "s"(ws) : "memory"); tag += 1; };

  if (w < 4) {
    if (busy) {
      epoch();
      constexpr int PF = 4;
      u32 oX[4], oH[2], oQ[3];
#pragma unroll
      for (int p = 0; p < 4; ++p) oX[p] = (4 * w + ((l & 31) >> 3)) * 1024 + (l & 7) * 128 + (((2 * p + (g >> 1)) ^ (l15 >> 1)) << 4);
      const int rq = w == 3 ? 0 : w;
#pragma unroll
      for (int i = 0; i < 3; ++i) oQ[i] = RING + (8 * (g >> 1) + q4) * 192 + ((i + rq) % 3) * 64 + (g & 1) * 32 + p4 * 8;
      const u32 selv = l15 == (g & 1) ? 0x3f803f80u : 0u;
      const bf16x8 sel = __builtin_bit_cast(bf16x8, u32x4{selv, selv, selv, selv});
      bf16x8 xf[28], wf[8];
      f32x16 aH[2], aQ[3] = {};
      f32x4 cs = {}, sx = {};
      float rs;
      bf16x8 lnb;
      const int ks = w >> 1;
      auto rdW = [&](auto SS, auto II) {
        constexpr int s = decltype(SS)::v, i = decltype(II)::v;
        constexpr int slot = (s < 14 ? s * 6 + i : 84 + i) % 8;
        if constexpr (s < 14) { const u32 o = oQ[i % 3] + (stage_addr(s) - RING) + (i / 3) * 3072; wf[slot] = trfrag(L, o, o + 768); }
        else if constexpr (s < 28) { const u32 o = oH[i & 1] + stage_addr(s) + (i >> 1) * 2048; wf[slot] = trfrag(L, o, o + 512); }
      };
      auto rdX = [&](auto SS, auto EE) { constexpr int k = 2 * decltype(SS)::v + decltype(EE)::v; if constexpr (k < 28) xf[k] = lds_ld<bf16x8>(L, oX[k & 3] + (k >> 2) * 16384); };
      auto putcs = [&](u32 base) {
        u32x4 v;
#pragma unroll
        for (int j = 0; j < 4; ++j) { const float hi = rbf(cs[j]); v[j] = pk(hi, cs[j] - hi); }
        if (l15 < 2) lds_st<u32x4>(L, base + (l15 * 16 + 4 * g) * 4, v);
        cs = f32x4{};
      };
      auto hepi = [&](int half) {
        u32 c[2][2], bb[2];
#pragma unroll
        for (int i = 0; i < 2; ++i) {
          const int ct = i ^ (w & 1);
          c[i][0] = lds_ld<u32>(L, L_CSH + half * 512 + (ct * 32 + (l & 31)) * 4); c[i][1] = lds_ld<u32>(L, L_CSH + half * 512 + 256 + (ct * 32 + (l & 31)) * 4);
          bb[i] = lds_ld<unsigned short>(L, L_B1 + (half * 64 + ct * 32 + (l & 31)) * 2);
        }
        SB();
#pragma unroll
        for (int i = 0; i < 2; ++i) aH[i] = mfma32(__builtin_bit_cast(bf16x8, u32x4{c[i][0], c[i][1], bb[i], 0u}), lnb, aH[i]);
        SB();
        const float rs2 = rs * rs;
#pragma unroll
        for (int i = 0; i < 2; ++i)
#pragma unroll
          for (int e = 0; e < 2; ++e) {
            u32x4 hp;
#pragma unroll
            for (int j = 0; j < 4; ++j) {
              const float t0 = aH[i][8 * e + 2 * j], t1 = aH[i][8 * e + 2 * j + 1];
              hp[j] = pk(t0 * rs2 * fmx(t0, 0.f), t1 * rs2 * fmx(t1, 0.f));
            }
            st16<16>(hp, rAC, l * 16 + ((rb * 4 + w) * 144 + cb * 8 + half * 4 + (i ^ (w & 1)) * 2 + e) * 1024);
          }
        SB();
      };
      bar();
      STAMP(1);
      rdX(IC<0>{}, IC<0>{}); rdX(IC<0>{}, IC<1>{});
      sfor<0, PF>([&](auto I) { rdW(IC<0>{}, I); });
      sfor<0, 28>([&](auto SS) {
        constexpr int s = decltype(SS)::v, NT = s < 14 ? 6 : 8;
        if constexpr (s == B_FA) wait_bar<0>();
        else if constexpr (s == B_FQ) wait_bar<(B_FQ > 21 ? 4 : 0)>();
        else if constexpr (s == 13 || s == 14 || s == 21) wait_bar<63>();
        else bar();
        if constexpr (s == 13) {
          lanes();
#pragma unroll
          for (int i = 0; i < 2; ++i) oH[i] = (g >> 1) * 1024 + q4 * 128 + (((i ^ (w & 1)) ^ (q4 >> 1)) << 6) + (g & 1) * 32 + p4 * 8;
        }
        if constexpr (s == 14) {
          STAMP(2);
          const u32x2 st = lds_ld<u32x2>(L, L_ST + (w * 32 + (l & 31)) * 8);
          u32 c[3];
#pragma unroll
          for (int i = 0; i < 3; ++i) c[i] = lds_ld<u32>(L, L_CSQ + (((i + rq) % 3) * 32 + (l & 31)) * 4);
          SB();
          const u32 mm2 = l < 32 ? (st[0] & 0xffffu) * 0x10001u : 0u;
          lnb = __builtin_bit_cast(bf16x8, u32x4{mm2, mm2, l < 32 ? st[0] >> 16 : 0u, 0u});
          rs = __uint_as_float(st[1]);
#pragma unroll
          for (int i = 0; i < 3; ++i) aQ[i] = mfma32(__builtin_bit_cast(bf16x8, u32x4{c[i], 0u, 0u, 0u}), lnb, aQ[i]);
          SB();
          const int fl = ((g & 1) << 1) | (g >> 1);
#pragma unroll
          for (int i = 0; i < 3; ++i)
#pragma unroll
            for (int e = 0; e < 2; ++e) {
              const int tq = 6 * cb + 2 * ((i + rq) % 3) + e;
              const float m = tq < 28 ? rs * (0.18898223650461361f * 1.4426950408889634f) : rs;
              const auto s0 = __builtin_amdgcn_permlane16_swap(pk(aQ[i][8 * e] * m, aQ[i][8 * e + 1] * m), pk(aQ[i][8 * e + 4] * m, aQ[i][8 * e + 5] * m), false, false);
              const auto s1 = __builtin_amdgcn_permlane16_swap(pk(aQ[i][8 * e + 2] * m, aQ[i][8 * e + 3] * m), pk(aQ[i][8 * e + 6] * m, aQ[i][8 * e + 7] * m), false, false);
              st16<16>(tq >= 56 && (fl & 1) ? u32x4{s0[1], s1[1], s0[0], s1[0]} : u32x4{s0[0], s1[0], s0[1], s1[1]}, rQKV,
                       fl * 256 + ((l15 ^ (tq >= 56 ? (2 * (tq & 1) + (fl >> 1)) << 2 : 0)) << 4) + ((rb * 4 + w) * 336 + 4 * tq) * 256);
            }
          SB();
          STAMP(3);
        }
        if constexpr (s == 21) hepi(0);
        sfor<0, NT>([&](auto II) {
          constexpr int i = decltype(II)::v, slot = (s < 14 ? s * 6 + i : 84 + i) % 8;
          if constexpr (i + PF < NT) rdW(SS, IC<i + PF>{});
          else rdW(IC<s + 1>{}, IC<i + PF - NT>{});
          if constexpr (i < 2) rdX(IC<s + 1>{}, II);
          if constexpr (s < 14) {
            aQ[i % 3] = mfma32(wf[slot], xf[2 * s + i / 3], aQ[i % 3]);
            if constexpr (i % 3 == 0) cs = mfma(wf[slot], sel, cs);
            if constexpr (s == 0 && (i == 1 || i == 2)) sx = mfma(xf[i - 1], sel, sx);
            if constexpr (s < 13 && i >= 4) sx = mfma(xf[2 * s + i - 2], sel, sx);
          } else {
            constexpr int k = 4 * ((s - 14) % 7) + (i >> 1);
            aH[i & 1] = mfma32(wf[slot], xf[k], k == 0 ? f32x16{} : aH[i & 1]);
            if constexpr (!(i & 1)) { if (((i >> 1) & 1) == ks) cs = mfma(wf[slot], sel, cs); }
          }
          SB();
        });
        if constexpr (s == 12) { if (l15 < 2) lds_st<f32x4>(L, L_SX + (w * 32 + l15 * 16 + 4 * g) * 4, sx); SB(); }
        if constexpr (s == 13) { putcs(L_CSQ + w * 128); SB(); }
        if constexpr (s == 20 || s == 27) { putcs(L_CSH + (s == 27) * 512 + ks * 256 + (w & 1) * 128); SB(); }
      });
      STAMP(4);
      wait_bar<63>();
      lanes();
      STAMP(5);
      hepi(1);
      const rsrc_t rWo = __builtin_amdgcn_make_buffer_rsrc((void*)wo, 0, 448 * 896, 0x00020000);
      u32x4 hA[36][2];
      const u32 vA[2] = {(u32)l * 16, (u32)l * 16 + 144 * 1024}, ring = L_WC + w * WC_RING;
      u32 vW[2];
      vW[0] = (l >> 3) * 896 + (((l & 7) ^ ((g & 1) << 2)) << 4); vW[1] = vW[0] + 7168 - 1024;
      auto ldA = [&](auto M, int a) {
        constexpr int m = decltype(M)::v;
        hA[m][a] = ld16_async<true>(vA[a], rAC, (cR * 288 + (m < 28 ? kstep(w, m) : 112 + 8 * w + (m - 28))) * 1024);
      };
      auto ldW = [&](auto M) {
        constexpr int m = decltype(M)::v;
        if constexpr (m < 28) dman<2>(lb + ring + (m % RW) * 2048, vW, rW2, kstep(w, m) * (16 * 896) + cc * 128);
        else dman<2>(lb + ring + (m % RW) * 2048, vW, rWo, (28 * (4 * w + ((m - 28) >> 1)) + 16 * (m & 1)) * 896 + cc * 128);
      };
      lds_wait(L + L_CNT + 16, 1u);
      STAMP(6);
      __builtin_amdgcn_s_setprio(PRIO_C);
      sfor<0, DA>([&](auto M) { ldA(M, 0); ldA(M, 1); });
      u32 oW[2];
#pragma unroll
      for (int b = 0; b < 2; ++b) oW[b] = ring + (4 * (g >> 1) + q4) * 128 + ((b ^ (q4 >> 1)) << 6) + (g & 1) * 32 + p4 * 8;
      bf16x8 wc[2][2];
      auto rdC = [&](int m) {
#pragma unroll
        for (int b = 0; b < 2; ++b) wc[m & 1][b] = trfrag(L, oW[b] + (m % RW) * 2048, oW[b] + (m % RW) * 2048 + 1024);
      };
      f32x16 acc[2][2] = {};
      u32x2 polled;
      const u32 vFB = WS_FB + (cR >> 1) * 128 + (4 * w + p4) * 8, vFG = WS_FG + (cR >> 1) * 128 + ((14 * w) / 4 + p4) * 8;
      rdC(0);
      sfor<0, 36>([&](auto N) {
        constexpr int n = decltype(N)::v;
        asm volatile("s_waitcnt vmcnt(%2)" : "+v"(hA[n][0]), "+v"(hA[n][1]) : "n"(SC.wC[n]) : "memory");
        if constexpr (n == 0) { if (last_of4(L + L_CNT) && l == 0) st_flag(rCTL, WS_FG + rb * 128 + cb * 8, tag); }
        if constexpr (n == TB - 2 || n == TO - 2) polled = ld8c_async(n == TB - 2 ? vFG : vFB, rCTL);
        if constexpr (n == TB || n == TO) {
          PIN(polled);
          if (!__all(polled[0] == (u32)tag && polled[1] == (u32)(tag >> 32))) spin<1>(rCTL, n == TB ? vFG : vFB, tag);
        }
        if constexpr (n + 1 < 36) rdC(n + 1);
        SB();
        auto mm = [&](int a, int b) { acc[a][b] = mfma32(wc[n & 1][b], __builtin_bit_cast(bf16x8, hA[n][a]), acc[a][b]); SB(); };
        mm(0, 0);
        if constexpr (SC.a0[n] < SC.a1[n]) { ldA(IC<SC.a0[n] % 36>{}, 0); mm(0, 1); ldA(IC<SC.a0[n] % 36>{}, 1); } else mm(0, 1);
        if constexpr (SC.a0[n] + 1 < SC.a1[n]) { ldA(IC<(SC.a0[n] + 1) % 36>{}, 0); ldA(IC<(SC.a0[n] + 1) % 36>{}, 1); }
        mm(1, 0);
        if constexpr (n >= 1 && n + RW - 1 < 36) ldW(IC<n + RW - 1>{});
        mm(1, 1);
        if constexpr (n == 33) ldres();
      });
      STAMP(10);
#pragma unroll
      for (int a = 0; a < 2; ++a)
#pragma unroll
        for (int b = 0; b < 2; ++b)
#pragma unroll
          for (int k = 0; k < 4; ++k)
            lds_st<f32x4>(L, ring + (a * 32 + (l & 31)) * RED_PITCH + (b * 32 + k * 8 + 4 * (g >> 1)) * 4,
                          f32x4{acc[a][b][4 * k], acc[a][b][4 * k + 1], acc[a][b][4 * k + 2], acc[a][b][4 * k + 3]});
      asm volatile("s_waitcnt vmcnt(0)" : "+v"(xres), "+v"(bres)::"memory");
    } else { bar();
      if (blockIdx.x == 255 && w == 0) { while (__builtin_amdgcn_s_memtime() - tent < 100000) __builtin_amdgcn_s_sleep(1); }
    }
  } else {
    const int li = w & 1;
    if (!busy) {
      epoch();
      if (w == 6 && l < 8) lds_st<u32>(L, L_CNT + l * 4, 0u);
      wait_bar<63>();
    } else if (w < 6) {
      const rsrc_t rW1 = __builtin_amdgcn_make_buffer_rsrc((void*)w1, 0, 448 * 3584, 0x00020000);
      const rsrc_t rWq = __builtin_amdgcn_make_buffer_rsrc((void*)wqkv, 0, 448 * 2688, 0x00020000);
      u32 vX[4], vH[4], vQ[3], vC[2], so7[7];
#pragma unroll
      for (int j = 0; j < 4; ++j) {
        vX[j] = (8 * j + (l >> 3)) * 896 + (((l & 7) ^ ((j & 1) * 4 + g)) << 4) - 1024 * j;
      }
#pragma unroll
      for (int j = 0; j < 3; ++j) { const int n = 64 * j + l; vQ[j] = (n / 12) * 2688 + (n % 12) * 16 - 1024 * j; }
#pragma unroll
      for (int j = 0; j < 7; ++j) so7[j] = rotl(j);
      const u32 sX = (rb * 128 + 64 * li) * 896, sH = cb * 256 + 32 * li * 3584, sQ = cb * 192 + 16 * li * 2688;
      auto issue = [&](auto O) {
        constexpr int op = decltype(O)::v, k = op / 256, a = (op % 256) / 2, e = op & 1;
        if constexpr (k == K_X) dman<4>(lb + (a * 16 + 8 * li + 4 * e) * 1024, vX, rX, sX + so7[a] * 128 + e * 28672);
        if constexpr (k == K_WA && a < 14) dman<3>(lb + stage_addr(a) + li * 3072, vQ, rWq, sQ + so7[a >> 1] * (64 * 2688) + (a & 1) * (32 * 2688));
        if constexpr (k == K_WA && a >= 14) dman<4>(lb + stage_addr(a) + li * 4096, vH, rW1, sH + so7[(a - 14) % 7] * (64 * 3584) + (a >= 21 ? 128 : 0));
        if constexpr (k == K_BIAS) dma4(lb + L_B1, cb * 256 + l * 4, __builtin_amdgcn_make_buffer_rsrc((void*)b1, 0, 3584, 0x00020000), 0);
        if constexpr (k == K_WCP) dman<2>(lb + L_WC + (2 * e + li) * WC_RING + a * 2048, vC, rW2, kstep(2 * e + li, a) * (16 * 896) + cc * 128);
      };
      sfor<0, NPRO>([&](auto K) { issue(IC<SC.pro[decltype(K)::v]>{}); });
      epoch();
#pragma unroll
      for (int j = 0; j < 4; ++j) vH[j] = (8 * j + (l >> 3)) * 3584 + (((l & 7) ^ ((g & 1) << 2)) << 4) - 1024 * j;
      vC[0] = (l >> 3) * 896 + (((l & 7) ^ ((g & 1) << 2)) << 4); vC[1] = vC[0] + 7168 - 1024;
      vm_bar<SC.wA[0]>();
      sfor<0, 28>([&](auto SS) {
        constexpr int s = decltype(SS)::v;
        vm_bar<SC.wA[s + 1]>();
        sfor<0, MAXO>([&](auto K) { issue(IC<SC.opA[s][decltype(K)::v]>{}); });
      });
      vm_bar<0>();
    } else {
      epoch();
      u32 oX[4];
#pragma unroll
      for (int p = 0; p < 4; ++p) oX[p] = (8 * li + ((l & 31) >> 3)) * 1024 + (l & 7) * 128 + (((2 * p + (g >> 1)) ^ (l15 >> 1)) << 4);
      float s2[2][2] = {};
      u32x4 xv[2][4];
      sfor<0, 15>([&](auto SS) {
        constexpr int s = decltype(SS)::v;
        wait_bar<63>();
        if constexpr (s < 14) {
#pragma unroll
          for (int k = 0; k < 4; ++k) xv[s & 1][k] = lds_ld<u32x4>(L, oX[(2 * s + (k >> 1)) & 3] + (s >> 1) * 16384 + (k & 1) * 4096);
        }
        SB();
        auto sq = [&](int p) {
#pragma unroll
          for (int k = 0; k < 4; ++k)
#pragma unroll
            for (int j = 0; j < 4; ++j) s2[k & 1][j & 1] = dot2(xv[p][k][j], xv[p][k][j], s2[k & 1][j & 1]);
          PIN(s2[0][0]); PIN(s2[0][1]); PIN(s2[1][0]); PIN(s2[1][1]);
        };
        if constexpr (s >= 1 && s < 14) sq(~s & 1);
        if constexpr (s == 13) sq(1);
        SB();
      });
#pragma unroll
      for (int r = 0; r < 2; ++r) {
        const u32 q2 = __float_as_uint(s2[r][0] + s2[r][1]);
        const auto b = __builtin_amdgcn_permlane32_swap(q2, q2, false, false);
        const float mean = lds_ld<float>(L, L_SX + ((2 * li + r) * 32 + (l & 31)) * 4) * (1.f / 448.f);
        const float var = fmaxf((__uint_as_float(b[0]) + __uint_as_float(b[1])) * (1.f / 448.f) - mean * mean, 0.f);
        const float sd = rbf(__builtin_amdgcn_sqrtf(rbf(rbf(var) + 1e-5f)));
        if (l < 32) lds_st<u32x2>(L, L_ST + ((2 * li + r) * 32 + l) * 8, u32x2{pk(-mean, sd), __float_as_uint(__builtin_amdgcn_rcpf(sd))});
      }
      if (w == 6 && l < 8) lds_st<u32>(L, L_CNT + l * 4, 0u);
      wait_bar<63>();
#pragma unroll 1
      for (int s = 15; s < 29; ++s) {
        bar();
        if (w == 7 && l == 0 && (s == B_FQ || s == B_FA)) st_flag(rCTL, (s == B_FQ ? WS_FQ : WS_FH) + rb * 128 + cb * 8, tag);
      }
      if (w == 7) {
        spin<1>(rCTL, WS_FH + (cR >> 1) * 128 + (l < 14 ? l : 0) * 8, tag);
        __hip_atomic_store((LAS u32*)(L + L_CNT + 16), 1u, __ATOMIC_RELAXED, __HIP_MEMORY_SCOPE_WORKGROUP);
      }
    }
    lanes();
    const int aw = w - 4;
    __builtin_amdgcn_s_setprio(PRIO_B);
#pragma unroll
    for (int k = 0; k < 2; ++k) if (k == 0 || l < 32) lds_st<u32x2>(L, L_V + (3 * aw + 2 * k + (l >> 5)) * 2048 + 1792 + (l & 31) * 8, u32x2{0x3f80u, 0u});
    if (w == 6) {
      const int e = l & 1, k = l < 14 ? l >> 1 : 0;
      const int ty = k == 0 ? 0 : (k < 4 ? 1 : 2), ro = k == 0 ? 0 : (k < 4 ? k - 1 : k - 4);
      const int rr = ai - ro < 0 ? 0 : ai - ro;
      spin<4>(rCTL, WS_FQ + rr * 128 + ((ty * 448 + 28 * ah + 27 * e) / 96) * 8, tag);
      __hip_atomic_store((LAS u32*)(L + L_CNT + 12), 1u, __ATOMIC_RELAXED, __HIP_MEMORY_SCOPE_WORKGROUP);
    } else lds_wait(L + L_CNT + 12, 1u);
    if (w == 4) STAMP(7);
#pragma unroll
    for (int j = 0; j < 6; ++j) {
      const int b = 3 * aw + (j >> 1), tb = 4 * ai - 8 + b < 0 ? 0 : 4 * ai - 8 + b;
      const u32 so = (tb * 336 + 112 + 112 * (j & 1) + 7 * ah) * 256, d = lb + ((j & 1) ? L_V + b * 2048 : L_K + b * 1792);
      dma16<true>(d, l * 16, rQKV, so);
      if (l < 48) dma16<true>(d + 1024, l * 16, rQKV, so + 1024);
    }
    const int hh = g >> 1;
    u32x2 qv[4];
#pragma unroll
    for (int k = 0; k < 4; ++k) qv[k] = ld8c_async(k == 3 && hh ? 0xfffffff0u : ((4 * ai + aw) * 336 + 7 * ah + 2 * k + hh) * 256 + l15 * 16 + (g & 1) * 8, rQKV);
    asm volatile("s_waitcnt vmcnt(0) lgkmcnt(0)" : "+v"(qv[0]), "+v"(qv[1]), "+v"(qv[2]), "+v"(qv[3])::"memory");
    if (!last_of4(L + L_CNT + 4)) lds_wait(L + L_CNT + 4, 4u);
    asm volatile("" ::: "memory");
    if (w == 4) STAMP(8);
    const bf16x8 qf[2] = {__builtin_bit_cast(bf16x8, u32x4{qv[0][0], qv[0][1], qv[1][0], qv[1][1]}), __builtin_bit_cast(bf16x8, u32x4{qv[2][0], qv[2][1], qv[3][0], qv[3][1]})};
    const u32 oK = L_K + aw * 1792 + hh * 256 + l15 * 16 + (g & 1) * 8, oK3 = hh ? oK + 1280 : oK + 1536;
    const int F = 7 * ah + 4 * (g & 1) + p4;
    u32 oV[2][2];
#pragma unroll
    for (int t = 0; t < 2; ++t)
#pragma unroll
      for (int e = 0; e < 2; ++e) oV[t][e] = L_V + aw * 2048 + (4 * (g & 1) + p4) * 256 + (((8 * e + 4 * hh + q4) ^ ((F & 6) << 1)) << 4) + ((t ^ (F & 1)) << 3);
    f32x16 sc[9];
    bf16x8 kf[9][2], vf[9][2];
    auto rdK = [&](int m) {
      const u32x2 a = lds_ld<u32x2>(L, oK + m * 1792), b = lds_ld<u32x2>(L, oK + m * 1792 + 512), c = lds_ld<u32x2>(L, oK + m * 1792 + 1024), d = lds_ld<u32x2>(L, oK3 + m * 1792);
      kf[m][0] = __builtin_bit_cast(bf16x8, u32x4{a[0], a[1], b[0], b[1]}); kf[m][1] = __builtin_bit_cast(bf16x8, u32x4{c[0], c[1], d[0], d[1]});
    };
    auto rdV = [&](int m) { vf[m][0] = trfrag(L, oV[0][0] + m * 2048, oV[0][1] + m * 2048); vf[m][1] = trfrag(L, oV[1][0] + m * 2048, oV[1][1] + m * 2048); };
    rdK(0); SB(); rdK(8); SB(); rdK(1); SB();
#pragma unroll
    for (int i = 0; i < 9; ++i) {
      const int m = i == 0 ? 0 : (i == 1 ? 8 : i - 1);
      if (i + 2 < 8) rdK(i + 2); else if (i >= 7) rdV(i - 6);
      sc[m] = mfma32(kf[m][1], qf[1], mfma32(kf[m][0], qf[0], f32x16{}));
      SB();
    }
    constexpr float NEG = -1e30f;
    if (ai < 2) {
      asm volatile("" ::: "memory");
      const int m0 = 8 - 4 * ai - aw;
#pragma unroll
      for (int m = 0; m < 8; ++m)
        if (m < m0) {
#pragma unroll
          for (int r = 0; r < 16; ++r) sc[m][r] = NEG;
        }
    }
    const int dq = (l & 31) - 4 * hh;
    u32 mk[8];
#pragma unroll
    for (int r = 0; r < 16; r += 2) {
      const bool c0 = (r & 3) + 8 * (r >> 2) <= dq, c1 = (r & 3) + 1 + 8 * (r >> 2) <= dq;
      sc[0][r] = c0 ? sc[8][r] : sc[0][r]; sc[0][r + 1] = c1 ? sc[8][r + 1] : sc[0][r + 1];
      mk[r >> 1] = c1 ? 0xffffffffu : (c0 ? 0xffffu : 0u);
    }
    float mx0 = fmx(sc[0][0], sc[0][1]), mx1 = fmx(sc[0][2], sc[0][3]);
#pragma unroll
    for (int k = 1; k < 32; ++k) { mx0 = fmx(fmx(mx0, sc[k >> 2][4 * (k & 3)]), sc[k >> 2][4 * (k & 3) + 1]); mx1 = fmx(fmx(mx1, sc[k >> 2][4 * (k & 3) + 2]), sc[k >> 2][4 * (k & 3) + 3]); }
    mx0 = fmx(mx0, mx1);
    const auto ms = __builtin_amdgcn_permlane32_swap(__float_as_uint(mx0), __float_as_uint(mx0), false, false);
    const float mx = fmx(__uint_as_float(ms[0]), __uint_as_float(ms[1]));
    SB();
    f32x16 o = {};
#pragma unroll
    for (int i = 0; i < 8; ++i) {
      const int m = (i + 1) & 7;
      if (i < 5) rdV(i + 3); else if (i == 5) rdV(0); else if (i == 6) rdV(8);
      u32x4 p[2];
#pragma unroll
      for (int j = 0; j < 8; ++j) {
        f32x2 d = f32x2{sc[m][2 * j], sc[m][2 * j + 1]} - f32x2{mx, mx};
        PIN(d);
        p[j >> 2][j & 3] = pk(__builtin_amdgcn_exp2f(d[0]), __builtin_amdgcn_exp2f(d[1]));
      }
#pragma unroll
      for (int s = 0; s < 2; ++s) {
        if (m) o = mfma32(vf[m][s], __builtin_bit_cast(bf16x8, p[s]), o);
        else {
          const u32x4 k4 = {mk[4 * s], mk[4 * s + 1], mk[4 * s + 2], mk[4 * s + 3]};
          o = mfma32(vf[0][s], __builtin_bit_cast(bf16x8, p[s] & ~k4), o);
          o = mfma32(vf[8][s], __builtin_bit_cast(bf16x8, p[s] & k4), o);
        }
      }
      SB();
    }
    const auto dn = __builtin_amdgcn_permlane32_swap(__float_as_uint(o[12]), __float_as_uint(o[12]), false, false);
    const float inv = __builtin_amdgcn_rcpf(__uint_as_float(dn[1]));
    const float in2 = hh ? 0.f : inv;
    const u32 so = ((ai * 4 + aw) * 144 + 112 + 2 * ah) * 1024;
    st16<16>(u32x4{pk(o[0] * inv, o[1] * inv), pk(o[2] * inv, o[3] * inv), pk(o[4] * inv, o[5] * inv), pk(o[6] * inv, o[7] * inv)}, rAC, l * 16 + so);
    st16<16>(u32x4{pk(o[8] * inv, o[9] * inv), pk(o[10] * inv, o[11] * inv), pk(o[12] * in2, o[13] * in2), pk(o[14] * in2, o[15] * in2)}, rAC, l * 16 + so + 1024);
    if (w == 4) STAMP(9);
    asm volatile("s_waitcnt vmcnt(0)" ::: "memory");
    if (last_of4(L + L_CNT + 8)) {
      if (l == 0) st_flag(rCTL, WS_FB + ai * 128 + ah * 8, tag);
      if (blockIdx.x == 255) {
#pragma unroll 1
        for (int k = 0; k < 4; ++k) spin<8>(rCTL, WS_FB + (k * 64 + l) * 8, tag);
        if (l == 0) st_flag(rCTL, 0, tag);
      }
    }
    if (busy) { ldres(); asm volatile("s_waitcnt vmcnt(0)" : "+v"(xres), "+v"(bres)::"memory"); }
  }
  if (busy) {
    wait_bar<63>();
    lanes();
    const int tid = w * 64 + l, row = tid >> 3, c8 = tid & 7;
    f32x4 r0 = {bflo(xres[0]) + bflo(bres[0]), bfhi(xres[0]) + bfhi(bres[0]), bflo(xres[1]) + bflo(bres[1]), bfhi(xres[1]) + bfhi(bres[1])};
    f32x4 r1 = {bflo(xres[2]) + bflo(bres[2]), bfhi(xres[2]) + bfhi(bres[2]), bflo(xres[3]) + bflo(bres[3]), bfhi(xres[3]) + bfhi(bres[3])};
#pragma unroll
    for (int j = 0; j < 4; ++j) {
      r0 += lds_ld<f32x4>(L, L_WC + j * WC_RING + row * RED_PITCH + c8 * 32);
      r1 += lds_ld<f32x4>(L, L_WC + j * WC_RING + row * RED_PITCH + c8 * 32 + 16);
    }
    st16<0>(u32x4{pk(r0[0], r0[1]), pk(r0[2], r0[3]), pk(r1[0], r1[1]), pk(r1[2], r1[3])},
                                           __builtin_amdgcn_make_buffer_rsrc(out, 0, 2048 * 896, 0x00020000), row * 896 + c8 * 16 + cR * 64 * 896 + cc * 128);
  }
  if (w == 0) STAMP(11);
}

extern "C" void kernel_launch(void* const* d_in, const int* in_sizes, int n_in, void* d_out, int out_size, void* d_ws, size_t ws_size, hipStream_t stream) {
  fused_block<<<256, 512, 0, stream>>>((const char*)d_in[0], (const char*)d_in[1], (const char*)d_in[2], (const char*)d_in[4], (const char*)d_in[5],
                                       (const char*)d_in[6], (const char*)d_in[7], (char*)d_out, (char*)d_ws);
}
```

```cpp
#define NOROT
#include <hip/hip_runtime.h>
#include <hip/hip_bf16.h>
#include <stdint.h>

#define DI __device__ __forceinline__
#define LAS __attribute__((address_space(3)))
#define GAS __attribute__((address_space(1)))
typedef unsigned u32;
typedef unsigned long long u64;
typedef __bf16 bf16x8 __attribute__((ext_vector_type(8)));
typedef __bf16 bf16x2 __attribute__((ext_vector_type(2)));
typedef short s16x4 __attribute__((ext_vector_type(4)));
typedef float f32x4 __attribute__((ext_vector_type(4)));
typedef float f32x2 __attribute__((ext_vector_type(2)));
typedef float f32x16 __attribute__((ext_vector_type(16)));
typedef unsigned u32x4 __attribute__((ext_vector_type(4)));
typedef unsigned u32x2 __attribute__((ext_vector_type(2)));
typedef __amdgpu_buffer_rsrc_t rsrc_t;

template <int N> struct IC { static constexpr int v = N; };
template <int B, int E, class F> DI void sfor(F&& f) { if constexpr (B < E) { f(IC<B>{}); sfor<B + 1, E>(f); } }

constexpr u32 WS_FH = 256, WS_FQ = WS_FH + 2048, WS_FB = WS_FQ + 2048, WS_FG = WS_FB + 2048, CTL_BYTES = 16384;
constexpr u32 WS_QKV = 16384, QKV_BYTES = 64u * 336u * 256u;
constexpr u32 WS_AC = WS_QKV + QKV_BYTES, AC_BYTES = 128u * 72u * 1024u;
constexpr u32 SLOT = 16384, RING = 7 * SLOT;
constexpr u32 L_SX = 45056, L_CSQ = 45568, L_B1 = 46080, L_CSH = 46336, L_ST = 47360, L_CNT = 48384;
constexpr u32 L_K = 0, L_V = 21504;
constexpr int RW = 12;
constexpr u32 L_WC = 49152, WC_RING = RW * 2048, RED_PITCH = 272;
constexpr u32 stage_addr(int s) { return s <= 20 ? RING + (s % 6) * 8192u : (s <= 25 ? (s - 21) * 8192u : RING + (s - 22) * 8192u); }

enum { K_NONE = 0, K_X, K_WA, K_BIAS, K_WCP };
constexpr int OP(int k, int a, int e = 0) { return k * 256 + 2 * a + e; }
constexpr int opn(int op) { int k = op / 256, a = (op % 256) / 2; return k == K_NONE ? 0 : k == K_X ? 4 : k == K_WA ? (a < 14 ? 3 : 4) : k == K_WCP ? 2 : 1; }
#define CAP_1 8
#define CAP_2 8
#define CAP_E 20
#define P_DA 8
#define P_TB 11
#define P_TO 25
#define P_FQ 23
#define P_FA 27
constexpr int MAXO = 12, NPRO = 8, DA = P_DA, TB = P_TB, TO = P_TO, B_FQ = P_FQ, B_FA = P_FA;
struct Sched {
  int pro[NPRO], opA[28][MAXO];
  int wA[29];
  int wC[36], a0[36], a1[36];
  int ok;
};
constexpr int cl(int n) { return n > 63 ? 63 : n; }
constexpr int free_at(u32 b) {
  return b < RING ? 2 * (int)(b / SLOT) + 1 : (int[6]){19, 20, 21, 16, 28, 28}[(b - RING) / 8192];
}
constexpr Sched make_sched() {
  Sched r{};
  r.ok = 1;
  int n = 0, iW[28] = {}, iX[7] = {}, iB = 0;
  int lo[128] = {}, le[128] = {}, nl = 0;
  for (int s = 0; s < 28; ++s) {
    if (s < 14 && !(s & 1)) { lo[nl] = OP(K_X, s >> 1, 0); le[nl++] = -1; lo[nl] = OP(K_X, s >> 1, 1); le[nl++] = -1; }
    lo[nl] = OP(K_WA, s); le[nl++] = s <= 20 ? s - 5 : (s <= 25 ? free_at(stage_addr(s) + 8191) : s - 9);
    if (s == 17) { lo[nl] = OP(K_BIAS, 0); le[nl++] = 5; }
  }
  for (int k = 0; k < RW; ++k) for (int j = 0; j < 2; ++j) {
    int f = 0;
    for (int e = 0; e < 4; ++e) { const int t = free_at(L_WC + (2 * j + (e >> 1)) * WC_RING + k * 2048 + (e & 1) * 2047); if (t > f) f = t; }
    lo[nl] = OP(K_WCP, k, j); le[nl++] = f;
  }
  bool done[128] = {};
  auto note = [&](int op) { n += opn(op); int k = op / 256, a = (op % 256) / 2; if (k == K_X) iX[a] = (op & 1) ? n : 0; if (k == K_WA) iW[a] = n; if (k == K_BIAS) iB = n; };
  for (int k = 0; k < NPRO; ++k) { r.pro[k] = lo[k]; done[k] = true; note(lo[k]); }
  r.wA[0] = cl(n - iW[0]);
  for (int s = 0; s < 28; ++s) {
    int need = 0;
    if (s + 1 < 28) {
      need = iW[s + 1]; if (!need) r.ok = 0;
      if (s + 1 <= 13) { int v = iX[(s + 1) >> 1]; if (!v) r.ok = 0; if (v > need) need = v; }
      if (s == 21) { if (!iB) r.ok = 0; if (iB > need) need = iB; }
    }
    r.wA[s + 1] = need ? cl(n - need) : 63;
    int cap = s == 14 || s == 21 ? CAP_E : (s < 14 ? CAP_1 : CAP_2), c = 0, used = 0;
    for (int k = 0; k < nl; ++k) if (!done[k] && le[k] <= s && used + opn(lo[k]) <= cap && c < MAXO) { done[k] = true; r.opA[s][c++] = lo[k]; used += opn(lo[k]); note(lo[k]); }
  }
  for (int k = 0; k < nl; ++k) if (!done[k]) r.ok = 0;
  n = 4;
  int iA[36] = {}, iWc[36] = {}, iP = 0, nA = DA;
  for (int m = 0; m < DA; ++m) { n += 2; iA[m] = n; }
  for (int s = 0; s < 36; ++s) {
    int need = iA[s]; if (!need) r.ok = 0;
    if (s + 1 < 36 && s + 1 >= RW) { if (!iWc[s + 1]) r.ok = 0; if (iWc[s + 1] > need) need = iWc[s + 1]; }
    if ((s == TB || s == TO) && iP > need) need = iP;
    r.wC[s] = cl(n - need);
    if (s == TB - 2 || s == TO - 2) { n += 1; iP = n; }
    r.a0[s] = nA;
    for (int c = 0; c < 2 && nA < 36 && nA <= s + DA && (nA < 14 || s >= TB) && (nA < 28 || s >= TO); ++c) { n += 2; iA[nA++] = n; }
    r.a1[s] = nA;
    if (s >= 1 && s + RW - 1 < 36) { n += 2; iWc[s + RW - 1] = n; }
    if (s == 33) n += 2;
  }
  return r;
}
constexpr Sched SC = make_sched();
static_assert(SC.ok, "schedule");

template <bool C> DI void dma16(u32 lds, u32 voff, rsrc_t rs, u32 soff) {
  if constexpr (C) asm volatile("s_mov_b32 m0, %0\n\ts_nop 0\n\tbuffer_load_dwordx4 %1, %2, %3 offen sc1 lds" ::"s"(lds), "v"(voff), "s"(rs), "s"(soff) : "memory");
  else asm volatile("s_mov_b32 m0, %0\n\ts_nop 0\n\tbuffer_load_dwordx4 %1, %2, %3 offen lds" ::"s"(lds), "v"(voff), "s"(rs), "s"(soff) : "memory");
}
template <bool C> DI u32x4 ld16_async(u32 voff, rsrc_t rs, u32 soff) {
  u32x4 r;
  if constexpr (C) asm volatile("buffer_load_dwordx4 %0, %1, %2, %3 offen sc1" : "=v"(r) : "v"(voff), "s"(rs), "s"(soff) : "memory");
  else asm volatile("buffer_load_dwordx4 %0, %1, %2, %3 offen" : "=v"(r) : "v"(voff), "s"(rs), "s"(soff) : "memory");
  return r;
}
DI void dma4(u32 lds, u32 voff, rsrc_t rs, u32 soff) {
  asm volatile("s_mov_b32 m0, %0\n\ts_nop 0\n\tbuffer_load_dword %1, %2, %3 offen lds" ::"s"(lds), "v"(voff), "s"(rs), "s"(soff) : "memory");
}
DI u32x2 ld8c_async(u32 voff, rsrc_t rs) {
  u32x2 r; asm volatile("buffer_load_dwordx2 %0, %1, %2, 0 offen sc1" : "=v"(r) : "v"(voff), "s"(rs) : "memory"); return r;
}
template <int N> DI void wait_bar() { asm volatile("s_waitcnt vmcnt(%0) lgkmcnt(0)\n\ts_barrier" ::"n"(N) : "memory"); }
template <int N> DI void vm_bar() { asm volatile("s_waitcnt vmcnt(%0)\n\ts_barrier" ::"n"(N) : "memory"); }
DI void bar() { asm volatile("s_barrier" ::: "memory"); }
#define PIN(x) asm volatile("" : "+v"(x))
#define SB() __builtin_amdgcn_sched_barrier(0)
DI f32x4 mfma(bf16x8 a, bf16x8 b, f32x4 c) { return __builtin_amdgcn_mfma_f32_16x16x32_bf16(a, b, c, 0, 0, 0); }
DI f32x16 mfma32(bf16x8 a, bf16x8 b, f32x16 c) { return __builtin_amdgcn_mfma_f32_32x32x16_bf16(a, b, c, 0, 0, 0); }
DI u32 pk(float a, float b) { bf16x2 v = {(__bf16)a, (__bf16)b}; return __builtin_bit_cast(u32, v); }
DI float fmx(float a, float b) { return __builtin_elementwise_maximum(a, b); }
DI float rbf(float a) { return (float)(__bf16)a; }
DI float bflo(u32 v) { return __uint_as_float(v << 16); }
DI float bfhi(u32 v) { return __uint_as_float(v & 0xffff0000u); }
DI float dot2(u32 a, u32 b, float c) { return __builtin_amdgcn_fdot2_f32_bf16(__builtin_bit_cast(bf16x2, a), __builtin_bit_cast(bf16x2, b), c, false); }
template <class T> DI T lds_ld(const LAS char* base, u32 off) { return *(const LAS T*)(base + off); }
template <class T> DI void lds_st(LAS char* base, u32 off, T v) { *(LAS T*)(base + off) = v; }
DI bf16x8 trfrag(LAS char* base, u32 off0, u32 off1) {
  s16x4 lo = __builtin_amdgcn_ds_read_tr16_b64_v4i16((LAS s16x4*)(base + off0));
  s16x4 hi = __builtin_amdgcn_ds_read_tr16_b64_v4i16((LAS s16x4*)(base + off1));
  return __builtin_bit_cast(bf16x8, __builtin_shufflevector(lo, hi, 0, 1, 2, 3, 4, 5, 6, 7));
}
template <int N> DI void dman(u32 lds, const u32* v, rsrc_t rs, u32 soff) {
  if constexpr (N == 4) asm volatile("s_mov_b32 m0, %0\n\ts_nop 0\n\tbuffer_load_dwordx4 %1, %5, %6 offen lds\n\tbuffer_load_dwordx4 %2, %5, %6 offen offset:1024 lds\n\t"
    "buffer_load_dwordx4 %3, %5, %6 offen offset:2048 lds\n\tbuffer_load_dwordx4 %4, %5, %6 offen offset:3072 lds" ::"s"(lds), "v"(v[0]), "v"(v[1]), "v"(v[2]), "v"(v[3]), "s"(rs), "s"(soff) : "memory");
  if constexpr (N == 3) asm volatile("s_mov_b32 m0, %0\n\ts_nop 0\n\tbuffer_load_dwordx4 %1, %4, %5 offen lds\n\tbuffer_load_dwordx4 %2, %4, %5 offen offset:1024 lds\n\t"
    "buffer_load_dwordx4 %3, %4, %5 offen offset:2048 lds" ::"s"(lds), "v"(v[0]), "v"(v[1]), "v"(v[2]), "s"(rs), "s"(soff) : "memory");
  if constexpr (N == 2) asm volatile("s_mov_b32 m0, %0\n\ts_nop 0\n\tbuffer_load_dwordx4 %1, %3, %4 offen lds\n\tbuffer_load_dwordx4 %2, %3, %4 offen offset:1024 lds" ::"s"(lds), "v"(v[0]), "v"(v[1]), "s"(rs), "s"(soff) : "memory");
}
template <int AUX> DI void st16(u32x4 v, rsrc_t rs, u32 voff) { __builtin_amdgcn_raw_buffer_store_b128(v, rs, voff, 0, AUX); }
DI void st_flag(rsrc_t rc, u32 off, u64 v) { __builtin_amdgcn_raw_buffer_store_b64(u32x2{(u32)v, (u32)(v >> 32)}, rc, off, 0, 16); }
template <int SLEEP> DI void spin(rsrc_t rc, u32 off, u64 tag) {
#pragma unroll 1
  for (u32 n = 0; n < (1u << 17); ++n) {
    asm volatile("" ::: "memory");
    const u32x2 v = __builtin_amdgcn_raw_buffer_load_b64(rc, off, 0, 16);
    if (__all(v[0] == (u32)tag && v[1] == (u32)(tag >> 32))) break;
    __builtin_amdgcn_s_sleep(SLEEP);
  }
  asm volatile("" ::: "memory");
}
DI void lds_wait(LAS char* p, u32 v) {
#pragma unroll 1
  for (u32 n = 0; n < (1u << 20); ++n) {
    if (__builtin_amdgcn_readfirstlane(__hip_atomic_load((LAS u32*)p, __ATOMIC_RELAXED, __HIP_MEMORY_SCOPE_WORKGROUP)) >= v) break;
    __builtin_amdgcn_s_sleep(1);
  }
  asm volatile("" ::: "memory");
}
DI bool last_of4(LAS char* cnt) {
  u32 old = 0;
  if (__builtin_amdgcn_mbcnt_hi(~0u, __builtin_amdgcn_mbcnt_lo(~0u, 0u)) == 0) old = __hip_atomic_fetch_add((LAS u32*)cnt, 1u, __ATOMIC_RELAXED, __HIP_MEMORY_SCOPE_WORKGROUP);
  return __builtin_amdgcn_readfirstlane(old) == 3;
}
#define STAMP(k)

__global__ __launch_bounds__(512, 2) void fused_block(const char* x_, const char* wqkv_, const char* wo_, const char* w1_, const char* b1_, const char* w2_, const char* b2_, char* out_, char* ws_) {
#define PRIO_C 0
#define PRIO_B 2
  __shared__ __attribute__((aligned(1024))) char smem[163840];
  u64 tent; const __attribute__((address_space(4))) u64* ka = (const __attribute__((address_space(4))) u64*)__builtin_amdgcn_kernarg_segment_ptr();
  asm volatile("s_memrealtime %0\n\ts_waitcnt lgkmcnt(0)" : "=s"(tent), "+s"(ka)::"memory");
  const char* x = (const char*)ka[0]; const char* wqkv = (const char*)ka[1]; const char* wo = (const char*)ka[2]; const char* w1 = (const char*)ka[3];
  const char* b1 = (const char*)ka[4]; const char* w2 = (const char*)ka[5]; const char* b2 = (const char*)ka[6]; char* out = (char*)ka[7]; char* ws = (char*)ka[8];
  LAS char* const L = (LAS char*)smem;
  const u32 lb = (u32)(uintptr_t)smem;
  int l = threadIdx.x & 63, g, q4, p4, l15;
  auto lanes = [&] { PIN(l); g = l >> 4; q4 = (l >> 2) & 3; p4 = l & 3; l15 = l & 15; };
  lanes();
  int w = __builtin_amdgcn_readfirstlane(threadIdx.x >> 6);
  asm volatile("s_nop 4" : "+s"(w));
  const int x8 = blockIdx.x & 7, rk = blockIdx.x >> 3;
  const bool busy = rk < 28;
  if (w == 0) STAMP(0);
  const rsrc_t rQKV = __builtin_amdgcn_make_buffer_rsrc(ws + WS_QKV, 0, QKV_BYTES, 0x00020000);
  const rsrc_t rAC = __builtin_amdgcn_make_buffer_rsrc(ws + WS_AC, 0, AC_BYTES, 0x00020000);
  const rsrc_t rX = __builtin_amdgcn_make_buffer_rsrc((void*)x, 0, 2048 * 896, 0x00020000);
  const rsrc_t rW2 = __builtin_amdgcn_make_buffer_rsrc((void*)w2, 0, 1792 * 896, 0x00020000);
  const rsrc_t rCTL = __builtin_amdgcn_make_buffer_rsrc(ws, 0, CTL_BYTES, 0x00020000);
  const int rb = (x8 & 3) * 4 + rk / 7, cb = (x8 >> 2) * 7 + rk % 7;
  const int ai = rk & 15, ah = 2 * x8 + (rk >> 4);
  const int cR = (x8 & 3) * 8 + (x8 >> 2) * 4 + rk / 7, cc = rk % 7;
  const int rot = 0;
  auto rotl = [&](int j) { const int t = j + rot; return t >= 7 ? t - 7 : t; };
  auto kstep = [&](int j, int n) {
    const int t = n % 14 + 2 * rot, u = 14 * j + (t >= 14 ? t - 14 : t);
    return u + (u & ~3) + (n >= 14 ? 4 : 0);
  };
  u64 tag;
  u32x4 xres, bres;
  auto ldres = [&] {
    const int tid = w * 64 + l;
    xres = ld16_async<false>((tid >> 3) * 896 + (tid & 7) * 16, rX, cR * 64 * 896 + cc * 128);
    bres = ld16_async<false>((l & 7) * 16, __builtin_amdgcn_make_buffer_rsrc((void*)b2, 0, 896, 0x00020000), cc * 128);
  };
  auto epoch = [&] { asm volatile("s_load_dwordx2 %0, %1, 0x0\n\ts_waitcnt lgkmcnt(0)" : "=s"(tag) : "s"(ws) : "memory"); tag += 1; };

  if (w < 4) {
    if (busy) {
      epoch();
      constexpr int PF = 4;
      u32 oX[4], oH[2], oQ[3];
#pragma unroll
      for (int p = 0; p < 4; ++p) oX[p] = (4 * w + ((l & 31) >> 3)) * 1024 + (l & 7) * 128 + (((2 * p + (g >> 1)) ^ (l15 >> 1)) << 4);
      const int rq = w == 3 ? 0 : w;
#pragma unroll
      for (int i = 0; i < 3; ++i) oQ[i] = RING + (8 * (g >> 1) + q4) * 192 + ((i + rq) % 3) * 64 + (g & 1) * 32 + p4 * 8;
      const u32 selv = l15 == (g & 1) ? 0x3f803f80u : 0u;
      const bf16x8 sel = __builtin_bit_cast(bf16x8, u32x4{selv, selv, selv, selv});
      bf16x8 xf[28], wf[8];
      f32x16 aH[2], aQ[3] = {};
      f32x4 cs = {}, sx = {};
      float rs;
      bf16x8 lnb;
      const int ks = w >> 1;
      auto rdW = [&](auto SS, auto II) {
        constexpr int s = decltype(SS)::v, i = decltype(II)::v;
        constexpr int slot = (s < 14 ? s * 6 + i : 84 + i) % 8;
        if constexpr (s < 14) { const u32 o = oQ[i % 3] + (stage_addr(s) - RING) + (i / 3) * 3072; wf[slot] = trfrag(L, o, o + 768); }
        else if constexpr (s < 28) { const u32 o = oH[i & 1] + stage_addr(s) + (i >> 1) * 2048; wf[slot] = trfrag(L, o, o + 512); }
      };
      auto rdX = [&](auto SS, auto EE) { constexpr int k = 2 * decltype(SS)::v + decltype(EE)::v; if constexpr (k < 28) xf[k] = lds_ld<bf16x8>(L, oX[k & 3] + (k >> 2) * 16384); };
      auto putcs = [&](u32 base) {
        u32x4 v;
#pragma unroll
        for (int j = 0; j < 4; ++j) { const float hi = rbf(cs[j]); v[j] = pk(hi, cs[j] - hi); }
        if (l15 < 2) lds_st<u32x4>(L, base + (l15 * 16 + 4 * g) * 4, v);
        cs = f32x4{};
      };
      auto hepi = [&](int half) {
        u32 c[2][2], bb[2];
#pragma unroll
        for (int i = 0; i < 2; ++i) {
          const int ct = i ^ (w & 1);
          c[i][0] = lds_ld<u32>(L, L_CSH + half * 512 + (ct * 32 + (l & 31)) * 4); c[i][1] = lds_ld<u32>(L, L_CSH + half * 512 + 256 + (ct * 32 + (l & 31)) * 4);
          bb[i] = lds_ld<unsigned short>(L, L_B1 + (half * 64 + ct * 32 + (l & 31)) * 2);
        }
        SB();
#pragma unroll
        for (int i = 0; i < 2; ++i) aH[i] = mfma32(__builtin_bit_cast(bf16x8, u32x4{c[i][0], c[i][1], bb[i], 0u}), lnb, aH[i]);
        SB();
        const float rs2 = rs * rs;
#pragma unroll
        for (int i = 0; i < 2; ++i)
#pragma unroll
          for (int e = 0; e < 2; ++e) {
            u32x4 hp;
#pragma unroll
            for (int j = 0; j < 4; ++j) {
              const float t0 = aH[i][8 * e + 2 * j], t1 = aH[i][8 * e + 2 * j + 1];
              hp[j] = pk(t0 * rs2 * fmx(t0, 0.f), t1 * rs2 * fmx(t1, 0.f));
            }
            st16<16>(hp, rAC, l * 16 + ((rb * 4 + w) * 144 + cb * 8 + half * 4 + (i ^ (w & 1)) * 2 + e) * 1024);
          }
        SB();
      };
      bar();
      STAMP(1);
      rdX(IC<0>{}, IC<0>{}); rdX(IC<0>{}, IC<1>{});
      sfor<0, PF>([&](auto I) { rdW(IC<0>{}, I); });
      sfor<0, 28>([&](auto SS) {
        constexpr int s = decltype(SS)::v, NT = s < 14 ? 6 : 8;
        if constexpr (s == B_FA) wait_bar<0>();
        else if constexpr (s == B_FQ) wait_bar<(B_FQ > 21 ? 4 : 0)>();
        else if constexpr (s == 13 || s == 14 || s == 21) wait_bar<63>();
        else bar();
        if constexpr (s == 13) {
          lanes();
#pragma unroll
          for (int i = 0; i < 2; ++i) oH[i] = (g >> 1) * 1024 + q4 * 128 + (((i ^ (w & 1)) ^ (q4 >> 1)) << 6) + (g & 1) * 32 + p4 * 8;
        }
        if constexpr (s == 14) {
          STAMP(2);
          const u32x2 st = lds_ld<u32x2>(L, L_ST + (w * 32 + (l & 31)) * 8);
          u32 c[3];
#pragma unroll
          for (int i = 0; i < 3; ++i) c[i] = lds_ld<u32>(L, L_CSQ + (((i + rq) % 3) * 32 + (l & 31)) * 4);
          SB();
          const u32 mm2 = l < 32 ? (st[0] & 0xffffu) * 0x10001u : 0u;
          lnb = __builtin_bit_cast(bf16x8, u32x4{mm2, mm2, l < 32 ? st[0] >> 16 : 0u, 0u});
          rs = __uint_as_float(st[1]);
#pragma unroll
          for (int i = 0; i < 3; ++i) aQ[i] = mfma32(__builtin_bit_cast(bf16x8, u32x4{c[i], 0u, 0u, 0u}), lnb, aQ[i]);
          SB();
          const int fl = ((g & 1) << 1) | (g >> 1);
#pragma unroll
          for (int i = 0; i < 3; ++i)
#pragma unroll
            for (int e = 0; e < 2; ++e) {
              const int tq = 6 * cb + 2 * ((i + rq) % 3) + e;
              const float m = tq < 28 ? rs * (0.18898223650461361f * 1.4426950408889634f) : rs;
              const auto s0 = __builtin_amdgcn_permlane16_swap(pk(aQ[i][8 * e] * m, aQ[i][8 * e + 1] * m), pk(aQ[i][8 * e + 4] * m, aQ[i][8 * e + 5] * m), false, false);
              const auto s1 = __builtin_amdgcn_permlane16_swap(pk(aQ[i][8 * e + 2] * m, aQ[i][8 * e + 3] * m), pk(aQ[i][8 * e + 6] * m, aQ[i][8 * e + 7] * m), false, false);
              st16<16>(tq >= 56 && (fl & 1) ? u32x4{s0[1], s1[1], s0[0], s1[0]} : u32x4{s0[0], s1[0], s0[1], s1[1]}, rQKV,
                       fl * 256 + ((l15 ^ (tq >= 56 ? (2 * (tq & 1) + (fl >> 1)) << 2 : 0)) << 4) + ((rb * 4 + w) * 336 + 4 * tq) * 256);
            }
          SB();
          STAMP(3);
        }
        if constexpr (s == 21) hepi(0);
        sfor<0, NT>([&](auto II) {
          constexpr int i = decltype(II)::v, slot = (s < 14 ? s * 6 + i : 84 + i) % 8;
          if constexpr (i + PF < NT) rdW(SS, IC<i + PF>{});
          else rdW(IC<s + 1>{}, IC<i + PF - NT>{});
          if constexpr (i < 2) rdX(IC<s + 1>{}, II);
          if constexpr (s < 14) {
            aQ[i % 3] = mfma32(wf[slot], xf[2 * s + i / 3], aQ[i % 3]);
            if constexpr (i % 3 == 0) cs = mfma(wf[slot], sel, cs);
            if constexpr (s == 0 && (i == 1 || i == 2)) sx = mfma(xf[i - 1], sel, sx);
            if constexpr (s < 13 && i >= 4) sx = mfma(xf[2 * s + i - 2], sel, sx);
          } else {
            constexpr int k = 4 * ((s - 14) % 7) + (i >> 1);
            aH[i & 1] = mfma32(wf[slot], xf[k], k == 0 ? f32x16{} : aH[i & 1]);
            if constexpr (!(i & 1)) { if (((i >> 1) & 1) == ks) cs = mfma(wf[slot], sel, cs); }
          }
          SB();
        });
        if constexpr (s == 12) { if (l15 < 2) lds_st<f32x4>(L, L_SX + (w * 32 + l15 * 16 + 4 * g) * 4, sx); SB(); }
        if constexpr (s == 13) { putcs(L_CSQ + w * 128); SB(); }
        if constexpr (s == 20 || s == 27) { putcs(L_CSH + (s == 27) * 512 + ks * 256 + (w & 1) * 128); SB(); }
      });
      STAMP(4);
      wait_bar<63>();
      lanes();
      STAMP(5);
      hepi(1);
      const rsrc_t rWo = __builtin_amdgcn_make_buffer_rsrc((void*)wo, 0, 448 * 896, 0x00020000);
      u32x4 hA[36][2];
      const u32 vA[2] = {(u32)l * 16, (u32)l * 16 + 144 * 1024}, ring = L_WC + w * WC_RING;
      u32 vW[2];
      vW[0] = (l >> 3) * 896 + (((l & 7) ^ ((g & 1) << 2)) << 4); vW[1] = vW[0] + 7168 - 1024;
      auto ldA = [&](auto M, int a) {
        constexpr int m = decltype(M)::v;
        hA[m][a] = ld16_async<true>(vA[a], rAC, (cR * 288 + (m < 28 ? kstep(w, m) : 112 + 8 * w + (m - 28))) * 1024);
      };
      auto ldW = [&](auto M) {
        constexpr int m = decltype(M)::v;
        if constexpr (m < 28) dman<2>(lb + ring + (m % RW) * 2048, vW, rW2, kstep(w, m) * (16 * 896) + cc * 128);
        else dman<2>(lb + ring + (m % RW) * 2048, vW, rWo, (28 * (4 * w + ((m - 28) >> 1)) + 16 * (m & 1)) * 896 + cc * 128);
      };
      lds_wait(L + L_CNT + 16, 1u);
      STAMP(6);
      __builtin_amdgcn_s_setprio(PRIO_C);
      sfor<0, DA>([&](auto M) { ldA(M, 0); ldA(M, 1); });
      u32 oW[2];
#pragma unroll
      for (int b = 0; b < 2; ++b) oW[b] = ring + (4 * (g >> 1) + q4) * 128 + ((b ^ (q4 >> 1)) << 6) + (g & 1) * 32 + p4 * 8;
      bf16x8 wc[2][2];
      auto rdC = [&](int m) {
#pragma unroll
        for (int b = 0; b < 2; ++b) wc[m & 1][b] = trfrag(L, oW[b] + (m % RW) * 2048, oW[b] + (m % RW) * 2048 + 1024);
      };
      f32x16 acc[2][2] = {};
      u32x2 polled;
      const u32 vFB = WS_FB + (cR >> 1) * 128 + (4 * w + p4) * 8, vFG = WS_FG + (cR >> 1) * 128 + ((14 * w) / 4 + p4) * 8;
      rdC(0);
      sfor<0, 36>([&](auto N) {
        constexpr int n = decltype(N)::v;
        asm volatile("s_waitcnt vmcnt(%2)" : "+v"(hA[n][0]), "+v"(hA[n][1]) : "n"(SC.wC[n]) : "memory");
        if constexpr (n == 0) { if (last_of4(L + L_CNT) && l == 0) st_flag(rCTL, WS_FG + rb * 128 + cb * 8, tag); }
        if constexpr (n == TB - 2 || n == TO - 2) polled = ld8c_async(n == TB - 2 ? vFG : vFB, rCTL);
        if constexpr (n == TB || n == TO) {
          PIN(polled);
          if (!__all(polled[0] == (u32)tag && polled[1] == (u32)(tag >> 32))) spin<1>(rCTL, n == TB ? vFG : vFB, tag);
        }
        if constexpr (n + 1 < 36) rdC(n + 1);
        SB();
        auto mm = [&](int a, int b) { acc[a][b] = mfma32(wc[n & 1][b], __builtin_bit_cast(bf16x8, hA[n][a]), acc[a][b]); SB(); };
        mm(0, 0);
        if constexpr (SC.a0[n] < SC.a1[n]) { ldA(IC<SC.a0[n] % 36>{}, 0); mm(0, 1); ldA(IC<SC.a0[n] % 36>{}, 1); } else mm(0, 1);
        if constexpr (SC.a0[n] + 1 < SC.a1[n]) { ldA(IC<(SC.a0[n] + 1) % 36>{}, 0); ldA(IC<(SC.a0[n] + 1) % 36>{}, 1); }
        mm(1, 0);
        if constexpr (n >= 1 && n + RW - 1 < 36) ldW(IC<n + RW - 1>{});
        mm(1, 1);
        if constexpr (n == 33) ldres();
      });
      STAMP(10);
#pragma unroll
      for (int a = 0; a < 2; ++a)
#pragma unroll
        for (int b = 0; b < 2; ++b)
#pragma unroll
          for (int k = 0; k < 4; ++k)
            lds_st<f32x4>(L, ring + (a * 32 + (l & 31)) * RED_PITCH + (b * 32 + k * 8 + 4 * (g >> 1)) * 4,
                          f32x4{acc[a][b][4 * k], acc[a][b][4 * k + 1], acc[a][b][4 * k + 2], acc[a][b][4 * k + 3]});
      asm volatile("s_waitcnt vmcnt(0)" : "+v"(xres), "+v"(bres)::"memory");
    } else { bar();
      if (blockIdx.x == 224 && w == 0) { while (__builtin_amdgcn_s_memrealtime() - tent < 5000) __builtin_amdgcn_s_sleep(1); }
    }
  } else {
    const int li = w & 1;
    if (!busy) {
      epoch();
      if (w == 6 && l < 8) lds_st<u32>(L, L_CNT + l * 4, 0u);
      wait_bar<63>();
    } else if (w < 6) {
      const rsrc_t rW1 = __builtin_amdgcn_make_buffer_rsrc((void*)w1, 0, 448 * 3584, 0x00020000);
      const rsrc_t rWq = __builtin_amdgcn_make_buffer_rsrc((void*)wqkv, 0, 448 * 2688, 0x00020000);
      u32 vX[4], vH[4], vQ[3], vC[2], so7[7];
#pragma unroll
      for (int j = 0; j < 4; ++j) {
        vX[j] = (8 * j + (l >> 3)) * 896 + (((l & 7) ^ ((j & 1) * 4 + g)) << 4) - 1024 * j;
      }
#pragma unroll
      for (int j = 0; j < 3; ++j) { const int n = 64 * j + l; vQ[j] = (n / 12) * 2688 + (n % 12) * 16 - 1024 * j; }
#pragma unroll
      for (int j = 0; j < 7; ++j) so7[j] = rotl(j);
      const u32 sX = (rb * 128 + 64 * li) * 896, sH = cb * 256 + 32 * li * 3584, sQ = cb * 192 + 16 * li * 2688;
      auto issue = [&](auto O) {
        constexpr int op = decltype(O)::v, k = op / 256, a = (op % 256) / 2, e = op & 1;
        if constexpr (k == K_X) dman<4>(lb + (a * 16 + 8 * li + 4 * e) * 1024, vX, rX, sX + so7[a] * 128 + e * 28672);
        if constexpr (k == K_WA && a < 14) dman<3>(lb + stage_addr(a) + li * 3072, vQ, rWq, sQ + so7[a >> 1] * (64 * 2688) + (a & 1) * (32 * 2688));
        if constexpr (k == K_WA && a >= 14) dman<4>(lb + stage_addr(a) + li * 4096, vH, rW1, sH + so7[(a - 14) % 7] * (64 * 3584) + (a >= 21 ? 128 : 0));
        if constexpr (k == K_BIAS) dma4(lb + L_B1, cb * 256 + l * 4, __builtin_amdgcn_make_buffer_rsrc((void*)b1, 0, 3584, 0x00020000), 0);
        if constexpr (k == K_WCP) dman<2>(lb + L_WC + (2 * e + li) * WC_RING + a * 2048, vC, rW2, kstep(2 * e + li, a) * (16 * 896) + cc * 128);
      };
      sfor<0, NPRO>([&](auto K) { issue(IC<SC.pro[decltype(K)::v]>{}); });
      epoch();
#pragma unroll
      for (int j = 0; j < 4; ++j) vH[j] = (8 * j + (l >> 3)) * 3584 + (((l & 7) ^ ((g & 1) << 2)) << 4) - 1024 * j;
      vC[0] = (l >> 3) * 896 + (((l & 7) ^ ((g & 1) << 2)) << 4); vC[1] = vC[0] + 7168 - 1024;
      vm_bar<SC.wA[0]>();
      sfor<0, 28>([&](auto SS) {
        constexpr int s = decltype(SS)::v;
        vm_bar<SC.wA[s + 1]>();
        sfor<0, MAXO>([&](auto K) { issue(IC<SC.opA[s][decltype(K)::v]>{}); });
      });
      vm_bar<0>();
    } else {
      epoch();
      u32 oX[4];
#pragma unroll
      for (int p = 0; p < 4; ++p) oX[p] = (8 * li + ((l & 31) >> 3)) * 1024 + (l & 7) * 128 + (((2 * p + (g >> 1)) ^ (l15 >> 1)) << 4);
      float s2[2][2] = {};
      u32x4 xv[2][4];
      sfor<0, 15>([&](auto SS) {
        constexpr int s = decltype(SS)::v;
        wait_bar<63>();
        if constexpr (s < 14) {
#pragma unroll
          for (int k = 0; k < 4; ++k) xv[s & 1][k] = lds_ld<u32x4>(L, oX[(2 * s + (k >> 1)) & 3] + (s >> 1) * 16384 + (k & 1) * 4096);
        }
        SB();
        auto sq = [&](int p) {
#pragma unroll
          for (int k = 0; k < 4; ++k)
#pragma unroll
            for (int j = 0; j < 4; ++j) s2[k & 1][j & 1] = dot2(xv[p][k][j], xv[p][k][j], s2[k & 1][j & 1]);
          PIN(s2[0][0]); PIN(s2[0][1]); PIN(s2[1][0]); PIN(s2[1][1]);
        };
        if constexpr (s >= 1 && s < 14) sq(~s & 1);
        if constexpr (s == 13) sq(1);
        SB();
      });
#pragma unroll
      for (int r = 0; r < 2; ++r) {
        const u32 q2 = __float_as_uint(s2[r][0] + s2[r][1]);
        const auto b = __builtin_amdgcn_permlane32_swap(q2, q2, false, false);
        const float mean = lds_ld<float>(L, L_SX + ((2 * li + r) * 32 + (l & 31)) * 4) * (1.f / 448.f);
        const float var = fmaxf((__uint_as_float(b[0]) + __uint_as_float(b[1])) * (1.f / 448.f) - mean * mean, 0.f);
        const float sd = rbf(__builtin_amdgcn_sqrtf(rbf(rbf(var) + 1e-5f)));
        if (l < 32) lds_st<u32x2>(L, L_ST + ((2 * li + r) * 32 + l) * 8, u32x2{pk(-mean, sd), __float_as_uint(__builtin_amdgcn_rcpf(sd))});
      }
      if (w == 6 && l < 8) lds_st<u32>(L, L_CNT + l * 4, 0u);
      wait_bar<63>();
#pragma unroll 1
      for (int s = 15; s < 29; ++s) {
        bar();
        if (w == 7 && l == 0 && (s == B_FQ || s == B_FA)) st_flag(rCTL, (s == B_FQ ? WS_FQ : WS_FH) + rb * 128 + cb * 8, tag);
      }
      if (w == 7) {
        spin<1>(rCTL, WS_FH + (cR >> 1) * 128 + (l < 14 ? l : 0) * 8, tag);
        __hip_atomic_store((LAS u32*)(L + L_CNT + 16), 1u, __ATOMIC_RELAXED, __HIP_MEMORY_SCOPE_WORKGROUP);
      }
    }
    lanes();
    const int aw = w - 4;
    __builtin_amdgcn_s_setprio(PRIO_B);
#pragma unroll
    for (int k = 0; k < 2; ++k) if (k == 0 || l < 32) lds_st<u32x2>(L, L_V + (3 * aw + 2 * k + (l >> 5)) * 2048 + 1792 + (l & 31) * 8, u32x2{0x3f80u, 0u});
    if (w == 6) {
      const int e = l & 1, k = l < 14 ? l >> 1 : 0;
      const int ty = k == 0 ? 0 : (k < 4 ? 1 : 2), ro = k == 0 ? 0 : (k < 4 ? k - 1 : k - 4);
      const int rr = ai - ro < 0 ? 0 : ai - ro;
      spin<4>(rCTL, WS_FQ + rr * 128 + ((ty * 448 + 28 * ah + 27 * e) / 96) * 8, tag);
      __hip_atomic_store((LAS u32*)(L + L_CNT + 12), 1u, __ATOMIC_RELAXED, __HIP_MEMORY_SCOPE_WORKGROUP);
    } else lds_wait(L + L_CNT + 12, 1u);
    if (w == 4) STAMP(7);
#pragma unroll
    for (int j = 0; j < 6; ++j) {
      const int b = 3 * aw + (j >> 1), tb = 4 * ai - 8 + b < 0 ? 0 : 4 * ai - 8 + b;
      const u32 so = (tb * 336 + 112 + 112 * (j & 1) + 7 * ah) * 256, d = lb + ((j & 1) ? L_V + b * 2048 : L_K + b * 1792);
      dma16<true>(d, l * 16, rQKV, so);
      if (l < 48) dma16<true>(d + 1024, l * 16, rQKV, so + 1024);
    }
    const int hh = g >> 1;
    u32x2 qv[4];
#pragma unroll
    for (int k = 0; k < 4; ++k) qv[k] = ld8c_async(k == 3 && hh ? 0xfffffff0u : ((4 * ai + aw) * 336 + 7 * ah + 2 * k + hh) * 256 + l15 * 16 + (g & 1) * 8, rQKV);
    asm volatile("s_waitcnt vmcnt(0) lgkmcnt(0)" : "+v"(qv[0]), "+v"(qv[1]), "+v"(qv[2]), "+v"(qv[3])::"memory");
    if (!last_of4(L + L_CNT + 4)) lds_wait(L + L_CNT + 4, 4u);
    asm volatile("" ::: "memory");
    if (w == 4) STAMP(8);
    const bf16x8 qf[2] = {__builtin_bit_cast(bf16x8, u32x4{qv[0][0], qv[0][1], qv[1][0], qv[1][1]}), __builtin_bit_cast(bf16x8, u32x4{qv[2][0], qv[2][1], qv[3][0], qv[3][1]})};
    const u32 oK = L_K + aw * 1792 + hh * 256 + l15 * 16 + (g & 1) * 8, oK3 = hh ? oK + 1280 : oK + 1536;
    const int F = 7 * ah + 4 * (g & 1) + p4;
    u32 oV[2][2];
#pragma unroll
    for (int t = 0; t < 2; ++t)
#pragma unroll
      for (int e = 0; e < 2; ++e) oV[t][e] = L_V + aw * 2048 + (4 * (g & 1) + p4) * 256 + (((8 * e + 4 * hh + q4) ^ ((F & 6) << 1)) << 4) + ((t ^ (F & 1)) << 3);
    f32x16 sc[9];
    bf16x8 kf[9][2], vf[9][2];
    auto rdK = [&](int m) {
      const u32x2 a = lds_ld<u32x2>(L, oK + m * 1792), b = lds_ld<u32x2>(L, oK + m * 1792 + 512), c = lds_ld<u32x2>(L, oK + m * 1792 + 1024), d = lds_ld<u32x2>(L, oK3 + m * 1792);
      kf[m][0] = __builtin_bit_cast(bf16x8, u32x4{a[0], a[1], b[0], b[1]}); kf[m][1] = __builtin_bit_cast(bf16x8, u32x4{c[0], c[1], d[0], d[1]});
    };
    auto rdV = [&](int m) { vf[m][0] = trfrag(L, oV[0][0] + m * 2048, oV[0][1] + m * 2048); vf[m][1] = trfrag(L, oV[1][0] + m * 2048, oV[1][1] + m * 2048); };
    rdK(0); SB(); rdK(8); SB(); rdK(1); SB();
#pragma unroll
    for (int i = 0; i < 9; ++i) {
      const int m = i == 0 ? 0 : (i == 1 ? 8 : i - 1);
      if (i + 2 < 8) rdK(i + 2); else if (i >= 7) rdV(i - 6);
      sc[m] = mfma32(kf[m][1], qf[1], mfma32(kf[m][0], qf[0], f32x16{}));
      SB();
    }
    constexpr float NEG = -1e30f;
    if (ai < 2) {
      asm volatile("" ::: "memory");
      const int m0 = 8 - 4 * ai - aw;
#pragma unroll
      for (int m = 0; m < 8; ++m)
        if (m < m0) {
#pragma unroll
          for (int r = 0; r < 16; ++r) sc[m][r] = NEG;
        }
    }
    const int dq = (l & 31) - 4 * hh;
    u32 mk[8];
#pragma unroll
    for (int r = 0; r < 16; r += 2) {
      const bool c0 = (r & 3) + 8 * (r >> 2) <= dq, c1 = (r & 3) + 1 + 8 * (r >> 2) <= dq;
      sc[0][r] = c0 ? sc[8][r] : sc[0][r]; sc[0][r + 1] = c1 ? sc[8][r + 1] : sc[0][r + 1];
      mk[r >> 1] = c1 ? 0xffffffffu : (c0 ? 0xffffu : 0u);
    }
    float mx0 = fmx(sc[0][0], sc[0][1]), mx1 = fmx(sc[0][2], sc[0][3]);
#pragma unroll
    for (int k = 1; k < 32; ++k) { mx0 = fmx(fmx(mx0, sc[k >> 2][4 * (k & 3)]), sc[k >> 2][4 * (k & 3) + 1]); mx1 = fmx(fmx(mx1, sc[k >> 2][4 * (k & 3) + 2]), sc[k >> 2][4 * (k & 3) + 3]); }
    mx0 = fmx(mx0, mx1);
    const auto ms = __builtin_amdgcn_permlane32_swap(__float_as_uint(mx0), __float_as_uint(mx0), false, false);
    const float mx = fmx(__uint_as_float(ms[0]), __uint_as_float(ms[1]));
    SB();
    f32x16 o = {};
#pragma unroll
    for (int i = 0; i < 8; ++i) {
      const int m = (i + 1) & 7;
      if (i < 5) rdV(i + 3); else if (i == 5) rdV(0); else if (i == 6) rdV(8);
      u32x4 p[2];
#pragma unroll
      for (int j = 0; j < 8; ++j) {
        f32x2 d = f32x2{sc[m][2 * j], sc[m][2 * j + 1]} - f32x2{mx, mx};
        PIN(d);
        p[j >> 2][j & 3] = pk(__builtin_amdgcn_exp2f(d[0]), __builtin_amdgcn_exp2f(d[1]));
      }
#pragma unroll
      for (int s = 0; s < 2; ++s) {
        if (m) o = mfma32(vf[m][s], __builtin_bit_cast(bf16x8, p[s]), o);
        else {
          const u32x4 k4 = {mk[4 * s], mk[4 * s + 1], mk[4 * s + 2], mk[4 * s + 3]};
          o = mfma32(vf[0][s], __builtin_bit_cast(bf16x8, p[s] & ~k4), o);
          o = mfma32(vf[8][s], __builtin_bit_cast(bf16x8, p[s] & k4), o);
        }
      }
      SB();
    }
    const auto dn = __builtin_amdgcn_permlane32_swap(__float_as_uint(o[12]), __float_as_uint(o[12]), false, false);
    const float inv = __builtin_amdgcn_rcpf(__uint_as_float(dn[1]));
    const float in2 = hh ? 0.f : inv;
    const u32 so = ((ai * 4 + aw) * 144 + 112 + 2 * ah) * 1024;
    st16<16>(u32x4{pk(o[0] * inv, o[1] * inv), pk(o[2] * inv, o[3] * inv), pk(o[4] * inv, o[5] * inv), pk(o[6] * inv, o[7] * inv)}, rAC, l * 16 + so);
    st16<16>(u32x4{pk(o[8] * inv, o[9] * inv), pk(o[10] * inv, o[11] * inv), pk(o[12] * in2, o[13] * in2), pk(o[14] * in2, o[15] * in2)}, rAC, l * 16 + so + 1024);
    if (w == 4) STAMP(9);
    asm volatile("s_waitcnt vmcnt(0)" ::: "memory");
    if (last_of4(L + L_CNT + 8)) {
      if (l == 0) st_flag(rCTL, WS_FB + ai * 128 + ah * 8, tag);
      if (blockIdx.x == 255) {
#pragma unroll 1
        for (int k = 0; k < 4; ++k) spin<8>(rCTL, WS_FB + (k * 64 + l) * 8, tag);
        if (l == 0) st_flag(rCTL, 0, tag);
      }
    }
    if (busy) { ldres(); asm volatile("s_waitcnt vmcnt(0)" : "+v"(xres), "+v"(bres)::"memory"); }
  }
  if (busy) {
    wait_bar<63>();
    lanes();
    const int tid = w * 64 + l, row = tid >> 3, c8 = tid & 7;
    f32x4 r0 = {bflo(xres[0]) + bflo(bres[0]), bfhi(xres[0]) + bfhi(bres[0]), bflo(xres[1]) + bflo(bres[1]), bfhi(xres[1]) + bfhi(bres[1])};
    f32x4 r1 = {bflo(xres[2]) + bflo(bres[2]), bfhi(xres[2]) + bfhi(bres[2]), bflo(xres[3]) + bflo(bres[3]), bfhi(xres[3]) + bfhi(bres[3])};
#pragma unroll
    for (int j = 0; j < 4; ++j) {
      r0 += lds_ld<f32x4>(L, L_WC + j * WC_RING + row * RED_PITCH + c8 * 32);
      r1 += lds_ld<f32x4>(L, L_WC + j * WC_RING + row * RED_PITCH + c8 * 32 + 16);
    }
    st16<16>(u32x4{pk(r0[0], r0[1]), pk(r0[2], r0[3]), pk(r1[0], r1[1]), pk(r1[2], r1[3])},
                                           __builtin_amdgcn_make_buffer_rsrc(out, 0, 2048 * 896, 0x00020000), row * 896 + c8 * 16 + cR * 64 * 896 + cc * 128);
  }
  if (w == 0) STAMP(11);
}

extern "C" void kernel_launch(void* const* d_in, const int* in_sizes, int n_in, void* d_out, int out_size, void* d_ws, size_t ws_size, hipStream_t stream) {
  fused_block<<<256, 512, 0, stream>>>((const char*)d_in[0], (const char*)d_in[1], (const char*)d_in[2], (const char*)d_in[4], (const char*)d_in[5],
                                       (const char*)d_in[6], (const char*)d_in[7], (char*)d_out, (char*)d_ws);
}
```

```cpp
#define NOROT
#include <hip/hip_runtime.h>
#include <hip/hip_bf16.h>
#include <stdint.h>

#define DI __device__ __forceinline__
#define LAS __attribute__((address_space(3)))
#define GAS __attribute__((address_space(1)))
typedef unsigned u32;
typedef unsigned long long u64;
typedef __bf16 bf16x8 __attribute__((ext_vector_type(8)));
typedef __bf16 bf16x2 __attribute__((ext_vector_type(2)));
typedef short s16x4 __attribute__((ext_vector_type(4)));
typedef float f32x4 __attribute__((ext_vector_type(4)));
typedef float f32x2 __attribute__((ext_vector_type(2)));
typedef float f32x16 __attribute__((ext_vector_type(16)));
typedef unsigned u32x4 __attribute__((ext_vector_type(4)));
typedef unsigned u32x2 __attribute__((ext_vector_type(2)));
typedef __amdgpu_buffer_rsrc_t rsrc_t;

template <int N> struct IC { static constexpr int v = N; };
template <int B, int E, class F> DI void sfor(F&& f) { if constexpr (B < E) { f(IC<B>{}); sfor<B + 1, E>(f); } }

constexpr u32 WS_FH = 256, WS_FQ = WS_FH + 2048, WS_FB = WS_FQ + 2048, WS_FG = WS_FB + 2048, CTL_BYTES = 16384;
constexpr u32 WS_QKV = 16384, QKV_BYTES = 64u * 336u * 256u;
constexpr u32 WS_AC = WS_QKV + QKV_BYTES, AC_BYTES = 128u * 72u * 1024u;
constexpr u32 SLOT = 16384, RING = 7 * SLOT;
constexpr u32 L_SX = 45056, L_CSQ = 45568, L_B1 = 46080, L_CSH = 46336, L_ST = 47360, L_CNT = 48384;
constexpr u32 L_K = 0, L_V = 21504;
constexpr int RW = 12;
constexpr u32 L_WC = 49152, WC_RING = RW * 2048, RED_PITCH = 272;
constexpr u32 stage_addr(int s) { return s <= 20 ? RING + (s % 6) * 8192u : (s <= 25 ? (s - 21) * 8192u : RING + (s - 22) * 8192u); }

enum { K_NONE = 0, K_X, K_WA, K_BIAS, K_WCP };
constexpr int OP(int k, int a, int e = 0) { return k * 256 + 2 * a + e; }
constexpr int opn(int op) { int k = op / 256, a = (op % 256) / 2; return k == K_NONE ? 0 : k == K_X ? 4 : k == K_WA ? (a < 14 ? 3 : 4) : k == K_WCP ? 2 : 1; }
#define CAP_1 8
#define CAP_2 8
#define CAP_E 20
#define P_DA 8
#define P_TB 11
#define P_TO 25
#define P_FQ 23
#define P_FA 27
constexpr int MAXO = 12, NPRO = 8, DA = P_DA, TB = P_TB, TO = P_TO, B_FQ = P_FQ, B_FA = P_FA;
struct Sched {
  int pro[NPRO], opA[28][MAXO];
  int wA[29];
  int wC[36], a0[36], a1[36];
  int ok;
};
constexpr int cl(int n) { return n > 63 ? 63 : n; }
constexpr int free_at(u32 b) {
  return b < RING ? 2 * (int)(b / SLOT) + 1 : (int[6]){19, 20, 21, 16, 28, 28}[(b - RING) / 8192];
}
constexpr Sched make_sched() {
  Sched r{};
  r.ok = 1;
  int n = 0, iW[28] = {}, iX[7] = {}, iB = 0;
  int lo[128] = {}, le[128] = {}, nl = 0;
  for (int s = 0; s < 28; ++s) {
    if (s < 14 && !(s & 1)) { lo[nl] = OP(K_X, s >> 1, 0); le[nl++] = -1; lo[nl] = OP(K_X, s >> 1, 1); le[nl++] = -1; }
    lo[nl] = OP(K_WA, s); le[nl++] = s <= 20 ? s - 5 : (s <= 25 ? free_at(stage_addr(s) + 8191) : s - 9);
    if (s == 17) { lo[nl] = OP(K_BIAS, 0); le[nl++] = 5; }
  }
  for (int k = 0; k < RW; ++k) for (int j = 0; j < 2; ++j) {
    int f = 0;
    for (int e = 0; e < 4; ++e) { const int t = free_at(L_WC + (2 * j + (e >> 1)) * WC_RING + k * 2048 + (e & 1) * 2047); if (t > f) f = t; }
    lo[nl] = OP(K_WCP, k, j); le[nl++] = f;
  }
  bool done[128] = {};
  auto note = [&](int op) { n += opn(op); int k = op / 256, a = (op % 256) / 2; if (k == K_X) iX[a] = (op & 1) ? n : 0; if (k == K_WA) iW[a] = n; if (k == K_BIAS) iB = n; };
  for (int k = 0; k < NPRO; ++k) { r.pro[k] = lo[k]; done[k] = true; note(lo[k]); }
  r.wA[0] = cl(n - iW[0]);
  for (int s = 0; s < 28; ++s) {
    int need = 0;
    if (s + 1 < 28) {
      need = iW[s + 1]; if (!need) r.ok = 0;
      if (s + 1 <= 13) { int v = iX[(s + 1) >> 1]; if (!v) r.ok = 0; if (v > need) need = v; }
      if (s == 21) { if (!iB) r.ok = 0; if (iB > need) need = iB; }
    }
    r.wA[s + 1] = need ? cl(n - need) : 63;
    int cap = s == 14 || s == 21 ? CAP_E : (s < 14 ? CAP_1 : CAP_2), c = 0, used = 0;
    for (int k = 0; k < nl; ++k) if (!done[k] && le[k] <= s && used + opn(lo[k]) <= cap && c < MAXO) { done[k] = true; r.opA[s][c++] = lo[k]; used += opn(lo[k]); note(lo[k]); }
  }
  for (int k = 0; k < nl; ++k) if (!done[k]) r.ok = 0;
  n = 4;
  int iA[36] = {}, iWc[36] = {}, iP = 0, nA = DA;
  for (int m = 0; m < DA; ++m) { n += 2; iA[m] = n; }
  for (int s = 0; s < 36; ++s) {
    int need = iA[s]; if (!need) r.ok = 0;
    if (s + 1 < 36 && s + 1 >= RW) { if (!iWc[s + 1]) r.ok = 0; if (iWc[s + 1] > need) need = iWc[s + 1]; }
    if ((s == TB || s == TO) && iP > need) need = iP;
    r.wC[s] = cl(n - need);
    if (s == TB - 2 || s == TO - 2) { n += 1; iP = n; }
    r.a0[s] = nA;
    for (int c = 0; c < 2 && nA < 36 && nA <= s + DA && (nA < 14 || s >= TB) && (nA < 28 || s >= TO); ++c) { n += 2; iA[nA++] = n; }
    r.a1[s] = nA;
    if (s >= 1 && s + RW - 1 < 36) { n += 2; iWc[s + RW - 1] = n; }
    if (s == 33) n += 2;
  }
  return r;
}
constexpr Sched SC = make_sched();
static_assert(SC.ok, "schedule");

template <bool C> DI void dma16(u32 lds, u32 voff, rsrc_t rs, u32 soff) {
  if constexpr (C) asm volatile("s_mov_b32 m0, %0\n\ts_nop 0\n\tbuffer_load_dwordx4 %1, %2, %3 offen sc1 lds" ::"s"(lds), "v"(voff), "s"(rs), "s"(soff) : "memory");
  else asm volatile("s_mov_b32 m0, %0\n\ts_nop 0\n\tbuffer_load_dwordx4 %1, %2, %3 offen lds" ::"s"(lds), "v"(voff), "s"(rs), "s"(soff) : "memory");
}
template <bool C> DI u32x4 ld16_async(u32 voff, rsrc_t rs, u32 soff) {
  u32x4 r;
  if constexpr (C) asm volatile("buffer_load_dwordx4 %0, %1, %2, %3 offen sc1" : "=v"(r) : "v"(voff), "s"(rs), "s"(soff) : "memory");
  else asm volatile("buffer_load_dwordx4 %0, %1, %2, %3 offen" : "=v"(r) : "v"(voff), "s"(rs), "s"(soff) : "memory");
  return r;
}
DI void dma4(u32 lds, u32 voff, rsrc_t rs, u32 soff) {
  asm volatile("s_mov_b32 m0, %0\n\ts_nop 0\n\tbuffer_load_dword %1, %2, %3 offen lds" ::"s"(lds), "v"(voff), "s"(rs), "s"(soff) : "memory");
}
DI u32x2 ld8c_async(u32 voff, rsrc_t rs) {
  u32x2 r; asm volatile("buffer_load_dwordx2 %0, %1, %2, 0 offen sc1" : "=v"(r) : "v"(voff), "s"(rs) : "memory"); return r;
}
template <int N> DI void wait_bar() { asm volatile("s_waitcnt vmcnt(%0) lgkmcnt(0)\n\ts_barrier" ::"n"(N) : "memory"); }
template <int N> DI void vm_bar() { asm volatile("s_waitcnt vmcnt(%0)\n\ts_barrier" ::"n"(N) : "memory"); }
DI void bar() { asm volatile("s_barrier" ::: "memory"); }
#define PIN(x) asm volatile("" : "+v"(x))
#define SB() __builtin_amdgcn_sched_barrier(0)
DI f32x4 mfma(bf16x8 a, bf16x8 b, f32x4 c) { return __builtin_amdgcn_mfma_f32_16x16x32_bf16(a, b, c, 0, 0, 0); }
DI f32x16 mfma32(bf16x8 a, bf16x8 b, f32x16 c) { return __builtin_amdgcn_mfma_f32_32x32x16_bf16(a, b, c, 0, 0, 0); }
DI u32 pk(float a, float b) { bf16x2 v = {(__bf16)a, (__bf16)b}; return __builtin_bit_cast(u32, v); }
DI float fmx(float a, float b) { return __builtin_elementwise_maximum(a, b); }
DI float rbf(float a) { return (float)(__bf16)a; }
DI float bflo(u32 v) { return __uint_as_float(v << 16); }
DI float bfhi(u32 v) { return __uint_as_float(v & 0xffff0000u); }
DI float dot2(u32 a, u32 b, float c) { return __builtin_amdgcn_fdot2_f32_bf16(__builtin_bit_cast(bf16x2, a), __builtin_bit_cast(bf16x2, b), c, false); }
template <class T> DI T lds_ld(const LAS char* base, u32 off) { return *(const LAS T*)(base + off); }
template <class T> DI void lds_st(LAS char* base, u32 off, T v) { *(LAS T*)(base + off) = v; }
DI bf16x8 trfrag(LAS char* base, u32 off0, u32 off1) {
  s16x4 lo = __builtin_amdgcn_ds_read_tr16_b64_v4i16((LAS s16x4*)(base + off0));
  s16x4 hi = __builtin_amdgcn_ds_read_tr16_b64_v4i16((LAS s16x4*)(base + off1));
  return __builtin_bit_cast(bf16x8, __builtin_shufflevector(lo, hi, 0, 1, 2, 3, 4, 5, 6, 7));
}
template <int N> DI void dman(u32 lds, const u32* v, rsrc_t rs, u32 soff) {
  if constexpr (N == 4) asm volatile("s_mov_b32 m0, %0\n\ts_nop 0\n\tbuffer_load_dwordx4 %1, %5, %6 offen lds\n\tbuffer_load_dwordx4 %2, %5, %6 offen offset:1024 lds\n\t"
    "buffer_load_dwordx4 %3, %5, %6 offen offset:2048 lds\n\tbuffer_load_dwordx4 %4, %5, %6 offen offset:3072 lds" ::"s"(lds), "v"(v[0]), "v"(v[1]), "v"(v[2]), "v"(v[3]), "s"(rs), "s"(soff) : "memory");
  if constexpr (N == 3) asm volatile("s_mov_b32 m0, %0\n\ts_nop 0\n\tbuffer_load_dwordx4 %1, %4, %5 offen lds\n\tbuffer_load_dwordx4 %2, %4, %5 offen offset:1024 lds\n\t"
    "buffer_load_dwordx4 %3, %4, %5 offen offset:2048 lds" ::"s"(lds), "v"(v[0]), "v"(v[1]), "v"(v[2]), "s"(rs), "s"(soff) : "memory");
  if constexpr (N == 2) asm volatile("s_mov_b32 m0, %0\n\ts_nop 0\n\tbuffer_load_dwordx4 %1, %3, %4 offen lds\n\tbuffer_load_dwordx4 %2, %3, %4 offen offset:1024 lds" ::"s"(lds), "v"(v[0]), "v"(v[1]), "s"(rs), "s"(soff) : "memory");
}
template <int AUX> DI void st16(u32x4 v, rsrc_t rs, u32 voff) { __builtin_amdgcn_raw_buffer_store_b128(v, rs, voff, 0, AUX); }
DI void st_flag(rsrc_t rc, u32 off, u64 v) { __builtin_amdgcn_raw_buffer_store_b64(u32x2{(u32)v, (u32)(v >> 32)}, rc, off, 0, 16); }
template <int SLEEP> DI void spin(rsrc_t rc, u32 off, u64 tag) {
#pragma unroll 1
  for (u32 n = 0; n < (1u << 17); ++n) {
    asm volatile("" ::: "memory");
    const u32x2 v = __builtin_amdgcn_raw_buffer_load_b64(rc, off, 0, 16);
    if (__all(v[0] == (u32)tag && v[1] == (u32)(tag >> 32))) break;
    __builtin_amdgcn_s_sleep(SLEEP);
  }
  asm volatile("" ::: "memory");
}
DI void lds_wait(LAS char* p, u32 v) {
#pragma unroll 1
  for (u32 n = 0; n < (1u << 20); ++n) {
    if (__builtin_amdgcn_readfirstlane(__hip_atomic_load((LAS u32*)p, __ATOMIC_RELAXED, __HIP_MEMORY_SCOPE_WORKGROUP)) >= v) break;
    __builtin_amdgcn_s_sleep(1);
  }
  asm volatile("" ::: "memory");
}
DI bool last_of4(LAS char* cnt) {
  u32 old = 0;
  if (__builtin_amdgcn_mbcnt_hi(~0u, __builtin_amdgcn_mbcnt_lo(~0u, 0u)) == 0) old = __hip_atomic_fetch_add((LAS u32*)cnt, 1u, __ATOMIC_RELAXED, __HIP_MEMORY_SCOPE_WORKGROUP);
  return __builtin_amdgcn_readfirstlane(old) == 3;
}
#define STAMP(k)

__global__ __launch_bounds__(512, 2) void fused_block(const char* __restrict__ x, const char* __restrict__ wqkv, const char* __restrict__ wo,
                                                      const char* __restrict__ w1, const char* __restrict__ b1, const char* __restrict__ w2,
                                                      const char* __restrict__ b2, char* __restrict__ out, char* ws) {
#define PRIO_C 0
#define PRIO_B 2
  __shared__ __attribute__((aligned(1024))) char smem[163840];
  LAS char* const L = (LAS char*)smem;
  const u32 lb = (u32)(uintptr_t)smem;
  int l = threadIdx.x & 63, g, q4, p4, l15;
  auto lanes = [&] { PIN(l); g = l >> 4; q4 = (l >> 2) & 3; p4 = l & 3; l15 = l & 15; };
  lanes();
  int w = __builtin_amdgcn_readfirstlane(threadIdx.x >> 6);
  asm volatile("s_nop 4" : "+s"(w));
  const int x8 = blockIdx.x & 7, rk = blockIdx.x >> 3;
  const bool busy = rk < 28;
  if (w == 0) STAMP(0);
  const rsrc_t rQKV = __builtin_amdgcn_make_buffer_rsrc(ws + WS_QKV, 0, QKV_BYTES, 0x00020000);
  const rsrc_t rAC = __builtin_amdgcn_make_buffer_rsrc(ws + WS_AC, 0, AC_BYTES, 0x00020000);
  const rsrc_t rX = __builtin_amdgcn_make_buffer_rsrc((void*)x, 0, 2048 * 896, 0x00020000);
  const rsrc_t rW2 = __builtin_amdgcn_make_buffer_rsrc((void*)w2, 0, 1792 * 896, 0x00020000);
  const rsrc_t rCTL = __builtin_amdgcn_make_buffer_rsrc(ws, 0, CTL_BYTES, 0x00020000);
  const int rb = (x8 & 3) * 4 + rk / 7, cb = (x8 >> 2) * 7 + rk % 7;
  const int ai = rk & 15, ah = 2 * x8 + (rk >> 4);
  const int cR = (x8 & 3) * 8 + (x8 >> 2) * 4 + rk / 7, cc = rk % 7;
  const int rot = 0;
  auto rotl = [&](int j) { const int t = j + rot; return t >= 7 ? t - 7 : t; };
  auto kstep = [&](int j, int n) {
    const int t = n % 14 + 2 * rot, u = 14 * j + (t >= 14 ? t - 14 : t);
    return u + (u & ~3) + (n >= 14 ? 4 : 0);
  };
  u64 tag;
  u32x4 xres, bres;
  auto ldres = [&] {
    const int tid = w * 64 + l;
    xres = ld16_async<false>((tid >> 3) * 896 + (tid & 7) * 16, rX, cR * 64 * 896 + cc * 128);
    bres = ld16_async<false>((l & 7) * 16, __builtin_amdgcn_make_buffer_rsrc((void*)b2, 0, 896, 0x00020000), cc * 128);
  };
  auto epoch = [&] { asm volatile("s_load_dwordx2 %0, %1, 0x0\n\ts_waitcnt lgkmcnt(0)" : "=s"(tag) : "s"(ws) : "memory"); tag += 1; };

  if (w < 4) {
    if (busy) {
      epoch();
      constexpr int PF = 4;
      u32 oX[4], oH[2], oQ[3];
#pragma unroll
      for (int p = 0; p < 4; ++p) oX[p] = (4 * w + ((l & 31) >> 3)) * 1024 + (l & 7) * 128 + (((2 * p + (g >> 1)) ^ (l15 >> 1)) << 4);
      const int rq = w == 3 ? 0 : w;
#pragma unroll
      for (int i = 0; i < 3; ++i) oQ[i] = RING + (8 * (g >> 1) + q4) * 192 + ((i + rq) % 3) * 64 + (g & 1) * 32 + p4 * 8;
      const u32 selv = l15 == (g & 1) ? 0x3f803f80u : 0u;
      const bf16x8 sel = __builtin_bit_cast(bf16x8, u32x4{selv, selv, selv, selv});
      bf16x8 xf[28], wf[8];
      f32x16 aH[2], aQ[3] = {};
      f32x4 cs = {}, sx = {};
      float rs;
      bf16x8 lnb;
      const int ks = w >> 1;
      auto rdW = [&](auto SS, auto II) {
        constexpr int s = decltype(SS)::v, i = decltype(II)::v;
        constexpr int slot = (s < 14 ? s * 6 + i : 84 + i) % 8;
        if constexpr (s < 14) { const u32 o = oQ[i % 3] + (stage_addr(s) - RING) + (i / 3) * 3072; wf[slot] = trfrag(L, o, o + 768); }
        else if constexpr (s < 28) { const u32 o = oH[i & 1] + stage_addr(s) + (i >> 1) * 2048; wf[slot] = trfrag(L, o, o + 512); }
      };
      auto rdX = [&](auto SS, auto EE) { constexpr int k = 2 * decltype(SS)::v + decltype(EE)::v; if constexpr (k < 28) xf[k] = lds_ld<bf16x8>(L, oX[k & 3] + (k >> 2) * 16384); };
      auto putcs = [&](u32 base) {
        u32x4 v;
#pragma unroll
        for (int j = 0; j < 4; ++j) { const float hi = rbf(cs[j]); v[j] = pk(hi, cs[j] - hi); }
        if (l15 < 2) lds_st<u32x4>(L, base + (l15 * 16 + 4 * g) * 4, v);
        cs = f32x4{};
      };
      auto hepi = [&](int half) {
        u32 c[2][2], bb[2];
#pragma unroll
        for (int i = 0; i < 2; ++i) {
          const int ct = i ^ (w & 1);
          c[i][0] = lds_ld<u32>(L, L_CSH + half * 512 + (ct * 32 + (l & 31)) * 4); c[i][1] = lds_ld<u32>(L, L_CSH + half * 512 + 256 + (ct * 32 + (l & 31)) * 4);
          bb[i] = lds_ld<unsigned short>(L, L_B1 + (half * 64 + ct * 32 + (l & 31)) * 2);
        }
        SB();
#pragma unroll
        for (int i = 0; i < 2; ++i) aH[i] = mfma32(__builtin_bit_cast(bf16x8, u32x4{c[i][0], c[i][1], bb[i], 0u}), lnb, aH[i]);
        SB();
        const float rs2 = rs * rs;
#pragma unroll
        for (int i = 0; i < 2; ++i)
#pragma unroll
          for (int e = 0; e < 2; ++e) {
            u32x4 hp;
#pragma unroll
            for (int j = 0; j < 4; ++j) {
              const float t0 = aH[i][8 * e + 2 * j], t1 = aH[i][8 * e + 2 * j + 1];
              hp[j] = pk(t0 * rs2 * fmx(t0, 0.f), t1 * rs2 * fmx(t1, 0.f));
            }
            st16<16>(hp, rAC, l * 16 + ((rb * 4 + w) * 144 + cb * 8 + half * 4 + (i ^ (w & 1)) * 2 + e) * 1024);
          }
        SB();
      };
      bar();
      STAMP(1);
      rdX(IC<0>{}, IC<0>{}); rdX(IC<0>{}, IC<1>{});
      sfor<0, PF>([&](auto I) { rdW(IC<0>{}, I); });
      sfor<0, 28>([&](auto SS) {
        constexpr int s = decltype(SS)::v, NT = s < 14 ? 6 : 8;
        if constexpr (s == B_FA) wait_bar<0>();
        else if constexpr (s == B_FQ) wait_bar<(B_FQ > 21 ? 4 : 0)>();
        else if constexpr (s == 13 || s == 14 || s == 21) wait_bar<63>();
        else bar();
        if constexpr (s == 13) {
          lanes();
#pragma unroll
          for (int i = 0; i < 2; ++i) oH[i] = (g >> 1) * 1024 + q4 * 128 + (((i ^ (w & 1)) ^ (q4 >> 1)) << 6) + (g & 1) * 32 + p4 * 8;
        }
        if constexpr (s == 14) {
          STAMP(2);
          const u32x2 st = lds_ld<u32x2>(L, L_ST + (w * 32 + (l & 31)) * 8);
          u32 c[3];
#pragma unroll
          for (int i = 0; i < 3; ++i) c[i] = lds_ld<u32>(L, L_CSQ + (((i + rq) % 3) * 32 + (l & 31)) * 4);
          SB();
          const u32 mm2 = l < 32 ? (st[0] & 0xffffu) * 0x10001u : 0u;
          lnb = __builtin_bit_cast(bf16x8, u32x4{mm2, mm2, l < 32 ? st[0] >> 16 : 0u, 0u});
          rs = __uint_as_float(st[1]);
#pragma unroll
          for (int i = 0; i < 3; ++i) aQ[i] = mfma32(__builtin_bit_cast(bf16x8, u32x4{c[i], 0u, 0u, 0u}), lnb, aQ[i]);
          SB();
          const int fl = ((g & 1) << 1) | (g >> 1);
#pragma unroll
          for (int i = 0; i < 3; ++i)
#pragma unroll
            for (int e = 0; e < 2; ++e) {
              const int tq = 6 * cb + 2 * ((i + rq) % 3) + e;
              const float m = tq < 28 ? rs * (0.18898223650461361f * 1.4426950408889634f) : rs;
              const auto s0 = __builtin_amdgcn_permlane16_swap(pk(aQ[i][8 * e] * m, aQ[i][8 * e + 1] * m), pk(aQ[i][8 * e + 4] * m, aQ[i][8 * e + 5] * m), false, false);
              const auto s1 = __builtin_amdgcn_permlane16_swap(pk(aQ[i][8 * e + 2] * m, aQ[i][8 * e + 3] * m), pk(aQ[i][8 * e + 6] * m, aQ[i][8 * e + 7] * m), false, false);
              st16<16>(tq >= 56 && (fl & 1) ? u32x4{s0[1], s1[1], s0[0], s1[0]} : u32x4{s0[0], s1[0], s0[1], s1[1]}, rQKV,
                       fl * 256 + ((l15 ^ (tq >= 56 ? (2 * (tq & 1) + (fl >> 1)) << 2 : 0)) << 4) + ((rb * 4 + w) * 336 + 4 * tq) * 256);
            }
          SB();
          STAMP(3);
        }
        if constexpr (s == 21) hepi(0);
        sfor<0, NT>([&](auto II) {
          constexpr int i = decltype(II)::v, slot = (s < 14 ? s * 6 + i : 84 + i) % 8;
          if constexpr (i + PF < NT) rdW(SS, IC<i + PF>{});
          else rdW(IC<s + 1>{}, IC<i + PF - NT>{});
          if constexpr (i < 2) rdX(IC<s + 1>{}, II);
          if constexpr (s < 14) {
            aQ[i % 3] = mfma32(wf[slot], xf[2 * s + i / 3], aQ[i % 3]);
            if constexpr (i % 3 == 0) cs = mfma(wf[slot], sel, cs);
            if constexpr (s == 0 && (i == 1 || i == 2)) sx = mfma(xf[i - 1], sel, sx);
            if constexpr (s < 13 && i >= 4) sx = mfma(xf[2 * s + i - 2], sel, sx);
          } else {
            constexpr int k = 4 * ((s - 14) % 7) + (i >> 1);
            aH[i & 1] = mfma32(wf[slot], xf[k], k == 0 ? f32x16{} : aH[i & 1]);
            if constexpr (!(i & 1)) { if (((i >> 1) & 1) == ks) cs = mfma(wf[slot], sel, cs); }
          }
          SB();
        });
        if constexpr (s == 12) { if (l15 < 2) lds_st<f32x4>(L, L_SX + (w * 32 + l15 * 16 + 4 * g) * 4, sx); SB(); }
        if constexpr (s == 13) { putcs(L_CSQ + w * 128); SB(); }
        if constexpr (s == 20 || s == 27) { putcs(L_CSH + (s == 27) * 512 + ks * 256 + (w & 1) * 128); SB(); }
      });
      STAMP(4);
      wait_bar<63>();
      lanes();
      STAMP(5);
      hepi(1);
      const rsrc_t rWo = __builtin_amdgcn_make_buffer_rsrc((void*)wo, 0, 448 * 896, 0x00020000);
      u32x4 hA[36][2];
      const u32 vA[2] = {(u32)l * 16, (u32)l * 16 + 144 * 1024}, ring = L_WC + w * WC_RING;
      u32 vW[2];
      vW[0] = (l >> 3) * 896 + (((l & 7) ^ ((g & 1) << 2)) << 4); vW[1] = vW[0] + 7168 - 1024;
      auto ldA = [&](auto M, int a) {
        constexpr int m = decltype(M)::v;
        hA[m][a] = ld16_async<true>(vA[a], rAC, (cR * 288 + (m < 28 ? kstep(w, m) : 112 + 8 * w + (m - 28))) * 1024);
      };
      auto ldW = [&](auto M) {
        constexpr int m = decltype(M)::v;
        if constexpr (m < 28) dman<2>(lb + ring + (m % RW) * 2048, vW, rW2, kstep(w, m) * (16 * 896) + cc * 128);
        else dman<2>(lb + ring + (m % RW) * 2048, vW, rWo, (28 * (4 * w + ((m - 28) >> 1)) + 16 * (m & 1)) * 896 + cc * 128);
      };
      lds_wait(L + L_CNT + 16, 1u);
      STAMP(6);
      __builtin_amdgcn_s_setprio(PRIO_C);
      sfor<0, DA>([&](auto M) { ldA(M, 0); ldA(M, 1); });
      u32 oW[2];
#pragma unroll
      for (int b = 0; b < 2; ++b) oW[b] = ring + (4 * (g >> 1) + q4) * 128 + ((b ^ (q4 >> 1)) << 6) + (g & 1) * 32 + p4 * 8;
      bf16x8 wc[2][2];
      auto rdC = [&](int m) {
#pragma unroll
        for (int b = 0; b < 2; ++b) wc[m & 1][b] = trfrag(L, oW[b] + (m % RW) * 2048, oW[b] + (m % RW) * 2048 + 1024);
      };
      f32x16 acc[2][2] = {};
      u32x2 polled;
      const u32 vFB = WS_FB + (cR >> 1) * 128 + (4 * w + p4) * 8, vFG = WS_FG + (cR >> 1) * 128 + ((14 * w) / 4 + p4) * 8;
      rdC(0);
      sfor<0, 36>([&](auto N) {
        constexpr int n = decltype(N)::v;
        asm volatile("s_waitcnt vmcnt(%2)" : "+v"(hA[n][0]), "+v"(hA[n][1]) : "n"(SC.wC[n]) : "memory");
        if constexpr (n == 0) { if (last_of4(L + L_CNT) && l == 0) st_flag(rCTL, WS_FG + rb * 128 + cb * 8, tag); }
        if constexpr (n == TB - 2 || n == TO - 2) polled = ld8c_async(n == TB - 2 ? vFG : vFB, rCTL);
        if constexpr (n == TB || n == TO) {
          PIN(polled);
          if (!__all(polled[0] == (u32)tag && polled[1] == (u32)(tag >> 32))) spin<1>(rCTL, n == TB ? vFG : vFB, tag);
        }
        if constexpr (n + 1 < 36) rdC(n + 1);
        SB();
        auto mm = [&](int a, int b) { acc[a][b] = mfma32(wc[n & 1][b], __builtin_bit_cast(bf16x8, hA[n][a]), acc[a][b]); SB(); };
        mm(0, 0);
        if constexpr (SC.a0[n] < SC.a1[n]) { ldA(IC<SC.a0[n] % 36>{}, 0); mm(0, 1); ldA(IC<SC.a0[n] % 36>{}, 1); } else mm(0, 1);
        if constexpr (SC.a0[n] + 1 < SC.a1[n]) { ldA(IC<(SC.a0[n] + 1) % 36>{}, 0); ldA(IC<(SC.a0[n] + 1) % 36>{}, 1); }
        mm(1, 0);
        if constexpr (n >= 1 && n + RW - 1 < 36) ldW(IC<n + RW - 1>{});
        mm(1, 1);
        if constexpr (n == 33) ldres();
      });
      STAMP(10);
#pragma unroll
      for (int a = 0; a < 2; ++a)
#pragma unroll
        for (int b = 0; b < 2; ++b)
#pragma unroll
          for (int k = 0; k < 4; ++k)
            lds_st<f32x4>(L, ring + (a * 32 + (l & 31)) * RED_PITCH + (b * 32 + k * 8 + 4 * (g >> 1)) * 4,
                          f32x4{acc[a][b][4 * k], acc[a][b][4 * k + 1], acc[a][b][4 * k + 2], acc[a][b][4 * k + 3]});
      asm volatile("s_waitcnt vmcnt(0)" : "+v"(xres), "+v"(bres)::"memory");
    } else bar();
  } else {
    const int li = w & 1;
    if (!busy) {
      epoch();
      if (w == 6 && l < 8) lds_st<u32>(L, L_CNT + l * 4, 0u);
      wait_bar<63>();
    } else if (w < 6) {
      const rsrc_t rW1 = __builtin_amdgcn_make_buffer_rsrc((void*)w1, 0, 448 * 3584, 0x00020000);
      const rsrc_t rWq = __builtin_amdgcn_make_buffer_rsrc((void*)wqkv, 0, 448 * 2688, 0x00020000);
      u32 vX[4], vH[4], vQ[3], vC[2], so7[7];
#pragma unroll
      for (int j = 0; j < 4; ++j) {
        vX[j] = (8 * j + (l >> 3)) * 896 + (((l & 7) ^ ((j & 1) * 4 + g)) << 4) - 1024 * j;
      }
#pragma unroll
      for (int j = 0; j < 3; ++j) { const int n = 64 * j + l; vQ[j] = (n / 12) * 2688 + (n % 12) * 16 - 1024 * j; }
#pragma unroll
      for (int j = 0; j < 7; ++j) so7[j] = rotl(j);
      const u32 sX = (rb * 128 + 64 * li) * 896, sH = cb * 256 + 32 * li * 3584, sQ = cb * 192 + 16 * li * 2688;
      auto issue = [&](auto O) {
        constexpr int op = decltype(O)::v, k = op / 256, a = (op % 256) / 2, e = op & 1;
        if constexpr (k == K_X) dman<4>(lb + (a * 16 + 8 * li + 4 * e) * 1024, vX, rX, sX + so7[a] * 128 + e * 28672);
        if constexpr (k == K_WA && a < 14) dman<3>(lb + stage_addr(a) + li * 3072, vQ, rWq, sQ + so7[a >> 1] * (64 * 2688) + (a & 1) * (32 * 2688));
        if constexpr (k == K_WA && a >= 14) dman<4>(lb + stage_addr(a) + li * 4096, vH, rW1, sH + so7[(a - 14) % 7] * (64 * 3584) + (a >= 21 ? 128 : 0));
        if constexpr (k == K_BIAS) dma4(lb + L_B1, cb * 256 + l * 4, __builtin_amdgcn_make_buffer_rsrc((void*)b1, 0, 3584, 0x00020000), 0);
        if constexpr (k == K_WCP) dman<2>(lb + L_WC + (2 * e + li) * WC_RING + a * 2048, vC, rW2, kstep(2 * e + li, a) * (16 * 896) + cc * 128);
      };
      sfor<0, NPRO>([&](auto K) { issue(IC<SC.pro[decltype(K)::v]>{}); });
      epoch();
#pragma unroll
      for (int j = 0; j < 4; ++j) vH[j] = (8 * j + (l >> 3)) * 3584 + (((l & 7) ^ ((g & 1) << 2)) << 4) - 1024 * j;
      vC[0] = (l >> 3) * 896 + (((l & 7) ^ ((g & 1) << 2)) << 4); vC[1] = vC[0] + 7168 - 1024;
      vm_bar<SC.wA[0]>();
      sfor<0, 28>([&](auto SS) {
        constexpr int s = decltype(SS)::v;
        vm_bar<SC.wA[s + 1]>();
        sfor<0, MAXO>([&](auto K) { issue(IC<SC.opA[s][decltype(K)::v]>{}); });
      });
      vm_bar<0>();
    } else {
      epoch();
      u32 oX[4];
#pragma unroll
      for (int p = 0; p < 4; ++p) oX[p] = (8 * li + ((l & 31) >> 3)) * 1024 + (l & 7) * 128 + (((2 * p + (g >> 1)) ^ (l15 >> 1)) << 4);
      float s2[2][2] = {};
      u32x4 xv[2][4];
      sfor<0, 15>([&](auto SS) {
        constexpr int s = decltype(SS)::v;
        wait_bar<63>();
        if constexpr (s < 14) {
#pragma unroll
          for (int k = 0; k < 4; ++k) xv[s & 1][k] = lds_ld<u32x4>(L, oX[(2 * s + (k >> 1)) & 3] + (s >> 1) * 16384 + (k & 1) * 4096);
        }
        SB();
        auto sq = [&](int p) {
#pragma unroll
          for (int k = 0; k < 4; ++k)
#pragma unroll
            for (int j = 0; j < 4; ++j) s2[k & 1][j & 1] = dot2(xv[p][k][j], xv[p][k][j], s2[k & 1][j & 1]);
          PIN(s2[0][0]); PIN(s2[0][1]); PIN(s2[1][0]); PIN(s2[1][1]);
        };
        if constexpr (s >= 1 && s < 14) sq(~s & 1);
        if constexpr (s == 13) sq(1);
        SB();
      });
#pragma unroll
      for (int r = 0; r < 2; ++r) {
        const u32 q2 = __float_as_uint(s2[r][0] + s2[r][1]);
        const auto b = __builtin_amdgcn_permlane32_swap(q2, q2, false, false);
        const float mean = lds_ld<float>(L, L_SX + ((2 * li + r) * 32 + (l & 31)) * 4) * (1.f / 448.f);
        const float var = fmaxf((__uint_as_float(b[0]) + __uint_as_float(b[1])) * (1.f / 448.f) - mean * mean, 0.f);
        const float sd = rbf(__builtin_amdgcn_sqrtf(rbf(rbf(var) + 1e-5f)));
        if (l < 32) lds_st<u32x2>(L, L_ST + ((2 * li + r) * 32 + l) * 8, u32x2{pk(-mean, sd), __float_as_uint(__builtin_amdgcn_rcpf(sd))});
      }
      if (w == 6 && l < 8) lds_st<u32>(L, L_CNT + l * 4, 0u);
      wait_bar<63>();
#pragma unroll 1
      for (int s = 15; s < 29; ++s) {
        bar();
        if (w == 7 && l == 0 && (s == B_FQ || s == B_FA)) st_flag(rCTL, (s == B_FQ ? WS_FQ : WS_FH) + rb * 128 + cb * 8, tag);
      }
      if (w == 7) {
        spin<1>(rCTL, WS_FH + (cR >> 1) * 128 + (l < 14 ? l : 0) * 8, tag);
        __hip_atomic_store((LAS u32*)(L + L_CNT + 16), 1u, __ATOMIC_RELAXED, __HIP_MEMORY_SCOPE_WORKGROUP);
      }
    }
    lanes();
    const int aw = w - 4;
    __builtin_amdgcn_s_setprio(PRIO_B);
#pragma unroll
    for (int k = 0; k < 2; ++k) if (k == 0 || l < 32) lds_st<u32x2>(L, L_V + (3 * aw + 2 * k + (l >> 5)) * 2048 + 1792 + (l & 31) * 8, u32x2{0x3f80u, 0u});
    if (w == 6) {
      const int e = l & 1, k = l < 14 ? l >> 1 : 0;
      const int ty = k == 0 ? 0 : (k < 4 ? 1 : 2), ro = k == 0 ? 0 : (k < 4 ? k - 1 : k - 4);
      const int rr = ai - ro < 0 ? 0 : ai - ro;
      spin<4>(rCTL, WS_FQ + rr * 128 + ((ty * 448 + 28 * ah + 27 * e) / 96) * 8, tag);
      __hip_atomic_store((LAS u32*)(L + L_CNT + 12), 1u, __ATOMIC_RELAXED, __HIP_MEMORY_SCOPE_WORKGROUP);
    } else lds_wait(L + L_CNT + 12, 1u);
    if (w == 4) STAMP(7);
#pragma unroll
    for (int j = 0; j < 6; ++j) {
      const int b = 3 * aw + (j >> 1), tb = 4 * ai - 8 + b < 0 ? 0 : 4 * ai - 8 + b;
      const u32 so = (tb * 336 + 112 + 112 * (j & 1) + 7 * ah) * 256, d = lb + ((j & 1) ? L_V + b * 2048 : L_K + b * 1792);
      dma16<true>(d, l * 16, rQKV, so);
      if (l < 48) dma16<true>(d + 1024, l * 16, rQKV, so + 1024);
    }
    const int hh = g >> 1;
    u32x2 qv[4];
#pragma unroll
    for (int k = 0; k < 4; ++k) qv[k] = ld8c_async(k == 3 && hh ? 0xfffffff0u : ((4 * ai + aw) * 336 + 7 * ah + 2 * k + hh) * 256 + l15 * 16 + (g & 1) * 8, rQKV);
    asm volatile("s_waitcnt vmcnt(0) lgkmcnt(0)" : "+v"(qv[0]), "+v"(qv[1]), "+v"(qv[2]), "+v"(qv[3])::"memory");
    if (!last_of4(L + L_CNT + 4)) lds_wait(L + L_CNT + 4, 4u);
    asm volatile("" ::: "memory");
    if (w == 4) STAMP(8);
    const bf16x8 qf[2] = {__builtin_bit_cast(bf16x8, u32x4{qv[0][0], qv[0][1], qv[1][0], qv[1][1]}), __builtin_bit_cast(bf16x8, u32x4{qv[2][0], qv[2][1], qv[3][0], qv[3][1]})};
    const u32 oK = L_K + aw * 1792 + hh * 256 + l15 * 16 + (g & 1) * 8, oK3 = hh ? oK + 1280 : oK + 1536;
    const int F = 7 * ah + 4 * (g & 1) + p4;
    u32 oV[2][2];
#pragma unroll
    for (int t = 0; t < 2; ++t)
#pragma unroll
      for (int e = 0; e < 2; ++e) oV[t][e] = L_V + aw * 2048 + (4 * (g & 1) + p4) * 256 + (((8 * e + 4 * hh + q4) ^ ((F & 6) << 1)) << 4) + ((t ^ (F & 1)) << 3);
    f32x16 sc[9];
    bf16x8 kf[9][2], vf[9][2];
    auto rdK = [&](int m) {
      const u32x2 a = lds_ld<u32x2>(L, oK + m * 1792), b = lds_ld<u32x2>(L, oK + m * 1792 + 512), c = lds_ld<u32x2>(L, oK + m * 1792 + 1024), d = lds_ld<u32x2>(L, oK3 + m * 1792);
      kf[m][0] = __builtin_bit_cast(bf16x8, u32x4{a[0], a[1], b[0], b[1]}); kf[m][1] = __builtin_bit_cast(bf16x8, u32x4{c[0], c[1], d[0], d[1]});
    };
    auto rdV = [&](int m) { vf[m][0] = trfrag(L, oV[0][0] + m * 2048, oV[0][1] + m * 2048); vf[m][1] = trfrag(L, oV[1][0] + m * 2048, oV[1][1] + m * 2048); };
    rdK(0); SB(); rdK(8); SB(); rdK(1); SB();
#pragma unroll
    for (int i = 0; i < 9; ++i) {
      const int m = i == 0 ? 0 : (i == 1 ? 8 : i - 1);
      if (i + 2 < 8) rdK(i + 2); else if (i >= 7) rdV(i - 6);
      sc[m] = mfma32(kf[m][1], qf[1], mfma32(kf[m][0], qf[0], f32x16{}));
      SB();
    }
    constexpr float NEG = -1e30f;
    if (ai < 2) {
      asm volatile("" ::: "memory");
      const int m0 = 8 - 4 * ai - aw;
#pragma unroll
      for (int m = 0; m < 8; ++m)
        if (m < m0) {
#pragma unroll
          for (int r = 0; r < 16; ++r) sc[m][r] = NEG;
        }
    }
    const int dq = (l & 31) - 4 * hh;
    u32 mk[8];
#pragma unroll
    for (int r = 0; r < 16; r += 2) {
      const bool c0 = (r & 3) + 8 * (r >> 2) <= dq, c1 = (r & 3) + 1 + 8 * (r >> 2) <= dq;
      sc[0][r] = c0 ? sc[8][r] : sc[0][r]; sc[0][r + 1] = c1 ? sc[8][r + 1] : sc[0][r + 1];
      mk[r >> 1] = c1 ? 0xffffffffu : (c0 ? 0xffffu : 0u);
    }
    float mx0 = fmx(sc[0][0], sc[0][1]), mx1 = fmx(sc[0][2], sc[0][3]);
#pragma unroll
    for (int k = 1; k < 32; ++k) { mx0 = fmx(fmx(mx0, sc[k >> 2][4 * (k & 3)]), sc[k >> 2][4 * (k & 3) + 1]); mx1 = fmx(fmx(mx1, sc[k >> 2][4 * (k & 3) + 2]), sc[k >> 2][4 * (k & 3) + 3]); }
    mx0 = fmx(mx0, mx1);
    const auto ms = __builtin_amdgcn_permlane32_swap(__float_as_uint(mx0), __float_as_uint(mx0), false, false);
    const float mx = fmx(__uint_as_float(ms[0]), __uint_as_float(ms[1]));
    SB();
    f32x16 o = {};
#pragma unroll
    for (int i = 0; i < 8; ++i) {
      const int m = (i + 1) & 7;
      if (i < 5) rdV(i + 3); else if (i == 5) rdV(0); else if (i == 6) rdV(8);
      u32x4 p[2];
#pragma unroll
      for (int j = 0; j < 8; ++j) {
        f32x2 d = f32x2{sc[m][2 * j], sc[m][2 * j + 1]} - f32x2{mx, mx};
        PIN(d);
        p[j >> 2][j & 3] = pk(__builtin_amdgcn_exp2f(d[0]), __builtin_amdgcn_exp2f(d[1]));
      }
#pragma unroll
      for (int s = 0; s < 2; ++s) {
        if (m) o = mfma32(vf[m][s], __builtin_bit_cast(bf16x8, p[s]), o);
        else {
          const u32x4 k4 = {mk[4 * s], mk[4 * s + 1], mk[4 * s + 2], mk[4 * s + 3]};
          o = mfma32(vf[0][s], __builtin_bit_cast(bf16x8, p[s] & ~k4), o);
          o = mfma32(vf[8][s], __builtin_bit_cast(bf16x8, p[s] & k4), o);
        }
      }
      SB();
    }
    const auto dn = __builtin_amdgcn_permlane32_swap(__float_as_uint(o[12]), __float_as_uint(o[12]), false, false);
    const float inv = __builtin_amdgcn_rcpf(__uint_as_float(dn[1]));
    const float in2 = hh ? 0.f : inv;
    const u32 so = ((ai * 4 + aw) * 144 + 112 + 2 * ah) * 1024;
    st16<16>(u32x4{pk(o[0] * inv, o[1] * inv), pk(o[2] * inv, o[3] * inv), pk(o[4] * inv, o[5] * inv), pk(o[6] * inv, o[7] * inv)}, rAC, l * 16 + so);
    st16<16>(u32x4{pk(o[8] * inv, o[9] * inv), pk(o[10] * inv, o[11] * inv), pk(o[12] * in2, o[13] * in2), pk(o[14] * in2, o[15] * in2)}, rAC, l * 16 + so + 1024);
    if (w == 4) STAMP(9);
    asm volatile("s_waitcnt vmcnt(0)" ::: "memory");
    if (last_of4(L + L_CNT + 8)) {
      if (l == 0) st_flag(rCTL, WS_FB + ai * 128 + ah * 8, tag);
      if (blockIdx.x == 255) {
#pragma unroll 1
        for (int k = 0; k < 4; ++k) spin<8>(rCTL, WS_FB + (k * 64 + l) * 8, tag);
        if (l == 0) st_flag(rCTL, 0, tag);
      }
    }
    if (busy) { ldres(); asm volatile("s_waitcnt vmcnt(0)" : "+v"(xres), "+v"(bres)::"memory"); }
  }
  if (busy) {
    wait_bar<63>();
    lanes();
    const int tid = w * 64 + l, row = tid >> 3, c8 = tid & 7;
    f32x4 r0 = {bflo(xres[0]) + bflo(bres[0]), bfhi(xres[0]) + bfhi(bres[0]), bflo(xres[1]) + bflo(bres[1]), bfhi(xres[1]) + bfhi(bres[1])};
    f32x4 r1 = {bflo(xres[2]) + bflo(bres[2]), bfhi(xres[2]) + bfhi(bres[2]), bflo(xres[3]) + bflo(bres[3]), bfhi(xres[3]) + bfhi(bres[3])};
#pragma unroll
    for (int j = 0; j < 4; ++j) {
      r0 += lds_ld<f32x4>(L, L_WC + j * WC_RING + row * RED_PITCH + c8 * 32);
      r1 += lds_ld<f32x4>(L, L_WC + j * WC_RING + row * RED_PITCH + c8 * 32 + 16);
    }
    st16<16>(u32x4{pk(r0[0], r0[1]), pk(r0[2], r0[3]), pk(r1[0], r1[1]), pk(r1[2], r1[3])},
                                           __builtin_amdgcn_make_buffer_rsrc(out, 0, 2048 * 896, 0x00020000), row * 896 + c8 * 16 + cR * 64 * 896 + cc * 128);
  }
  if (w == 0) STAMP(11);
}

extern "C" void kernel_launch(void* const* d_in, const int* in_sizes, int n_in, void* d_out, int out_size, void* d_ws, size_t ws_size, hipStream_t stream) {
  fused_block<<<256, 512, 0, stream>>>((const char*)d_in[0], (const char*)d_in[1], (const char*)d_in[2], (const char*)d_in[4], (const char*)d_in[5],
                                       (const char*)d_in[6], (const char*)d_in[7], (char*)d_out, (char*)d_ws);
}
```

```cpp
#define NOROT
#include <hip/hip_runtime.h>
#include <hip/hip_bf16.h>
#include <stdint.h>

#define DI __device__ __forceinline__
#define LAS __attribute__((address_space(3)))
#define GAS __attribute__((address_space(1)))
typedef unsigned u32;
typedef unsigned long long u64;
typedef __bf16 bf16x8 __attribute__((ext_vector_type(8)));
typedef __bf16 bf16x2 __attribute__((ext_vector_type(2)));
typedef short s16x4 __attribute__((ext_vector_type(4)));
typedef float f32x4 __attribute__((ext_vector_type(4)));
typedef float f32x2 __attribute__((ext_vector_type(2)));
typedef float f32x16 __attribute__((ext_vector_type(16)));
typedef unsigned u32x4 __attribute__((ext_vector_type(4)));
typedef unsigned u32x2 __attribute__((ext_vector_type(2)));
typedef __amdgpu_buffer_rsrc_t rsrc_t;

template <int N> struct IC { static constexpr int v = N; };
template <int B, int E, class F> DI void sfor(F&& f) { if constexpr (B < E) { f(IC<B>{}); sfor<B + 1, E>(f); } }

constexpr u32 WS_FH = 256, WS_FQ = WS_FH + 2048, WS_FB = WS_FQ + 2048, WS_FG = WS_FB + 2048, CTL_BYTES = 16384;
constexpr u32 WS_QKV = 16384, QKV_BYTES = 64u * 336u * 256u;
constexpr u32 WS_AC = WS_QKV + QKV_BYTES, AC_BYTES = 128u * 72u * 1024u;
constexpr u32 SLOT = 16384, RING = 7 * SLOT;
constexpr u32 L_SX = 45056, L_CSQ = 45568, L_B1 = 46080, L_CSH = 46336, L_ST = 47360, L_CNT = 48384;
constexpr u32 L_K = 0, L_V = 21504;
constexpr int RW = 12;
constexpr u32 L_WC = 49152, WC_RING = RW * 2048, RED_PITCH = 272;
constexpr u32 stage_addr(int s) { return s <= 20 ? RING + (s % 6) * 8192u : (s <= 25 ? (s - 21) * 8192u : RING + (s - 22) * 8192u); }

enum { K_NONE = 0, K_X, K_WA, K_BIAS, K_WCP };
constexpr int OP(int k, int a, int e = 0) { return k * 256 + 2 * a + e; }
constexpr int opn(int op) { int k = op / 256, a = (op % 256) / 2; return k == K_NONE ? 0 : k == K_X ? 4 : k == K_WA ? (a < 14 ? 3 : 4) : k == K_WCP ? 2 : 1; }
#define CAP_1 8
#define CAP_2 8
#define CAP_E 20
#define P_DA 8
#define P_TB 11
#define P_TO 25
#define P_FQ 23
#define P_FA 27
constexpr int MAXO = 12, NPRO = 8, DA = P_DA, TB = P_TB, TO = P_TO, B_FQ = P_FQ, B_FA = P_FA;
struct Sched {
  int pro[NPRO], opA[28][MAXO];
  int wA[29];
  int wC[36], a0[36], a1[36];
  int ok;
};
constexpr int cl(int n) { return n > 63 ? 63 : n; }
constexpr int free_at(u32 b) {
  return b < RING ? 2 * (int)(b / SLOT) + 1 : (int[6]){19, 20, 21, 16, 28, 28}[(b - RING) / 8192];
}
constexpr Sched make_sched() {
  Sched r{};
  r.ok = 1;
  int n = 0, iW[28] = {}, iX[7] = {}, iB = 0;
  int lo[128] = {}, le[128] = {}, nl = 0;
  for (int s = 0; s < 28; ++s) {
    if (s < 14 && !(s & 1)) { lo[nl] = OP(K_X, s >> 1, 0); le[nl++] = -1; lo[nl] = OP(K_X, s >> 1, 1); le[nl++] = -1; }
    lo[nl] = OP(K_WA, s); le[nl++] = s <= 20 ? s - 5 : (s <= 25 ? free_at(stage_addr(s) + 8191) : s - 9);
    if (s == 17) { lo[nl] = OP(K_BIAS, 0); le[nl++] = 5; }
  }
  for (int k = 0; k < RW; ++k) for (int j = 0; j < 2; ++j) {
    int f = 0;
    for (int e = 0; e < 4; ++e) { const int t = free_at(L_WC + (2 * j + (e >> 1)) * WC_RING + k * 2048 + (e & 1) * 2047); if (t > f) f = t; }
    lo[nl] = OP(K_WCP, k, j); le[nl++] = f;
  }
  bool done[128] = {};
  auto note = [&](int op) { n += opn(op); int k = op / 256, a = (op % 256) / 2; if (k == K_X) iX[a] = (op & 1) ? n : 0; if (k == K_WA) iW[a] = n; if (k == K_BIAS) iB = n; };
  for (int k = 0; k < NPRO; ++k) { r.pro[k] = lo[k]; done[k] = true; note(lo[k]); }
  r.wA[0] = cl(n - iW[0]);
  for (int s = 0; s < 28; ++s) {
    int need = 0;
    if (s + 1 < 28) {
      need = iW[s + 1]; if (!need) r.ok = 0;
      if (s + 1 <= 13) { int v = iX[(s + 1) >> 1]; if (!v) r.ok = 0; if (v > need) need = v; }
      if (s == 21) { if (!iB) r.ok = 0; if (iB > need) need = iB; }
    }
    r.wA[s + 1] = need ? cl(n - need) : 63;
    int cap = s == 14 || s == 21 ? CAP_E : (s < 14 ? CAP_1 : CAP_2), c = 0, used = 0;
    for (int k = 0; k < nl; ++k) if (!done[k] && le[k] <= s && used + opn(lo[k]) <= cap && c < MAXO) { done[k] = true; r.opA[s][c++] = lo[k]; used += opn(lo[k]); note(lo[k]); }
  }
  for (int k = 0; k < nl; ++k) if (!done[k]) r.ok = 0;
  n = 4;
  int iA[36] = {}, iWc[36] = {}, iP = 0, nA = DA;
  for (int m = 0; m < DA; ++m) { n += 2; iA[m] = n; }
  for (int s = 0; s < 36; ++s) {
    int need = iA[s]; if (!need) r.ok = 0;
    if (s + 1 < 36 && s + 1 >= RW) { if (!iWc[s + 1]) r.ok = 0; if (iWc[s + 1] > need) need = iWc[s + 1]; }
    if ((s == TB || s == TO) && iP > need) need = iP;
    r.wC[s] = cl(n - need);
    if (s == TB - 2 || s == TO - 2) { n += 1; iP = n; }
    r.a0[s] = nA;
    for (int c = 0; c < 2 && nA < 36 && nA <= s + DA && (nA < 14 || s >= TB) && (nA < 28 || s >= TO); ++c) { n += 2; iA[nA++] = n; }
    r.a1[s] = nA;
    if (s >= 1 && s + RW - 1 < 36) { n += 2; iWc[s + RW - 1] = n; }
    if (s == 33) n += 2;
  }
  return r;
}
constexpr Sched SC = make_sched();
static_assert(SC.ok, "schedule");

template <bool C> DI void dma16(u32 lds, u32 voff, rsrc_t rs, u32 soff) {
  if constexpr (C) asm volatile("s_mov_b32 m0, %0\n\ts_nop 0\n\tbuffer_load_dwordx4 %1, %2, %3 offen sc1 lds" ::"s"(lds), "v"(voff), "s"(rs), "s"(soff) : "memory");
  else asm volatile("s_mov_b32 m0, %0\n\ts_nop 0\n\tbuffer_load_dwordx4 %1, %2, %3 offen lds" ::"s"(lds), "v"(voff), "s"(rs), "s"(soff) : "memory");
}
template <bool C> DI u32x4 ld16_async(u32 voff, rsrc_t rs, u32 soff) {
  u32x4 r;
  if constexpr (C) asm volatile("buffer_load_dwordx4 %0, %1, %2, %3 offen sc1" : "=v"(r) : "v"(voff), "s"(rs), "s"(soff) : "memory");
  else asm volatile("buffer_load_dwordx4 %0, %1, %2, %3 offen" : "=v"(r) : "v"(voff), "s"(rs), "s"(soff) : "memory");
  return r;
}
DI void dma4(u32 lds, u32 voff, rsrc_t rs, u32 soff) {
  asm volatile("s_mov_b32 m0, %0\n\ts_nop 0\n\tbuffer_load_dword %1, %2, %3 offen lds" ::"s"(lds), "v"(voff), "s"(rs), "s"(soff) : "memory");
}
DI u32x2 ld8c_async(u32 voff, rsrc_t rs) {
  u32x2 r; asm volatile("buffer_load_dwordx2 %0, %1, %2, 0 offen sc1" : "=v"(r) : "v"(voff), "s"(rs) : "memory"); return r;
}
template <int N> DI void wait_bar() { asm volatile("s_waitcnt vmcnt(%0) lgkmcnt(0)\n\ts_barrier" ::"n"(N) : "memory"); }
template <int N> DI void vm_bar() { asm volatile("s_waitcnt vmcnt(%0)\n\ts_barrier" ::"n"(N) : "memory"); }
DI void bar() { asm volatile("s_barrier" ::: "memory"); }
#define PIN(x) asm volatile("" : "+v"(x))
#define SB() __builtin_amdgcn_sched_barrier(0)
DI f32x4 mfma(bf16x8 a, bf16x8 b, f32x4 c) { return __builtin_amdgcn_mfma_f32_16x16x32_bf16(a, b, c, 0, 0, 0); }
DI f32x16 mfma32(bf16x8 a, bf16x8 b, f32x16 c) { return __builtin_amdgcn_mfma_f32_32x32x16_bf16(a, b, c, 0, 0, 0); }
DI u32 pk(float a, float b) { bf16x2 v = {(__bf16)a, (__bf16)b}; return __builtin_bit_cast(u32, v); }
DI float fmx(float a, float b) { return __builtin_elementwise_maximum(a, b); }
DI float rbf(float a) { return (float)(__bf16)a; }
DI float bflo(u32 v) { return __uint_as_float(v << 16); }
DI float bfhi(u32 v) { return __uint_as_float(v & 0xffff0000u); }
DI float dot2(u32 a, u32 b, float c) { return __builtin_amdgcn_fdot2_f32_bf16(__builtin_bit_cast(bf16x2, a), __builtin_bit_cast(bf16x2, b), c, false); }
template <class T> DI T lds_ld(const LAS char* base, u32 off) { return *(const LAS T*)(base + off); }
template <class T> DI void lds_st(LAS char* base, u32 off, T v) { *(LAS T*)(base + off) = v; }
DI bf16x8 trfrag(LAS char* base, u32 off0, u32 off1) {
  s16x4 lo = __builtin_amdgcn_ds_read_tr16_b64_v4i16((LAS s16x4*)(base + off0));
  s16x4 hi = __builtin_amdgcn_ds_read_tr16_b64_v4i16((LAS s16x4*)(base + off1));
  return __builtin_bit_cast(bf16x8, __builtin_shufflevector(lo, hi, 0, 1, 2, 3, 4, 5, 6, 7));
}
template <int N> DI void dman(u32 lds, const u32* v, rsrc_t rs, u32 soff) {
  if constexpr (N == 4) asm volatile("s_mov_b32 m0, %0\n\ts_nop 0\n\tbuffer_load_dwordx4 %1, %5, %6 offen lds\n\tbuffer_load_dwordx4 %2, %5, %6 offen offset:1024 lds\n\t"
    "buffer_load_dwordx4 %3, %5, %6 offen offset:2048 lds\n\tbuffer_load_dwordx4 %4, %5, %6 offen offset:3072 lds" ::"s"(lds), "v"(v[0]), "v"(v[1]), "v"(v[2]), "v"(v[3]), "s"(rs), "s"(soff) : "memory");
  if constexpr (N == 3) asm volatile("s_mov_b32 m0, %0\n\ts_nop 0\n\tbuffer_load_dwordx4 %1, %4, %5 offen lds\n\tbuffer_load_dwordx4 %2, %4, %5 offen offset:1024 lds\n\t"
    "buffer_load_dwordx4 %3, %4, %5 offen offset:2048 lds" ::"s"(lds), "v"(v[0]), "v"(v[1]), "v"(v[2]), "s"(rs), "s"(soff) : "memory");
  if constexpr (N == 2) asm volatile("s_mov_b32 m0, %0\n\ts_nop 0\n\tbuffer_load_dwordx4 %1, %3, %4 offen lds\n\tbuffer_load_dwordx4 %2, %3, %4 offen offset:1024 lds" ::"s"(lds), "v"(v[0]), "v"(v[1]), "s"(rs), "s"(soff) : "memory");
}
template <int AUX> DI void st16(u32x4 v, rsrc_t rs, u32 voff) { __builtin_amdgcn_raw_buffer_store_b128(v, rs, voff, 0, AUX); }
DI void st_flag(rsrc_t rc, u32 off, u64 v) { __builtin_amdgcn_raw_buffer_store_b64(u32x2{(u32)v, (u32)(v >> 32)}, rc, off, 0, 16); }
template <int SLEEP> DI void spin(rsrc_t rc, u32 off, u64 tag) {
#pragma unroll 1
  for (u32 n = 0; n < (1u << 17); ++n) {
    asm volatile("" ::: "memory");
    const u32x2 v = __builtin_amdgcn_raw_buffer_load_b64(rc, off, 0, 16);
    if (__all(v[0] == (u32)tag && v[1] == (u32)(tag >> 32))) break;
    __builtin_amdgcn_s_sleep(SLEEP);
  }
  asm volatile("" ::: "memory");
}
DI void lds_wait(LAS char* p, u32 v) {
#pragma unroll 1
  for (u32 n = 0; n < (1u << 20); ++n) {
    if (__builtin_amdgcn_readfirstlane(__hip_atomic_load((LAS u32*)p, __ATOMIC_RELAXED, __HIP_MEMORY_SCOPE_WORKGROUP)) >= v) break;
    __builtin_amdgcn_s_sleep(1);
  }
  asm volatile("" ::: "memory");
}
DI bool last_of4(LAS char* cnt) {
  u32 old = 0;
  if (__builtin_amdgcn_mbcnt_hi(~0u, __builtin_amdgcn_mbcnt_lo(~0u, 0u)) == 0) old = __hip_atomic_fetch_add((LAS u32*)cnt, 1u, __ATOMIC_RELAXED, __HIP_MEMORY_SCOPE_WORKGROUP);
  return __builtin_amdgcn_readfirstlane(old) == 3;
}
#define STAMP(k)

__global__ __launch_bounds__(512, 2) void fused_block(const char* __restrict__ x, const char* __restrict__ wqkv, const char* __restrict__ wo,
                                                      const char* __restrict__ w1, const char* __restrict__ b1, const char* __restrict__ w2,
                                                      const char* __restrict__ b2, char* __restrict__ out, char* ws) {
#define PRIO_C 0
#define PRIO_B 2
  __shared__ __attribute__((aligned(1024))) char smem[163840];
  LAS char* const L = (LAS char*)smem;
  const u32 lb = (u32)(uintptr_t)smem;
  int l = threadIdx.x & 63, g, q4, p4, l15;
  auto lanes = [&] { PIN(l); g = l >> 4; q4 = (l >> 2) & 3; p4 = l & 3; l15 = l & 15; };
  lanes();
  int w = __builtin_amdgcn_readfirstlane(threadIdx.x >> 6);
  asm volatile("s_nop 4" : "+s"(w));
  const int x8 = blockIdx.x & 7, rk = blockIdx.x >> 3;
  const bool busy = rk < 28;
  if (w == 0) STAMP(0);
  const rsrc_t rQKV = __builtin_amdgcn_make_buffer_rsrc(ws + WS_QKV, 0, QKV_BYTES, 0x00020000);
  const rsrc_t rAC = __builtin_amdgcn_make_buffer_rsrc(ws + WS_AC, 0, AC_BYTES, 0x00020000);
  const rsrc_t rX = __builtin_amdgcn_make_buffer_rsrc((void*)x, 0, 2048 * 896, 0x00020000);
  const rsrc_t rW2 = __builtin_amdgcn_make_buffer_rsrc((void*)w2, 0, 1792 * 896, 0x00020000);
  const rsrc_t rCTL = __builtin_amdgcn_make_buffer_rsrc(ws, 0, CTL_BYTES, 0x00020000);
  const int rb = (x8 & 3) * 4 + rk / 7, cb = (x8 >> 2) * 7 + rk % 7;
  const int ai = rk & 15, ah = 2 * x8 + (rk >> 4);
  const int cR = (x8 & 3) * 8 + (x8 >> 2) * 4 + rk / 7, cc = rk % 7;
  const int rot = 0;
  auto rotl = [&](int j) { const int t = j + rot; return t >= 7 ? t - 7 : t; };
  auto kstep = [&](int j, int n) {
    const int t = n % 14 + 2 * rot, u = 14 * j + (t >= 14 ? t - 14 : t);
    return u + (u & ~3) + (n >= 14 ? 4 : 0);
  };
  u64 tag;
  u32x4 xres, bres;
  auto ldres = [&] {
    const int tid = w * 64 + l;
    xres = ld16_async<false>((tid >> 3) * 896 + (tid & 7) * 16, rX, cR * 64 * 896 + cc * 128);
    bres = ld16_async<false>((l & 7) * 16, __builtin_amdgcn_make_buffer_rsrc((void*)b2, 0, 896, 0x00020000), cc * 128);
  };
  auto epoch = [&] { asm volatile("s_load_dwordx2 %0, %1, 0x0\n\ts_waitcnt lgkmcnt(0)" : "=s"(tag) : "s"(ws) : "memory"); tag += 1; };

  if (w < 4) {
    if (busy) {
      epoch();
      constexpr int PF = 4;
      u32 oX[4], oH[2], oQ[3];
#pragma unroll
      for (int p = 0; p < 4; ++p) oX[p] = (4 * w + ((l & 31) >> 3)) * 1024 + (l & 7) * 128 + (((2 * p + (g >> 1)) ^ (l15 >> 1)) << 4);
      const int rq = w == 3 ? 0 : w;
#pragma unroll
      for (int i = 0; i < 3; ++i) oQ[i] = RING + (8 * (g >> 1) + q4) * 192 + ((i + rq) % 3) * 64 + (g & 1) * 32 + p4 * 8;
      const u32 selv = l15 == (g & 1) ? 0x3f803f80u : 0u;
      const bf16x8 sel = __builtin_bit_cast(bf16x8, u32x4{selv, selv, selv, selv});
      bf16x8 xf[28], wf[8];
      f32x16 aH[2], aQ[3] = {};
      f32x4 cs = {}, sx = {};
      float rs;
      bf16x8 lnb;
      const int ks = w >> 1;
      auto rdW = [&](auto SS, auto II) {
        constexpr int s = decltype(SS)::v, i = decltype(II)::v;
        constexpr int slot = (s < 14 ? s * 6 + i : 84 + i) % 8;
        if constexpr (s < 14) { const u32 o = oQ[i % 3] + (stage_addr(s) - RING) + (i / 3) * 3072; wf[slot] = trfrag(L, o, o + 768); }
        else if constexpr (s < 28) { const u32 o = oH[i & 1] + stage_addr(s) + (i >> 1) * 2048; wf[slot] = trfrag(L, o, o + 512); }
      };
      auto rdX = [&](auto SS, auto EE) { constexpr int k = 2 * decltype(SS)::v + decltype(EE)::v; if constexpr (k < 28) xf[k] = lds_ld<bf16x8>(L, oX[k & 3] + (k >> 2) * 16384); };
      auto putcs = [&](u32 base) {
        u32x4 v;
#pragma unroll
        for (int j = 0; j < 4; ++j) { const float hi = rbf(cs[j]); v[j] = pk(hi, cs[j] - hi); }
        if (l15 < 2) lds_st<u32x4>(L, base + (l15 * 16 + 4 * g) * 4, v);
        cs = f32x4{};
      };
      auto hepi = [&](int half) {
        u32 c[2][2], bb[2];
#pragma unroll
        for (int i = 0; i < 2; ++i) {
          const int ct = i ^ (w & 1);
          c[i][0] = lds_ld<u32>(L, L_CSH + half * 512 + (ct * 32 + (l & 31)) * 4); c[i][1] = lds_ld<u32>(L, L_CSH + half * 512 + 256 + (ct * 32 + (l & 31)) * 4);
          bb[i] = lds_ld<unsigned short>(L, L_B1 + (half * 64 + ct * 32 + (l & 31)) * 2);
        }
        SB();
#pragma unroll
        for (int i = 0; i < 2; ++i) aH[i] = mfma32(__builtin_bit_cast(bf16x8, u32x4{c[i][0], c[i][1], bb[i], 0u}), lnb, aH[i]);
        SB();
        const float rs2 = rs * rs;
#pragma unroll
        for (int i = 0; i < 2; ++i)
#pragma unroll
          for (int e = 0; e < 2; ++e) {
            u32x4 hp;
#pragma unroll
            for (int j = 0; j < 4; ++j) {
              const float t0 = aH[i][8 * e + 2 * j], t1 = aH[i][8 * e + 2 * j + 1];
              hp[j] = pk(t0 * rs2 * fmx(t0, 0.f), t1 * rs2 * fmx(t1, 0.f));
            }
            st16<16>(hp, rAC, l * 16 + ((rb * 4 + w) * 144 + cb * 8 + half * 4 + (i ^ (w & 1)) * 2 + e) * 1024);
          }
        SB();
      };
      bar();
      STAMP(1);
      rdX(IC<0>{}, IC<0>{}); rdX(IC<0>{}, IC<1>{});
      sfor<0, PF>([&](auto I) { rdW(IC<0>{}, I); });
      sfor<0, 28>([&](auto SS) {
        constexpr int s = decltype(SS)::v, NT = s < 14 ? 6 : 8;
        if constexpr (s == B_FA) wait_bar<0>();
        else if constexpr (s == B_FQ) wait_bar<(B_FQ > 21 ? 4 : 0)>();
        else if constexpr (s == 13 || s == 14 || s == 21) wait_bar<63>();
        else bar();
        if constexpr (s == 13) {
          lanes();
#pragma unroll
          for (int i = 0; i < 2; ++i) oH[i] = (g >> 1) * 1024 + q4 * 128 + (((i ^ (w & 1)) ^ (q4 >> 1)) << 6) + (g & 1) * 32 + p4 * 8;
        }
        if constexpr (s == 14) {
          STAMP(2);
          const u32x2 st = lds_ld<u32x2>(L, L_ST + (w * 32 + (l & 31)) * 8);
          u32 c[3];
#pragma unroll
          for (int i = 0; i < 3; ++i) c[i] = lds_ld<u32>(L, L_CSQ + (((i + rq) % 3) * 32 + (l & 31)) * 4);
          SB();
          const u32 mm2 = l < 32 ? (st[0] & 0xffffu) * 0x10001u : 0u;
          lnb = __builtin_bit_cast(bf16x8, u32x4{mm2, mm2, l < 32 ? st[0] >> 16 : 0u, 0u});
          rs = __uint_as_float(st[1]);
#pragma unroll
          for (int i = 0; i < 3; ++i) aQ[i] = mfma32(__builtin_bit_cast(bf16x8, u32x4{c[i], 0u, 0u, 0u}), lnb, aQ[i]);
          SB();
          const int fl = ((g & 1) << 1) | (g >> 1);
#pragma unroll
          for (int i = 0; i < 3; ++i)
#pragma unroll
            for (int e = 0; e < 2; ++e) {
              const int tq = 6 * cb + 2 * ((i + rq) % 3) + e;
              const float m = tq < 28 ? rs * (0.18898223650461361f * 1.4426950408889634f) : rs;
              const auto s0 = __builtin_amdgcn_permlane16_swap(pk(aQ[i][8 * e] * m, aQ[i][8 * e + 1] * m), pk(aQ[i][8 * e + 4] * m, aQ[i][8 * e + 5] * m), false, false);
              const auto s1 = __builtin_amdgcn_permlane16_swap(pk(aQ[i][8 * e + 2] * m, aQ[i][8 * e + 3] * m), pk(aQ[i][8 * e + 6] * m, aQ[i][8 * e + 7] * m), false, false);
              st16<16>(tq >= 56 && (fl & 1) ? u32x4{s0[1], s1[1], s0[0], s1[0]} : u32x4{s0[0], s1[0], s0[1], s1[1]}, rQKV,
                       fl * 256 + ((l15 ^ (tq >= 56 ? (2 * (tq & 1) + (fl >> 1)) << 2 : 0)) << 4) + ((rb * 4 + w) * 336 + 4 * tq) * 256);
            }
          SB();
          STAMP(3);
        }
        if constexpr (s == 21) hepi(0);
        sfor<0, NT>([&](auto II) {
          constexpr int i = decltype(II)::v, slot = (s < 14 ? s * 6 + i : 84 + i) % 8;
          if constexpr (i + PF < NT) rdW(SS, IC<i + PF>{});
          else rdW(IC<s + 1>{}, IC<i + PF - NT>{});
          if constexpr (i < 2) rdX(IC<s + 1>{}, II);
          if constexpr (s < 14) {
            aQ[i % 3] = mfma32(wf[slot], xf[2 * s + i / 3], aQ[i % 3]);
            if constexpr (i % 3 == 0) cs = mfma(wf[slot], sel, cs);
            if constexpr (s == 0 && (i == 1 || i == 2)) sx = mfma(xf[i - 1], sel, sx);
            if constexpr (s < 13 && i >= 4) sx = mfma(xf[2 * s + i - 2], sel, sx);
          } else {
            constexpr int k = 4 * ((s - 14) % 7) + (i >> 1);
            aH[i & 1] = mfma32(wf[slot], xf[k], k == 0 ? f32x16{} : aH[i & 1]);
            if constexpr (!(i & 1)) { if (((i >> 1) & 1) == ks) cs = mfma(wf[slot], sel, cs); }
          }
          SB();
        });
        if constexpr (s == 12) { if (l15 < 2) lds_st<f32x4>(L, L_SX + (w * 32 + l15 * 16 + 4 * g) * 4, sx); SB(); }
        if constexpr (s == 13) { putcs(L_CSQ + w * 128); SB(); }
        if constexpr (s == 20 || s == 27) { putcs(L_CSH + (s == 27) * 512 + ks * 256 + (w & 1) * 128); SB(); }
      });
      STAMP(4);
      wait_bar<63>();
      lanes();
      STAMP(5);
      hepi(1);
      const rsrc_t rWo = __builtin_amdgcn_make_buffer_rsrc((void*)wo, 0, 448 * 896, 0x00020000);
      u32x4 hA[36][2];
      const u32 vA[2] = {(u32)l * 16, (u32)l * 16 + 144 * 1024}, ring = L_WC + w * WC_RING;
      u32 vW[2];
      vW[0] = (l >> 3) * 896 + (((l & 7) ^ ((g & 1) << 2)) << 4); vW[1] = vW[0] + 7168 - 1024;
      auto ldA = [&](auto M, int a) {
        constexpr int m = decltype(M)::v;
        hA[m][a] = ld16_async<true>(vA[a], rAC, (cR * 288 + (m < 28 ? kstep(w, m) : 112 + 8 * w + (m - 28))) * 1024);
      };
      auto ldW = [&](auto M) {
        constexpr int m = decltype(M)::v;
        if constexpr (m < 28) dman<2>(lb + ring + (m % RW) * 2048, vW, rW2, kstep(w, m) * (16 * 896) + cc * 128);
        else dman<2>(lb + ring + (m % RW) * 2048, vW, rWo, (28 * (4 * w + ((m - 28) >> 1)) + 16 * (m & 1)) * 896 + cc * 128);
      };
      lds_wait(L + L_CNT + 16, 1u);
      STAMP(6);
      __builtin_amdgcn_s_setprio(PRIO_C);
      sfor<0, DA>([&](auto M) { ldA(M, 0); ldA(M, 1); });
      u32 oW[2];
#pragma unroll
      for (int b = 0; b < 2; ++b) oW[b] = ring + (4 * (g >> 1) + q4) * 128 + ((b ^ (q4 >> 1)) << 6) + (g & 1) * 32 + p4 * 8;
      bf16x8 wc[2][2];
      auto rdC = [&](int m) {
#pragma unroll
        for (int b = 0; b < 2; ++b) wc[m & 1][b] = trfrag(L, oW[b] + (m % RW) * 2048, oW[b] + (m % RW) * 2048 + 1024);
      };
      f32x16 acc[2][2] = {};
      u32x2 polled;
      const u32 vFB = WS_FB + (cR >> 1) * 128 + (4 * w + p4) * 8, vFG = WS_FG + (cR >> 1) * 128 + ((14 * w) / 4 + p4) * 8;
      rdC(0);
      sfor<0, 36>([&](auto N) {
        constexpr int n = decltype(N)::v;
        asm volatile("s_waitcnt vmcnt(%2)" : "+v"(hA[n][0]), "+v"(hA[n][1]) : "n"(SC.wC[n]) : "memory");
        if constexpr (n == 0) { if (last_of4(L + L_CNT) && l == 0) st_flag(rCTL, WS_FG + rb * 128 + cb * 8, tag); }
        if constexpr (n == TB - 2 || n == TO - 2) polled = ld8c_async(n == TB - 2 ? vFG : vFB, rCTL);
        if constexpr (n == TB || n == TO) {
          PIN(polled);
          if (!__all(polled[0] == (u32)tag && polled[1] == (u32)(tag >> 32))) spin<1>(rCTL, n == TB ? vFG : vFB, tag);
        }
        if constexpr (n + 1 < 36) rdC(n + 1);
        SB();
        auto mm = [&](int a, int b) { acc[a][b] = mfma32(wc[n & 1][b], __builtin_bit_cast(bf16x8, hA[n][a]), acc[a][b]); SB(); };
        mm(0, 0);
        if constexpr (SC.a0[n] < SC.a1[n]) { ldA(IC<SC.a0[n] % 36>{}, 0); mm(0, 1); ldA(IC<SC.a0[n] % 36>{}, 1); } else mm(0, 1);
        if constexpr (SC.a0[n] + 1 < SC.a1[n]) { ldA(IC<(SC.a0[n] + 1) % 36>{}, 0); ldA(IC<(SC.a0[n] + 1) % 36>{}, 1); }
        mm(1, 0);
        if constexpr (n >= 1 && n + RW - 1 < 36) ldW(IC<n + RW - 1>{});
        mm(1, 1);
        if constexpr (n == 33) ldres();
      });
      STAMP(10);
#pragma unroll
      for (int a = 0; a < 2; ++a)
#pragma unroll
        for (int b = 0; b < 2; ++b)
#pragma unroll
          for (int k = 0; k < 4; ++k)
            lds_st<f32x4>(L, ring + (a * 32 + (l & 31)) * RED_PITCH + (b * 32 + k * 8 + 4 * (g >> 1)) * 4,
                          f32x4{acc[a][b][4 * k], acc[a][b][4 * k + 1], acc[a][b][4 * k + 2], acc[a][b][4 * k + 3]});
      asm volatile("s_waitcnt vmcnt(0)" : "+v"(xres), "+v"(bres)::"memory");
    } else bar();
  } else {
    const int li = w & 1;
    if (!busy) {
      epoch();
      if (w == 6 && l < 8) lds_st<u32>(L, L_CNT + l * 4, 0u);
      wait_bar<63>();
    } else if (w < 6) {
      const rsrc_t rW1 = __builtin_amdgcn_make_buffer_rsrc((void*)w1, 0, 448 * 3584, 0x00020000);
      const rsrc_t rWq = __builtin_amdgcn_make_buffer_rsrc((void*)wqkv, 0, 448 * 2688, 0x00020000);
      u32 vX[4], vH[4], vQ[3], vC[2], so7[7];
#pragma unroll
      for (int j = 0; j < 4; ++j) {
        vX[j] = (8 * j + (l >> 3)) * 896 + (((l & 7) ^ ((j & 1) * 4 + g)) << 4) - 1024 * j;
      }
#pragma unroll
      for (int j = 0; j < 3; ++j) { const int n = 64 * j + l; vQ[j] = (n / 12) * 2688 + (n % 12) * 16 - 1024 * j; }
#pragma unroll
      for (int j = 0; j < 7; ++j) so7[j] = rotl(j);
      const u32 sX = (rb * 128 + 64 * li) * 896, sH = cb * 256 + 32 * li * 3584, sQ = cb * 192 + 16 * li * 2688;
      auto issue = [&](auto O) {
        constexpr int op = decltype(O)::v, k = op / 256, a = (op % 256) / 2, e = op & 1;
        if constexpr (k == K_X) dman<4>(lb + (a * 16 + 8 * li + 4 * e) * 1024, vX, rX, sX + so7[a] * 128 + e * 28672);
        if constexpr (k == K_WA && a < 14) dman<3>(lb + stage_addr(a) + li * 3072, vQ, rWq, sQ + so7[a >> 1] * (64 * 2688) + (a & 1) * (32 * 2688));
        if constexpr (k == K_WA && a >= 14) dman<4>(lb + stage_addr(a) + li * 4096, vH, rW1, sH + so7[(a - 14) % 7] * (64 * 3584) + (a >= 21 ? 128 : 0));
        if constexpr (k == K_BIAS) dma4(lb + L_B1, cb * 256 + l * 4, __builtin_amdgcn_make_buffer_rsrc((void*)b1, 0, 3584, 0x00020000), 0);
        if constexpr (k == K_WCP) dman<2>(lb + L_WC + (2 * e + li) * WC_RING + a * 2048, vC, rW2, kstep(2 * e + li, a) * (16 * 896) + cc * 128);
      };
      sfor<0, NPRO>([&](auto K) { issue(IC<SC.pro[decltype(K)::v]>{}); });
      epoch();
#pragma unroll
      for (int j = 0; j < 4; ++j) vH[j] = (8 * j + (l >> 3)) * 3584 + (((l & 7) ^ ((g & 1) << 2)) << 4) - 1024 * j;
      vC[0] = (l >> 3) * 896 + (((l & 7) ^ ((g & 1) << 2)) << 4); vC[1] = vC[0] + 7168 - 1024;
      vm_bar<SC.wA[0]>();
      sfor<0, 28>([&](auto SS) {
        constexpr int s = decltype(SS)::v;
        vm_bar<SC.wA[s + 1]>();
        sfor<0, MAXO>([&](auto K) { issue(IC<SC.opA[s][decltype(K)::v]>{}); });
      });
      vm_bar<0>();
    } else {
      epoch();
      u32 oX[4];
#pragma unroll
      for (int p = 0; p < 4; ++p) oX[p] = (8 * li + ((l & 31) >> 3)) * 1024 + (l & 7) * 128 + (((2 * p + (g >> 1)) ^ (l15 >> 1)) << 4);
      float s2[2][2] = {};
      u32x4 xv[2][4];
      sfor<0, 15>([&](auto SS) {
        constexpr int s = decltype(SS)::v;
        wait_bar<63>();
        if constexpr (s < 14) {
#pragma unroll
          for (int k = 0; k < 4; ++k) xv[s & 1][k] = lds_ld<u32x4>(L, oX[(2 * s + (k >> 1)) & 3] + (s >> 1) * 16384 + (k & 1) * 4096);
        }
        SB();
        auto sq = [&](int p) {
#pragma unroll
          for (int k = 0; k < 4; ++k)
#pragma unroll
            for (int j = 0; j < 4; ++j) s2[k & 1][j & 1] = dot2(xv[p][k][j], xv[p][k][j], s2[k & 1][j & 1]);
          PIN(s2[0][0]); PIN(s2[0][1]); PIN(s2[1][0]); PIN(s2[1][1]);
        };
        if constexpr (s >= 1 && s < 14) sq(~s & 1);
        if constexpr (s == 13) sq(1);
        SB();
      });
#pragma unroll
      for (int r = 0; r < 2; ++r) {
        const u32 q2 = __float_as_uint(s2[r][0] + s2[r][1]);
        const auto b = __builtin_amdgcn_permlane32_swap(q2, q2, false, false);
        const float mean = lds_ld<float>(L, L_SX + ((2 * li + r) * 32 + (l & 31)) * 4) * (1.f / 448.f);
        const float var = fmaxf((__uint_as_float(b[0]) + __uint_as_float(b[1])) * (1.f / 448.f) - mean * mean, 0.f);
        const float sd = rbf(__builtin_amdgcn_sqrtf(rbf(rbf(var) + 1e-5f)));
        if (l < 32) lds_st<u32x2>(L, L_ST + ((2 * li + r) * 32 + l) * 8, u32x2{pk(-mean, sd), __float_as_uint(__builtin_amdgcn_rcpf(sd))});
      }
      if (w == 6 && l < 8) lds_st<u32>(L, L_CNT + l * 4, 0u);
      wait_bar<63>();
#pragma unroll 1
      for (int s = 15; s < 29; ++s) {
        bar();
        if (w == 7 && l == 0 && (s == B_FQ || s == B_FA)) st_flag(rCTL, (s == B_FQ ? WS_FQ : WS_FH) + rb * 128 + cb * 8, tag);
      }
      if (w == 7) {
        spin<1>(rCTL, WS_FH + (cR >> 1) * 128 + (l < 14 ? l : 0) * 8, tag);
        __hip_atomic_store((LAS u32*)(L + L_CNT + 16), 1u, __ATOMIC_RELAXED, __HIP_MEMORY_SCOPE_WORKGROUP);
      }
    }
    lanes();
    const int aw = w - 4;
    __builtin_amdgcn_s_setprio(PRIO_B);
#pragma unroll
    for (int k = 0; k < 2; ++k) if (k == 0 || l < 32) lds_st<u32x2>(L, L_V + (3 * aw + 2 * k + (l >> 5)) * 2048 + 1792 + (l & 31) * 8, u32x2{0x3f80u, 0u});
    if (w == 6) {
      const int e = l & 1, k = l < 14 ? l >> 1 : 0;
      const int ty = k == 0 ? 0 : (k < 4 ? 1 : 2), ro = k == 0 ? 0 : (k < 4 ? k - 1 : k - 4);
      const int rr = ai - ro < 0 ? 0 : ai - ro;
      spin<4>(rCTL, WS_FQ + rr * 128 + ((ty * 448 + 28 * ah + 27 * e) / 96) * 8, tag);
      __hip_atomic_store((LAS u32*)(L + L_CNT + 12), 1u, __ATOMIC_RELAXED, __HIP_MEMORY_SCOPE_WORKGROUP);
    } else lds_wait(L + L_CNT + 12, 1u);
    if (w == 4) STAMP(7);
#pragma unroll
    for (int j = 0; j < 6; ++j) {
      const int b = 3 * aw + (j >> 1), tb = 4 * ai - 8 + b < 0 ? 0 : 4 * ai - 8 + b;
      const u32 so = (tb * 336 + 112 + 112 * (j & 1) + 7 * ah) * 256, d = lb + ((j & 1) ? L_V + b * 2048 : L_K + b * 1792);
      dma16<true>(d, l * 16, rQKV, so);
      if (l < 48) dma16<true>(d + 1024, l * 16, rQKV, so + 1024);
    }
    const int hh = g >> 1;
    u32x2 qv[4];
#pragma unroll
    for (int k = 0; k < 4; ++k) qv[k] = ld8c_async(k == 3 && hh ? 0xfffffff0u : ((4 * ai + aw) * 336 + 7 * ah + 2 * k + hh) * 256 + l15 * 16 + (g & 1) * 8, rQKV);
    asm volatile("s_waitcnt vmcnt(0) lgkmcnt(0)" : "+v"(qv[0]), "+v"(qv[1]), "+v"(qv[2]), "+v"(qv[3])::"memory");
    if (!last_of4(L + L_CNT + 4)) lds_wait(L + L_CNT + 4, 4u);
    asm volatile("" ::: "memory");
    if (w == 4) STAMP(8);
    const bf16x8 qf[2] = {__builtin_bit_cast(bf16x8, u32x4{qv[0][0], qv[0][1], qv[1][0], qv[1][1]}), __builtin_bit_cast(bf16x8, u32x4{qv[2][0], qv[2][1], qv[3][0], qv[3][1]})};
    const u32 oK = L_K + aw * 1792 + hh * 256 + l15 * 16 + (g & 1) * 8, oK3 = hh ? oK + 1280 : oK + 1536;
    const int F = 7 * ah + 4 * (g & 1) + p4;
    u32 oV[2][2];
#pragma unroll
    for (int t = 0; t < 2; ++t)
#pragma unroll
      for (int e = 0; e < 2; ++e) oV[t][e] = L_V + aw * 2048 + (4 * (g & 1) + p4) * 256 + (((8 * e + 4 * hh + q4) ^ ((F & 6) << 1)) << 4) + ((t ^ (F & 1)) << 3);
    f32x16 sc[9];
    bf16x8 kf[9][2], vf[9][2];
    auto rdK = [&](int m) {
      const u32x2 a = lds_ld<u32x2>(L, oK + m * 1792), b = lds_ld<u32x2>(L, oK + m * 1792 + 512), c = lds_ld<u32x2>(L, oK + m * 1792 + 1024), d = lds_ld<u32x2>(L, oK3 + m * 1792);
      kf[m][0] = __builtin_bit_cast(bf16x8, u32x4{a[0], a[1], b[0], b[1]}); kf[m][1] = __builtin_bit_cast(bf16x8, u32x4{c[0], c[1], d[0], d[1]});
    };
    auto rdV = [&](int m) { vf[m][0] = trfrag(L, oV[0][0] + m * 2048, oV[0][1] + m * 2048); vf[m][1] = trfrag(L, oV[1][0] + m * 2048, oV[1][1] + m * 2048); };
    rdK(0); SB(); rdK(8); SB(); rdK(1); SB();
#pragma unroll
    for (int i = 0; i < 9; ++i) {
      const int m = i == 0 ? 0 : (i == 1 ? 8 : i - 1);
      if (i + 2 < 8) rdK(i + 2); else if (i >= 7) rdV(i - 6);
      sc[m] = mfma32(kf[m][1], qf[1], mfma32(kf[m][0], qf[0], f32x16{}));
      SB();
    }
    constexpr float NEG = -1e30f;
    if (ai < 2) {
      asm volatile("" ::: "memory");
      const int m0 = 8 - 4 * ai - aw;
#pragma unroll
      for (int m = 0; m < 8; ++m)
        if (m < m0) {
#pragma unroll
          for (int r = 0; r < 16; ++r) sc[m][r] = NEG;
        }
    }
    const int dq = (l & 31) - 4 * hh;
    u32 mk[8];
#pragma unroll
    for (int r = 0; r < 16; r += 2) {
      const bool c0 = (r & 3) + 8 * (r >> 2) <= dq, c1 = (r & 3) + 1 + 8 * (r >> 2) <= dq;
      sc[0][r] = c0 ? sc[8][r] : sc[0][r]; sc[0][r + 1] = c1 ? sc[8][r + 1] : sc[0][r + 1];
      mk[r >> 1] = c1 ? 0xffffffffu : (c0 ? 0xffffu : 0u);
    }
    float mx0 = fmx(sc[0][0], sc[0][1]), mx1 = fmx(sc[0][2], sc[0][3]);
#pragma unroll
    for (int k = 1; k < 32; ++k) { mx0 = fmx(fmx(mx0, sc[k >> 2][4 * (k & 3)]), sc[k >> 2][4 * (k & 3) + 1]); mx1 = fmx(fmx(mx1, sc[k >> 2][4 * (k & 3) + 2]), sc[k >> 2][4 * (k & 3) + 3]); }
    mx0 = fmx(mx0, mx1);
    const auto ms = __builtin_amdgcn_permlane32_swap(__float_as_uint(mx0), __float_as_uint(mx0), false, false);
    const float mx = fmx(__uint_as_float(ms[0]), __uint_as_float(ms[1]));
    SB();
    f32x16 o = {};
#pragma unroll
    for (int i = 0; i < 8; ++i) {
      const int m = (i + 1) & 7;
      if (i < 5) rdV(i + 3); else if (i == 5) rdV(0); else if (i == 6) rdV(8);
      u32x4 p[2];
#pragma unroll
      for (int j = 0; j < 8; ++j) {
        f32x2 d = f32x2{sc[m][2 * j], sc[m][2 * j + 1]} - f32x2{mx, mx};
        PIN(d);
        p[j >> 2][j & 3] = pk(__builtin_amdgcn_exp2f(d[0]), __builtin_amdgcn_exp2f(d[1]));
      }
#pragma unroll
      for (int s = 0; s < 2; ++s) {
        if (m) o = mfma32(vf[m][s], __builtin_bit_cast(bf16x8, p[s]), o);
        else {
          const u32x4 k4 = {mk[4 * s], mk[4 * s + 1], mk[4 * s + 2], mk[4 * s + 3]};
          o = mfma32(vf[0][s], __builtin_bit_cast(bf16x8, p[s] & ~k4), o);
          o = mfma32(vf[8][s], __builtin_bit_cast(bf16x8, p[s] & k4), o);
        }
      }
      SB();
    }
    const auto dn = __builtin_amdgcn_permlane32_swap(__float_as_uint(o[12]), __float_as_uint(o[12]), false, false);
    const float inv = __builtin_amdgcn_rcpf(__uint_as_float(dn[1]));
    const float in2 = hh ? 0.f : inv;
    const u32 so = ((ai * 4 + aw) * 144 + 112 + 2 * ah) * 1024;
    st16<16>(u32x4{pk(o[0] * inv, o[1] * inv), pk(o[2] * inv, o[3] * inv), pk(o[4] * inv, o[5] * inv), pk(o[6] * inv, o[7] * inv)}, rAC, l * 16 + so);
    st16<16>(u32x4{pk(o[8] * inv, o[9] * inv), pk(o[10] * inv, o[11] * inv), pk(o[12] * in2, o[13] * in2), pk(o[14] * in2, o[15] * in2)}, rAC, l * 16 + so + 1024);
    if (w == 4) STAMP(9);
    asm volatile("s_waitcnt vmcnt(0)" ::: "memory");
    if (last_of4(L + L_CNT + 8)) {
      if (l == 0) st_flag(rCTL, WS_FB + ai * 128 + ah * 8, tag);
      if (blockIdx.x == 255) {
#pragma unroll 1
        for (int k = 0; k < 4; ++k) spin<8>(rCTL, WS_FB + (k * 64 + l) * 8, tag);
        if (l == 0) st_flag(rCTL, 0, tag);
      }
    }
    if (busy) { ldres(); asm volatile("s_waitcnt vmcnt(0)" : "+v"(xres), "+v"(bres)::"memory"); }
  }
  if (busy) {
    wait_bar<63>();
    lanes();
    const int tid = w * 64 + l, row = tid >> 3, c8 = tid & 7;
    f32x4 r0 = {bflo(xres[0]) + bflo(bres[0]), bfhi(xres[0]) + bfhi(bres[0]), bflo(xres[1]) + bflo(bres[1]), bfhi(xres[1]) + bfhi(bres[1])};
    f32x4 r1 = {bflo(xres[2]) + bflo(bres[2]), bfhi(xres[2]) + bfhi(bres[2]), bflo(xres[3]) + bflo(bres[3]), bfhi(xres[3]) + bfhi(bres[3])};
#pragma unroll
    for (int j = 0; j < 4; ++j) {
      r0 += lds_ld<f32x4>(L, L_WC + j * WC_RING + row * RED_PITCH + c8 * 32);
      r1 += lds_ld<f32x4>(L, L_WC + j * WC_RING + row * RED_PITCH + c8 * 32 + 16);
    }
    st16<16>(u32x4{pk(r0[0], r0[1]), pk(r0[2], r0[3]), pk(r1[0], r1[1]), pk(r1[2], r1[3])},
                                           __builtin_amdgcn_make_buffer_rsrc(out, 0, 2048 * 896, 0x00020000), row * 896 + c8 * 16 + cR * 64 * 896 + cc * 128);
  }
  if (w == 0) STAMP(11);
}

__global__ void mark_k(u64* p) { u64 t0 = __builtin_amdgcn_s_memrealtime(); if (threadIdx.x == 0) p[0] = t0; }
__global__ void until_k(const u64* p) { const u64 t0 = p[0]; u32 n = 0; while (__builtin_amdgcn_s_memrealtime() - t0 < 6000 && ++n < 1000000) __builtin_amdgcn_s_sleep(1); }
extern "C" void kernel_launch(void* const* d_in, const int* in_sizes, int n_in, void* d_out, int out_size, void* d_ws, size_t ws_size, hipStream_t stream) {
  mark_k<<<1, 64, 0, stream>>>((u64*)((char*)d_ws + (32u << 20)));
  fused_block<<<256, 512, 0, stream>>>((const char*)d_in[0], (const char*)d_in[1], (const char*)d_in[2], (const char*)d_in[4], (const char*)d_in[5],
                                       (const char*)d_in[6], (const char*)d_in[7], (char*)d_out, (char*)d_ws);
  until_k<<<1, 64, 0, stream>>>((u64*)((char*)d_ws + (32u << 20)));
}
```

```cpp
#define QPI 2
#define HPI 2
#include <hip/hip_runtime.h>
#include <hip/hip_bf16.h>
#include <stdint.h>

#define DI __device__ __forceinline__
#define LAS __attribute__((address_space(3)))
typedef unsigned u32;
typedef unsigned long long u64;
typedef __bf16 bf16x8 __attribute__((ext_vector_type(8)));
typedef __bf16 bf16x2 __attribute__((ext_vector_type(2)));
typedef short s16x4 __attribute__((ext_vector_type(4)));
typedef float f32x4 __attribute__((ext_vector_type(4)));
typedef float f32x2 __attribute__((ext_vector_type(2)));
typedef float f32x16 __attribute__((ext_vector_type(16)));
typedef unsigned u32x4 __attribute__((ext_vector_type(4)));
typedef unsigned u32x2 __attribute__((ext_vector_type(2)));
typedef __amdgpu_buffer_rsrc_t rsrc_t;

template <int N> struct IC { static constexpr int v = N; };
template <int B, int E, class F> DI void sfor(F&& f) { if constexpr (B < E) { f(IC<B>{}); sfor<B + 1, E>(f); } }

constexpr u32 FS = 128;
constexpr u32 WS_FH = FS, WS_FQ = WS_FH + 256 * FS, WS_FB = WS_FQ + 256 * FS, WS_FG = WS_FB + 256 * FS, WS_FP = WS_FG + 256 * FS, WS_FC = WS_FP + 448 * FS;
constexpr u32 WS_CS = WS_FC + 63 * FS, CTL_BYTES = WS_CS + 16384;
constexpr u32 WS_QKV = CTL_BYTES, QKV_BYTES = 64u * 336u * 256u;
constexpr u32 WS_AC = WS_QKV + QKV_BYTES, AC_BYTES = 64u * 144u * 1024u + 448u * 4096u;
constexpr u32 PX = 64u * 144u * 1024u;
constexpr u32 RING = 7 * 16384;
constexpr u32 L_SX = 45056, L_CSQ = 46080, L_CSH = 46848, L_ST = 47872, L_CNT = 48896;
constexpr u32 L_K = 0, L_V = 21504, L_Q = RING + 5 * 8192;
constexpr u32 L_WC = 49152, L_RED = L_WC + 16384;
enum { C_DONE, C_LAND, C_FQ, C_FA, C_FG, C_KV, C_FB, C_WLO, C_WHI, C_P6, C_P33, C_ATT, C_LATE, C_PAIR0, C_PAIR1, C_CSH, C_FH, C_SQ, C_CS0, C_N };
constexpr u32 stage_addr(int s) { return RING + (s % 6) * 8192u; }

enum { K_NONE = 0, K_X, K_WA, K_BIAS, K_WCP };
constexpr int OP(int k, int a, int e = 0) { return k * 256 + 2 * a + e; }
constexpr int opn(int op) { int k = op / 256, a = (op % 256) / 2; return k == K_NONE ? 0 : k == K_X ? 4 : k == K_WA ? (a < 14 ? 3 : 4) : k == K_WCP ? 2 : 1; }
#define CAP_1 8
#define P_NPRO 8
#define P_DA 12
#define P_TB 24
#define P_TO 50
#define P_FQ 24
#define PRIO_C 0
#define PRIO_B 2
#define ROTA 1
#define WS_OFF 0
#define CODE_KB 41
#define PF_DELAY 20
#define P_TFP 60
#define SLP 2
#define LSLP 1
constexpr u32 LANDED(int s) { return 2 * (s - 15); }
constexpr int MAXO = 12, NPRO = P_NPRO, DA = P_DA, TB = P_TB, TO = P_TO, S_FQ = P_FQ, T_FP = P_TFP;
struct Sched {
  int pro[NPRO], opA[28][MAXO];
  int wA[29];
  int wC[72], a0[72], a1[72];
  int wP, ok;
};
constexpr int cl(int n) { return n > 63 ? 63 : n; }
constexpr Sched make_sched() {
  Sched r{};
  r.ok = 1;
  int n = 0, iW[28] = {}, iX[7] = {}, iB = 0;
  int lo[128] = {}, le[128] = {}, nl = 0;
  for (int s = 0; s < 28; ++s) {
    if (s < 14 && !(s & 1)) { lo[nl] = OP(K_X, s >> 1, 0); le[nl++] = -1; lo[nl] = OP(K_X, s >> 1, 1); le[nl++] = -1; }
    lo[nl] = OP(K_WA, s); le[nl++] = s - 5;
  }
  for (int k = 0; k < 16; ++k) { lo[nl] = OP(K_WCP, k); le[nl++] = 7 + 2 * (k >> 2); }
  bool done[128] = {};
  auto note = [&](int op) { n += opn(op); int k = op / 256, a = (op % 256) / 2; if (k == K_X) iX[a] = (op & 1) ? n : 0; if (k == K_WA) iW[a] = n; if (k == K_BIAS) iB = n; };
  for (int k = 0; k < NPRO; ++k) { r.pro[k] = lo[k]; done[k] = true; note(lo[k]); }
  r.wA[0] = cl(n - iW[0]);
  for (int s = 0; s < 27; ++s) {
    int need = iW[s + 1]; if (!need) r.ok = 0;
    if (s + 1 <= 13) { int v = iX[(s + 1) >> 1]; if (!v) r.ok = 0; if (v > need) need = v; }
    r.wA[s + 1] = cl(n - need);
    int cap = s == 14 ? 20 : (s < 14 ? CAP_1 : 8), c = 0, used = 0;
    for (int k = 0; k < nl; ++k) if (!done[k] && le[k] <= s && used + opn(lo[k]) <= cap && c < MAXO) { done[k] = true; r.opA[s][c++] = lo[k]; used += opn(lo[k]); note(lo[k]); }
  }
  for (int k = 0; k < nl; ++k) if (!done[k]) r.ok = 0;
  n = 0;
  int iA[72] = {}, iP = 0, nA = 8 + DA, n56 = 0;
  for (int m = 8; m < 8 + DA; ++m) { n += 1; iA[m] = n; }
  for (int s = 8; s < 72; ++s) {
    int need = iA[s]; if (!need) r.ok = 0;
    if ((s == TB || s == TO) && iP > need) need = iP;
    r.wC[s] = cl(n - need);
    if (s == 56) n56 = n;
    if (s == T_FP) r.wP = n - n56;
    if (s == TB - 3 || s == TO - 3) { n += 1; iP = n; }
    r.a0[s] = nA;
    for (int c = 0; c < 2 && nA < 72 && nA <= s + DA && (nA < 32 || s >= TB) && (nA < 56 || s >= TO); ++c) { n += 1; iA[nA++] = n; }
    r.a1[s] = nA;
  }
  return r;
}
constexpr Sched SC = make_sched();
static_assert(SC.ok, "schedule");

DI rsrc_t mkrs(const void* p, u32 bytes) { return __builtin_amdgcn_make_buffer_rsrc((void*)p, 0, bytes, 0x00020000); }
template <int N> DI void dman(u32 lds, const u32* v, rsrc_t rs, u32 soff) {
  if constexpr (N == 4) asm volatile("s_mov_b32 m0, %0\n\ts_nop 0\n\tbuffer_load_dwordx4 %1, %5, %6 offen lds\n\tbuffer_load_dwordx4 %2, %5, %6 offen offset:1024 lds\n\t"
    "buffer_load_dwordx4 %3, %5, %6 offen offset:2048 lds\n\tbuffer_load_dwordx4 %4, %5, %6 offen offset:3072 lds" ::"s"(lds), "v"(v[0]), "v"(v[1]), "v"(v[2]), "v"(v[3]), "s"(rs), "s"(soff) : "memory");
  if constexpr (N == 3) asm volatile("s_mov_b32 m0, %0\n\ts_nop 0\n\tbuffer_load_dwordx4 %1, %4, %5 offen lds\n\tbuffer_load_dwordx4 %2, %4, %5 offen offset:1024 lds\n\t"
    "buffer_load_dwordx4 %3, %4, %5 offen offset:2048 lds" ::"s"(lds), "v"(v[0]), "v"(v[1]), "v"(v[2]), "s"(rs), "s"(soff) : "memory");
  if constexpr (N == 2) asm volatile("s_mov_b32 m0, %0\n\ts_nop 0\n\tbuffer_load_dwordx4 %1, %3, %4 offen lds\n\tbuffer_load_dwordx4 %2, %3, %4 offen offset:1024 lds" ::"s"(lds), "v"(v[0]), "v"(v[1]), "s"(rs), "s"(soff) : "memory");
  if constexpr (N == 1) asm volatile("s_mov_b32 m0, %0\n\ts_nop 0\n\tbuffer_load_dwordx4 %1, %2, %3 offen lds" ::"s"(lds), "v"(v[0]), "s"(rs), "s"(soff) : "memory");
}
DI void dma1792c(u32 lds, u32 voff, rsrc_t rs, u32 soff) {
  asm volatile("s_mov_b32 m0, %0\n\ts_nop 0\n\tbuffer_load_dwordx4 %1, %2, %3 offen sc1 lds\n\ts_mov_b32 exec_hi, 0xffff\n\t"
               "buffer_load_dwordx4 %1, %2, %3 offen offset:1024 sc1 lds\n\ts_mov_b32 exec_hi, -1" ::"s"(lds), "v"(voff), "s"(rs), "s"(soff) : "memory");
}
DI void dma4(u32 lds, u32 voff, rsrc_t rs, u32 soff) {
  asm volatile("s_mov_b32 m0, %0\n\ts_nop 0\n\tbuffer_load_dword %1, %2, %3 offen lds" ::"s"(lds), "v"(voff), "s"(rs), "s"(soff) : "memory");
}
template <bool C> DI u32x4 ld16_async(u32 voff, rsrc_t rs, u32 soff) {
  u32x4 r;
  if constexpr (C) asm volatile("buffer_load_dwordx4 %0, %1, %2, %3 offen sc1" : "=v"(r) : "v"(voff), "s"(rs), "s"(soff) : "memory");
  else asm volatile("buffer_load_dwordx4 %0, %1, %2, %3 offen" : "=v"(r) : "v"(voff), "s"(rs), "s"(soff) : "memory");
  return r;
}
DI u32x2 ld8c_async(u32 voff, rsrc_t rs) {
  u32x2 r; asm volatile("buffer_load_dwordx2 %0, %1, %2, 0 offen sc1" : "=v"(r) : "v"(voff), "s"(rs) : "memory"); return r;
}
template <int N> DI void wait_bar() { asm volatile("s_waitcnt vmcnt(%0) lgkmcnt(0)\n\ts_barrier" ::"n"(N) : "memory"); }
template <int N> DI void vm_bar() { asm volatile("s_waitcnt vmcnt(%0)\n\ts_barrier" ::"n"(N) : "memory"); }
template <int N> DI void vm() { asm volatile("s_waitcnt vmcnt(%0)" ::"n"(N) : "memory"); }
DI void bar() { asm volatile("s_barrier" ::: "memory"); }
#define PIN(x) asm volatile("" : "+v"(x))
#define SB() __builtin_amdgcn_sched_barrier(0)
DI f32x4 mfma(bf16x8 a, bf16x8 b, f32x4 c) { return __builtin_amdgcn_mfma_f32_16x16x32_bf16(a, b, c, 0, 0, 0); }
DI f32x16 mfma32(bf16x8 a, bf16x8 b, f32x16 c) { return __builtin_amdgcn_mfma_f32_32x32x16_bf16(a, b, c, 0, 0, 0); }
DI u32 pk(float a, float b) { bf16x2 v = {(__bf16)a, (__bf16)b}; return __builtin_bit_cast(u32, v); }
DI float fmx(float a, float b) { return __builtin_elementwise_maximum(a, b); }
DI float rbf(float a) { return (float)(__bf16)a; }
DI float bflo(u32 v) { return __uint_as_float(v << 16); }
DI float bfhi(u32 v) { return __uint_as_float(v & 0xffff0000u); }
DI float dot2(u32 a, u32 b, float c) { return __builtin_amdgcn_fdot2_f32_bf16(__builtin_bit_cast(bf16x2, a), __builtin_bit_cast(bf16x2, b), c, false); }
template <class T> DI T lds_ld(const LAS char* base, u32 off) { return *(const LAS T*)(base + off); }
template <class T> DI void lds_st(LAS char* base, u32 off, T v) { *(LAS T*)(base + off) = v; }
DI bf16x8 trfrag(LAS char* base, u32 off0, u32 off1) {
  s16x4 lo = __builtin_amdgcn_ds_read_tr16_b64_v4i16((LAS s16x4*)(base + off0));
  s16x4 hi = __builtin_amdgcn_ds_read_tr16_b64_v4i16((LAS s16x4*)(base + off1));
  return __builtin_bit_cast(bf16x8, __builtin_shufflevector(lo, hi, 0, 1, 2, 3, 4, 5, 6, 7));
}
#define ST_AUX 16
template <int AUX> DI void st16(u32x4 v, rsrc_t rs, u32 voff) { __builtin_amdgcn_raw_buffer_store_b128(v, rs, voff, 0, AUX ? ST_AUX : 0); }
DI void st_flag(rsrc_t rc, u32 off, u64 v) { __builtin_amdgcn_raw_buffer_store_b64(u32x2{(u32)v, (u32)(v >> 32)}, rc, off, 0, 16); }
DI bool tagged(u32x2 v, u64 tag) { return __all(v[0] == (u32)tag && v[1] == (u32)(tag >> 32)); }
template <int SLEEP> DI void spin(rsrc_t rc, u32 off, u64 tag) {
#pragma unroll 1
  for (u32 n = 0; n < (1u << 17); ++n) {
    asm volatile("" ::: "memory");
    if (tagged(__builtin_amdgcn_raw_buffer_load_b64(rc, off, 0, 16), tag)) break;
    __builtin_amdgcn_s_sleep(SLEEP);
  }
  asm volatile("" ::: "memory");
}
DI u32 cnt_ld(LAS char* L, int k) { return __hip_atomic_load((LAS u32*)(L + L_CNT + 4 * k), __ATOMIC_RELAXED, __HIP_MEMORY_SCOPE_WORKGROUP); }
DI void cnt_wait(LAS char* L, int k, u32 v) {
#pragma unroll 1
  for (u32 n = 0; n < (1u << 20); ++n) {
    if ((u32)__builtin_amdgcn_readfirstlane(cnt_ld(L, k)) >= v) break;
    __builtin_amdgcn_s_sleep(LSLP);
  }
  asm volatile("" ::: "memory");
}
DI void cnt_add(u32 lb, int k) {
  asm volatile("s_mov_b64 exec, 1\n\tds_add_u32 %0, %1\n\ts_mov_b64 exec, -1" ::"v"(lb + L_CNT + 4 * k), "v"(1u) : "memory");
}
template <int N> DI bool vm_le() {
  u32 r; asm volatile("s_getreg_b32 %0, hwreg(HW_REG_IB_STS)" : "=s"(r)::"memory");
  return (r & 0xc0000fu) <= (u32)N;
}
DI bool last_of4(LAS char* L, int k) {
  u32 old = 0;
  asm volatile("" ::: "memory");
  if (__builtin_amdgcn_mbcnt_hi(~0u, __builtin_amdgcn_mbcnt_lo(~0u, 0u)) == 0) old = __hip_atomic_fetch_add((LAS u32*)(L + L_CNT + 4 * k), 1u, __ATOMIC_RELAXED, __HIP_MEMORY_SCOPE_WORKGROUP);
  asm volatile("" ::: "memory");
  return __builtin_amdgcn_readfirstlane(old) == 3;
}
#define STAMP(k)
#define FLUSH()

__global__ __launch_bounds__(512, 2) void fused_block(const char* __restrict__ x, const char* __restrict__ wqkv, const char* __restrict__ wo,
                                                      const char* __restrict__ w1, const char* __restrict__ b1, const char* __restrict__ w2,
                                                      const char* __restrict__ b2, char* __restrict__ out, char* ws) {
  __shared__ __attribute__((aligned(1024))) char smem[163840];
  u64 pc; asm volatile("s_getpc_b64 %0" : "=s"(pc));
  LAS char* const L = (LAS char*)smem;
  const u32 lb = (u32)(uintptr_t)smem;
  int l = threadIdx.x & 63, g, q4, p4, l15;
  auto lanes = [&] { PIN(l); g = l >> 4; q4 = (l >> 2) & 3; p4 = l & 3; l15 = l & 15; };
  lanes();
  int w = __builtin_amdgcn_readfirstlane(threadIdx.x >> 6);
  asm volatile("s_nop 4" : "+s"(w));
  const int x8 = blockIdx.x & 7, rk = blockIdx.x >> 3;
  const bool busy = rk < 28;
  if (w == 0) STAMP(0);
  const rsrc_t rQKV = mkrs(ws + WS_QKV, QKV_BYTES), rAC = mkrs(ws + WS_AC, AC_BYTES), rCTL = mkrs(ws, CTL_BYTES);
  const rsrc_t rX = mkrs(x, 2048 * 896), rW2 = mkrs(w2, 1792 * 896);
  const int kh = x8 >> 2, cc = rk % 7, rb = (x8 & 3) * 4 + rk / 7, cb = kh * 7 + cc;
  const int ai = rk & 15, ah = 2 * x8 + (rk >> 4);
  const int ro6 = ROTA ? (cc + ((3 * (rk / 7)) >> 1)) % 6 : 0;
  auto rotl = [&](int j) { const int t = j + ro6; return j == 0 ? 0 : (t > 6 ? t - 6 : t); };
  auto kstep = [&](int p) {
    if (p < 8) return 8 * cb + p;
    const int t = cc + 1 + ((p - 8) % 24) / 4;
    return 8 * (7 * kh + (t >= 7 ? t - 7 : t)) + (p & 3) + (p >= 32 ? 4 : 0);
  };
  u64 tag;
  auto epoch = [&] { asm volatile("s_load_dwordx2 %0, %1, 0x0\n\ts_waitcnt lgkmcnt(0)" : "=s"(tag) : "s"(ws) : "memory"); tag += 1; };
  auto vW2 = [&](u32* v) { v[0] = (l >> 3) * 896 + (((l & 7) ^ ((g & 1) << 2)) << 4); v[1] = v[0] + 7168 - 1024; };

  if (w < 4) {
    if (!busy) {
      const rsrc_t rW1 = mkrs(w1, 448 * 3584), rWo = mkrs(wo, 448 * 896);
      const int blk = (rk - 28) * 8 + x8, q = (rk - 28) * 4 + w;
      u32x4 d[14];
#pragma unroll
      for (int k = 0; k < 14; ++k) d[k] = __builtin_amdgcn_raw_buffer_load_b128(rW1, (112 * w + (l >> 3)) * 3584 + (l & 7) * 16 + (blk < 28 ? blk * 128 : 0x7fffffff), k * 8 * 3584, 0);
      const u32 bias = __builtin_amdgcn_raw_buffer_load_b16(mkrs(b1, 3584), (blk * 64 + l) * 2, 0, 0);
      SB();
      dma4(lb + L_WC + w * 16384, (q * 64 + l) * 64, mkrs((const void*)(pc & ~255ull), CODE_KB * 1024), 0);
      epoch();
      bar();
      float a[8] = {};
#pragma unroll
      for (int k = 0; k < 14; ++k)
#pragma unroll
        for (int j = 0; j < 4; ++j) { a[2 * j] += bflo(d[k][j]); a[2 * j + 1] += bfhi(d[k][j]); }
      lds_st<f32x4>(L, RING + (w * 64 + l) * 32, f32x4{a[0], a[1], a[2], a[3]});
      lds_st<f32x4>(L, RING + (w * 64 + l) * 32 + 16, f32x4{a[4], a[5], a[6], a[7]});
      if (last_of4(L, C_CS0) && blk < 28) {
        float t = 0.f;
#pragma unroll
        for (int k = 0; k < 32; ++k) t += lds_ld<float>(L, RING + k * 256 + l * 4);
        const float hi = rbf(t);
        __builtin_amdgcn_raw_buffer_store_b64(u32x2{pk(hi, t - hi), bias}, rCTL, WS_CS + (blk * 64 + l) * 8, 0, 16);
        vm<0>();
        if (l == 0) st_flag(rCTL, WS_FC + blk * FS, tag);
      }
      const u32 v = l * 16;
#pragma unroll 1
      for (int k = 0; k < 56; ++k) {
        int r = q * 28 + (k >> 1) + (x8 & 3) * 112; r = r >= 448 ? r - 448 : r;
        dman<1>(lb + L_WC + w * 16384 + (k & 15) * 1024, &v, rW1, r * 3584 + kh * 1792 + (k & 1) * 768);
        vm<10>();
      }
#pragma unroll 1
      for (int k = 0; k < 49; ++k) {
        int r = k * 16 + q + (x8 & 3) * 196; r = r >= 784 ? r - 784 : r;
        dman<1>(lb + L_WC + w * 16384 + (k & 15) * 1024, &v, rW2, kh * 802816 + r * 1024);
        vm<10>();
      }
#pragma unroll 1
      for (int k = 0; k < 25; ++k) { dman<1>(lb + L_WC + w * 16384 + (k & 15) * 1024, &v, rWo, (k * 16 + q) * 1024); vm<10>(); }
      vm<0>();
      FLUSH();
      return;
    }
    epoch();
    constexpr int PF = 4;
    u32 oX[4], oH[2], oQ[3];
#pragma unroll
    for (int p = 0; p < 4; ++p) oX[p] = (4 * w + ((l & 31) >> 3)) * 1024 + (l & 7) * 128 + (((2 * p + (g >> 1)) ^ (l15 >> 1)) << 4);
#pragma unroll
    for (int i = 0; i < 3; ++i) oQ[i] = RING + (8 * (g >> 1) + q4) * 192 + ((i + (w == 3 ? 0 : w)) % 3) * 64 + (g & 1) * 32 + p4 * 8;
    const u32 selv = l15 == (g & 1) ? 0x3f803f80u : 0u;
    const bf16x8 sel = __builtin_bit_cast(bf16x8, u32x4{selv, selv, selv, selv});
    bf16x8 xf[28], wf[8];
    f32x16 aH[2][2], aQ[3] = {};
    f32x4 cs = {}, sx = {};
    u32x2 st;
    u32 cq[3];
    const int rq = w == 3 ? 0 : w;
    float rs;
    bf16x8 lnb;
    u32x4 hA[72];
    auto rdW = [&](auto SS, auto II) {
      constexpr int s = decltype(SS)::v, i = decltype(II)::v;
      constexpr int slot = (s < 14 ? s * 6 + i : 84 + i) % 8;
      if constexpr (s < 14) { const u32 o = oQ[i % 3] + (stage_addr(s) - RING) + (i / 3) * 3072; wf[slot] = trfrag(L, o, o + 768); }
      else if constexpr (s < 28) { const u32 o = oH[i & 1] + (stage_addr(s) - RING) + (i >> 1) * 2048; wf[slot] = trfrag(L, o, o + 512); }
    };
    auto rdX = [&](auto SS, auto EE) { constexpr int k = 2 * decltype(SS)::v + decltype(EE)::v; if constexpr (k < 28) xf[k] = lds_ld<bf16x8>(L, oX[k & 3] + (k >> 2) * 16384); };
    u32x2 ch[2];
    auto rdcs = [&](int half) {
#pragma unroll
      for (int i = 0; i < 2; ++i) ch[i] = lds_ld<u32x2>(L, L_CSH + (half * 64 + i * 32 + (l & 31)) * 8);
    };
    auto hpack = [&](int half, int c) {
      const float rs2 = rs * rs;
      u32x4 hp;
#pragma unroll
      for (int j = 0; j < 4; ++j) {
        const float t0 = aH[half][c >> 1][8 * (c & 1) + 2 * j], t1 = aH[half][c >> 1][8 * (c & 1) + 2 * j + 1];
        hp[j] = pk(t0 * rs2 * fmx(t0, 0.f), t1 * rs2 * fmx(t1, 0.f));
      }
      hA[half * 4 + c] = hp;
    };
    auto hstore = [&](int m) { st16<16>(hA[m], rAC, l * 16 + ((rb * 4 + w) * 144 + cb * 8 + m) * 1024); };
    auto qstore = [&](int c) {
      const int i = c >> 1, e = c & 1, fl = ((g & 1) << 1) | (g >> 1);
      const int tq = 6 * cb + 2 * ((i + rq) % 3) + e;
      const float m = tq < 28 ? rs * (0.18898223650461361f * 1.4426950408889634f) : rs;
      const auto s0 = __builtin_amdgcn_permlane16_swap(pk(aQ[i][8 * e] * m, aQ[i][8 * e + 1] * m), pk(aQ[i][8 * e + 4] * m, aQ[i][8 * e + 5] * m), false, false);
      const auto s1 = __builtin_amdgcn_permlane16_swap(pk(aQ[i][8 * e + 2] * m, aQ[i][8 * e + 3] * m), pk(aQ[i][8 * e + 6] * m, aQ[i][8 * e + 7] * m), false, false);
      u32x4 v = {s0[0], s1[0], s0[1], s1[1]};
      u32 vo = fl * 256 + l15 * 16;
      if (tq >= 56) {
        asm volatile("" ::: "memory");
        if (fl & 1) v = u32x4{s0[1], s1[1], s0[0], s1[0]};
        vo ^= (2 * (tq & 1) + (fl >> 1)) << 6;
      }
      st16<16>(v, rQKV, vo + ((rb * 4 + w) * 336 + 4 * tq) * 256);
    };
    bool sigq = false, siga = false;
    bar();
    STAMP(1);
    rdX(IC<0>{}, IC<0>{}); rdX(IC<0>{}, IC<1>{});
    sfor<0, PF>([&](auto I) { rdW(IC<0>{}, I); });
    sfor<0, 28>([&](auto SS) {
      constexpr int s = decltype(SS)::v, NT = s < 14 ? 6 : 8;
      if constexpr (s == 12) wait_bar<63>();
      else if constexpr (s == 14) wait_bar<63>();
      else if constexpr (s < 14) bar();
      if constexpr (s == 13) {
        lanes();
        st = lds_ld<u32x2>(L, L_ST + (w * 32 + (l & 31)) * 8);

#pragma unroll
        for (int i = 0; i < 2; ++i) oH[i] = RING + (g >> 1) * 1024 + q4 * 128 + ((i ^ (q4 >> 1)) << 6) + (g & 1) * 32 + p4 * 8;
      }
      if constexpr (s == 14) {
        STAMP(2);
#pragma unroll
        for (int i = 0; i < 3; ++i) cq[i] = lds_ld<u32>(L, L_CSQ + (((i + rq) % 3) * 32 + (l & 31)) * 4);
        cnt_wait(L, C_CSH, 1);
        rdcs(0);
        SB();
#pragma unroll
        for (int i = 0; i < 3; ++i) aQ[i] = mfma32(__builtin_bit_cast(bf16x8, u32x4{cq[i], 0u, 0u, 0u}), lnb, aQ[i]);
        SB();
        STAMP(3);
      }
      u32 land;
      if constexpr (s > 14 && s < 27) land = cnt_ld(L, C_LAND);
      sfor<0, NT>([&](auto II) {
        constexpr int i = decltype(II)::v, slot = (s < 14 ? s * 6 + i : 84 + i) % 8;
        if constexpr (s > 14 && s < 27 && i == NT - PF) {
          if ((u32)__builtin_amdgcn_readfirstlane(land) < LANDED(s + 1)) cnt_wait(L, C_LAND, LANDED(s + 1));
          asm volatile("" ::: "memory");
        }
        if constexpr (i + PF < NT) rdW(SS, IC<i + PF>{});
        else rdW(IC<s + 1>{}, IC<i + PF - NT>{});
        if constexpr (i < 2 && s != 12) rdX(IC<s + 1>{}, II);
        if constexpr (s == 11 && (i == 2 || i == 3)) rdX(IC<13>{}, IC<i - 2>{});
        if constexpr (s < 14) {
          aQ[i % 3] = mfma32(wf[slot], xf[2 * s + i / 3], aQ[i % 3]);
          if constexpr (s == 0 && (i == 1 || i == 2)) sx = mfma(xf[i - 1], sel, sx);
          if constexpr (s < 12 && i >= 4) sx = mfma(xf[2 * s + i - 2], sel, sx);
          if constexpr (s == 11 && i == 5) { sx = mfma(xf[26], sel, sx); sx = mfma(xf[27], sel, sx); }
          if constexpr (s == 13 && i == 2) {
            lnb = __builtin_bit_cast(bf16x8, u32x4{l < 32 ? (st[0] & 0xffffu) * 0x10001u : 0u, l < 32 ? st[0] >> 16 : 0u, 0u, 0u});
            rs = __uint_as_float(st[1]);
          }
          if constexpr (i % 3 == 0) cs = mfma(wf[slot], sel, cs);
          if constexpr (s == 13 && i == 4) {
            u32x4 v;
#pragma unroll
            for (int j = 0; j < 4; ++j) { const float hi = rbf(cs[j]); v[j] = pk(hi, cs[j] - hi); }
            if (l15 < 2) lds_st<u32x4>(L, L_CSQ + w * 128 + (l15 * 16 + 4 * g) * 4, v);
          }
        } else {
          constexpr int k = 4 * ((s - 14) % 7) + (i >> 1);
          constexpr int hf = s >= 21;
          if constexpr (k == 0) aH[hf][i] = mfma32(__builtin_bit_cast(bf16x8, u32x4{ch[i][0], ch[i][1], 0u, 0u}), lnb, f32x16{});
          aH[hf][i & 1] = mfma32(wf[slot], xf[k], aH[hf][i & 1]);
          if constexpr (s == 20 && i == 4) rdcs(1);
          if constexpr (s < 14 + 6 / QPI && i % (8 / QPI) == 1) qstore((s - 14) * QPI + i / (8 / QPI));
          if constexpr (hf && s < 21 + 4 / HPI && i % (8 / HPI) == 1) { hpack(0, (s - 21) * HPI + i / (8 / HPI)); hstore((s - 21) * HPI + i / (8 / HPI)); }
        }
        SB();
      });
      if constexpr (s == 11) { if (l15 < 2) lds_st<f32x4>(L, L_SX + (w * 32 + l15 * 16 + 4 * g) * 4, sx); SB(); }
      if constexpr (s >= 14) cnt_add(lb, C_DONE);
      constexpr int nh = s < 21 ? 0 : ((s - 20) * HPI > 4 ? 4 : (s - 20) * HPI);
      if constexpr (s >= 15 + 6 / QPI && s <= S_FQ) { if (!sigq && (s == S_FQ || vm_le<nh>())) { vm<nh>(); cnt_add(lb, C_FQ); sigq = true; } }
      if constexpr (s >= 22 + 4 / HPI) { if (!siga && vm_le<0>()) { vm<0>(); cnt_add(lb, C_FA); siga = true; } }
    });
    STAMP(4);
    lanes();
    const bool giver = (w >> 1) != kh;
    const u32 vFH = (rb * 16 + 7 * kh + (l < 7 ? l : 0)) * FS, vFB = WS_FB + (rb * 16 + (giver ? 8 : 0) + (l & 7)) * FS;
#pragma unroll
    for (int c = 0; c < 4; ++c) hpack(1, c);
    SB();
    if (!siga) { vm<0>(); cnt_add(lb, C_FA); }
    hstore(4); hstore(5);
    STAMP(5);
    u32x2 polled;
    u32 vW[2];
    vW2(vW);
    cnt_wait(L, C_DONE, 56);
#pragma unroll
    for (int k = 0; k < 5; ++k) dman<2>(lb + L_WC + (32 + 5 * w + k) * 2048, vW, rW2, ((8 * (7 * kh + (cc + 1 + ((5 * w + k) >> 2)) % 7) + 4 + ((5 * w + k) & 3)) * 16) * 896 + cc * 128);
    hstore(6); hstore(7);
    cnt_wait(L, C_FH, 1);
    STAMP(6);
    __builtin_amdgcn_s_setprio(PRIO_C);
    const u32 vA = l * 16, sA = (rb * 4 + w) * 144, sO = (rb * 4 + (giver ? w ^ 2 : w)) * 144 + (giver ? 128 : 112);
    auto ldA = [&](auto M) { constexpr int m = decltype(M)::v; hA[m] = ld16_async<true>(vA, rAC, (m < 56 ? sA + kstep(m) : sO + (m - 56)) * 1024); };
    sfor<8, 8 + DA>(ldA);
    u32 oW[2], oO[2];
#pragma unroll
    for (int b = 0; b < 2; ++b) { oW[b] = (4 * (g >> 1) + q4) * 128 + ((b ^ (q4 >> 1)) << 6) + (g & 1) * 32 + p4 * 8; oO[b] = oW[b] + (giver ? 8 * 3584 : 0); }
    bf16x8 wc[2][2];
    auto rdC = [&](int m) {
#pragma unroll
      for (int b = 0; b < 2; ++b) {
        u32 o;
        if (m < 56) o = oW[b] + L_WC + m * 2048;
        else o = oO[b] + ((m - 56) >> 1) * 3584 + (m & 1) * 2048 + (m >= 66 && giver ? L_WC - 364 * 128 : 0);
        wc[m & 1][b] = trfrag(L, o, o + 1024);
      }
    };
    f32x16 acc[2];
    u32x4 xres[2], bres;
    cnt_wait(L, C_WLO, 2);
    rdC(0);
    sfor<0, 72>([&](auto N) {
      constexpr int n = decltype(N)::v;
      if constexpr (n >= 8) asm volatile("s_waitcnt vmcnt(%1)" : "+v"(hA[n]) : "n"(SC.wC[n]) : "memory");
      if constexpr (n == 33) cnt_add(lb, C_P33);
      if constexpr (n == 8) { cnt_add(lb, C_P6); cnt_add(lb, C_WHI); if (last_of4(L, C_FG) && l == 0) st_flag(rCTL, WS_FG + (rb * 16 + cb) * FS, tag); }
      if constexpr (n == TB - 3 || n == TO - 3) polled = ld8c_async(n == TB - 3 ? WS_FG + vFH : vFB, rCTL);
      if constexpr (n == TB || n == TO) { PIN(polled); if (!tagged(polled, tag)) spin<SLP>(rCTL, n == TB ? WS_FG + vFH : vFB, tag); }
      if constexpr (n == 31) cnt_wait(L, C_WHI, 4);
      if constexpr (n == 51) cnt_wait(L, C_LATE, 4);
      if constexpr (n == 56) {
        if (giver) {
#pragma unroll
          for (int k = 0; k < 4; ++k) {
            const f32x16& a = acc[k >> 1];
            const int o = 8 * (k & 1);
            st16<16>(u32x4{pk(a[o], a[o + 1]), pk(a[o + 2], a[o + 3]), pk(a[o + 4], a[o + 5]), pk(a[o + 6], a[o + 7])}, rAC, PX + ((rb * 7 + cc) * 4 + w) * 4096 + k * 1024 + l * 16);
          }
          acc[0] = f32x16{}; acc[1] = f32x16{};
        }
      }
      if constexpr (n == T_FP) { if (giver) { vm<SC.wP>(); if (l == 0) st_flag(rCTL, WS_FP + ((rb * 7 + cc) * 4 + w) * FS, tag); } }
      if constexpr (n + 1 < 72) rdC(n + 1);
      SB();
      acc[0] = mfma32(wc[n & 1][0], __builtin_bit_cast(bf16x8, hA[n]), n == 0 ? f32x16{} : acc[0]);
      SB();
      if constexpr (n >= 8) sfor<SC.a0[n], SC.a1[n]>(ldA);
      acc[1] = mfma32(wc[n & 1][1], __builtin_bit_cast(bf16x8, hA[n]), n == 0 ? f32x16{} : acc[1]);
      SB();
    });
    STAMP(10);
    const int tile = giver ? w ^ 2 : w, r0 = (giver ? 16 : 0) + (l >> 3);
#pragma unroll
    for (int k = 0; k < 2; ++k) xres[k] = ld16_async<false>((r0 + 8 * k) * 896 + (l & 7) * 16, rX, (rb * 128 + tile * 32) * 896 + cc * 128);
    bres = ld16_async<false>((l & 7) * 16, mkrs(b2, 896), cc * 128);
    if (!giver) {
      spin<SLP>(rCTL, WS_FP + ((rb * 7 + cc) * 4 + w) * FS, tag);
#pragma unroll
      for (int k = 0; k < 4; ++k) {
        const u32x4 p = __builtin_amdgcn_raw_buffer_load_b128(rAC, PX + ((rb * 7 + cc) * 4 + w) * 4096 + k * 1024 + l * 16, 0, 16);
#pragma unroll
        for (int j = 0; j < 4; ++j) { acc[k >> 1][8 * (k & 1) + 2 * j] += bflo(p[j]); acc[k >> 1][8 * (k & 1) + 2 * j + 1] += bfhi(p[j]); }
      }
    }
    cnt_wait(L, C_P33, 4);
#pragma unroll
    for (int b = 0; b < 2; ++b)
#pragma unroll
      for (int k = 0; k < 4; ++k)
        lds_st<f32x4>(L, L_RED + w * 8192 + (l & 31) * 256 + (((b * 8 + k * 2 + (g >> 1)) ^ (l & 7)) << 4), f32x4{acc[b][4 * k], acc[b][4 * k + 1], acc[b][4 * k + 2], acc[b][4 * k + 3]});
    cnt_add(lb, C_PAIR0 + (w & 1));
    asm volatile("s_waitcnt vmcnt(0)" : "+v"(xres[0]), "+v"(xres[1]), "+v"(bres)::"memory");
    cnt_wait(L, C_PAIR0 + (w & 1), 2);
    const rsrc_t rO = mkrs(out, 2048 * 896);
#pragma unroll
    for (int k = 0; k < 2; ++k) {
      const int row = r0 + 8 * k;
      f32x4 a = {bflo(xres[k][0]) + bflo(bres[0]), bfhi(xres[k][0]) + bfhi(bres[0]), bflo(xres[k][1]) + bflo(bres[1]), bfhi(xres[k][1]) + bfhi(bres[1])};
      f32x4 b = {bflo(xres[k][2]) + bflo(bres[2]), bfhi(xres[k][2]) + bfhi(bres[2]), bflo(xres[k][3]) + bflo(bres[3]), bfhi(xres[k][3]) + bfhi(bres[3])};
#pragma unroll
      for (int j = 0; j < 2; ++j) {
        a += lds_ld<f32x4>(L, L_RED + ((w & 1) + 2 * j) * 8192 + row * 256 + (((2 * (l & 7)) ^ (row & 7)) << 4));
        b += lds_ld<f32x4>(L, L_RED + ((w & 1) + 2 * j) * 8192 + row * 256 + (((2 * (l & 7) + 1) ^ (row & 7)) << 4));
      }
      st16<16>(u32x4{pk(a[0], a[1]), pk(a[2], a[3]), pk(b[0], b[1]), pk(b[2], b[3])}, rO, (rb * 128 + tile * 32 + row) * 896 + cc * 128 + (l & 7) * 16);
    }
    if (w == 0) STAMP(11);
    FLUSH();
    return;
  }

  const int li = w & 1;
  if (!busy) {
    epoch();
    if (w == 6 && l < C_N) lds_st<u32>(L, L_CNT + l * 4, l == C_DONE ? 64u : 0u);
    wait_bar<63>();
  } else if (w < 6) {
    const rsrc_t rW1 = mkrs(w1, 448 * 3584), rWq = mkrs(wqkv, 448 * 2688);
    u32 vX[4], vH[4], vQ[3], vC[2], so7[7];
#pragma unroll
    for (int j = 0; j < 4; ++j) vX[j] = (8 * j + (l >> 3)) * 896 + (((l & 7) ^ ((j & 1) * 4 + g)) << 4) - 1024 * j;
#pragma unroll
    for (int j = 0; j < 3; ++j) { const int n = 64 * j + l; vQ[j] = (n / 12) * 2688 + (n % 12) * 16 - 1024 * j; }
#pragma unroll
    for (int j = 0; j < 7; ++j) so7[j] = rotl(j);
    const u32 sX = (rb * 128 + 64 * li) * 896, sH = cb * 256 + 32 * li * 3584, sQ = cb * 192 + 16 * li * 2688;
    auto issue = [&](auto O) {
      constexpr int op = decltype(O)::v, k = op / 256, a = (op % 256) / 2, e = op & 1;
      if constexpr (k == K_X) dman<4>(lb + (a * 16 + 8 * li + 4 * e) * 1024, vX, rX, sX + so7[a] * 128 + e * 28672);
      if constexpr (k == K_WA && a < 14) dman<3>(lb + stage_addr(a) + li * 3072, vQ, rWq, sQ + so7[a >> 1] * (64 * 2688) + (a & 1) * (32 * 2688));
      if constexpr (k == K_WA && a >= 14) dman<4>(lb + stage_addr(a) + li * 4096, vH, rW1, sH + so7[(a - 14) % 7] * (64 * 3584) + (a >= 21 ? 128 : 0));
      if constexpr (k == K_WCP) dman<2>(lb + L_WC + (2 * a + li) * 2048, vC, rW2, (kstep(2 * a) + li) * (16 * 896) + cc * 128);
    };
    sfor<0, NPRO>([&](auto K) { issue(IC<SC.pro[decltype(K)::v]>{}); });
    epoch();
#pragma unroll
    for (int j = 0; j < 4; ++j) vH[j] = (8 * j + (l >> 3)) * 3584 + (((l & 7) ^ ((g & 1) << 2)) << 4) - 1024 * j;
    vW2(vC);
    vm_bar<SC.wA[0]>();
    sfor<0, 27>([&](auto SS) {
      constexpr int s = decltype(SS)::v;
      if constexpr (s <= 14) vm_bar<SC.wA[s + 1]>();
      else {
        vm<SC.wA[s + 1]>();
        if constexpr (s > 15) cnt_wait(L, C_LAND, LANDED(s));
        cnt_add(lb, C_LAND);
        if constexpr (s + 5 < 28) cnt_wait(L, C_DONE, 4 * (s - 14));
      }
      sfor<0, MAXO>([&](auto K) { issue(IC<SC.opA[s][decltype(K)::v]>{}); });
    });
    vm<0>();
    cnt_add(lb, C_WLO);
    if (w == 4) { STAMP(13); cnt_wait(L, C_FA, 4); STAMP(14); if (l == 0) st_flag(rCTL, WS_FH + (rb * 16 + cb) * FS, tag); }
    else { cnt_wait(L, C_FA, 4); spin<SLP>(rCTL, WS_FH + (rb * 16 + 7 * kh + (l < 7 ? l : 0)) * FS, tag); STAMP(15); cnt_add(lb, C_FH); }
  } else {
    epoch();
    u32 oX[4];
#pragma unroll
    for (int p = 0; p < 4; ++p) oX[p] = (8 * li + ((l & 31) >> 3)) * 1024 + (l & 7) * 128 + (((2 * p + (g >> 1)) ^ (l15 >> 1)) << 4);
    float s2[2][2] = {};
    const u32 fc = WS_FC + (2 * cb + (l & 1)) * FS;
    u32x2 polled;
    bool got;
    auto ldcs = [&] { asm volatile("s_mov_b32 m0, %0\n\ts_nop 0\n\tbuffer_load_dwordx4 %1, %2, %3 offen sc1 lds" ::"s"(lb + L_CSH), "v"(l * 16), "s"(rCTL), "s"(WS_CS + cb * 1024) : "memory"); };
    u32x4 xv[8];
    auto sq = [&](int p) {
#pragma unroll
      for (int k = 0; k < 4; ++k)
#pragma unroll
        for (int j = 0; j < 4; ++j) s2[k & 1][j & 1] = dot2(xv[4 * p + k][j], xv[4 * p + k][j], s2[k & 1][j & 1]);
      PIN(s2[0][0]); PIN(s2[0][1]); PIN(s2[1][0]); PIN(s2[1][1]);
    };
    sfor<0, 13>([&](auto SS) {
      constexpr int s = decltype(SS)::v;
      bar();
      if constexpr (!(s & 1)) {
#pragma unroll
        for (int k = 0; k < 8; ++k) xv[k] = lds_ld<u32x4>(L, oX[k >> 1] + (s >> 1) * 16384 + (k & 1) * 4096);
      }
      sq(s & 1);
      if constexpr (s == 9) polled = ld8c_async(fc, rCTL);
      if constexpr (s == 12) {
        asm volatile("s_waitcnt vmcnt(0)" : "+v"(polled)::"memory");
        got = tagged(polled, tag);
        if (w == 7 && got) ldcs();
      }
      SB();
    });
    bar();
    sq(1);
#pragma unroll
    for (int r = 0; r < 2; ++r) {
      const u32 q2 = __float_as_uint(s2[r][0] + s2[r][1]);
      const auto b = __builtin_amdgcn_permlane32_swap(q2, q2, false, false);
      const float mean = lds_ld<float>(L, L_SX + ((2 * li + r) * 32 + (l & 31)) * 4) * (1.f / 448.f);
      const float var = fmaxf((__uint_as_float(b[0]) + __uint_as_float(b[1])) * (1.f / 448.f) - mean * mean, 0.f);
      const float sd = rbf(__builtin_amdgcn_sqrtf(rbf(rbf(var) + 1e-5f)));
      if (l < 32) lds_st<u32x2>(L, L_ST + ((2 * li + r) * 32 + l) * 8, u32x2{pk(-mean, sd), __float_as_uint(__builtin_amdgcn_rcpf(sd))});
    }
    if (w == 6 && l < C_N) lds_st<u32>(L, L_CNT + l * 4, 0u);
    wait_bar<63>();
    if (w == 7 && got) { vm<0>(); cnt_add(lb, C_CSH); }
    bar();
    if (w == 7 && !got) { spin<SLP>(rCTL, fc, tag); ldcs(); vm<0>(); cnt_add(lb, C_CSH); }
  }
  lanes();
  const int aw = w - 4, hh = g >> 1;
  if (w >= 6) {
#pragma unroll
    for (int k = 0; k < 3; ++k) lds_st<u32x2>(L, L_V + (6 * li + 2 * k + (l >> 5)) * 2048 + 1792 + (l & 31) * 8, u32x2{0x3f80u, 0u});
    const int e = l & 1, k = l < 14 ? l >> 1 : 0;
    const int ty = k == 0 ? 0 : (k < 4 ? 1 : 2), ro = k == 0 ? 0 : (k < 4 ? k - 1 : k - 4);
    const int rr = ai - ro < 0 ? 0 : ai - ro;
    if (!busy) { for (int k = 0; k < 12; ++k) __builtin_amdgcn_s_sleep(15); }
    if (busy) { cnt_wait(L, C_FQ, 4); if (w == 6) STAMP(12); if (w == 6 && l == 0) st_flag(rCTL, WS_FQ + (rb * 16 + cb) * FS, tag); }
    if (w == 6) { spin<SLP>(rCTL, WS_FQ + (rr * 16 + (ty * 448 + 28 * ah + 27 * e) / 96) * FS, tag); cnt_add(lb, C_SQ); }
    else cnt_wait(L, C_SQ, 1);
    if (w == 6) STAMP(7);
#pragma unroll
    for (int j = 0; j < 12; ++j) {
      const int tb = 4 * ai - 8 + j < 0 ? 0 : 4 * ai - 8 + j;
      dma1792c(lb + (li ? L_V + j * 2048 : L_K + j * 1792), l * 16, rQKV, (tb * 336 + 112 + 112 * li + 7 * ah) * 256);
    }
    cnt_wait(L, C_DONE, 40);
#pragma unroll
    for (int j = 0; j < 2; ++j) dma1792c(lb + L_Q + (2 * li + j) * 2048, l * 16, rQKV, ((4 * ai + 2 * li + j) * 336 + 7 * ah) * 256);
    asm volatile("s_waitcnt vmcnt(0) lgkmcnt(0)" ::: "memory");
    cnt_add(lb, C_KV);
  }
  cnt_wait(L, C_KV, 2);
  __builtin_amdgcn_s_setprio(PRIO_B);
  if (w == 4) STAMP(8);
  u32x2 qv[4];
#pragma unroll
  for (int k = 0; k < 4; ++k) qv[k] = lds_ld<u32x2>(L, L_Q + aw * 2048 + (k == 3 ? 6 : 2 * k + hh) * 256 + l15 * 16 + (g & 1) * 8);
  if (hh) qv[3] = u32x2{0u, 0u};
  const bf16x8 qf[2] = {__builtin_bit_cast(bf16x8, u32x4{qv[0][0], qv[0][1], qv[1][0], qv[1][1]}), __builtin_bit_cast(bf16x8, u32x4{qv[2][0], qv[2][1], qv[3][0], qv[3][1]})};
  const u32 oK = L_K + aw * 1792 + hh * 256 + l15 * 16 + (g & 1) * 8, oK3 = hh ? oK + 1280 : oK + 1536;
  const int F = 7 * ah + 4 * (g & 1) + p4;
  u32 oV[2][2];
#pragma unroll
  for (int t = 0; t < 2; ++t)
#pragma unroll
    for (int e = 0; e < 2; ++e) oV[t][e] = L_V + aw * 2048 + (4 * (g & 1) + p4) * 256 + (((8 * e + 4 * hh + q4) ^ ((F & 6) << 1)) << 4) + ((t ^ (F & 1)) << 3);
  f32x16 sc[9];
  bf16x8 kf[9][2], vf[9][2];
  auto rdK = [&](int m) {
    const u32x2 a = lds_ld<u32x2>(L, oK + m * 1792), b = lds_ld<u32x2>(L, oK + m * 1792 + 512), c = lds_ld<u32x2>(L, oK + m * 1792 + 1024), d = lds_ld<u32x2>(L, oK3 + m * 1792);
    kf[m][0] = __builtin_bit_cast(bf16x8, u32x4{a[0], a[1], b[0], b[1]}); kf[m][1] = __builtin_bit_cast(bf16x8, u32x4{c[0], c[1], d[0], d[1]});
  };
  auto rdV = [&](int m) { vf[m][0] = trfrag(L, oV[0][0] + m * 2048, oV[0][1] + m * 2048); vf[m][1] = trfrag(L, oV[1][0] + m * 2048, oV[1][1] + m * 2048); };
  rdK(0); SB(); rdK(8); SB(); rdK(1); SB();
#pragma unroll
  for (int i = 0; i < 9; ++i) {
    const int m = i == 0 ? 0 : (i == 1 ? 8 : i - 1);
    if (i + 2 < 8) rdK(i + 2); else if (i >= 7) rdV(i - 6);
    sc[m] = mfma32(kf[m][1], qf[1], mfma32(kf[m][0], qf[0], f32x16{}));
    SB();
  }
  constexpr float NEG = -1e30f;
  if (ai < 2) {
    asm volatile("" ::: "memory");
    const int m0 = 8 - 4 * ai - aw;
#pragma unroll
    for (int m = 0; m < 8; ++m)
      if (m < m0) {
#pragma unroll
        for (int r = 0; r < 16; ++r) sc[m][r] = NEG;
      }
  }
  const int dq = (l & 31) - 4 * hh;
  u32 mk[8];
#pragma unroll
  for (int r = 0; r < 16; r += 2) {
    const bool c0 = (r & 3) + 8 * (r >> 2) <= dq, c1 = (r & 3) + 1 + 8 * (r >> 2) <= dq;
    sc[0][r] = c0 ? sc[8][r] : sc[0][r]; sc[0][r + 1] = c1 ? sc[8][r + 1] : sc[0][r + 1];
    mk[r >> 1] = c1 ? 0xffffffffu : (c0 ? 0xffffu : 0u);
  }
  float mx0 = fmx(sc[0][0], sc[0][1]), mx1 = fmx(sc[0][2], sc[0][3]);
#pragma unroll
  for (int k = 1; k < 32; ++k) { mx0 = fmx(fmx(mx0, sc[k >> 2][4 * (k & 3)]), sc[k >> 2][4 * (k & 3) + 1]); mx1 = fmx(fmx(mx1, sc[k >> 2][4 * (k & 3) + 2]), sc[k >> 2][4 * (k & 3) + 3]); }
  mx0 = fmx(mx0, mx1);
  const auto ms = __builtin_amdgcn_permlane32_swap(__float_as_uint(mx0), __float_as_uint(mx0), false, false);
  const float mx = fmx(__uint_as_float(ms[0]), __uint_as_float(ms[1]));
  SB();
  f32x16 o = {};
#pragma unroll
  for (int i = 0; i < 8; ++i) {
    const int m = (i + 1) & 7;
    if (i < 5) rdV(i + 3); else if (i == 5) rdV(0); else if (i == 6) rdV(8);
    u32x4 p[2];
#pragma unroll
    for (int j = 0; j < 8; ++j) {
      f32x2 d = f32x2{sc[m][2 * j], sc[m][2 * j + 1]} - f32x2{mx, mx};
      PIN(d);
      p[j >> 2][j & 3] = pk(__builtin_amdgcn_exp2f(d[0]), __builtin_amdgcn_exp2f(d[1]));
    }
#pragma unroll
    for (int s = 0; s < 2; ++s) {
      if (m) o = mfma32(vf[m][s], __builtin_bit_cast(bf16x8, p[s]), o);
      else {
        const u32x4 k4 = {mk[4 * s], mk[4 * s + 1], mk[4 * s + 2], mk[4 * s + 3]};
        o = mfma32(vf[0][s], __builtin_bit_cast(bf16x8, p[s] & ~k4), o);
        o = mfma32(vf[8][s], __builtin_bit_cast(bf16x8, p[s] & k4), o);
      }
    }
    SB();
  }
  cnt_add(lb, C_ATT);
  const auto dn = __builtin_amdgcn_permlane32_swap(__float_as_uint(o[12]), __float_as_uint(o[12]), false, false);
  const float inv = __builtin_amdgcn_rcpf(__uint_as_float(dn[1]));
  const float in2 = hh ? 0.f : inv;
  const u32 so = ((ai * 4 + aw) * 144 + 112 + 2 * ah) * 1024;
  st16<16>(u32x4{pk(o[0] * inv, o[1] * inv), pk(o[2] * inv, o[3] * inv), pk(o[4] * inv, o[5] * inv), pk(o[6] * inv, o[7] * inv)}, rAC, l * 16 + so);
  st16<16>(u32x4{pk(o[8] * inv, o[9] * inv), pk(o[10] * inv, o[11] * inv), pk(o[12] * in2, o[13] * in2), pk(o[14] * in2, o[15] * in2)}, rAC, l * 16 + so + 1024);
  if (w == 4) STAMP(9);
  if (busy) {
    u32 vW[2];
    vW2(vW);
    dman<2>(lb + L_Q + aw * 2048, vW, rW2, (kstep(52) + aw) * (16 * 896) + cc * 128);
    vm<2>();
    if (last_of4(L, C_FB) && l == 0) st_flag(rCTL, WS_FB + (ai * 16 + ah) * FS, tag);
    cnt_wait(L, C_ATT, 4);
    cnt_wait(L, C_P6, 4);
    const rsrc_t rWo = mkrs(wo, 448 * 896);
#pragma unroll
    for (int k = 0; k < 6; ++k) if (k < 5 || aw < 3) dman<2>(lb + (4 * k + aw) * 2048, vW, rWo, (4 * k + aw) * (16 * 896) + cc * 128);
#pragma unroll
    for (int k = 0; k < 2; ++k) if (k == 0 || aw < 2) dman<2>(lb + L_WC + (4 * k + aw) * 2048, vW, rWo, (364 + 16 * (4 * k + aw)) * 896 + cc * 128);
    vm<0>();
    cnt_add(lb, C_LATE);
  } else {
    vm<0>();
    if (last_of4(L, C_FB)) {
      if (l == 0) st_flag(rCTL, WS_FB + (ai * 16 + ah) * FS, tag);
      if (blockIdx.x == 255) {
#pragma unroll 1
        for (int k = 0; k < 4; ++k) spin<32>(rCTL, WS_FB + (k * 64 + l) * FS, tag);
        if (l == 0) st_flag(rCTL, 0, tag);
      }
    }
  }
  FLUSH();
}

extern "C" void kernel_launch(void* const* d_in, const int* in_sizes, int n_in, void* d_out, int out_size, void* d_ws, size_t ws_size, hipStream_t stream) {
  fused_block<<<256, 512, 0, stream>>>((const char*)d_in[0], (const char*)d_in[1], (const char*)d_in[2], (const char*)d_in[4], (const char*)d_in[5],
                                       (const char*)d_in[6], (const char*)d_in[7], (char*)d_out, (char*)d_ws + WS_OFF);
}
```

```cpp
#define WS_OFF (0u  __H0__ )
#include <hip/hip_runtime.h>
#include <hip/hip_bf16.h>
#include <stdint.h>

#define DI __device__ __forceinline__
#define LAS __attribute__((address_space(3)))
typedef unsigned u32;
typedef unsigned long long u64;
typedef __bf16 bf16x8 __attribute__((ext_vector_type(8)));
typedef __bf16 bf16x2 __attribute__((ext_vector_type(2)));
typedef short s16x4 __attribute__((ext_vector_type(4)));
typedef float f32x4 __attribute__((ext_vector_type(4)));
typedef float f32x2 __attribute__((ext_vector_type(2)));
typedef float f32x16 __attribute__((ext_vector_type(16)));
typedef unsigned u32x4 __attribute__((ext_vector_type(4)));
typedef unsigned u32x2 __attribute__((ext_vector_type(2)));
typedef __amdgpu_buffer_rsrc_t rsrc_t;

template <int N> struct IC { static constexpr int v = N; };
template <int B, int E, class F> DI void sfor(F&& f) { if constexpr (B < E) { f(IC<B>{}); sfor<B + 1, E>(f); } }

constexpr u32 FS = 128;
constexpr u32 WS_FH = FS, WS_FQ = WS_FH + 256 * FS, WS_FB = WS_FQ + 256 * FS, WS_FG = WS_FB + 256 * FS, WS_FP = WS_FG + 256 * FS, WS_FC = WS_FP + 448 * FS;
constexpr u32 WS_CS = WS_FC + 63 * FS, CTL_BYTES = WS_CS + 16384;
constexpr u32 WS_QKV = CTL_BYTES, QKV_BYTES = 64u * 336u * 256u;
constexpr u32 WS_AC = WS_QKV + QKV_BYTES, AC_BYTES = 64u * 144u * 1024u + 448u * 4096u;
constexpr u32 PX = 64u * 144u * 1024u;
constexpr u32 RING = 7 * 16384;
constexpr u32 L_SX = 45056, L_CSQ = 46080, L_CSH = 46848, L_ST = 47872, L_CNT = 48896;
constexpr u32 L_K = 0, L_V = 21504, L_Q = RING + 5 * 8192;
constexpr u32 L_WC = 49152, L_RED = L_WC + 16384;
enum { C_DONE, C_LAND = 4, C_FQ = 6, C_FA, C_FG, C_KV, C_FB, C_WLO, C_WHI, C_P6, C_P33, C_ATT, C_LATE, C_PAIR0, C_PAIR1, C_CSH, C_FH, C_SQ, C_V, C_CS0, C_N };
constexpr u32 stage_addr(int s) { return RING + (s % 6) * 8192u; }

enum { K_NONE = 0, K_X, K_WA, K_BIAS, K_WCP };
constexpr int OP(int k, int a, int e = 0) { return k * 256 + 2 * a + e; }
constexpr int opn(int op) { int k = op / 256, a = (op % 256) / 2; return k == K_NONE ? 0 : k == K_X ? 4 : k == K_WA ? (a < 14 ? 3 : 4) : k == K_WCP ? 2 : 1; }
#define CAP_1 8
#define P_NPRO 8
#define P_DA 12
#define P_TB 24
#define P_TO 50
#define P_FQ 24
#define PRIO_C 0
#define PRIO_B 2
#define ROTA 1
#define CODE_KB 41
#define PF_DELAY 20
#define P_TFP 66
#define SLP 2
#define LSLP 1
#define QPI 2
#define HPI 2
constexpr int MAXO = 12, NPRO = P_NPRO, DA = P_DA, TB = P_TB, TO = P_TO, S_FQ = P_FQ, T_FP = P_TFP;
struct Sched {
  int pro[NPRO], opA[28][MAXO];
  int wA[29];
  int wC[72], a0[72], a1[72];
  int wP[72], ok;
};
constexpr int cl(int n) { return n > 63 ? 63 : n; }
constexpr Sched make_sched() {
  Sched r{};
  r.ok = 1;
  int n = 0, iW[28] = {}, iX[7] = {}, iB = 0;
  int lo[128] = {}, le[128] = {}, nl = 0;
  for (int s = 0; s < 28; ++s) {
    if (s < 14 && !(s & 1)) { lo[nl] = OP(K_X, s >> 1, 0); le[nl++] = -1; lo[nl] = OP(K_X, s >> 1, 1); le[nl++] = -1; }
    lo[nl] = OP(K_WA, s); le[nl++] = s - 5;
  }
  for (int k = 0; k < 16; ++k) { lo[nl] = OP(K_WCP, k); le[nl++] = 7 + 2 * (k >> 2); }
  bool done[128] = {};
  auto note = [&](int op) { n += opn(op); int k = op / 256, a = (op % 256) / 2; if (k == K_X) iX[a] = (op & 1) ? n : 0; if (k == K_WA) iW[a] = n; if (k == K_BIAS) iB = n; };
  for (int k = 0; k < NPRO; ++k) { r.pro[k] = lo[k]; done[k] = true; note(lo[k]); }
  r.wA[0] = cl(n - iW[0]);
  for (int s = 0; s < 27; ++s) {
    int need = iW[s > 14 && s < 26 ? s + 2 : s + 1]; if (!need) r.ok = 0;
    if (s + 1 <= 13) { int v = iX[(s + 1) >> 1]; if (!v) r.ok = 0; if (v > need) need = v; }
    r.wA[s + 1] = cl(n - need);
    int cap = s == 14 ? 20 : (s < 14 ? CAP_1 : 8), c = 0, used = 0;
    for (int k = 0; k < nl; ++k) if (!done[k] && le[k] <= s && used + opn(lo[k]) <= cap && c < MAXO) { done[k] = true; r.opA[s][c++] = lo[k]; used += opn(lo[k]); note(lo[k]); }
  }
  for (int k = 0; k < nl; ++k) if (!done[k]) r.ok = 0;
  n = 0;
  int iA[72] = {}, iP = 0, nA = 8 + DA, n56 = 0;
  for (int m = 8; m < 8 + DA; ++m) { n += 1; iA[m] = n; }
  for (int s = 8; s < 72; ++s) {
    int need = iA[s]; if (!need) r.ok = 0;
    if ((s == TB || s == TO) && iP > need) need = iP;
    r.wC[s] = cl(n - need);
    if (s == 56) n56 = n;
    if (s >= 56) r.wP[s] = n - n56;
    if (s == TB - 3 || s == TO - 3) { n += 1; iP = n; }
    r.a0[s] = nA;
    for (int c = 0; c < 2 && nA < 72 && nA <= s + DA && (nA < 32 || s >= TB) && (nA < 56 || s >= TO); ++c) { n += 1; iA[nA++] = n; }
    r.a1[s] = nA;
  }
  return r;
}
constexpr Sched SC = make_sched();
static_assert(SC.ok, "schedule");

DI rsrc_t mkrs(const void* p, u32 bytes) { return __builtin_amdgcn_make_buffer_rsrc((void*)p, 0, bytes, 0x00020000); }
template <int N> DI void dman(u32 lds, const u32* v, rsrc_t rs, u32 soff) {
  if constexpr (N == 4) asm volatile("s_mov_b32 m0, %0\n\ts_nop 0\n\tbuffer_load_dwordx4 %1, %5, %6 offen lds\n\tbuffer_load_dwordx4 %2, %5, %6 offen offset:1024 lds\n\t"
    "buffer_load_dwordx4 %3, %5, %6 offen offset:2048 lds\n\tbuffer_load_dwordx4 %4, %5, %6 offen offset:3072 lds" ::"s"(lds), "v"(v[0]), "v"(v[1]), "v"(v[2]), "v"(v[3]), "s"(rs), "s"(soff) : "memory");
  if constexpr (N == 3) asm volatile("s_mov_b32 m0, %0\n\ts_nop 0\n\tbuffer_load_dwordx4 %1, %4, %5 offen lds\n\tbuffer_load_dwordx4 %2, %4, %5 offen offset:1024 lds\n\t"
    "buffer_load_dwordx4 %3, %4, %5 offen offset:2048 lds" ::"s"(lds), "v"(v[0]), "v"(v[1]), "v"(v[2]), "s"(rs), "s"(soff) : "memory");
  if constexpr (N == 2) asm volatile("s_mov_b32 m0, %0\n\ts_nop 0\n\tbuffer_load_dwordx4 %1, %3, %4 offen lds\n\tbuffer_load_dwordx4 %2, %3, %4 offen offset:1024 lds" ::"s"(lds), "v"(v[0]), "v"(v[1]), "s"(rs), "s"(soff) : "memory");
  if constexpr (N == 1) asm volatile("s_mov_b32 m0, %0\n\ts_nop 0\n\tbuffer_load_dwordx4 %1, %2, %3 offen lds" ::"s"(lds), "v"(v[0]), "s"(rs), "s"(soff) : "memory");
}
DI void dma1792c(u32 lds, u32 voff, rsrc_t rs, u32 soff) {
  asm volatile("s_mov_b32 m0, %0\n\ts_nop 0\n\tbuffer_load_dwordx4 %1, %2, %3 offen sc1 lds\n\ts_mov_b32 exec_hi, 0xffff\n\t"
               "buffer_load_dwordx4 %1, %2, %3 offen offset:1024 sc1 lds\n\ts_mov_b32 exec_hi, -1" ::"s"(lds), "v"(voff), "s"(rs), "s"(soff) : "memory");
}
DI void dma4(u32 lds, u32 voff, rsrc_t rs, u32 soff) {
  asm volatile("s_mov_b32 m0, %0\n\ts_nop 0\n\tbuffer_load_dword %1, %2, %3 offen lds" ::"s"(lds), "v"(voff), "s"(rs), "s"(soff) : "memory");
}
template <bool C, int IMM = 0> DI u32x4 ld16_async(u32 voff, rsrc_t rs, u32 soff) {
  u32x4 r;
  if constexpr (C) asm volatile("buffer_load_dwordx4 %0, %1, %2, %3 offen offset:%4 sc1" : "=v"(r) : "v"(voff), "s"(rs), "s"(soff), "n"(IMM) : "memory");
  else asm volatile("buffer_load_dwordx4 %0, %1, %2, %3 offen" : "=v"(r) : "v"(voff), "s"(rs), "s"(soff) : "memory");
  return r;
}
DI u32x2 ld8c_async(u32 voff, rsrc_t rs) {
  u32x2 r; asm volatile("buffer_load_dwordx2 %0, %1, %2, 0 offen sc1" : "=v"(r) : "v"(voff), "s"(rs) : "memory"); return r;
}
template <int N> DI void wait_bar() { asm volatile("s_waitcnt vmcnt(%0) lgkmcnt(0)\n\ts_barrier" ::"n"(N) : "memory"); }
template <int N> DI void vm_bar() { asm volatile("s_waitcnt vmcnt(%0)\n\ts_barrier" ::"n"(N) : "memory"); }
template <int N> DI void vm() { asm volatile("s_waitcnt vmcnt(%0)" ::"n"(N) : "memory"); }
DI void bar() { asm volatile("s_barrier" ::: "memory"); }
#define PIN(x) asm volatile("" : "+v"(x))
#define SB() __builtin_amdgcn_sched_barrier(0)
DI f32x4 mfma(bf16x8 a, bf16x8 b, f32x4 c) { return __builtin_amdgcn_mfma_f32_16x16x32_bf16(a, b, c, 0, 0, 0); }
DI f32x16 mfma32(bf16x8 a, bf16x8 b, f32x16 c) { return __builtin_amdgcn_mfma_f32_32x32x16_bf16(a, b, c, 0, 0, 0); }
DI u32 pk(float a, float b) { bf16x2 v = {(__bf16)a, (__bf16)b}; return __builtin_bit_cast(u32, v); }
DI float fmx(float a, float b) { return __builtin_elementwise_maximum(a, b); }
DI float rbf(float a) { return (float)(__bf16)a; }
DI float bflo(u32 v) { return __uint_as_float(v << 16); }
DI float bfhi(u32 v) { return __uint_as_float(v & 0xffff0000u); }
DI float dot2(u32 a, u32 b, float c) { return __builtin_amdgcn_fdot2_f32_bf16(__builtin_bit_cast(bf16x2, a), __builtin_bit_cast(bf16x2, b), c, false); }
template <class T> DI T lds_ld(const LAS char* base, u32 off) { return *(const LAS T*)(base + off); }
template <class T> DI void lds_st(LAS char* base, u32 off, T v) { *(LAS T*)(base + off) = v; }
DI bf16x8 trfrag(LAS char* base, u32 off0, u32 off1) {
  s16x4 lo = __builtin_amdgcn_ds_read_tr16_b64_v4i16((LAS s16x4*)(base + off0));
  s16x4 hi = __builtin_amdgcn_ds_read_tr16_b64_v4i16((LAS s16x4*)(base + off1));
  return __builtin_bit_cast(bf16x8, __builtin_shufflevector(lo, hi, 0, 1, 2, 3, 4, 5, 6, 7));
}
#define ST_AUX 16
template <int AUX> DI void st16(u32x4 v, rsrc_t rs, u32 voff) { __builtin_amdgcn_raw_buffer_store_b128(v, rs, voff, 0, AUX ? ST_AUX : 0); }
DI void st_flag(rsrc_t rc, u32 off, u64 v) { __builtin_amdgcn_raw_buffer_store_b64(u32x2{(u32)v, (u32)(v >> 32)}, rc, off, 0, 16); }
DI bool tagged(u32x2 v, u64 tag) { return __all(v[0] == (u32)tag && v[1] == (u32)(tag >> 32)); }
template <int SLEEP> DI void spin(rsrc_t rc, u32 off, u64 tag) {
#pragma unroll 1
  for (u32 n = 0; n < (1u << 17); ++n) {
    asm volatile("" ::: "memory");
    if (tagged(__builtin_amdgcn_raw_buffer_load_b64(rc, off, 0, 16), tag)) break;
    __builtin_amdgcn_s_sleep(SLEEP);
  }
  asm volatile("" ::: "memory");
}
DI u32 cnt_ld(LAS char* L, int k) { return __hip_atomic_load((LAS u32*)(L + L_CNT + 4 * k), __ATOMIC_RELAXED, __HIP_MEMORY_SCOPE_WORKGROUP); }
DI void cnt_wait(LAS char* L, int k, u32 v) {
#pragma unroll 1
  for (u32 n = 0; n < (1u << 20); ++n) {
    if ((u32)__builtin_amdgcn_readfirstlane(cnt_ld(L, k)) >= v) break;
    __builtin_amdgcn_s_sleep(LSLP);
  }
  asm volatile("" ::: "memory");
}
DI void prog_st(LAS char* L, int k, u32 v) { asm volatile("" ::: "memory"); __hip_atomic_store((LAS u32*)(L + L_CNT + 4 * k), v, __ATOMIC_RELAXED, __HIP_MEMORY_SCOPE_WORKGROUP); asm volatile("" ::: "memory"); }
DI u32 land_ld(LAS char* L) { const u64 v = __hip_atomic_load((LAS u64*)(L + L_CNT + 4 * C_LAND), __ATOMIC_RELAXED, __HIP_MEMORY_SCOPE_WORKGROUP); return min((u32)v, (u32)(v >> 32)); }
DI u32 done_ld(LAS char* L) {
  const u64 a = __hip_atomic_load((LAS u64*)(L + L_CNT), __ATOMIC_RELAXED, __HIP_MEMORY_SCOPE_WORKGROUP), b = __hip_atomic_load((LAS u64*)(L + L_CNT + 8), __ATOMIC_RELAXED, __HIP_MEMORY_SCOPE_WORKGROUP);
  return min(min((u32)a, (u32)(a >> 32)), min((u32)b, (u32)(b >> 32)));
}
template <bool DONE> DI void prog_wait(LAS char* L, u32 v) {
#pragma unroll 1
  for (u32 n = 0; n < (1u << 20); ++n) {
    if ((u32)__builtin_amdgcn_readfirstlane(DONE ? done_ld(L) : land_ld(L)) >= v) break;
    __builtin_amdgcn_s_sleep(LSLP);
  }
  asm volatile("" ::: "memory");
}
DI void cnt_add(u32 lb, int k) {
  asm volatile("s_mov_b64 exec, 1\n\tds_add_u32 %0, %1\n\ts_mov_b64 exec, -1" ::"v"(lb + L_CNT + 4 * k), "v"(1u) : "memory");
}
template <int N> DI bool vm_le() {
  u32 r; asm volatile("s_getreg_b32 %0, hwreg(HW_REG_IB_STS)" : "=s"(r)::"memory");
  return (r & 0xc0000fu) <= (u32)N;
}
DI bool last_of4(LAS char* L, int k) {
  u32 old = 0;
  asm volatile("" ::: "memory");
  if (__builtin_amdgcn_mbcnt_hi(~0u, __builtin_amdgcn_mbcnt_lo(~0u, 0u)) == 0) old = __hip_atomic_fetch_add((LAS u32*)(L + L_CNT + 4 * k), 1u, __ATOMIC_RELAXED, __HIP_MEMORY_SCOPE_WORKGROUP);
  asm volatile("" ::: "memory");
  return __builtin_amdgcn_readfirstlane(old) == 3;
}
#define STAMP(k)
#define FLUSH()

__global__ __launch_bounds__(512, 2) void fused_block(const char* __restrict__ x, const char* __restrict__ wqkv, const char* __restrict__ wo,
                                                      const char* __restrict__ w1, const char* __restrict__ b1, const char* __restrict__ w2,
                                                      const char* __restrict__ b2, char* __restrict__ out, char* ws) {
  __shared__ __attribute__((aligned(1024))) char smem[163840];
  asm volatile("" : "+s"(x), "+s"(wqkv), "+s"(wo), "+s"(w1), "+s"(b1), "+s"(w2), "+s"(b2), "+s"(out), "+s"(ws));
  u64 pc; asm volatile("s_getpc_b64 %0" : "=s"(pc));
  LAS char* const L = (LAS char*)smem;
  const u32 lb = (u32)(uintptr_t)smem;
  int l = threadIdx.x & 63, g, q4, p4, l15;
  auto lanes = [&] { PIN(l); g = l >> 4; q4 = (l >> 2) & 3; p4 = l & 3; l15 = l & 15; };
  lanes();
  int w = __builtin_amdgcn_readfirstlane(threadIdx.x >> 6);
  asm volatile("s_nop 4" : "+s"(w));
  const int x8 = blockIdx.x & 7, rk = blockIdx.x >> 3;
  const bool busy = rk < 28;
  if (w == 0) STAMP(0);
  const rsrc_t rQKV = mkrs(ws + WS_QKV, QKV_BYTES), rAC = mkrs(ws + WS_AC, AC_BYTES), rCTL = mkrs(ws, CTL_BYTES);
  const rsrc_t rX = mkrs(x, 2048 * 896), rW2 = mkrs(w2, 1792 * 896);
  const int kh = x8 >> 2, cc = rk % 7, rb = (x8 & 3) * 4 + rk / 7, cb = kh * 7 + cc;
  const int ai = rk & 15, ah = 2 * x8 + (rk >> 4);
  const int ro6 = ROTA ? (cc + ((3 * (rk / 7)) >> 1)) % 6 : 0;
  auto rotl = [&](int j) { const int t = j + ro6; return j == 0 ? 0 : (t > 6 ? t - 6 : t); };
  auto kstep = [&](int p) {
    if (p < 8) return 8 * cb + p;
    const int t = cc + 1 + ((p - 8) % 24) / 4;
    return 8 * (7 * kh + (t >= 7 ? t - 7 : t)) + (p & 3) + (p >= 32 ? 4 : 0);
  };
  u64 tag;
  auto epoch = [&] { asm volatile("s_load_dwordx2 %0, %1, 0x0\n\ts_waitcnt lgkmcnt(0)" : "=s"(tag) : "s"(ws) : "memory"); tag += 1; };
  auto vW2 = [&](u32* v) { v[0] = (l >> 3) * 896 + (((l & 7) ^ ((g & 1) << 2)) << 4); v[1] = v[0] + 7168 - 1024; };

  if (w < 4) {
    if (!busy) {
      const rsrc_t rW1 = mkrs(w1, 448 * 3584), rWo = mkrs(wo, 448 * 896);
      const int blk = (rk - 28) * 8 + x8, q = (rk - 28) * 4 + w;
      u32x4 d[14];
#pragma unroll
      for (int k = 0; k < 14; ++k) d[k] = __builtin_amdgcn_raw_buffer_load_b128(rW1, (112 * w + (l >> 3)) * 3584 + (l & 7) * 16 + (blk < 28 ? blk * 128 : 0x7fffffff), k * 8 * 3584, 0);
      const u32 bias = __builtin_amdgcn_raw_buffer_load_b16(mkrs(b1, 3584), (blk * 64 + l) * 2, 0, 0);
      SB();
      dma4(lb + L_WC + w * 16384, (q * 64 + l) * 64, mkrs((const void*)(pc & ~255ull), CODE_KB * 1024), 0);
      epoch();
      bar();
      float a[8] = {};
#pragma unroll
      for (int k = 0; k < 14; ++k)
#pragma unroll
        for (int j = 0; j < 4; ++j) { a[2 * j] += bflo(d[k][j]); a[2 * j + 1] += bfhi(d[k][j]); }
      lds_st<f32x4>(L, RING + (w * 64 + l) * 32, f32x4{a[0], a[1], a[2], a[3]});
      lds_st<f32x4>(L, RING + (w * 64 + l) * 32 + 16, f32x4{a[4], a[5], a[6], a[7]});
      if (last_of4(L, C_CS0) && blk < 28) {
        float t = 0.f;
#pragma unroll
        for (int k = 0; k < 32; ++k) t += lds_ld<float>(L, RING + k * 256 + l * 4);
        const float hi = rbf(t);
        __builtin_amdgcn_raw_buffer_store_b64(u32x2{pk(hi, t - hi), bias}, rCTL, WS_CS + (blk * 64 + l) * 8, 0, 16);
        vm<0>();
        if (l == 0) st_flag(rCTL, WS_FC + blk * FS, tag);
      }
      const u32 v = l * 16;
#pragma unroll 1
      for (int k = 0; k < 49; ++k) {
        int r = k * 16 + q + (x8 & 3) * 196; r = r >= 784 ? r - 784 : r;
        dman<1>(lb + L_WC + w * 16384 + (k & 15) * 1024, &v, rW2, kh * 802816 + r * 1024);
        vm<10>();
      }
#pragma unroll 1
      for (int k = 0; k < 25; ++k) { dman<1>(lb + L_WC + w * 16384 + (k & 15) * 1024, &v, rWo, (k * 16 + q) * 1024); vm<10>(); }
      vm<0>();
      FLUSH();
      return;
    }
    epoch();
    constexpr int PF = 4;
    u32 oX[4], oH[2], oQ[3];
#pragma unroll
    for (int p = 0; p < 4; ++p) oX[p] = (4 * w + ((l & 31) >> 3)) * 1024 + (l & 7) * 128 + (((2 * p + (g >> 1)) ^ (l15 >> 1)) << 4);
#pragma unroll
    for (int i = 0; i < 3; ++i) oQ[i] = RING + (8 * (g >> 1) + q4) * 192 + ((i + (w == 3 ? 0 : w)) % 3) * 64 + (g & 1) * 32 + p4 * 8;
    const u32 selv = l15 == (g & 1) ? 0x3f803f80u : 0u;
    const bf16x8 sel = __builtin_bit_cast(bf16x8, u32x4{selv, selv, selv, selv});
    bf16x8 xf[28], wf[8];
    f32x16 aH[2][2], aQ[3] = {};
    f32x4 cs = {}, sx = {};
    u32x2 st;
    u32 cq[3];
    const int rq = w == 3 ? 0 : w;
    float rs;
    bf16x8 lnb;
    u32x4 hA[72];
    auto rdW = [&](auto SS, auto II) {
      constexpr int s = decltype(SS)::v, i = decltype(II)::v;
      constexpr int slot = (s < 14 ? s * 6 + i : 84 + i) % 8;
      if constexpr (s < 14) { const u32 o = oQ[i % 3] + (stage_addr(s) - RING) + (i / 3) * 3072; wf[slot] = trfrag(L, o, o + 768); }
      else if constexpr (s < 28) { const u32 o = oH[i & 1] + (stage_addr(s) - RING) + (i >> 1) * 2048; wf[slot] = trfrag(L, o, o + 512); }
    };
    auto rdX = [&](auto SS, auto EE) { constexpr int k = 2 * decltype(SS)::v + decltype(EE)::v; if constexpr (k < 28) xf[k] = lds_ld<bf16x8>(L, oX[k & 3] + (k >> 2) * 16384); };
    u32x2 ch[2];
    auto rdcs = [&](int half) {
#pragma unroll
      for (int i = 0; i < 2; ++i) ch[i] = lds_ld<u32x2>(L, L_CSH + (half * 64 + i * 32 + (l & 31)) * 8);
    };
    auto hpack = [&](int half, int c) {
      const float rs2 = rs * rs;
      u32x4 hp;
#pragma unroll
      for (int j = 0; j < 4; ++j) {
        const float t0 = aH[half][c >> 1][8 * (c & 1) + 2 * j], t1 = aH[half][c >> 1][8 * (c & 1) + 2 * j + 1];
        hp[j] = pk(t0 * rs2 * fmx(t0, 0.f), t1 * rs2 * fmx(t1, 0.f));
      }
      hA[half * 4 + c] = hp;
    };
    auto hstore = [&](int m) { st16<16>(hA[m], rAC, l * 16 + ((rb * 4 + w) * 144 + cb * 8 + m) * 1024); };
    auto qstore = [&](int c) {
      const int i = c >> 1, e = c & 1, fl = ((g & 1) << 1) | (g >> 1);
      const int tq = 6 * cb + 2 * ((i + rq) % 3) + e;
      const float m = tq < 28 ? rs * (0.18898223650461361f * 1.4426950408889634f) : rs;
      const auto s0 = __builtin_amdgcn_permlane16_swap(pk(aQ[i][8 * e] * m, aQ[i][8 * e + 1] * m), pk(aQ[i][8 * e + 4] * m, aQ[i][8 * e + 5] * m), false, false);
      const auto s1 = __builtin_amdgcn_permlane16_swap(pk(aQ[i][8 * e + 2] * m, aQ[i][8 * e + 3] * m), pk(aQ[i][8 * e + 6] * m, aQ[i][8 * e + 7] * m), false, false);
      u32x4 v = {s0[0], s1[0], s0[1], s1[1]};
      u32 vo = fl * 256 + l15 * 16;
      if (tq >= 56) {
        asm volatile("" ::: "memory");
        if (fl & 1) v = u32x4{s0[1], s1[1], s0[0], s1[0]};
        vo ^= (2 * (tq & 1) + (fl >> 1)) << 6;
      }
      st16<16>(v, rQKV, vo + ((rb * 4 + w) * 336 + 4 * tq) * 256);
    };
    bool sigq = false, siga = false;
    bar();
    STAMP(1);
    rdX(IC<0>{}, IC<0>{}); rdX(IC<0>{}, IC<1>{});
    sfor<0, PF>([&](auto I) { rdW(IC<0>{}, I); });
    sfor<0, 28>([&](auto SS) {
      constexpr int s = decltype(SS)::v, NT = s < 14 ? 6 : 8;
      if constexpr (s == 12) wait_bar<63>();
      else if constexpr (s == 14) wait_bar<63>();
      else if constexpr (s < 14) bar();
      if constexpr (s == 13) {
        lanes();
        st = lds_ld<u32x2>(L, L_ST + (w * 32 + (l & 31)) * 8);

#pragma unroll
        for (int i = 0; i < 2; ++i) oH[i] = RING + (g >> 1) * 1024 + q4 * 128 + ((i ^ (q4 >> 1)) << 6) + (g & 1) * 32 + p4 * 8;
      }
      if constexpr (s == 14) {
        STAMP(2);
#pragma unroll
        for (int i = 0; i < 3; ++i) cq[i] = lds_ld<u32>(L, L_CSQ + (((i + rq) % 3) * 32 + (l & 31)) * 4);
        cnt_wait(L, C_CSH, 1);
        rdcs(0);
        SB();
#pragma unroll
        for (int i = 0; i < 3; ++i) aQ[i] = mfma32(__builtin_bit_cast(bf16x8, u32x4{cq[i], 0u, 0u, 0u}), lnb, aQ[i]);
        SB();
        STAMP(3);
      }
      u32 land;
      sfor<0, NT>([&](auto II) {
        constexpr int i = decltype(II)::v, slot = (s < 14 ? s * 6 + i : 84 + i) % 8;
        if constexpr (s > 14 && s < 27 && i == 1) land = land_ld(L);
        if constexpr (s > 14 && s < 27 && i == NT - PF) {
          if ((u32)__builtin_amdgcn_readfirstlane(land) <= s) prog_wait<false>(L, s + 1);
          asm volatile("" ::: "memory");
        }
        if constexpr (i + PF < NT) rdW(SS, IC<i + PF>{});
        else rdW(IC<s + 1>{}, IC<i + PF - NT>{});
        if constexpr (i < 2 && s != 12) rdX(IC<s + 1>{}, II);
        if constexpr (s == 11 && (i == 2 || i == 3)) rdX(IC<13>{}, IC<i - 2>{});
        if constexpr (s < 14) {
          aQ[i % 3] = mfma32(wf[slot], xf[2 * s + i / 3], aQ[i % 3]);
          if constexpr (s == 0 && (i == 1 || i == 2)) sx = mfma(xf[i - 1], sel, sx);
          if constexpr (s < 12 && i >= 4) sx = mfma(xf[2 * s + i - 2], sel, sx);
          if constexpr (s == 11 && i == 5) { sx = mfma(xf[26], sel, sx); sx = mfma(xf[27], sel, sx); }
          if constexpr (s == 13 && i == 2) {
            lnb = __builtin_bit_cast(bf16x8, u32x4{l < 32 ? (st[0] & 0xffffu) * 0x10001u : 0u, l < 32 ? st[0] >> 16 : 0u, 0u, 0u});
            rs = __uint_as_float(st[1]);
          }
          if constexpr (i % 3 == 0) cs = mfma(wf[slot], sel, cs);
          if constexpr (s == 13 && i == 4) {
            u32x4 v;
#pragma unroll
            for (int j = 0; j < 4; ++j) { const float hi = rbf(cs[j]); v[j] = pk(hi, cs[j] - hi); }
            if (l15 < 2) lds_st<u32x4>(L, L_CSQ + w * 128 + (l15 * 16 + 4 * g) * 4, v);
          }
        } else {
          constexpr int k = 4 * ((s - 14) % 7) + (i >> 1);
          constexpr int hf = s >= 21;
          if constexpr (k == 0) aH[hf][i] = mfma32(__builtin_bit_cast(bf16x8, u32x4{ch[i][0], ch[i][1], 0u, 0u}), lnb, f32x16{});
          aH[hf][i & 1] = mfma32(wf[slot], xf[k], aH[hf][i & 1]);
          if constexpr (s == 20 && i == 4) rdcs(1);
          if constexpr (s < 14 + 6 / QPI && i % (8 / QPI) == 1) qstore((s - 14) * QPI + i / (8 / QPI));
          if constexpr (hf && s < 21 + 4 / HPI && i % (8 / HPI) == 1) { hpack(0, (s - 21) * HPI + i / (8 / HPI)); hstore((s - 21) * HPI + i / (8 / HPI)); }
        }
        SB();
      });
      if constexpr (s == 11) { if (l15 < 2) lds_st<f32x4>(L, L_SX + (w * 32 + l15 * 16 + 4 * g) * 4, sx); SB(); }
      if constexpr (s >= 14) prog_st(L, C_DONE + w, s);
      constexpr int nh = s < 21 ? 0 : ((s - 20) * HPI > 4 ? 4 : (s - 20) * HPI);
      if constexpr (s >= 15 + 6 / QPI && s <= S_FQ) { if (!sigq && (s == S_FQ || vm_le<nh>())) { vm<nh>(); cnt_add(lb, C_FQ); sigq = true; } }
      if constexpr (s >= 22 + 4 / HPI) { if (!siga && vm_le<0>()) { vm<0>(); cnt_add(lb, C_FA); siga = true; } }
    });
    STAMP(4);
    lanes();
    const bool giver = (w >> 1) != kh;
    const u32 vFH = (rb * 16 + 7 * kh + (l < 7 ? l : 0)) * FS, vFB = WS_FB + (rb * 16 + (giver ? 8 : 0) + (l & 7)) * FS;
#pragma unroll
    for (int c = 0; c < 4; ++c) hpack(1, c);
    SB();
    if (!siga) { vm<0>(); cnt_add(lb, C_FA); }
    hstore(4); hstore(5);
    STAMP(5);
    u32x2 polled;
    u32 vW[2];
    vW2(vW);
    prog_wait<true>(L, 27);
#pragma unroll
    for (int k = 0; k < 5; ++k) dman<2>(lb + L_WC + (32 + 5 * w + k) * 2048, vW, rW2, ((8 * (7 * kh + (cc + 1 + ((5 * w + k) >> 2)) % 7) + 4 + ((5 * w + k) & 3)) * 16) * 896 + cc * 128);
    hstore(6); hstore(7);
    cnt_wait(L, C_FH, 1);
    STAMP(6);
    __builtin_amdgcn_s_setprio(PRIO_C);
    const u32 vA = l * 16, sA = (rb * 4 + w) * 144, sO = (rb * 4 + (giver ? w ^ 2 : w)) * 144 + (giver ? 128 : 112);
    u32 sN[6];
#pragma unroll
    for (int j = 0; j < 6; ++j) { sN[j] = (sA + kstep(8 + 4 * j)) * 1024; asm volatile("" : "+s"(sN[j])); }
    const u32 vB = vA + 4096;
    auto ldA = [&](auto M) {
      constexpr int m = decltype(M)::v;
      hA[m] = ld16_async<true, (m & 3) * 1024>(m >= 32 && m < 56 ? vB : vA, rAC, m < 56 ? sN[((m - 8) % 24) / 4] : (sO + ((m - 56) & ~3)) * 1024);
    };
    sfor<8, 8 + DA>(ldA);
    u32 oW[2], oO[2];
#pragma unroll
    for (int b = 0; b < 2; ++b) { oW[b] = (4 * (g >> 1) + q4) * 128 + ((b ^ (q4 >> 1)) << 6) + (g & 1) * 32 + p4 * 8; oO[b] = oW[b] + (giver ? 8 * 3584 : 0); }
    bf16x8 wc[2][2];
    auto rdC = [&](int m) {
#pragma unroll
      for (int b = 0; b < 2; ++b) {
        u32 o;
        if (m < 56) o = oW[b] + L_WC + m * 2048;
        else o = oO[b] + ((m - 56) >> 1) * 3584 + (m & 1) * 2048 + (m >= 66 && giver ? L_WC - 364 * 128 : 0);
        wc[m & 1][b] = trfrag(L, o, o + 1024);
      }
    };
    f32x16 acc[2];
    bool sigp = false;
    u32x4 xres[2], bres;
    cnt_wait(L, C_WLO, 2);
    rdC(0);
    sfor<0, 72>([&](auto N) {
      constexpr int n = decltype(N)::v;
      if constexpr (n >= 8) asm volatile("s_waitcnt vmcnt(%1)" : "+v"(hA[n]) : "n"(SC.wC[n]) : "memory");
      if constexpr (n == 33) cnt_add(lb, C_P33);
      if constexpr (n == 8) { cnt_add(lb, C_P6); cnt_add(lb, C_WHI); if (last_of4(L, C_FG) && l == 0) st_flag(rCTL, WS_FG + (rb * 16 + cb) * FS, tag); }
      if constexpr (n == TB - 3 || n == TO - 3) polled = ld8c_async(n == TB - 3 ? WS_FG + vFH : vFB, rCTL);
      if constexpr (n == TB || n == TO) { PIN(polled); if (!tagged(polled, tag)) spin<SLP>(rCTL, n == TB ? WS_FG + vFH : vFB, tag); }
      if constexpr (n == 31) cnt_wait(L, C_WHI, 4);
      if constexpr (n == 51) cnt_wait(L, C_LATE, 4);
      if constexpr (n == 56) {
        if (giver) {
#pragma unroll
          for (int k = 0; k < 4; ++k) {
            const f32x16& a = acc[k >> 1];
            const int o = 8 * (k & 1);
            st16<16>(u32x4{pk(a[o], a[o + 1]), pk(a[o + 2], a[o + 3]), pk(a[o + 4], a[o + 5]), pk(a[o + 6], a[o + 7])}, rAC, PX + ((rb * 7 + cc) * 4 + w) * 4096 + k * 1024 + l * 16);
          }
          acc[0] = f32x16{}; acc[1] = f32x16{};
        }
      }
      if constexpr (n > 57 && n <= T_FP) {
        if (giver && !sigp && (n == T_FP || vm_le<SC.wP[n]>())) { vm<SC.wP[n]>(); if (l == 0) st_flag(rCTL, WS_FP + ((rb * 7 + cc) * 4 + w) * FS, tag); sigp = true; }
      }
      if constexpr (n + 1 < 72) rdC(n + 1);
      SB();
      acc[0] = mfma32(wc[n & 1][0], __builtin_bit_cast(bf16x8, hA[n]), n == 0 ? f32x16{} : acc[0]);
      SB();
      if constexpr (n >= 8) sfor<SC.a0[n], SC.a1[n]>(ldA);
      acc[1] = mfma32(wc[n & 1][1], __builtin_bit_cast(bf16x8, hA[n]), n == 0 ? f32x16{} : acc[1]);
      SB();
    });
    STAMP(10);
    const int tile = giver ? w ^ 2 : w, r0 = (giver ? 16 : 0) + (l >> 3);
#pragma unroll
    for (int k = 0; k < 2; ++k) xres[k] = ld16_async<false>((r0 + 8 * k) * 896 + (l & 7) * 16, rX, (rb * 128 + tile * 32) * 896 + cc * 128);
    bres = ld16_async<false>((l & 7) * 16, mkrs(b2, 896), cc * 128);
    if (!giver) {
      spin<SLP>(rCTL, WS_FP + ((rb * 7 + cc) * 4 + w) * FS, tag);
#pragma unroll
      for (int k = 0; k < 4; ++k) {
        const u32x4 p = __builtin_amdgcn_raw_buffer_load_b128(rAC, PX + ((rb * 7 + cc) * 4 + w) * 4096 + k * 1024 + l * 16, 0, 16);
#pragma unroll
        for (int j = 0; j < 4; ++j) { acc[k >> 1][8 * (k & 1) + 2 * j] += bflo(p[j]); acc[k >> 1][8 * (k & 1) + 2 * j + 1] += bfhi(p[j]); }
      }
    }
    cnt_wait(L, C_P33, 4);
#pragma unroll
    for (int b = 0; b < 2; ++b)
#pragma unroll
      for (int k = 0; k < 4; ++k)
        lds_st<f32x4>(L, L_RED + w * 8192 + (l & 31) * 256 + (((b * 8 + k * 2 + (g >> 1)) ^ (l & 7)) << 4), f32x4{acc[b][4 * k], acc[b][4 * k + 1], acc[b][4 * k + 2], acc[b][4 * k + 3]});
    cnt_add(lb, C_PAIR0 + (w & 1));
    asm volatile("s_waitcnt vmcnt(0)" : "+v"(xres[0]), "+v"(xres[1]), "+v"(bres)::"memory");
    cnt_wait(L, C_PAIR0 + (w & 1), 2);
    const rsrc_t rO = mkrs(out, 2048 * 896);
#pragma unroll
    for (int k = 0; k < 2; ++k) {
      const int row = r0 + 8 * k;
      f32x4 a = {bflo(xres[k][0]) + bflo(bres[0]), bfhi(xres[k][0]) + bfhi(bres[0]), bflo(xres[k][1]) + bflo(bres[1]), bfhi(xres[k][1]) + bfhi(bres[1])};
      f32x4 b = {bflo(xres[k][2]) + bflo(bres[2]), bfhi(xres[k][2]) + bfhi(bres[2]), bflo(xres[k][3]) + bflo(bres[3]), bfhi(xres[k][3]) + bfhi(bres[3])};
#pragma unroll
      for (int j = 0; j < 2; ++j) {
        a += lds_ld<f32x4>(L, L_RED + ((w & 1) + 2 * j) * 8192 + row * 256 + (((2 * (l & 7)) ^ (row & 7)) << 4));
        b += lds_ld<f32x4>(L, L_RED + ((w & 1) + 2 * j) * 8192 + row * 256 + (((2 * (l & 7) + 1) ^ (row & 7)) << 4));
      }
      st16<16>(u32x4{pk(a[0], a[1]), pk(a[2], a[3]), pk(b[0], b[1]), pk(b[2], b[3])}, rO, (rb * 128 + tile * 32 + row) * 896 + cc * 128 + (l & 7) * 16);
    }
    if (w == 0) STAMP(11);
    FLUSH();
    return;
  }

  const int li = w & 1;
  if (!busy) {
    epoch();
    if (w == 6 && l < C_N) lds_st<u32>(L, L_CNT + l * 4, l < 4 ? 64u : 0u);
    wait_bar<63>();
  } else if (w < 6) {
    const rsrc_t rW1 = mkrs(w1, 448 * 3584), rWq = mkrs(wqkv, 448 * 2688);
    u32 vX[4], vH[4], vQ[3], vC[2], so7[7];
#pragma unroll
    for (int j = 0; j < 4; ++j) vX[j] = (8 * j + (l >> 3)) * 896 + (((l & 7) ^ ((j & 1) * 4 + g)) << 4) - 1024 * j;
    so7[0] = 0;
    const u32 sX = (rb * 128 + 64 * li) * 896, sH = cb * 256 + 32 * li * 3584, sQ = cb * 192 + 16 * li * 2688;
    auto issue = [&](auto O) {
      constexpr int op = decltype(O)::v, k = op / 256, a = (op % 256) / 2, e = op & 1;
      if constexpr (k == K_X) dman<4>(lb + (a * 16 + 8 * li + 4 * e) * 1024, vX, rX, sX + so7[a] * 128 + e * 28672);
      if constexpr (k == K_WA && a < 14) dman<3>(lb + stage_addr(a) + li * 3072, vQ, rWq, sQ + so7[a >> 1] * (64 * 2688) + (a & 1) * (32 * 2688));
      if constexpr (k == K_WA && a >= 14) dman<4>(lb + stage_addr(a) + li * 4096, vH, rW1, sH + so7[(a - 14) % 7] * (64 * 3584) + (a >= 21 ? 128 : 0));
      if constexpr (k == K_WCP) dman<2>(lb + L_WC + (2 * a + li) * 2048, vC, rW2, (kstep(2 * a) + li) * (16 * 896) + cc * 128);
    };
    sfor<0, 2>([&](auto K) { issue(IC<SC.pro[decltype(K)::v]>{}); });
    lanes();
#pragma unroll
    for (int j = 0; j < 3; ++j) { const u32 n = 64 * j + l; vQ[j] = (n / 12) * 2688 + (n % 12) * 16 - 1024 * j; }
    sfor<2, 4>([&](auto K) { issue(IC<SC.pro[decltype(K)::v]>{}); });
#pragma unroll
    for (int j = 1; j < 7; ++j) so7[j] = rotl(j);
    sfor<4, NPRO>([&](auto K) { issue(IC<SC.pro[decltype(K)::v]>{}); });
    epoch();
#pragma unroll
    for (int j = 0; j < 4; ++j) vH[j] = (8 * j + (l >> 3)) * 3584 + (((l & 7) ^ ((g & 1) << 2)) << 4) - 1024 * j;
    vW2(vC);
    vm_bar<SC.wA[0]>();
    sfor<0, 26>([&](auto SS) {
      constexpr int s = decltype(SS)::v;
      if constexpr (s <= 14) vm_bar<SC.wA[s + 1]>();
      else {
        vm<SC.wA[s + 1]>();
        prog_st(L, C_LAND + li, s + 2);
        if constexpr (s + 5 < 28) prog_wait<true>(L, s - 1);
      }
      sfor<0, MAXO>([&](auto K) { issue(IC<SC.opA[s][decltype(K)::v]>{}); });
    });
    vm<0>();
    cnt_add(lb, C_WLO);
    if (w == 4) { STAMP(13); cnt_wait(L, C_FA, 4); STAMP(14); if (l == 0) st_flag(rCTL, WS_FH + (rb * 16 + cb) * FS, tag); }
    else { cnt_wait(L, C_FA, 4); spin<SLP>(rCTL, WS_FH + (rb * 16 + 7 * kh + (l < 7 ? l : 0)) * FS, tag); STAMP(15); cnt_add(lb, C_FH); }
  } else {
    epoch();
    u32 oX[4];
#pragma unroll
    for (int p = 0; p < 4; ++p) oX[p] = (8 * li + ((l & 31) >> 3)) * 1024 + (l & 7) * 128 + (((2 * p + (g >> 1)) ^ (l15 >> 1)) << 4);
    float s2[2][2] = {};
    const u32 fc = WS_FC + (2 * cb + (l & 1)) * FS;
    u32x2 polled;
    bool got;
    auto ldcs = [&] { asm volatile("s_mov_b32 m0, %0\n\ts_nop 0\n\tbuffer_load_dwordx4 %1, %2, %3 offen sc1 lds" ::"s"(lb + L_CSH), "v"(l * 16), "s"(rCTL), "s"(WS_CS + cb * 1024) : "memory"); };
    u32x4 xv[8];
    auto sq = [&](int p) {
#pragma unroll
      for (int k = 0; k < 4; ++k)
#pragma unroll
        for (int j = 0; j < 4; ++j) s2[k & 1][j & 1] = dot2(xv[4 * p + k][j], xv[4 * p + k][j], s2[k & 1][j & 1]);
      PIN(s2[0][0]); PIN(s2[0][1]); PIN(s2[1][0]); PIN(s2[1][1]);
    };
    sfor<0, 13>([&](auto SS) {
      constexpr int s = decltype(SS)::v;
      bar();
      if constexpr (!(s & 1)) {
#pragma unroll
        for (int k = 0; k < 8; ++k) xv[k] = lds_ld<u32x4>(L, oX[k >> 1] + (s >> 1) * 16384 + (k & 1) * 4096);
      }
      sq(s & 1);
      if constexpr (s == 9) polled = ld8c_async(fc, rCTL);
      if constexpr (s == 12) {
        asm volatile("s_waitcnt vmcnt(0)" : "+v"(polled)::"memory");
        got = tagged(polled, tag);
        if (w == 7 && got) ldcs();
      }
      SB();
    });
    bar();
    sq(1);
#pragma unroll
    for (int r = 0; r < 2; ++r) {
      const u32 q2 = __float_as_uint(s2[r][0] + s2[r][1]);
      const auto b = __builtin_amdgcn_permlane32_swap(q2, q2, false, false);
      const float mean = lds_ld<float>(L, L_SX + ((2 * li + r) * 32 + (l & 31)) * 4) * (1.f / 448.f);
      const float var = fmaxf((__uint_as_float(b[0]) + __uint_as_float(b[1])) * (1.f / 448.f) - mean * mean, 0.f);
      const float sd = rbf(__builtin_amdgcn_sqrtf(rbf(rbf(var) + 1e-5f)));
      if (l < 32) lds_st<u32x2>(L, L_ST + ((2 * li + r) * 32 + l) * 8, u32x2{pk(-mean, sd), __float_as_uint(__builtin_amdgcn_rcpf(sd))});
    }
    if (w == 6 && l < C_N) lds_st<u32>(L, L_CNT + l * 4, 0u);
    wait_bar<63>();
    if (w == 7 && got) { vm<0>(); cnt_add(lb, C_CSH); }
    bar();
    if (w == 7 && !got) { spin<SLP>(rCTL, fc, tag); ldcs(); vm<0>(); cnt_add(lb, C_CSH); }
  }
  lanes();
  const int aw = w - 4, hh = g >> 1;
  if (w >= 6) {
#pragma unroll
    for (int k = 0; k < 3; ++k) lds_st<u32x2>(L, L_V + (6 * li + 2 * k + (l >> 5)) * 2048 + 1792 + (l & 31) * 8, u32x2{0x3f80u, 0u});
    const int e = l & 1, k = l < 14 ? l >> 1 : 0;
    const int ty = k == 0 ? 0 : (k < 4 ? 1 : 2), ro = k == 0 ? 0 : (k < 4 ? k - 1 : k - 4);
    const int rr = ai - ro < 0 ? 0 : ai - ro;
    if (!busy) { for (int k = 0; k < 12; ++k) __builtin_amdgcn_s_sleep(15); }
    if (busy) { cnt_wait(L, C_FQ, 4); if (w == 6) STAMP(12); if (w == 6 && l == 0) st_flag(rCTL, WS_FQ + (rb * 16 + cb) * FS, tag); }
    if (w == 6) { spin<SLP>(rCTL, WS_FQ + (rr * 16 + (ty * 448 + 28 * ah + 27 * e) / 96) * FS, tag); cnt_add(lb, C_SQ); }
    else cnt_wait(L, C_SQ, 1);
    if (w == 6) STAMP(7);
#pragma unroll
    for (int e = 0; e < 2; ++e) {
#pragma unroll
      for (int j = 0; j < 6; ++j) {
        const int b = 2 * j + li, tb = 4 * ai - 8 + b < 0 ? 0 : 4 * ai - 8 + b;
        dma1792c(lb + (e ? L_V + b * 2048 : L_K + b * 1792), l * 16, rQKV, (tb * 336 + 112 + 112 * e + 7 * ah) * 256);
      }
      if (e == 0) {
        prog_wait<true>(L, 23);
#pragma unroll
        for (int j = 0; j < 2; ++j) dma1792c(lb + L_Q + (2 * li + j) * 2048, l * 16, rQKV, ((4 * ai + 2 * li + j) * 336 + 7 * ah) * 256);
      }
    }
    vm<12>();
    cnt_add(lb, C_KV);
  }
  cnt_wait(L, C_KV, 2);
  __builtin_amdgcn_s_setprio(PRIO_B);
  if (w == 4) STAMP(8);
  u32x2 qv[4];
#pragma unroll
  for (int k = 0; k < 4; ++k) qv[k] = lds_ld<u32x2>(L, L_Q + aw * 2048 + (k == 3 ? 6 : 2 * k + hh) * 256 + l15 * 16 + (g & 1) * 8);
  if (hh) qv[3] = u32x2{0u, 0u};
  const bf16x8 qf[2] = {__builtin_bit_cast(bf16x8, u32x4{qv[0][0], qv[0][1], qv[1][0], qv[1][1]}), __builtin_bit_cast(bf16x8, u32x4{qv[2][0], qv[2][1], qv[3][0], qv[3][1]})};
  const u32 oK = L_K + aw * 1792 + hh * 256 + l15 * 16 + (g & 1) * 8, oK3 = hh ? oK + 1280 : oK + 1536;
  const int F = 7 * ah + 4 * (g & 1) + p4;
  u32 oV[2][2];
#pragma unroll
  for (int t = 0; t < 2; ++t)
#pragma unroll
    for (int e = 0; e < 2; ++e) oV[t][e] = L_V + aw * 2048 + (4 * (g & 1) + p4) * 256 + (((8 * e + 4 * hh + q4) ^ ((F & 6) << 1)) << 4) + ((t ^ (F & 1)) << 3);
  f32x16 sc[9];
  bf16x8 kf[9][2], vf[9][2];
  auto rdK = [&](int m) {
    const u32x2 a = lds_ld<u32x2>(L, oK + m * 1792), b = lds_ld<u32x2>(L, oK + m * 1792 + 512), c = lds_ld<u32x2>(L, oK + m * 1792 + 1024), d = lds_ld<u32x2>(L, oK3 + m * 1792);
    kf[m][0] = __builtin_bit_cast(bf16x8, u32x4{a[0], a[1], b[0], b[1]}); kf[m][1] = __builtin_bit_cast(bf16x8, u32x4{c[0], c[1], d[0], d[1]});
  };
  auto rdV = [&](int m) { vf[m][0] = trfrag(L, oV[0][0] + m * 2048, oV[0][1] + m * 2048); vf[m][1] = trfrag(L, oV[1][0] + m * 2048, oV[1][1] + m * 2048); };
  rdK(0); SB(); rdK(8); SB(); rdK(1); SB();
#pragma unroll
  for (int i = 0; i < 9; ++i) {
    const int m = i == 0 ? 0 : (i == 1 ? 8 : i - 1);
    if (i + 2 < 8) rdK(i + 2);
    sc[m] = mfma32(kf[m][1], qf[1], mfma32(kf[m][0], qf[0], f32x16{}));
    SB();
  }
  if (w >= 6) { vm<0>(); cnt_add(lb, C_V); }
  constexpr float NEG = -1e30f;
  if (ai < 2) {
    asm volatile("" ::: "memory");
    const int m0 = 8 - 4 * ai - aw;
#pragma unroll
    for (int m = 0; m < 8; ++m)
      if (m < m0) {
#pragma unroll
        for (int r = 0; r < 16; ++r) sc[m][r] = NEG;
      }
  }
  const int dq = (l & 31) - 4 * hh;
  u32 mk[8];
#pragma unroll
  for (int r = 0; r < 16; r += 2) {
    const bool c0 = (r & 3) + 8 * (r >> 2) <= dq, c1 = (r & 3) + 1 + 8 * (r >> 2) <= dq;
    sc[0][r] = c0 ? sc[8][r] : sc[0][r]; sc[0][r + 1] = c1 ? sc[8][r + 1] : sc[0][r + 1];
    mk[r >> 1] = c1 ? 0xffffffffu : (c0 ? 0xffffu : 0u);
  }
  cnt_wait(L, C_V, 2);
  rdV(1); rdV(2);
  SB();
  float mx0 = fmx(sc[0][0], sc[0][1]), mx1 = fmx(sc[0][2], sc[0][3]);
#pragma unroll
  for (int k = 1; k < 32; ++k) { mx0 = fmx(fmx(mx0, sc[k >> 2][4 * (k & 3)]), sc[k >> 2][4 * (k & 3) + 1]); mx1 = fmx(fmx(mx1, sc[k >> 2][4 * (k & 3) + 2]), sc[k >> 2][4 * (k & 3) + 3]); }
  mx0 = fmx(mx0, mx1);
  const auto ms = __builtin_amdgcn_permlane32_swap(__float_as_uint(mx0), __float_as_uint(mx0), false, false);
  const float mx = fmx(__uint_as_float(ms[0]), __uint_as_float(ms[1]));
  SB();
  f32x16 o = {};
#pragma unroll
  for (int i = 0; i < 8; ++i) {
    const int m = (i + 1) & 7;
    if (i < 5) rdV(i + 3); else if (i == 5) rdV(0); else if (i == 6) rdV(8);
    u32x4 p[2];
#pragma unroll
    for (int j = 0; j < 8; ++j) {
      f32x2 d = f32x2{sc[m][2 * j], sc[m][2 * j + 1]} - f32x2{mx, mx};
      PIN(d);
      p[j >> 2][j & 3] = pk(__builtin_amdgcn_exp2f(d[0]), __builtin_amdgcn_exp2f(d[1]));
    }
#pragma unroll
    for (int s = 0; s < 2; ++s) {
      if (m) o = mfma32(vf[m][s], __builtin_bit_cast(bf16x8, p[s]), o);
      else {
        const u32x4 k4 = {mk[4 * s], mk[4 * s + 1], mk[4 * s + 2], mk[4 * s + 3]};
        o = mfma32(vf[0][s], __builtin_bit_cast(bf16x8, p[s] & ~k4), o);
        o = mfma32(vf[8][s], __builtin_bit_cast(bf16x8, p[s] & k4), o);
      }
    }
    SB();
  }
  cnt_add(lb, C_ATT);
  const auto dn = __builtin_amdgcn_permlane32_swap(__float_as_uint(o[12]), __float_as_uint(o[12]), false, false);
  const float inv = __builtin_amdgcn_rcpf(__uint_as_float(dn[1]));
  const float in2 = hh ? 0.f : inv;
  const u32 so = ((ai * 4 + aw) * 144 + 112 + 2 * ah) * 1024;
  st16<16>(u32x4{pk(o[0] * inv, o[1] * inv), pk(o[2] * inv, o[3] * inv), pk(o[4] * inv, o[5] * inv), pk(o[6] * inv, o[7] * inv)}, rAC, l * 16 + so);
  st16<16>(u32x4{pk(o[8] * inv, o[9] * inv), pk(o[10] * inv, o[11] * inv), pk(o[12] * in2, o[13] * in2), pk(o[14] * in2, o[15] * in2)}, rAC, l * 16 + so + 1024);
  if (w == 4) STAMP(9);
  if (busy) {
    u32 vW[2];
    vW2(vW);
    dman<2>(lb + L_Q + aw * 2048, vW, rW2, (kstep(52) + aw) * (16 * 896) + cc * 128);
    vm<2>();
    if (last_of4(L, C_FB) && l == 0) st_flag(rCTL, WS_FB + (ai * 16 + ah) * FS, tag);
    cnt_wait(L, C_ATT, 4);
    cnt_wait(L, C_P6, 4);
    const rsrc_t rWo = mkrs(wo, 448 * 896);
#pragma unroll
    for (int k = 0; k < 6; ++k) if (k < 5 || aw < 3) dman<2>(lb + (4 * k + aw) * 2048, vW, rWo, (4 * k + aw) * (16 * 896) + cc * 128);
#pragma unroll
    for (int k = 0; k < 2; ++k) if (k == 0 || aw < 2) dman<2>(lb + L_WC + (4 * k + aw) * 2048, vW, rWo, (364 + 16 * (4 * k + aw)) * 896 + cc * 128);
    vm<0>();
    cnt_add(lb, C_LATE);
  } else {
    vm<0>();
    if (last_of4(L, C_FB)) {
      if (l == 0) st_flag(rCTL, WS_FB + (ai * 16 + ah) * FS, tag);
      if (blockIdx.x == 255) {
#pragma unroll 1
        for (int k = 0; k < 4; ++k) spin<32>(rCTL, WS_FB + (k * 64 + l) * FS, tag);
        if (l == 0) st_flag(rCTL, 0, tag);
      }
    }
  }
  FLUSH();
}

extern "C" void kernel_launch(void* const* d_in, const int* in_sizes, int n_in, void* d_out, int out_size, void* d_ws, size_t ws_size, hipStream_t stream) {
  fused_block<<<256, 512, 0, stream>>>((const char*)d_in[0], (const char*)d_in[1], (const char*)d_in[2], (const char*)d_in[4], (const char*)d_in[5],
                                       (const char*)d_in[6], (const char*)d_in[7], (char*)d_out, (char*)d_ws + WS_OFF);
}
```

```cpp
#include <hip/hip_runtime.h>
#include <hip/hip_bf16.h>
#include <stdint.h>

#define DI __device__ __forceinline__
#define LAS __attribute__((address_space(3)))
typedef unsigned u32;
typedef unsigned long long u64;
typedef __bf16 bf16x8 __attribute__((ext_vector_type(8)));
typedef __bf16 bf16x2 __attribute__((ext_vector_type(2)));
typedef short s16x4 __attribute__((ext_vector_type(4)));
typedef float f32x4 __attribute__((ext_vector_type(4)));
typedef float f32x2 __attribute__((ext_vector_type(2)));
typedef float f32x16 __attribute__((ext_vector_type(16)));
typedef unsigned u32x4 __attribute__((ext_vector_type(4)));
typedef unsigned u32x2 __attribute__((ext_vector_type(2)));
typedef __amdgpu_buffer_rsrc_t rsrc_t;

template <int N> struct IC { static constexpr int v = N; };
template <int B, int E, class F> DI void sfor(F&& f) { if constexpr (B < E) { f(IC<B>{}); sfor<B + 1, E>(f); } }

constexpr u32 FS = 128;
constexpr u32 WS_FH = FS, WS_FQ = WS_FH + 256 * FS, WS_FB = WS_FQ + 256 * FS, WS_FG = WS_FB + 256 * FS, WS_FP = WS_FG + 256 * FS, WS_FC = WS_FP + 448 * FS;
constexpr u32 WS_FS = WS_FC + 32 * FS, WS_FZ = WS_FS + 128 * FS, WS_CS = WS_FZ + 287 * FS, WS_ST = WS_CS + 16384, CTL_BYTES = WS_ST + 16384;
constexpr u32 WS_QKV = CTL_BYTES, QKV_BYTES = 64u * 336u * 256u;
constexpr u32 WS_AC = WS_QKV + QKV_BYTES, AC_BYTES = 64u * 144u * 1024u + 448u * 4096u;
constexpr u32 PX = 64u * 144u * 1024u;
constexpr u32 RING = 7 * 16384;
constexpr u32 L_POLL = 45056, L_CSQ = 46080, L_CSH = 46848, L_ST = 47872, L_CNT = 48896;
constexpr u32 L_K = 0, L_V = 21504, L_Q = RING + 5 * 8192;
constexpr u32 L_WC = 49152, L_RED = L_WC + 16384;
enum { C_DONE, C_LAND = 4, C_FQ = 6, C_FA, C_FG, C_KV, C_FB, C_WLO, C_WHI, C_P6, C_P33, C_ATT, C_LATE, C_PAIR0, C_PAIR1, C_CSH, C_FH, C_SQ, C_V, C_ST, C_CS0, C_N };
constexpr u32 stage_addr(int s) { return s < 14 ? RING + (s % 4) * 10240u : (s < 19 ? (s - 14) * 8192u : RING + (s - 19) * 8192u); }

enum { K_NONE = 0, K_X, K_WA, K_BIAS, K_WCP };
constexpr int OP(int k, int a, int e = 0) { return k * 256 + 2 * a + e; }
constexpr int opn(int op) { int k = op / 256, a = (op % 256) / 2; return k == K_NONE ? 0 : k == K_X ? 4 : k == K_WA ? (a < 14 ? 5 : 4) : k == K_WCP ? 2 : 1; }
#define CAP_1 9
#define P_NPRO 8
#define P_DA 12
#define P_TB 20
#define P_TO 46
#define P_FQ 19
#define PRIO_C 0
#define PRIO_B 2
#define ROTA 1
#define WS_OFF 0
#define CODE_KB 39
#define PF_DELAY 20
#define P_TFP 66
#define SLP 2
#define LSLP 1
#define GATE_K 1
#define OWN1 4
#define CLR0 3
#define QNOW 6
constexpr int MAXO = 24, NPRO = P_NPRO, DA = P_DA, TB = P_TB, S_FQ = P_FQ, T_WH = 26, T_O = P_TO;
struct Sched {
  int pro[NPRO], opA[15][MAXO];
  int wA[16];
  int wL[2];
  int wC[56], a0[56], a1[56];
  int ok;
};
constexpr int cl(int n) { return n > 63 ? 63 : n; }
constexpr Sched make_sched() {
  Sched r{};
  r.ok = 1;
  int n = 0, iW[21] = {}, iX[7] = {};
  int lo[128] = {}, le[128] = {}, nl = 0;
  for (int s = 0; s < 21; ++s) {
    if (s < 14 && !(s & 1)) { lo[nl] = OP(K_X, s >> 1, 0); le[nl++] = -1; lo[nl] = OP(K_X, s >> 1, 1); le[nl++] = -1; }
    lo[nl] = OP(K_WA, s); le[nl++] = s < 14 ? s - 3 : (s < 19 ? 5 : 14);
  }
  for (int k = 0; k < 16; ++k) { lo[nl] = OP(K_WCP, k); le[nl++] = 7 + 2 * (k >> 2); }
  bool done[128] = {};
  auto note = [&](int op) { n += opn(op); int k = op / 256, a = (op % 256) / 2; if (k == K_X) iX[a] = (op & 1) ? n : 0; if (k == K_WA) iW[a] = n; };
  for (int k = 0; k < NPRO; ++k) { r.pro[k] = lo[k]; done[k] = true; note(lo[k]); }
  r.wA[0] = cl(n - iW[0]);
  for (int s = 0; s < 15; ++s) {
    int need = iW[s + 1]; if (!need) r.ok = 0;
    if (s + 1 <= 13) { int v = iX[(s + 1) >> 1]; if (!v) r.ok = 0; if (v > need) need = v; }
    r.wA[s + 1] = cl(n - need);
    int cap = s == 14 ? 64 : CAP_1, c = 0, used = 0;
    for (int k = 0; k < nl; ++k) if (!done[k] && le[k] <= s && used + opn(lo[k]) <= cap && c < MAXO) { done[k] = true; r.opA[s][c++] = lo[k]; used += opn(lo[k]); note(lo[k]); if (s == 14 && c == 2) r.wL[0] = cl(n - iW[18]); if (s == 14 && c == 8) r.wL[1] = cl(n - iW[20]); }
  }
  if (r.opA[14][1] != OP(K_WA, 20)) r.ok = 0;
  for (int k = 0; k < nl; ++k) if (!done[k]) r.ok = 0;
  n = 0;
  int iA[56] = {}, iP = 0, iH = 0, nA = 8 + DA;
  for (int m = 8; m < 8 + DA; ++m) { n += 1; iA[m] = n; }
  for (int s = 8; s < 56; ++s) {
    int need = iA[s]; if (!need) r.ok = 0;
    if (s == TB && iP > need) need = iP;
    if (s == T_WH && iH > need) need = iH;
    r.wC[s] = cl(n - need);
    if (s == TB - 3) { n += 1; iP = n; }
    r.a0[s] = nA;
    for (int c = 0; c < 2 && nA < 56 && nA <= s + DA && (nA < 32 || s >= TB); ++c) { n += 1; iA[nA++] = n; }
    r.a1[s] = nA;
    if (s <= 16 && !(s & 1)) { n += 2; iH = n; }
    if (s >= T_O && s < T_O + 8) n += 2;
  }
  return r;
}
constexpr Sched SC = make_sched();
static_assert(SC.ok, "schedule");

DI rsrc_t mkrs(const void* p, u32 bytes) { return __builtin_amdgcn_make_buffer_rsrc((void*)p, 0, bytes, 0x00020000); }
template <int N> DI void dman(u32 lds, const u32* v, rsrc_t rs, u32 soff) {
  if constexpr (N == 4) asm volatile("s_mov_b32 m0, %0\n\ts_nop 0\n\tbuffer_load_dwordx4 %1, %5, %6 offen lds\n\tbuffer_load_dwordx4 %2, %5, %6 offen offset:1024 lds\n\t"
    "buffer_load_dwordx4 %3, %5, %6 offen offset:2048 lds\n\tbuffer_load_dwordx4 %4, %5, %6 offen offset:3072 lds" ::"s"(lds), "v"(v[0]), "v"(v[1]), "v"(v[2]), "v"(v[3]), "s"(rs), "s"(soff) : "memory");
  if constexpr (N == 3) asm volatile("s_mov_b32 m0, %0\n\ts_nop 0\n\tbuffer_load_dwordx4 %1, %4, %5 offen lds\n\tbuffer_load_dwordx4 %2, %4, %5 offen offset:1024 lds\n\t"
    "buffer_load_dwordx4 %3, %4, %5 offen offset:2048 lds" ::"s"(lds), "v"(v[0]), "v"(v[1]), "v"(v[2]), "s"(rs), "s"(soff) : "memory");
  if constexpr (N == 2) asm volatile("s_mov_b32 m0, %0\n\ts_nop 0\n\tbuffer_load_dwordx4 %1, %3, %4 offen lds\n\tbuffer_load_dwordx4 %2, %3, %4 offen offset:1024 lds" ::"s"(lds), "v"(v[0]), "v"(v[1]), "s"(rs), "s"(soff) : "memory");
  if constexpr (N == 1) asm volatile("s_mov_b32 m0, %0\n\ts_nop 0\n\tbuffer_load_dwordx4 %1, %2, %3 offen lds" ::"s"(lds), "v"(v[0]), "s"(rs), "s"(soff) : "memory");
}
DI void dma1792c(u32 lds, u32 voff, rsrc_t rs, u32 soff) {
  asm volatile("s_mov_b32 m0, %0\n\ts_nop 0\n\tbuffer_load_dwordx4 %1, %2, %3 offen sc1 lds\n\ts_mov_b32 exec_hi, 0xffff\n\t"
               "buffer_load_dwordx4 %1, %2, %3 offen offset:1024 sc1 lds\n\ts_mov_b32 exec_hi, -1" ::"s"(lds), "v"(voff), "s"(rs), "s"(soff) : "memory");
}
DI void dma4(u32 lds, u32 voff, rsrc_t rs, u32 soff) {
  asm volatile("s_mov_b32 m0, %0\n\ts_nop 0\n\tbuffer_load_dword %1, %2, %3 offen lds" ::"s"(lds), "v"(voff), "s"(rs), "s"(soff) : "memory");
}
template <bool C, int IMM = 0> DI u32x4 ld16_async(u32 voff, rsrc_t rs, u32 soff) {
  u32x4 r;
  if constexpr (C) asm volatile("buffer_load_dwordx4 %0, %1, %2, %3 offen offset:%4 sc1" : "=v"(r) : "v"(voff), "s"(rs), "s"(soff), "n"(IMM) : "memory");
  else asm volatile("buffer_load_dwordx4 %0, %1, %2, %3 offen" : "=v"(r) : "v"(voff), "s"(rs), "s"(soff) : "memory");
  return r;
}
DI u32x2 ld8c_async(u32 voff, rsrc_t rs) {
  u32x2 r; asm volatile("buffer_load_dwordx2 %0, %1, %2, 0 offen sc1" : "=v"(r) : "v"(voff), "s"(rs) : "memory"); return r;
}
template <int N> DI void wait_bar() { asm volatile("s_waitcnt vmcnt(%0) lgkmcnt(0)\n\ts_barrier" ::"n"(N) : "memory"); }
template <int N> DI void vm_bar() { asm volatile("s_waitcnt vmcnt(%0)\n\ts_barrier" ::"n"(N) : "memory"); }
template <int N> DI void vm() { asm volatile("s_waitcnt vmcnt(%0)" ::"n"(N) : "memory"); }
DI void bar() { asm volatile("s_barrier" ::: "memory"); }
#define PIN(x) asm volatile("" : "+v"(x))
#define SB() __builtin_amdgcn_sched_barrier(0)
DI f32x4 mfma(bf16x8 a, bf16x8 b, f32x4 c) { return __builtin_amdgcn_mfma_f32_16x16x32_bf16(a, b, c, 0, 0, 0); }
DI f32x16 mfma32(bf16x8 a, bf16x8 b, f32x16 c) { return __builtin_amdgcn_mfma_f32_32x32x16_bf16(a, b, c, 0, 0, 0); }
DI u32 pk(float a, float b) { bf16x2 v = {(__bf16)a, (__bf16)b}; return __builtin_bit_cast(u32, v); }
DI float fmx(float a, float b) { return __builtin_elementwise_maximum(a, b); }
DI float rbf(float a) { return (float)(__bf16)a; }
DI float bflo(u32 v) { return __uint_as_float(v << 16); }
DI float bfhi(u32 v) { return __uint_as_float(v & 0xffff0000u); }
DI float dot2(u32 a, u32 b, float c) { return __builtin_amdgcn_fdot2_f32_bf16(__builtin_bit_cast(bf16x2, a), __builtin_bit_cast(bf16x2, b), c, false); }
DI float sum8(float v) {
  v += __builtin_bit_cast(float, __builtin_amdgcn_update_dpp(0, __builtin_bit_cast(int, v), 0xb1, 0xf, 0xf, true));
  v += __builtin_bit_cast(float, __builtin_amdgcn_update_dpp(0, __builtin_bit_cast(int, v), 0x4e, 0xf, 0xf, true));
  v += __builtin_bit_cast(float, __builtin_amdgcn_update_dpp(0, __builtin_bit_cast(int, v), 0x141, 0xf, 0xf, true));
  return v;
}
template <class T> DI T lds_ld(const LAS char* base, u32 off) { return *(const LAS T*)(base + off); }
template <class T> DI void lds_st(LAS char* base, u32 off, T v) { *(LAS T*)(base + off) = v; }
DI bf16x8 trfrag(LAS char* base, u32 off0, u32 off1) {
  s16x4 lo = __builtin_amdgcn_ds_read_tr16_b64_v4i16((LAS s16x4*)(base + off0));
  s16x4 hi = __builtin_amdgcn_ds_read_tr16_b64_v4i16((LAS s16x4*)(base + off1));
  return __builtin_bit_cast(bf16x8, __builtin_shufflevector(lo, hi, 0, 1, 2, 3, 4, 5, 6, 7));
}
#define ST_AUX 16
template <int AUX> DI void st16(u32x4 v, rsrc_t rs, u32 voff) { __builtin_amdgcn_raw_buffer_store_b128(v, rs, voff, 0, AUX ? ST_AUX : 0); }
DI void st_flag(rsrc_t rc, u32 off, u64 v) { __builtin_amdgcn_raw_buffer_store_b64(u32x2{(u32)v, (u32)(v >> 32)}, rc, off, 0, 16); }
DI bool tagged(u32x2 v, u64 tag) { return __all(v[0] == (u32)tag && v[1] == (u32)(tag >> 32)); }
template <int SLEEP> DI void spin(rsrc_t rc, u32 off, u64 tag) {
#pragma unroll 1
  for (u32 n = 0; n < (1u << 17); ++n) {
    asm volatile("" ::: "memory");
    if (tagged(__builtin_amdgcn_raw_buffer_load_b64(rc, off, 0, 16), tag)) break;
    __builtin_amdgcn_s_sleep(SLEEP);
  }
  asm volatile("" ::: "memory");
}
DI u32 cnt_ld(LAS char* L, int k) { return __hip_atomic_load((LAS u32*)(L + L_CNT + 4 * k), __ATOMIC_RELAXED, __HIP_MEMORY_SCOPE_WORKGROUP); }
DI void cnt_wait(LAS char* L, int k, u32 v) {
#pragma unroll 1
  for (u32 n = 0; n < (1u << 20); ++n) {
    if ((u32)__builtin_amdgcn_readfirstlane(cnt_ld(L, k)) >= v) break;
    __builtin_amdgcn_s_sleep(LSLP);
  }
  asm volatile("" ::: "memory");
}
DI void prog_st(LAS char* L, int k, u32 v) { asm volatile("" ::: "memory"); __hip_atomic_store((LAS u32*)(L + L_CNT + 4 * k), v, __ATOMIC_RELAXED, __HIP_MEMORY_SCOPE_WORKGROUP); asm volatile("" ::: "memory"); }
DI u32 land_ld(LAS char* L) { const u64 v = __hip_atomic_load((LAS u64*)(L + L_CNT + 4 * C_LAND), __ATOMIC_RELAXED, __HIP_MEMORY_SCOPE_WORKGROUP); return min((u32)v, (u32)(v >> 32)); }
DI u32 done_ld(LAS char* L) {
  const u64 a = __hip_atomic_load((LAS u64*)(L + L_CNT), __ATOMIC_RELAXED, __HIP_MEMORY_SCOPE_WORKGROUP), b = __hip_atomic_load((LAS u64*)(L + L_CNT + 8), __ATOMIC_RELAXED, __HIP_MEMORY_SCOPE_WORKGROUP);
  return min(min((u32)a, (u32)(a >> 32)), min((u32)b, (u32)(b >> 32)));
}
template <bool DONE> DI void prog_wait(LAS char* L, u32 v) {
#pragma unroll 1
  for (u32 n = 0; n < (1u << 20); ++n) {
    if ((u32)__builtin_amdgcn_readfirstlane(DONE ? done_ld(L) : land_ld(L)) >= v) break;
    __builtin_amdgcn_s_sleep(LSLP);
  }
  asm volatile("" ::: "memory");
}
DI void cnt_add(u32 lb, int k) {
  asm volatile("s_mov_b64 exec, 1\n\tds_add_u32 %0, %1\n\ts_mov_b64 exec, -1" ::"v"(lb + L_CNT + 4 * k), "v"(1u) : "memory");
}
template <int N> DI bool vm_le() {
  u32 r; asm volatile("s_getreg_b32 %0, hwreg(HW_REG_IB_STS)" : "=s"(r)::"memory");
  return (r & 0xc0000fu) <= (u32)N;
}
DI bool last_of4(LAS char* L, int k) {
  u32 old = 0;
  asm volatile("" ::: "memory");
  if (__builtin_amdgcn_mbcnt_hi(~0u, __builtin_amdgcn_mbcnt_lo(~0u, 0u)) == 0) old = __hip_atomic_fetch_add((LAS u32*)(L + L_CNT + 4 * k), 1u, __ATOMIC_RELAXED, __HIP_MEMORY_SCOPE_WORKGROUP);
  asm volatile("" ::: "memory");
  return __builtin_amdgcn_readfirstlane(old) == 3;
}
#define STAMP(k)
#define FLUSH()

__global__ __launch_bounds__(512, 2) void fused_block(const char* __restrict__ x, const char* __restrict__ wqkv, const char* __restrict__ wo,
                                                      const char* __restrict__ w1, const char* __restrict__ b1, const char* __restrict__ w2,
                                                      const char* __restrict__ b2, char* __restrict__ out, char* ws) {
  __shared__ __attribute__((aligned(1024))) char smem[163840];
  asm volatile("" : "+s"(x), "+s"(wqkv), "+s"(wo), "+s"(w1), "+s"(b1), "+s"(w2), "+s"(b2), "+s"(out), "+s"(ws));
  u64 pc; asm volatile("s_getpc_b64 %0" : "=s"(pc));
  LAS char* const L = (LAS char*)smem;
  const u32 lb = (u32)(uintptr_t)smem;
  int l = threadIdx.x & 63, g, q4, p4, l15;
  auto lanes = [&] { PIN(l); g = l >> 4; q4 = (l >> 2) & 3; p4 = l & 3; l15 = l & 15; };
  lanes();
  int w = __builtin_amdgcn_readfirstlane(threadIdx.x >> 6);
  asm volatile("s_nop 4" : "+s"(w));
  const int x8 = blockIdx.x & 7, rk = blockIdx.x >> 3;
  const bool busy = rk < 28;
  if (w == 0) STAMP(0);
  const rsrc_t rQKV = mkrs(ws + WS_QKV, QKV_BYTES), rAC = mkrs(ws + WS_AC, AC_BYTES), rCTL = mkrs(ws, CTL_BYTES);
  const rsrc_t rX = mkrs(x, 2048 * 896), rW2 = mkrs(w2, 1792 * 896);
  const int kh = x8 >> 2, cc = rk % 7, rb = (x8 & 3) * 4 + rk / 7, cb = kh * 7 + cc;
  const int ai = rk & 15, ah = 2 * x8 + (rk >> 4);
  const int ro6 = ROTA ? (cc + ((3 * (rk / 7)) >> 1)) % 6 : 0;
  auto rotl = [&](int j) { const int t = j + ro6; return j == 0 ? 0 : (t > 6 ? t - 6 : t); };
  auto kstep = [&](int p) {
    if (p < 8) return 8 * cb + p;
    const int t = cc + 1 + ((p - 8) % 24) / 4;
    return 8 * (7 * kh + (t >= 7 ? t - 7 : t)) + (p & 3) + (p >= 32 ? 4 : 0);
  };
  u64 tag;
  auto epoch = [&] { asm volatile("s_load_dwordx2 %0, %1, 0x0\n\ts_waitcnt lgkmcnt(0)" : "=s"(tag) : "s"(ws) : "memory"); tag += 1; };
  auto vW2 = [&](u32* v) { v[0] = (l >> 3) * 896 + (((l & 7) ^ ((g & 1) << 2)) << 4); v[1] = v[0] + 7168 - 1024; };

  if (w < 4) {
    if (!busy) {
      const int q = (rk - 28) * 4 + w;
      dma4(lb + L_WC + w * 16384, (q * 64 + l) * 64, mkrs((const void*)(pc & ~255ull), CODE_KB * 1024), 0);
      bar();
      for (int k = 0; k < PF_DELAY; ++k) __builtin_amdgcn_s_sleep(2);
      const rsrc_t rW1 = mkrs(w1, 448 * 3584), rWo = mkrs(wo, 448 * 896);
      const u32 v = l * 16;
#pragma unroll 1
      for (int k = 0; k < 49; ++k) {
        int r = k * 16 + q + (x8 & 3) * 196; r = r >= 784 ? r - 784 : r;
        dman<1>(lb + L_WC + w * 16384 + (k & 15) * 1024, &v, rW2, kh * 802816 + r * 1024);
        vm<10>();
      }
#pragma unroll 1
      for (int k = 0; k < 25; ++k) { dman<1>(lb + L_WC + w * 16384 + (k & 15) * 1024, &v, rWo, (k * 16 + q) * 1024); vm<10>(); }
      vm<0>();
      FLUSH();
      return;
    }
    epoch();
    constexpr int PF = 4;
    u32 oX[4], oH[2], oQ[3];
#pragma unroll
    for (int p = 0; p < 4; ++p) oX[p] = (4 * w + ((l & 31) >> 3)) * 1024 + (l & 7) * 128 + (((2 * p + (g >> 1)) ^ (l15 >> 1)) << 4);
    const int rq = w == 3 ? 0 : w;
#pragma unroll
    for (int i = 0; i < 3; ++i) oQ[i] = RING + (8 * (g >> 1) + q4) * 192 + ((i + rq) % 3) * 64 + (g & 1) * 32 + p4 * 8;
#pragma unroll
    for (int i = 0; i < 2; ++i) oH[i] = RING + (g >> 1) * 1024 + q4 * 128 + ((i ^ (q4 >> 1)) << 6) + (g & 1) * 32 + p4 * 8;
    const u32 selv = l15 == (g & 1) ? 0x3f803f80u : 0u;
    const bf16x8 sel = __builtin_bit_cast(bf16x8, u32x4{selv, selv, selv, selv});
    bf16x8 xf[28], wf[8];
    f32x16 aH[2] = {}, aQ[3] = {};
    f32x4 cs = {};
    u32x2 ch[2];
    float rs;
    bf16x8 lnb;
    u32x4 hA[72], qp[6];
    auto rdW = [&](auto SS, auto II) {
      constexpr int s = decltype(SS)::v, i = decltype(II)::v;
      constexpr int slot = (s < 14 ? s * 10 + i : 140 + i) % 8;
      if constexpr (s < 14 && i % 5 < 3) { const u32 o = oQ[i % 5] + (stage_addr(s) - RING) + (i / 5) * 3072; wf[slot] = trfrag(L, o, o + 768); }
      else if constexpr (s < 14) { const u32 o = oH[i % 5 - 3] + (stage_addr(s) - RING) + 6144 + (i / 5) * 2048; wf[slot] = trfrag(L, o, o + 512); }
      else if constexpr (s < 21) { const u32 o = oH[i & 1] + (stage_addr(s) - RING) + (i >> 1) * 2048; wf[slot] = trfrag(L, o, o + 512); }
    };
    auto rdX = [&](auto SS, auto EE) { constexpr int k = 2 * decltype(SS)::v + decltype(EE)::v; if constexpr (k < 28) xf[k] = lds_ld<bf16x8>(L, oX[k & 3] + (k >> 2) * 16384); };
    auto rdcs = [&](int half) {
#pragma unroll
      for (int i = 0; i < 2; ++i) ch[i] = lds_ld<u32x2>(L, L_CSH + (half * 64 + i * 32 + (l & 31)) * 8);
    };
    auto hpack = [&](int half, int c) {
      const float rs2 = rs * rs;
      u32x4 hp;
#pragma unroll
      for (int j = 0; j < 4; ++j) {
        const float t0 = aH[c >> 1][8 * (c & 1) + 2 * j], t1 = aH[c >> 1][8 * (c & 1) + 2 * j + 1];
        hp[j] = pk(t0 * rs2 * fmx(t0, 0.f), t1 * rs2 * fmx(t1, 0.f));
      }
      hA[half * 4 + c] = hp;
    };
    auto hstore = [&](int m) { st16<16>(hA[m], rAC, l * 16 + ((rb * 4 + w) * 144 + cb * 8 + m) * 1024); };
    auto qpack = [&](int c) {
      const int i = c >> 1, e = c & 1, tq = 6 * cb + 2 * ((i + rq) % 3) + e;
      const float m = tq < 28 ? rs * (0.18898223650461361f * 1.4426950408889634f) : rs;
      const auto s0 = __builtin_amdgcn_permlane16_swap(pk(aQ[i][8 * e] * m, aQ[i][8 * e + 1] * m), pk(aQ[i][8 * e + 4] * m, aQ[i][8 * e + 5] * m), false, false);
      const auto s1 = __builtin_amdgcn_permlane16_swap(pk(aQ[i][8 * e + 2] * m, aQ[i][8 * e + 3] * m), pk(aQ[i][8 * e + 6] * m, aQ[i][8 * e + 7] * m), false, false);
      qp[c] = u32x4{s0[0], s1[0], s0[1], s1[1]};
    };
    auto qstore = [&](int c) {
      const int fl = ((g & 1) << 1) | (g >> 1), tq = 6 * cb + 2 * (((c >> 1) + rq) % 3) + (c & 1);
      u32x4 v = qp[c];
      u32 vo = fl * 256 + l15 * 16;
      if (tq >= 56) {
        asm volatile("" ::: "memory");
        if (fl & 1) v = u32x4{v[2], v[3], v[0], v[1]};
        vo ^= (2 * (tq & 1) + (fl >> 1)) << 6;
      }
      st16<16>(v, rQKV, vo + ((rb * 4 + w) * 336 + 4 * tq) * 256);
    };
    bool sigq = false, siga = false;
    const bool giver = (w >> 1) != kh;
    bar();
    STAMP(1);
    rdX(IC<0>{}, IC<0>{}); rdX(IC<0>{}, IC<1>{});
    sfor<0, PF>([&](auto I) { rdW(IC<0>{}, I); });
    sfor<0, 21>([&](auto SS) {
      constexpr int s = decltype(SS)::v, NT = s < 14 ? 10 : 8;
      if constexpr (s == 14) wait_bar<63>();
      else if constexpr (s < 14) bar();
      if constexpr (s == 13) lanes();
      if constexpr (s == 14) {
        STAMP(2);
        u32 cq[3];
#pragma unroll
        for (int i = 0; i < 3; ++i) cq[i] = lds_ld<u32>(L, L_CSQ + (((i + rq) % 3) * 32 + (l & 31)) * 4);
        const u32 rdy = cnt_ld(L, C_ST);
        u32x2 st = lds_ld<u32x2>(L, L_ST + (w * 32 + (l & 31)) * 8);
        rdcs(0);
        if (!__builtin_amdgcn_readfirstlane(rdy)) { cnt_wait(L, C_ST, 1); st = lds_ld<u32x2>(L, L_ST + (w * 32 + (l & 31)) * 8); rdcs(0); }
        SB();
        lnb = __builtin_bit_cast(bf16x8, u32x4{l < 32 ? (st[0] & 0xffffu) * 0x10001u : 0u, l < 32 ? st[0] >> 16 : 0u, 0u, 0u});
        rs = __uint_as_float(st[1]);
#pragma unroll
        for (int i = 0; i < 3; ++i) aQ[i] = mfma32(__builtin_bit_cast(bf16x8, u32x4{cq[i], 0u, 0u, 0u}), lnb, aQ[i]);
#pragma unroll
        for (int i = 0; i < 2; ++i) aH[i] = mfma32(__builtin_bit_cast(bf16x8, u32x4{ch[i][0], ch[i][1], 0u, 0u}), lnb, aH[i]);
        rdcs(1);
        SB();
#pragma unroll
        for (int c = 0; c < 6; ++c) { qpack(c); if (c % (6 / QNOW) == 6 / QNOW - 1) qstore(c / (6 / QNOW)); SB(); }
#pragma unroll
        for (int c = 0; c < 4; ++c) hpack(0, c);
        SB();
        STAMP(3);
      }
      u32 land;
      sfor<0, NT>([&](auto II) {
        constexpr int i = decltype(II)::v, slot = (s < 14 ? s * 10 + i : 140 + i) % 8;
        if constexpr ((s == 15 || s == 18) && i == 1) land = land_ld(L);
        if constexpr ((s == 15 || s == 18) && i == NT - PF) {
          if ((u32)__builtin_amdgcn_readfirstlane(land) < (s == 15 ? 1u : 2u)) prog_wait<false>(L, s == 15 ? 1 : 2);
          asm volatile("" ::: "memory");
        }
        if constexpr (i + PF < NT) rdW(SS, IC<i + PF>{});
        else rdW(IC<s + 1>{}, IC<i + PF - NT>{});
        if constexpr (i < 2) rdX(IC<s + 1>{}, II);
        if constexpr (s < 14) {
          if constexpr (i % 5 < 3) aQ[i % 5] = mfma32(wf[slot], xf[2 * s + i / 5], aQ[i % 5]);
          else aH[i % 5 - 3] = mfma32(wf[slot], xf[2 * s + i / 5], aH[i % 5 - 3]);
          if constexpr (i % 5 == 0) cs = mfma(wf[slot], sel, cs);
          if constexpr (s == 13 && i == 7) {
            u32x4 v;
#pragma unroll
            for (int j = 0; j < 4; ++j) { const float hi = rbf(cs[j]); v[j] = pk(hi, cs[j] - hi); }
            if (l15 < 2) lds_st<u32x4>(L, L_CSQ + w * 128 + (l15 * 16 + 4 * g) * 4, v);
          }
        } else {
          constexpr int k = 4 * (s - 14) + (i >> 1), m = QNOW + 2 * (s - 14) + (i >> 2);
          if constexpr (k == 0) aH[i] = mfma32(__builtin_bit_cast(bf16x8, u32x4{ch[i][0], ch[i][1], 0u, 0u}), lnb, f32x16{});
          aH[i & 1] = mfma32(wf[slot], xf[k], aH[i & 1]);
          if constexpr ((i & 3) == 1 && m < 6) qstore(m);
          if constexpr ((i & 3) == 1 && m >= 6 && m < 10) hstore(m - 6);
        }
        SB();
      });
      if constexpr (s == 18 || s == 20) prog_st(L, C_DONE + w, s);
      if constexpr (s >= 14) {
        constexpr int ns = QNOW + 2 * (s - 13), nh = ns < 6 ? 0 : (ns > 10 ? 4 : ns - 6);
        if constexpr (ns > 6 && s <= S_FQ) { if (!sigq && (s == S_FQ || vm_le<nh>())) { vm<nh>(); cnt_add(lb, C_FQ); sigq = true; } }
        if constexpr (ns > 10) { if (!siga && vm_le<0>()) { vm<0>(); cnt_add(lb, C_FA); siga = true; } }
      }
    });
    STAMP(4);
    lanes();
    const u32 vFG = WS_FG + (rb * 16 + 7 * kh + (l < 7 ? l : 0)) * FS;
#pragma unroll
    for (int c = 0; c < 4; ++c) hpack(1, c);
    SB();
    if (!siga) { vm<0>(); cnt_add(lb, C_FA); }
#pragma unroll
    for (int m = 4; m < 8; ++m) hstore(m);
    STAMP(5);
    u32x2 polled;
    u32 vW[2];
    vW2(vW);
    const u32 vA = l * 16, sA = (rb * 4 + w) * 144, sO = ((rb * 4 + (giver ? w ^ 2 : w)) * 144 + (giver ? 128 : 112)) * 1024;
    u32 sN[6];
#pragma unroll
    for (int j = 0; j < 6; ++j) { sN[j] = (sA + kstep(8 + 4 * j)) * 1024; asm volatile("" : "+s"(sN[j])); }
    const u32 vB = vA + 4096;
    auto ldA = [&](auto M) { constexpr int m = decltype(M)::v; hA[m] = ld16_async<true, (m & 3) * 1024>(m >= 32 ? vB : vA, rAC, sN[((m - 8) % 24) / 4]); };
    auto ldO = [&](auto M) { constexpr int m = decltype(M)::v; hA[m] = ld16_async<true, (m & 3) * 1024>(vA, rAC, sO + ((m - 56) & ~3) * 1024); };
    u32 oW[2], oO[2];
#pragma unroll
    for (int b = 0; b < 2; ++b) { oW[b] = (4 * (g >> 1) + q4) * 128 + ((b ^ (q4 >> 1)) << 6) + (g & 1) * 32 + p4 * 8; oO[b] = oW[b] + (giver ? 8 * 3584 : 0); }
    bf16x8 wc[2][2];
    auto rdC = [&](int m) {
#pragma unroll
      for (int b = 0; b < 2; ++b) {
        const u32 o = m < 56 ? oW[b] + L_WC + m * 2048 : oO[b] + ((m - 56) >> 1) * 3584 + (m & 1) * 2048 + (m >= 66 && giver ? L_WC - 364 * 128 : 0);
        wc[m & 1][b] = trfrag(L, o, o + 1024);
      }
    };
    f32x16 acc[2];
    u32x4 xres[2], bres, pp[4];
    auto step = [&](auto N) {
      constexpr int n = decltype(N)::v;
      if constexpr (n + 1 < 72) rdC(n + 1);
      SB();
      acc[0] = mfma32(wc[n & 1][0], __builtin_bit_cast(bf16x8, hA[n]), n == 0 ? f32x16{} : acc[0]);
      SB();
      if constexpr (n >= 8 && n < 56) sfor<SC.a0[n], SC.a1[n]>(ldA);
      if constexpr (n >= 8 && n <= 16 && !(n & 1)) {
        const int t = 5 * w + (n - 8) / 2;
        dman<2>(lb + L_WC + (32 + t) * 2048, vW, rW2, ((8 * (7 * kh + (cc + 1 + (t >> 2)) % 7) + 4 + (t & 3)) * 16) * 896 + cc * 128);
      }
      acc[1] = mfma32(wc[n & 1][1], __builtin_bit_cast(bf16x8, hA[n]), n == 0 ? f32x16{} : acc[1]);
      SB();
    };
    cnt_wait(L, C_WLO, 2);
    rdC(0);
    sfor<0, OWN1>(step);
    cnt_wait(L, C_FH, 1);
    cnt_wait(L, C_KV, 2);
    STAMP(6);
    __builtin_amdgcn_s_setprio(PRIO_C);
    sfor<8, 8 + DA>(ldA);
    prog_wait<true>(L, 20);
    sfor<OWN1, 8>(step);
    sfor<8, 56>([&](auto N) {
      constexpr int n = decltype(N)::v;
      asm volatile("s_waitcnt vmcnt(%1)" : "+v"(hA[n]) : "n"(SC.wC[n]) : "memory");
      if constexpr (n == 33) cnt_add(lb, C_P33);
      if constexpr (n == 8) { cnt_add(lb, C_P6); if (last_of4(L, C_FG) && l == 0) st_flag(rCTL, WS_FG + (rb * 16 + cb) * FS, tag); }
      if constexpr (n == T_WH) cnt_add(lb, C_WHI);
      if constexpr (n == TB - 3) polled = ld8c_async(vFG, rCTL);
      if constexpr (n == TB) { PIN(polled); if (!tagged(polled, tag)) spin<SLP>(rCTL, vFG, tag); }
      if constexpr (n == 31) cnt_wait(L, C_WHI, 4);
      if constexpr (n == 51) cnt_wait(L, C_LATE, 4);
      step(N);
      if constexpr (n >= T_O && n < T_O + 8) { ldO(IC<56 + 2 * (n - T_O)>{}); ldO(IC<57 + 2 * (n - T_O)>{}); }
    });
    asm volatile("s_waitcnt vmcnt(0)" : "+v"(hA[56]), "+v"(hA[57]), "+v"(hA[58]), "+v"(hA[59]), "+v"(hA[60]), "+v"(hA[61]), "+v"(hA[62]), "+v"(hA[63]),
                 "+v"(hA[64]), "+v"(hA[65]), "+v"(hA[66]), "+v"(hA[67]), "+v"(hA[68]), "+v"(hA[69]), "+v"(hA[70]), "+v"(hA[71])::"memory");
    if (giver) {
#pragma unroll
      for (int k = 0; k < 4; ++k) {
        const f32x16& a = acc[k >> 1];
        const int o = 8 * (k & 1);
        st16<16>(u32x4{pk(a[o], a[o + 1]), pk(a[o + 2], a[o + 3]), pk(a[o + 4], a[o + 5]), pk(a[o + 6], a[o + 7])}, rAC, PX + ((rb * 7 + cc) * 4 + w) * 4096 + k * 1024 + l * 16);
      }
      acc[0] = f32x16{}; acc[1] = f32x16{};
    }
    auto fresh = [&](const u32x4* v, int n) {
      u32 m = 0;
#pragma unroll
      for (int k = 0; k < n; ++k) m = max(max(m, max(v[k][0], v[k][1])), max(v[k][2], v[k][3]));
      return __all(m != ~0u);
    };
#pragma unroll 1
    for (u32 n = 0; n < (1u << 17) && !fresh(&hA[56], 16); ++n) {
#pragma unroll 1
      for (; n < (1u << 17); ++n) {
        asm volatile("" ::: "memory");
        if (__all(__builtin_amdgcn_raw_buffer_load_b32(rAC, sO + (l & 7) * 2048 + 1024, 0, 16) != ~0u)) break;
        __builtin_amdgcn_s_sleep(SLP);
      }
#pragma unroll
      for (int k = 0; k < 16; ++k) hA[56 + k] = __builtin_amdgcn_raw_buffer_load_b128(rAC, vA + sO + k * 1024, 0, 16);
    }
    const int tile = giver ? w ^ 2 : w, r0 = (giver ? 16 : 0) + (l >> 3);
#pragma unroll
    for (int k = 0; k < 2; ++k) xres[k] = ld16_async<false>((r0 + 8 * k) * 896 + (l & 7) * 16, rX, (rb * 128 + tile * 32) * 896 + cc * 128);
    bres = ld16_async<false>((l & 7) * 16, mkrs(b2, 896), cc * 128);
    sfor<56, 66>(step);
    sfor<0, 4>([&](auto K) { constexpr int k = decltype(K)::v; pp[k] = ld16_async<true, k * 1024>(vA, rAC, giver ? 0xfffff000u : PX + ((rb * 7 + cc) * 4 + w) * 4096); });
    sfor<66, 72>(step);
    asm volatile("s_waitcnt vmcnt(0)" : "+v"(xres[0]), "+v"(xres[1]), "+v"(bres), "+v"(pp[0]), "+v"(pp[1]), "+v"(pp[2]), "+v"(pp[3])::"memory");
    STAMP(10);
#pragma unroll 1
    for (u32 n = 0; n < (1u << 17) && !fresh(pp, 4); ++n) {
      asm volatile("" ::: "memory");
#pragma unroll
      for (int k = 0; k < 4; ++k) pp[k] = __builtin_amdgcn_raw_buffer_load_b128(rAC, PX + ((rb * 7 + cc) * 4 + w) * 4096 + k * 1024 + vA, 0, 16);
    }
#pragma unroll
    for (int k = 0; k < 4; ++k)
#pragma unroll
      for (int j = 0; j < 4; ++j) { acc[k >> 1][8 * (k & 1) + 2 * j] += bflo(pp[k][j]); acc[k >> 1][8 * (k & 1) + 2 * j + 1] += bfhi(pp[k][j]); }
    cnt_wait(L, C_P33, 4);
#pragma unroll
    for (int b = 0; b < 2; ++b)
#pragma unroll
      for (int k = 0; k < 4; ++k)
        lds_st<f32x4>(L, L_RED + w * 8192 + (l & 31) * 256 + (((b * 8 + k * 2 + (g >> 1)) ^ (l & 7)) << 4), f32x4{acc[b][4 * k], acc[b][4 * k + 1], acc[b][4 * k + 2], acc[b][4 * k + 3]});
    cnt_add(lb, C_PAIR0 + (w & 1));
    cnt_wait(L, C_PAIR0 + (w & 1), 2);
    const rsrc_t rO = mkrs(out, 2048 * 896);
#pragma unroll
    for (int k = 0; k < 2; ++k) {
      const int row = r0 + 8 * k;
      f32x4 a = {bflo(xres[k][0]) + bflo(bres[0]), bfhi(xres[k][0]) + bfhi(bres[0]), bflo(xres[k][1]) + bflo(bres[1]), bfhi(xres[k][1]) + bfhi(bres[1])};
      f32x4 b = {bflo(xres[k][2]) + bflo(bres[2]), bfhi(xres[k][2]) + bfhi(bres[2]), bflo(xres[k][3]) + bflo(bres[3]), bfhi(xres[k][3]) + bfhi(bres[3])};
#pragma unroll
      for (int j = 0; j < 2; ++j) {
        a += lds_ld<f32x4>(L, L_RED + ((w & 1) + 2 * j) * 8192 + row * 256 + (((2 * (l & 7)) ^ (row & 7)) << 4));
        b += lds_ld<f32x4>(L, L_RED + ((w & 1) + 2 * j) * 8192 + row * 256 + (((2 * (l & 7) + 1) ^ (row & 7)) << 4));
      }
      st16<16>(u32x4{pk(a[0], a[1]), pk(a[2], a[3]), pk(b[0], b[1]), pk(b[2], b[3])}, rO, (rb * 128 + tile * 32 + row) * 896 + cc * 128 + (l & 7) * 16);
    }
    if (w == 0) STAMP(11);
    FLUSH();
    return;
  }

  const int li = w & 1;
  auto clear_o = [&] {
#pragma unroll
    for (int k = 0; k < 8; ++k) st16<16>(u32x4{~0u, ~0u, ~0u, ~0u}, rAC, ((ai * 4 + (k >> 1)) * 144 + 112 + 2 * ah + (k & 1)) * 1024 + l * 16);
  };
  if (!busy) {
    const int blk = (rk - 28) * 8 + x8;
    u32x4 d[14];
#pragma unroll
    for (int k = 0; k < 14; ++k) d[k] = __builtin_amdgcn_raw_buffer_load_b128(mkrs(w1, 448 * 3584), (112 * (w - 4) + (l >> 3)) * 3584 + (l & 7) * 16 + (blk < 28 ? blk * 128 : 0x7fffffff), k * 8 * 3584, 0);
    const u32 bias = __builtin_amdgcn_raw_buffer_load_b16(mkrs(b1, 3584), (blk * 64 + l) * 2, 0, 0);
    SB();
    epoch();
    if (w == 6 && l < C_N) lds_st<u32>(L, L_CNT + l * 4, l < 4 ? 64u : 0u);
    wait_bar<63>();
    if (w == 4) { vm<0>(); STAMP(38); }
    float a[8] = {};
#pragma unroll
    for (int k = 0; k < 14; ++k)
#pragma unroll
      for (int j = 0; j < 4; ++j) { a[2 * j] += bflo(d[k][j]); a[2 * j + 1] += bfhi(d[k][j]); }
    lds_st<f32x4>(L, RING + ((w - 4) * 64 + l) * 32, f32x4{a[0], a[1], a[2], a[3]});
    lds_st<f32x4>(L, RING + ((w - 4) * 64 + l) * 32 + 16, f32x4{a[4], a[5], a[6], a[7]});
    if (last_of4(L, C_CS0) && blk < 28) {
      float t = 0.f;
#pragma unroll
      for (int k = 0; k < 32; ++k) t += lds_ld<float>(L, RING + k * 256 + l * 4);
      const float hi = rbf(t);
      __builtin_amdgcn_raw_buffer_store_b64(u32x2{pk(hi, t - hi), bias}, rCTL, WS_CS + (blk * 64 + l) * 8, 0, 16);
      vm<0>();
      if (l == 0) st_flag(rCTL, WS_FC + blk * FS, tag);
      STAMP(39);
    }
    if (w == 7) { clear_o(); vm<0>(); if (l == 0) st_flag(rCTL, WS_FZ + (ai * 16 + ah) * FS, tag); }
  } else if (w < 6) {
    const rsrc_t rW1 = mkrs(w1, 448 * 3584), rWq = mkrs(wqkv, 448 * 2688);
    u32 vX[4], vH[4], vQ[3], vC[2], so7[7];
#pragma unroll
    for (int j = 0; j < 4; ++j) vX[j] = (8 * j + (l >> 3)) * 896 + (((l & 7) ^ ((j & 1) * 4 + g)) << 4) - 1024 * j;
    so7[0] = 0;
    const u32 sX = (rb * 128 + 64 * li) * 896, sH = cb * 256 + 16 * li * 3584, sQ = cb * 192 + 16 * li * 2688;
    auto issue = [&](auto O) {
      constexpr int op = decltype(O)::v, k = op / 256, a = (op % 256) / 2, e = op & 1;
      if constexpr (k == K_X) dman<4>(lb + (a * 16 + 8 * li + 4 * e) * 1024, vX, rX, sX + so7[a] * 128 + e * 28672);
      if constexpr (k == K_WA && a < 14) {
        dman<3>(lb + stage_addr(a) + li * 3072, vQ, rWq, sQ + so7[a >> 1] * (64 * 2688) + (a & 1) * (32 * 2688));
        dman<2>(lb + stage_addr(a) + 6144 + li * 2048, vH, rW1, sH + so7[a >> 1] * (64 * 3584) + (a & 1) * (32 * 3584));
      }
      if constexpr (k == K_WA && a >= 14) dman<4>(lb + stage_addr(a) + li * 4096, vH, rW1, sH + 16 * li * 3584 + so7[a - 14] * (64 * 3584) + 128);
      if constexpr (k == K_WCP) dman<2>(lb + L_WC + (2 * a + li) * 2048, vC, rW2, (kstep(2 * a) + li) * (16 * 896) + cc * 128);
    };
    sfor<0, 2>([&](auto K) { issue(IC<SC.pro[decltype(K)::v]>{}); });
    lanes();
#pragma unroll
    for (int j = 0; j < 3; ++j) { const u32 n = 64 * j + l; vQ[j] = (n / 12) * 2688 + (n % 12) * 16 - 1024 * j; }
#pragma unroll
    for (int j = 0; j < 4; ++j) vH[j] = (8 * j + (l >> 3)) * 3584 + (((l & 7) ^ ((g & 1) << 2)) << 4) - 1024 * j;
    sfor<2, 4>([&](auto K) { issue(IC<SC.pro[decltype(K)::v]>{}); });
#pragma unroll
    for (int j = 1; j < 7; ++j) so7[j] = rotl(j);
    sfor<4, NPRO>([&](auto K) { issue(IC<SC.pro[decltype(K)::v]>{}); });
    epoch();
    vW2(vC);
    vm_bar<SC.wA[0]>();
    sfor<0, 15>([&](auto SS) {
      constexpr int s = decltype(SS)::v;
      vm_bar<SC.wA[s + 1]>();
      sfor<0, (s == 14 ? 2 : MAXO)>([&](auto K) { issue(IC<SC.opA[s][decltype(K)::v]>{}); });
    });
    vm<SC.wL[0]>();
    prog_st(L, C_LAND + li, 1);
    sfor<2, 8>([&](auto K) { issue(IC<SC.opA[14][decltype(K)::v]>{}); });
    vm<SC.wL[1]>();
    prog_st(L, C_LAND + li, 2);
    sfor<8, MAXO>([&](auto K) { issue(IC<SC.opA[14][decltype(K)::v]>{}); });
    vm<0>();
    cnt_add(lb, C_WLO);
    if (w == 4) { STAMP(13); cnt_wait(L, C_FA, 5); STAMP(14); if (l == 0) st_flag(rCTL, WS_FH + (rb * 16 + cb) * FS, tag); }
    else { cnt_wait(L, C_FA, 5); spin<SLP>(rCTL, l >= 16 && l < 32 ? WS_FZ + (rb * 16 + l - 16) * FS : WS_FH + (rb * 16 + (l < 7 ? 7 * kh + l : 7 * (1 - kh) + cc)) * FS, tag); STAMP(15); cnt_add(lb, C_FH); }
  } else {
    if (w == 6 && cb < 8) {
      u32x4 d[14];
#pragma unroll
      for (int k = 0; k < 14; ++k) d[k] = __builtin_amdgcn_raw_buffer_load_b128(rX, (rb * 128 + cb * 16 + (l >> 3)) * 896 + (l & 7) * 16, (k / 7) * 7168 + (k % 7) * 128, 0);
      SB();
      epoch();
      bar(); bar();
#pragma unroll
      for (int r = 0; r < 2; ++r) {
        bar();
        float s1 = 0.f, s2 = 0.f;
#pragma unroll
        for (int k = 0; k < 7; ++k)
#pragma unroll
          for (int j = 0; j < 4; ++j) { s1 = dot2(d[7 * r + k][j], 0x3f803f80u, s1); s2 = dot2(d[7 * r + k][j], d[7 * r + k][j], s2); }
        const float mean = sum8(s1) * (1.f / 448.f);
        const float var = fmaxf(sum8(s2) * (1.f / 448.f) - mean * mean, 0.f);
        const float sd = rbf(__builtin_amdgcn_sqrtf(rbf(rbf(var) + 1e-5f)));
        if ((l & 7) == 0) __builtin_amdgcn_raw_buffer_store_b64(u32x2{pk(-mean, sd), __float_as_uint(__builtin_amdgcn_rcpf(sd))}, rCTL, WS_ST + (rb * 128 + cb * 16 + r * 8 + (l >> 3)) * 8, 0, 16);
        SB();
      }
    } else { epoch(); bar(); bar(); bar(); bar(); }
    if (w == 6) {
      const u32 fo = l < 8 ? WS_FS + (rb * 8 + l) * FS : WS_FC + (2 * cb + (l & 1)) * FS;
      const u32 fp = (l < 16 ? WS_FS + (rb * 8 + (l >> 1)) * FS : WS_FC + (2 * cb + ((l >> 1) & 1)) * FS) + (l & 1) * 4;
      auto fetch = [&] {
        asm volatile("s_mov_b32 m0, %0\n\ts_nop 0\n\tbuffer_load_dwordx4 %1, %3, 0 offen sc1 lds\n\tbuffer_load_dwordx4 %2, %3, 0 offen offset:1024 sc1 lds"
                     ::"s"(lb + L_CSH), "v"(l * 16 + WS_CS + cb * 1024), "v"(l * 16 + WS_ST + rb * 1024 - 1024), "s"(rCTL) : "memory");
      };
      int state = -1;
#pragma unroll 1
      for (int s = 3; s <= 14; ++s) {
        wait_bar<63>();
        if (s == 4 && cb < 8) { vm<0>(); if (l == 0) st_flag(rCTL, WS_FS + (rb * 8 + cb) * FS, tag); }
        if (s == 5 && l < C_N) lds_st<u32>(L, L_CNT + l * 4, 0u);
        if (s >= 7 && (s & 1) && state < 2) {
          vm<0>();
          if (state == 1) { cnt_add(lb, C_ST); state = 2; STAMP(33); }
          else if (state == 0 && __all(lds_ld<u32>(L, L_POLL + l * 4) == (u32)(l & 1 ? tag >> 32 : tag))) { fetch(); state = 1; }
          else { asm volatile("s_mov_b32 m0, %0\n\ts_nop 0\n\tbuffer_load_dword %1, %2, 0 offen sc1 lds" ::"s"(lb + L_POLL), "v"(fp), "s"(rCTL) : "memory"); state = 0; }
        }
      }
      vm<0>();
      if (state <= 0) { spin<SLP>(rCTL, fo, tag); fetch(); vm<0>(); }
      if (state < 2) { cnt_add(lb, C_ST); STAMP(33); }
    } else {
#pragma unroll 1
      for (int s = 3; s <= 14; ++s) {
        bar();
        const int k = s - CLR0;
        if (k >= 0 && k < 8) {
          st16<16>(u32x4{~0u, ~0u, ~0u, ~0u}, rAC, ((ai * 4 + (k >> 1)) * 144 + 112 + 2 * ah + (k & 1)) * 1024 + l * 16);
          st16<16>(u32x4{~0u, ~0u, ~0u, ~0u}, rAC, PX + ((rb * 7 + cc) * 4 + 2 * kh) * 4096 + k * 1024 + l * 16);
        }
      }
      vm<0>();
      cnt_add(lb, C_FA);
      if (l == 0) st_flag(rCTL, WS_FZ + (ai * 16 + ah) * FS, tag);
    }
  }
  lanes();
  const int aw = w - 4, hh = g >> 1;
  if (w >= 6) {
    const int e = l & 1, k = l < 14 ? l >> 1 : 0;
    const int ty = k == 0 ? 0 : (k < 4 ? 1 : 2), ro = k == 0 ? 0 : (k < 4 ? k - 1 : k - 4);
    const int rr = ai - ro < 0 ? 0 : ai - ro;
    if (!busy) { for (int k = 0; k < 12; ++k) __builtin_amdgcn_s_sleep(15); }
    if (busy) { cnt_wait(L, C_FQ, 4); if (w == 6) STAMP(12); if (w == 6 && l == 0) st_flag(rCTL, WS_FQ + (rb * 16 + cb) * FS, tag); }
    prog_wait<true>(L, 18);
#pragma unroll
    for (int k = 0; k < 3; ++k) lds_st<u32x2>(L, L_V + (6 * li + 2 * k + (l >> 5)) * 2048 + 1792 + (l & 31) * 8, u32x2{0x3f80u, 0u});
    if (w == 6) { spin<SLP>(rCTL, WS_FQ + (rr * 16 + (ty * 448 + 28 * ah + 27 * e) / 96) * FS, tag); cnt_add(lb, C_SQ); }
    else cnt_wait(L, C_SQ, 1);
    if (w == 6) STAMP(7);
#pragma unroll
    for (int e = 0; e < 2; ++e) {
#pragma unroll
      for (int j = 0; j < 6; ++j) {
        const int b = 2 * j + li, tb = 4 * ai - 8 + b < 0 ? 0 : 4 * ai - 8 + b;
        dma1792c(lb + (e ? L_V + b * 2048 : L_K + b * 1792), l * 16, rQKV, (tb * 336 + 112 + 112 * e + 7 * ah) * 256);
      }
      if (e == 0) {
#pragma unroll
        for (int j = 0; j < 2; ++j) dma1792c(lb + L_Q + (2 * li + j) * 2048, l * 16, rQKV, ((4 * ai + 2 * li + j) * 336 + 7 * ah) * 256);
      }
    }
    vm<12>();
    cnt_add(lb, C_KV);
  }
  cnt_wait(L, C_KV, 2);
  __builtin_amdgcn_s_setprio(PRIO_B);
  if (w == 4) STAMP(8);
  u32x2 qv[4];
#pragma unroll
  for (int k = 0; k < 4; ++k) qv[k] = lds_ld<u32x2>(L, L_Q + aw * 2048 + (k == 3 ? 6 : 2 * k + hh) * 256 + l15 * 16 + (g & 1) * 8);
  if (hh) qv[3] = u32x2{0u, 0u};
  const bf16x8 qf[2] = {__builtin_bit_cast(bf16x8, u32x4{qv[0][0], qv[0][1], qv[1][0], qv[1][1]}), __builtin_bit_cast(bf16x8, u32x4{qv[2][0], qv[2][1], qv[3][0], qv[3][1]})};
  const u32 oK = L_K + aw * 1792 + hh * 256 + l15 * 16 + (g & 1) * 8, oK3 = hh ? oK + 1280 : oK + 1536;
  const int F = 7 * ah + 4 * (g & 1) + p4;
  u32 oV[2][2];
#pragma unroll
  for (int t = 0; t < 2; ++t)
#pragma unroll
    for (int e = 0; e < 2; ++e) oV[t][e] = L_V + aw * 2048 + (4 * (g & 1) + p4) * 256 + (((8 * e + 4 * hh + q4) ^ ((F & 6) << 1)) << 4) + ((t ^ (F & 1)) << 3);
  f32x16 sc[9];
  bf16x8 kf[9][2], vf[9][2];
  auto rdK = [&](int m) {
    const u32x2 a = lds_ld<u32x2>(L, oK + m * 1792), b = lds_ld<u32x2>(L, oK + m * 1792 + 512), c = lds_ld<u32x2>(L, oK + m * 1792 + 1024), d = lds_ld<u32x2>(L, oK3 + m * 1792);
    kf[m][0] = __builtin_bit_cast(bf16x8, u32x4{a[0], a[1], b[0], b[1]}); kf[m][1] = __builtin_bit_cast(bf16x8, u32x4{c[0], c[1], d[0], d[1]});
  };
  auto rdV = [&](int m) { vf[m][0] = trfrag(L, oV[0][0] + m * 2048, oV[0][1] + m * 2048); vf[m][1] = trfrag(L, oV[1][0] + m * 2048, oV[1][1] + m * 2048); };
  rdK(0); SB(); rdK(8); SB(); rdK(1); SB();
#pragma unroll
  for (int i = 0; i < 9; ++i) {
    const int m = i == 0 ? 0 : (i == 1 ? 8 : i - 1);
    if (i + 2 < 8) rdK(i + 2);
    sc[m] = mfma32(kf[m][1], qf[1], mfma32(kf[m][0], qf[0], f32x16{}));
    SB();
  }
  if (w >= 6) { vm<0>(); cnt_add(lb, C_V); }
  constexpr float NEG = -1e30f;
  if (ai < 2) {
    asm volatile("" ::: "memory");
    const int m0 = 8 - 4 * ai - aw;
#pragma unroll
    for (int m = 0; m < 8; ++m)
      if (m < m0) {
#pragma unroll
        for (int r = 0; r < 16; ++r) sc[m][r] = NEG;
      }
  }
  const int dq = (l & 31) - 4 * hh;
  u32 mk[8];
#pragma unroll
  for (int r = 0; r < 16; r += 2) {
    const bool c0 = (r & 3) + 8 * (r >> 2) <= dq, c1 = (r & 3) + 1 + 8 * (r >> 2) <= dq;
    sc[0][r] = c0 ? sc[8][r] : sc[0][r]; sc[0][r + 1] = c1 ? sc[8][r + 1] : sc[0][r + 1];
    mk[r >> 1] = c1 ? 0xffffffffu : (c0 ? 0xffffu : 0u);
  }
  cnt_wait(L, C_V, 2);
  rdV(1); rdV(2);
  SB();
  float mx0 = fmx(sc[0][0], sc[0][1]), mx1 = fmx(sc[0][2], sc[0][3]);
#pragma unroll
  for (int k = 1; k < 32; ++k) { mx0 = fmx(fmx(mx0, sc[k >> 2][4 * (k & 3)]), sc[k >> 2][4 * (k & 3) + 1]); mx1 = fmx(fmx(mx1, sc[k >> 2][4 * (k & 3) + 2]), sc[k >> 2][4 * (k & 3) + 3]); }
  mx0 = fmx(mx0, mx1);
  const auto ms = __builtin_amdgcn_permlane32_swap(__float_as_uint(mx0), __float_as_uint(mx0), false, false);
  const float mx = fmx(__uint_as_float(ms[0]), __uint_as_float(ms[1]));
  SB();
  f32x16 o = {};
#pragma unroll
  for (int i = 0; i < 8; ++i) {
    const int m = (i + 1) & 7;
    if (i < 5) rdV(i + 3); else if (i == 5) rdV(0); else if (i == 6) rdV(8);
    u32x4 p[2];
#pragma unroll
    for (int j = 0; j < 8; ++j) {
      f32x2 d = f32x2{sc[m][2 * j], sc[m][2 * j + 1]} - f32x2{mx, mx};
      PIN(d);
      p[j >> 2][j & 3] = pk(__builtin_amdgcn_exp2f(d[0]), __builtin_amdgcn_exp2f(d[1]));
    }
#pragma unroll
    for (int s = 0; s < 2; ++s) {
      if (m) o = mfma32(vf[m][s], __builtin_bit_cast(bf16x8, p[s]), o);
      else {
        const u32x4 k4 = {mk[4 * s], mk[4 * s + 1], mk[4 * s + 2], mk[4 * s + 3]};
        o = mfma32(vf[0][s], __builtin_bit_cast(bf16x8, p[s] & ~k4), o);
        o = mfma32(vf[8][s], __builtin_bit_cast(bf16x8, p[s] & k4), o);
      }
    }
    SB();
  }
  cnt_add(lb, C_ATT);
  const auto dn = __builtin_amdgcn_permlane32_swap(__float_as_uint(o[12]), __float_as_uint(o[12]), false, false);
  const float inv = __builtin_amdgcn_rcpf(__uint_as_float(dn[1]));
  const float in2 = hh ? 0.f : inv;
  const u32 so = ((ai * 4 + aw) * 144 + 112 + 2 * ah) * 1024;
  st16<16>(u32x4{pk(o[0] * inv, o[1] * inv), pk(o[2] * inv, o[3] * inv), pk(o[4] * inv, o[5] * inv), pk(o[6] * inv, o[7] * inv)}, rAC, l * 16 + so);
  st16<16>(u32x4{pk(o[8] * inv, o[9] * inv), pk(o[10] * inv, o[11] * inv), pk(o[12] * in2, o[13] * in2), pk(o[14] * in2, o[15] * in2)}, rAC, l * 16 + so + 1024);
  if (w == 4) STAMP(9);
  if (busy) {
    u32 vW[2];
    vW2(vW);
    dman<2>(lb + L_Q + aw * 2048, vW, rW2, (kstep(52) + aw) * (16 * 896) + cc * 128);
    vm<2>();
    if (last_of4(L, C_FB) && l == 0) st_flag(rCTL, WS_FB + (ai * 16 + ah) * FS, tag);
    cnt_wait(L, C_ATT, 4);
    cnt_wait(L, C_P6, 4);
    const rsrc_t rWo = mkrs(wo, 448 * 896);
#pragma unroll
    for (int k = 0; k < 6; ++k) if (k < 5 || aw < 3) dman<2>(lb + (4 * k + aw) * 2048, vW, rWo, (4 * k + aw) * (16 * 896) + cc * 128);
#pragma unroll
    for (int k = 0; k < 2; ++k) if (k == 0 || aw < 2) dman<2>(lb + L_WC + (4 * k + aw) * 2048, vW, rWo, (364 + 16 * (4 * k + aw)) * 896 + cc * 128);
    vm<0>();
    cnt_add(lb, C_LATE);
  } else {
    vm<0>();
    if (last_of4(L, C_FB)) {
      if (l == 0) st_flag(rCTL, WS_FB + (ai * 16 + ah) * FS, tag);
      if (blockIdx.x == 255) {
#pragma unroll 1
        for (int k = 0; k < 4; ++k) spin<32>(rCTL, WS_FB + (k * 64 + l) * FS, tag);
        if (l == 0) st_flag(rCTL, 0, tag);
      }
    }
  }
  FLUSH();
}

extern "C" void kernel_launch(void* const* d_in, const int* in_sizes, int n_in, void* d_out, int out_size, void* d_ws, size_t ws_size, hipStream_t stream) {
  fused_block<<<256, 512, 0, stream>>>((const char*)d_in[0], (const char*)d_in[1], (const char*)d_in[2], (const char*)d_in[4], (const char*)d_in[5],
                                       (const char*)d_in[6], (const char*)d_in[7], (char*)d_out, (char*)d_ws + WS_OFF);
}
```

```cpp
#define PROBE 1
#include <hip/hip_runtime.h>
#include <hip/hip_bf16.h>
#include <stdint.h>

#define DI __device__ __forceinline__
#define LAS __attribute__((address_space(3)))
typedef unsigned u32;
typedef unsigned long long u64;
typedef __bf16 bf16x8 __attribute__((ext_vector_type(8)));
typedef __bf16 bf16x2 __attribute__((ext_vector_type(2)));
typedef short s16x4 __attribute__((ext_vector_type(4)));
typedef float f32x4 __attribute__((ext_vector_type(4)));
typedef float f32x2 __attribute__((ext_vector_type(2)));
typedef float f32x16 __attribute__((ext_vector_type(16)));
typedef unsigned u32x4 __attribute__((ext_vector_type(4)));
typedef unsigned u32x2 __attribute__((ext_vector_type(2)));
typedef __amdgpu_buffer_rsrc_t rsrc_t;

template <int N> struct IC { static constexpr int v = N; };
template <int B, int E, class F> DI void sfor(F&& f) { if constexpr (B < E) { f(IC<B>{}); sfor<B + 1, E>(f); } }

constexpr u32 FS = 128;
constexpr u32 WS_FH = FS, WS_FQ = WS_FH + 256 * FS, WS_FB = WS_FQ + 256 * FS, WS_FG = WS_FB + 256 * FS, WS_FP = WS_FG + 256 * FS, WS_FC = WS_FP + 448 * FS;
constexpr u32 WS_FS = WS_FC + 32 * FS, WS_FZ = WS_FS + 128 * FS, WS_CS = WS_FZ + 287 * FS, WS_ST = WS_CS + 16384, CTL_BYTES = WS_ST + 16384;
constexpr u32 WS_QKV = CTL_BYTES, QKV_BYTES = 64u * 336u * 256u;
constexpr u32 WS_AC = WS_QKV + QKV_BYTES, AC_BYTES = 64u * 144u * 1024u + 448u * 4096u;
constexpr u32 PX = 64u * 144u * 1024u;
constexpr u32 RING = 7 * 16384;
constexpr u32 L_POLL = 45056, L_CSQ = 46080, L_CSH = 46848, L_ST = 47872, L_CNT = 48896;
constexpr u32 L_K = 0, L_V = 21504, L_Q = RING + 5 * 8192;
constexpr u32 L_WC = 49152, L_RED = L_WC + 16384;
enum { C_DONE, C_LAND = 4, C_FQ = 6, C_FA, C_FG, C_KV, C_FB, C_WLO, C_WHI, C_P6, C_P33, C_ATT, C_LATE, C_PAIR0, C_PAIR1, C_CSH, C_FH, C_SQ, C_V, C_ST, C_CS0, C_N };
constexpr u32 stage_addr(int s) { return s < 14 ? RING + (s % 4) * 10240u : (s < 19 ? (s - 14) * 8192u : RING + (s - 19) * 8192u); }

enum { K_NONE = 0, K_X, K_WA, K_BIAS, K_WCP };
constexpr int OP(int k, int a, int e = 0) { return k * 256 + 2 * a + e; }
constexpr int opn(int op) { int k = op / 256, a = (op % 256) / 2; return k == K_NONE ? 0 : k == K_X ? 4 : k == K_WA ? (a < 14 ? 5 : 4) : k == K_WCP ? 2 : 1; }
#define CAP_1 9
#define P_NPRO 8
#define P_DA 12
#define P_TB 20
#define P_TO 46
#define P_FQ 19
#define PRIO_C 0
#define PRIO_B 2
#define ROTA 1
#define WS_OFF 0
#define CODE_KB 39
#define PF_DELAY 20
#define P_TFP 66
#define SLP 2
#define LSLP 1
#define GATE_K 1
#define OWN1 4
#define CLR0 3
#define QNOW 6
constexpr int MAXO = 24, NPRO = P_NPRO, DA = P_DA, TB = P_TB, S_FQ = P_FQ, T_WH = 26, T_O = P_TO;
struct Sched {
  int pro[NPRO], opA[15][MAXO];
  int wA[16];
  int wL[2];
  int wC[56], a0[56], a1[56];
  int ok;
};
constexpr int cl(int n) { return n > 63 ? 63 : n; }
constexpr Sched make_sched() {
  Sched r{};
  r.ok = 1;
  int n = 0, iW[21] = {}, iX[7] = {};
  int lo[128] = {}, le[128] = {}, nl = 0;
  for (int s = 0; s < 21; ++s) {
    if (s < 14 && !(s & 1)) { lo[nl] = OP(K_X, s >> 1, 0); le[nl++] = -1; lo[nl] = OP(K_X, s >> 1, 1); le[nl++] = -1; }
    lo[nl] = OP(K_WA, s); le[nl++] = s < 14 ? s - 3 : (s < 19 ? 5 : 14);
  }
  for (int k = 0; k < 16; ++k) { lo[nl] = OP(K_WCP, k); le[nl++] = 7 + 2 * (k >> 2); }
  bool done[128] = {};
  auto note = [&](int op) { n += opn(op); int k = op / 256, a = (op % 256) / 2; if (k == K_X) iX[a] = (op & 1) ? n : 0; if (k == K_WA) iW[a] = n; };
  for (int k = 0; k < NPRO; ++k) { r.pro[k] = lo[k]; done[k] = true; note(lo[k]); }
  r.wA[0] = cl(n - iW[0]);
  for (int s = 0; s < 15; ++s) {
    int need = iW[s + 1]; if (!need) r.ok = 0;
    if (s + 1 <= 13) { int v = iX[(s + 1) >> 1]; if (!v) r.ok = 0; if (v > need) need = v; }
    r.wA[s + 1] = cl(n - need);
    int cap = s == 14 ? 64 : CAP_1, c = 0, used = 0;
    for (int k = 0; k < nl; ++k) if (!done[k] && le[k] <= s && used + opn(lo[k]) <= cap && c < MAXO) { done[k] = true; r.opA[s][c++] = lo[k]; used += opn(lo[k]); note(lo[k]); if (s == 14 && c == 2) r.wL[0] = cl(n - iW[18]); if (s == 14 && c == 8) r.wL[1] = cl(n - iW[20]); }
  }
  if (r.opA[14][1] != OP(K_WA, 20)) r.ok = 0;
  for (int k = 0; k < nl; ++k) if (!done[k]) r.ok = 0;
  n = 0;
  int iA[56] = {}, iP = 0, iH = 0, nA = 8 + DA;
  for (int m = 8; m < 8 + DA; ++m) { n += 1; iA[m] = n; }
  for (int s = 8; s < 56; ++s) {
    int need = iA[s]; if (!need) r.ok = 0;
    if (s == TB && iP > need) need = iP;
    if (s == T_WH && iH > need) need = iH;
    r.wC[s] = cl(n - need);
    if (s == TB - 3) { n += 1; iP = n; }
    r.a0[s] = nA;
    for (int c = 0; c < 2 && nA < 56 && nA <= s + DA && (nA < 32 || s >= TB); ++c) { n += 1; iA[nA++] = n; }
    r.a1[s] = nA;
    if (s <= 16 && !(s & 1)) { n += 2; iH = n; }
    if (s >= T_O && s < T_O + 8) n += 2;
  }
  return r;
}
constexpr Sched SC = make_sched();
static_assert(SC.ok, "schedule");

DI rsrc_t mkrs(const void* p, u32 bytes) { return __builtin_amdgcn_make_buffer_rsrc((void*)p, 0, bytes, 0x00020000); }
template <int N> DI void dman(u32 lds, const u32* v, rsrc_t rs, u32 soff) {
  if constexpr (N == 4) asm volatile("s_mov_b32 m0, %0\n\ts_nop 0\n\tbuffer_load_dwordx4 %1, %5, %6 offen lds\n\tbuffer_load_dwordx4 %2, %5, %6 offen offset:1024 lds\n\t"
    "buffer_load_dwordx4 %3, %5, %6 offen offset:2048 lds\n\tbuffer_load_dwordx4 %4, %5, %6 offen offset:3072 lds" ::"s"(lds), "v"(v[0]), "v"(v[1]), "v"(v[2]), "v"(v[3]), "s"(rs), "s"(soff) : "memory");
  if constexpr (N == 3) asm volatile("s_mov_b32 m0, %0\n\ts_nop 0\n\tbuffer_load_dwordx4 %1, %4, %5 offen lds\n\tbuffer_load_dwordx4 %2, %4, %5 offen offset:1024 lds\n\t"
    "buffer_load_dwordx4 %3, %4, %5 offen offset:2048 lds" ::"s"(lds), "v"(v[0]), "v"(v[1]), "v"(v[2]), "s"(rs), "s"(soff) : "memory");
  if constexpr (N == 2) asm volatile("s_mov_b32 m0, %0\n\ts_nop 0\n\tbuffer_load_dwordx4 %1, %3, %4 offen lds\n\tbuffer_load_dwordx4 %2, %3, %4 offen offset:1024 lds" ::"s"(lds), "v"(v[0]), "v"(v[1]), "s"(rs), "s"(soff) : "memory");
  if constexpr (N == 1) asm volatile("s_mov_b32 m0, %0\n\ts_nop 0\n\tbuffer_load_dwordx4 %1, %2, %3 offen lds" ::"s"(lds), "v"(v[0]), "s"(rs), "s"(soff) : "memory");
}
DI void dma1792c(u32 lds, u32 voff, rsrc_t rs, u32 soff) {
  asm volatile("s_mov_b32 m0, %0\n\ts_nop 0\n\tbuffer_load_dwordx4 %1, %2, %3 offen sc1 lds\n\ts_mov_b32 exec_hi, 0xffff\n\t"
               "buffer_load_dwordx4 %1, %2, %3 offen offset:1024 sc1 lds\n\ts_mov_b32 exec_hi, -1" ::"s"(lds), "v"(voff), "s"(rs), "s"(soff) : "memory");
}
DI void dma4(u32 lds, u32 voff, rsrc_t rs, u32 soff) {
  asm volatile("s_mov_b32 m0, %0\n\ts_nop 0\n\tbuffer_load_dword %1, %2, %3 offen lds" ::"s"(lds), "v"(voff), "s"(rs), "s"(soff) : "memory");
}
template <bool C, int IMM = 0> DI u32x4 ld16_async(u32 voff, rsrc_t rs, u32 soff) {
  u32x4 r;
  if constexpr (C) asm volatile("buffer_load_dwordx4 %0, %1, %2, %3 offen offset:%4 sc1" : "=v"(r) : "v"(voff), "s"(rs), "s"(soff), "n"(IMM) : "memory");
  else asm volatile("buffer_load_dwordx4 %0, %1, %2, %3 offen" : "=v"(r) : "v"(voff), "s"(rs), "s"(soff) : "memory");
  return r;
}
DI u32x2 ld8c_async(u32 voff, rsrc_t rs) {
  u32x2 r; asm volatile("buffer_load_dwordx2 %0, %1, %2, 0 offen sc1" : "=v"(r) : "v"(voff), "s"(rs) : "memory"); return r;
}
template <int N> DI void wait_bar() { asm volatile("s_waitcnt vmcnt(%0) lgkmcnt(0)\n\ts_barrier" ::"n"(N) : "memory"); }
template <int N> DI void vm_bar() { asm volatile("s_waitcnt vmcnt(%0)\n\ts_barrier" ::"n"(N) : "memory"); }
template <int N> DI void vm() { asm volatile("s_waitcnt vmcnt(%0)" ::"n"(N) : "memory"); }
DI void bar() { asm volatile("s_barrier" ::: "memory"); }
#define PIN(x) asm volatile("" : "+v"(x))
#define SB() __builtin_amdgcn_sched_barrier(0)
DI f32x4 mfma(bf16x8 a, bf16x8 b, f32x4 c) { return __builtin_amdgcn_mfma_f32_16x16x32_bf16(a, b, c, 0, 0, 0); }
DI f32x16 mfma32(bf16x8 a, bf16x8 b, f32x16 c) { return __builtin_amdgcn_mfma_f32_32x32x16_bf16(a, b, c, 0, 0, 0); }
DI u32 pk(float a, float b) { bf16x2 v = {(__bf16)a, (__bf16)b}; return __builtin_bit_cast(u32, v); }
DI float fmx(float a, float b) { return __builtin_elementwise_maximum(a, b); }
DI float rbf(float a) { return (float)(__bf16)a; }
DI float bflo(u32 v) { return __uint_as_float(v << 16); }
DI float bfhi(u32 v) { return __uint_as_float(v & 0xffff0000u); }
DI float dot2(u32 a, u32 b, float c) { return __builtin_amdgcn_fdot2_f32_bf16(__builtin_bit_cast(bf16x2, a), __builtin_bit_cast(bf16x2, b), c, false); }
DI float sum8(float v) {
  v += __builtin_bit_cast(float, __builtin_amdgcn_update_dpp(0, __builtin_bit_cast(int, v), 0xb1, 0xf, 0xf, true));
  v += __builtin_bit_cast(float, __builtin_amdgcn_update_dpp(0, __builtin_bit_cast(int, v), 0x4e, 0xf, 0xf, true));
  v += __builtin_bit_cast(float, __builtin_amdgcn_update_dpp(0, __builtin_bit_cast(int, v), 0x141, 0xf, 0xf, true));
  return v;
}
template <class T> DI T lds_ld(const LAS char* base, u32 off) { return *(const LAS T*)(base + off); }
template <class T> DI void lds_st(LAS char* base, u32 off, T v) { *(LAS T*)(base + off) = v; }
DI bf16x8 trfrag(LAS char* base, u32 off0, u32 off1) {
  s16x4 lo = __builtin_amdgcn_ds_read_tr16_b64_v4i16((LAS s16x4*)(base + off0));
  s16x4 hi = __builtin_amdgcn_ds_read_tr16_b64_v4i16((LAS s16x4*)(base + off1));
  return __builtin_bit_cast(bf16x8, __builtin_shufflevector(lo, hi, 0, 1, 2, 3, 4, 5, 6, 7));
}
#define ST_AUX 16
template <int AUX> DI void st16(u32x4 v, rsrc_t rs, u32 voff) { __builtin_amdgcn_raw_buffer_store_b128(v, rs, voff, 0, AUX ? ST_AUX : 0); }
DI void st_flag(rsrc_t rc, u32 off, u64 v) { __builtin_amdgcn_raw_buffer_store_b64(u32x2{(u32)v, (u32)(v >> 32)}, rc, off, 0, 16); }
DI bool tagged(u32x2 v, u64 tag) { return __all(v[0] == (u32)tag && v[1] == (u32)(tag >> 32)); }
template <int SLEEP> DI void spin(rsrc_t rc, u32 off, u64 tag) {
#pragma unroll 1
  for (u32 n = 0; n < (1u << 17); ++n) {
    asm volatile("" ::: "memory");
    if (tagged(__builtin_amdgcn_raw_buffer_load_b64(rc, off, 0, 16), tag)) break;
    __builtin_amdgcn_s_sleep(SLEEP);
  }
  asm volatile("" ::: "memory");
}
DI u32 cnt_ld(LAS char* L, int k) { return __hip_atomic_load((LAS u32*)(L + L_CNT + 4 * k), __ATOMIC_RELAXED, __HIP_MEMORY_SCOPE_WORKGROUP); }
DI void cnt_wait(LAS char* L, int k, u32 v) {
#pragma unroll 1
  for (u32 n = 0; n < (1u << 20); ++n) {
    if ((u32)__builtin_amdgcn_readfirstlane(cnt_ld(L, k)) >= v) break;
    __builtin_amdgcn_s_sleep(LSLP);
  }
  asm volatile("" ::: "memory");
}
DI void prog_st(LAS char* L, int k, u32 v) { asm volatile("" ::: "memory"); __hip_atomic_store((LAS u32*)(L + L_CNT + 4 * k), v, __ATOMIC_RELAXED, __HIP_MEMORY_SCOPE_WORKGROUP); asm volatile("" ::: "memory"); }
DI u32 land_ld(LAS char* L) { const u64 v = __hip_atomic_load((LAS u64*)(L + L_CNT + 4 * C_LAND), __ATOMIC_RELAXED, __HIP_MEMORY_SCOPE_WORKGROUP); return min((u32)v, (u32)(v >> 32)); }
DI u32 done_ld(LAS char* L) {
  const u64 a = __hip_atomic_load((LAS u64*)(L + L_CNT), __ATOMIC_RELAXED, __HIP_MEMORY_SCOPE_WORKGROUP), b = __hip_atomic_load((LAS u64*)(L + L_CNT + 8), __ATOMIC_RELAXED, __HIP_MEMORY_SCOPE_WORKGROUP);
  return min(min((u32)a, (u32)(a >> 32)), min((u32)b, (u32)(b >> 32)));
}
template <bool DONE> DI void prog_wait(LAS char* L, u32 v) {
#pragma unroll 1
  for (u32 n = 0; n < (1u << 20); ++n) {
    if ((u32)__builtin_amdgcn_readfirstlane(DONE ? done_ld(L) : land_ld(L)) >= v) break;
    __builtin_amdgcn_s_sleep(LSLP);
  }
  asm volatile("" ::: "memory");
}
DI void cnt_add(u32 lb, int k) {
  asm volatile("s_mov_b64 exec, 1\n\tds_add_u32 %0, %1\n\ts_mov_b64 exec, -1" ::"v"(lb + L_CNT + 4 * k), "v"(1u) : "memory");
}
template <int N> DI bool vm_le() {
  u32 r; asm volatile("s_getreg_b32 %0, hwreg(HW_REG_IB_STS)" : "=s"(r)::"memory");
  return (r & 0xc0000fu) <= (u32)N;
}
DI bool last_of4(LAS char* L, int k) {
  u32 old = 0;
  asm volatile("" ::: "memory");
  if (__builtin_amdgcn_mbcnt_hi(~0u, __builtin_amdgcn_mbcnt_lo(~0u, 0u)) == 0) old = __hip_atomic_fetch_add((LAS u32*)(L + L_CNT + 4 * k), 1u, __ATOMIC_RELAXED, __HIP_MEMORY_SCOPE_WORKGROUP);
  asm volatile("" ::: "memory");
  return __builtin_amdgcn_readfirstlane(old) == 3;
}
#define STAMP(k) do { if ((k) == PROBE) { SB(); asm volatile("s_memrealtime %0\n\ts_waitcnt lgkmcnt(0)" : "=s"(mark)::"memory"); SB(); } } while (0)
#define FLUSH() do { if (mark) { u32 n_ = 0; while (__builtin_amdgcn_s_memrealtime() - mark < 4000 && ++n_ < 1000000) __builtin_amdgcn_s_sleep(4); } } while (0)

__global__ __launch_bounds__(512, 2) void fused_block(const char* __restrict__ x, const char* __restrict__ wqkv, const char* __restrict__ wo,
                                                      const char* __restrict__ w1, const char* __restrict__ b1, const char* __restrict__ w2,
                                                      const char* __restrict__ b2, char* __restrict__ out, char* ws) {
  __shared__ __attribute__((aligned(1024))) char smem[163840];
  asm volatile("" : "+s"(x), "+s"(wqkv), "+s"(wo), "+s"(w1), "+s"(b1), "+s"(w2), "+s"(b2), "+s"(out), "+s"(ws));
  u64 pc; asm volatile("s_getpc_b64 %0" : "=s"(pc));
  LAS char* const L = (LAS char*)smem;
  const u32 lb = (u32)(uintptr_t)smem;
  int l = threadIdx.x & 63, g, q4, p4, l15;
  auto lanes = [&] { PIN(l); g = l >> 4; q4 = (l >> 2) & 3; p4 = l & 3; l15 = l & 15; };
  lanes();
  int w = __builtin_amdgcn_readfirstlane(threadIdx.x >> 6);
  asm volatile("s_nop 4" : "+s"(w));
  const int x8 = blockIdx.x & 7, rk = blockIdx.x >> 3;
  const bool busy = rk < 28;
  u64 mark = 0;
  if (w == 0) STAMP(0);
  const rsrc_t rQKV = mkrs(ws + WS_QKV, QKV_BYTES), rAC = mkrs(ws + WS_AC, AC_BYTES), rCTL = mkrs(ws, CTL_BYTES);
  const rsrc_t rX = mkrs(x, 2048 * 896), rW2 = mkrs(w2, 1792 * 896);
  const int kh = x8 >> 2, cc = rk % 7, rb = (x8 & 3) * 4 + rk / 7, cb = kh * 7 + cc;
  const int ai = rk & 15, ah = 2 * x8 + (rk >> 4);
  const int ro6 = ROTA ? (cc + ((3 * (rk / 7)) >> 1)) % 6 : 0;
  auto rotl = [&](int j) { const int t = j + ro6; return j == 0 ? 0 : (t > 6 ? t - 6 : t); };
  auto kstep = [&](int p) {
    if (p < 8) return 8 * cb + p;
    const int t = cc + 1 + ((p - 8) % 24) / 4;
    return 8 * (7 * kh + (t >= 7 ? t - 7 : t)) + (p & 3) + (p >= 32 ? 4 : 0);
  };
  u64 tag;
  auto epoch = [&] { asm volatile("s_load_dwordx2 %0, %1, 0x0\n\ts_waitcnt lgkmcnt(0)" : "=s"(tag) : "s"(ws) : "memory"); tag += 1; };
  auto vW2 = [&](u32* v) { v[0] = (l >> 3) * 896 + (((l & 7) ^ ((g & 1) << 2)) << 4); v[1] = v[0] + 7168 - 1024; };

  if (w < 4) {
    if (!busy) {
      const int q = (rk - 28) * 4 + w;
      dma4(lb + L_WC + w * 16384, (q * 64 + l) * 64, mkrs((const void*)(pc & ~255ull), CODE_KB * 1024), 0);
      bar();
      for (int k = 0; k < PF_DELAY; ++k) __builtin_amdgcn_s_sleep(2);
      const rsrc_t rW1 = mkrs(w1, 448 * 3584), rWo = mkrs(wo, 448 * 896);
      const u32 v = l * 16;
#pragma unroll 1
      for (int k = 0; k < 49; ++k) {
        int r = k * 16 + q + (x8 & 3) * 196; r = r >= 784 ? r - 784 : r;
        dman<1>(lb + L_WC + w * 16384 + (k & 15) * 1024, &v, rW2, kh * 802816 + r * 1024);
        vm<10>();
      }
#pragma unroll 1
      for (int k = 0; k < 25; ++k) { dman<1>(lb + L_WC + w * 16384 + (k & 15) * 1024, &v, rWo, (k * 16 + q) * 1024); vm<10>(); }
      vm<0>();
      FLUSH();
      return;
    }
    epoch();
    constexpr int PF = 4;
    u32 oX[4], oH[2], oQ[3];
#pragma unroll
    for (int p = 0; p < 4; ++p) oX[p] = (4 * w + ((l & 31) >> 3)) * 1024 + (l & 7) * 128 + (((2 * p + (g >> 1)) ^ (l15 >> 1)) << 4);
    const int rq = w == 3 ? 0 : w;
#pragma unroll
    for (int i = 0; i < 3; ++i) oQ[i] = RING + (8 * (g >> 1) + q4) * 192 + ((i + rq) % 3) * 64 + (g & 1) * 32 + p4 * 8;
#pragma unroll
    for (int i = 0; i < 2; ++i) oH[i] = RING + (g >> 1) * 1024 + q4 * 128 + ((i ^ (q4 >> 1)) << 6) + (g & 1) * 32 + p4 * 8;
    const u32 selv = l15 == (g & 1) ? 0x3f803f80u : 0u;
    const bf16x8 sel = __builtin_bit_cast(bf16x8, u32x4{selv, selv, selv, selv});
    bf16x8 xf[28], wf[8];
    f32x16 aH[2] = {}, aQ[3] = {};
    f32x4 cs = {};
    u32x2 ch[2];
    float rs;
    bf16x8 lnb;
    u32x4 hA[72], qp[6];
    auto rdW = [&](auto SS, auto II) {
      constexpr int s = decltype(SS)::v, i = decltype(II)::v;
      constexpr int slot = (s < 14 ? s * 10 + i : 140 + i) % 8;
      if constexpr (s < 14 && i % 5 < 3) { const u32 o = oQ[i % 5] + (stage_addr(s) - RING) + (i / 5) * 3072; wf[slot] = trfrag(L, o, o + 768); }
      else if constexpr (s < 14) { const u32 o = oH[i % 5 - 3] + (stage_addr(s) - RING) + 6144 + (i / 5) * 2048; wf[slot] = trfrag(L, o, o + 512); }
      else if constexpr (s < 21) { const u32 o = oH[i & 1] + (stage_addr(s) - RING) + (i >> 1) * 2048; wf[slot] = trfrag(L, o, o + 512); }
    };
    auto rdX = [&](auto SS, auto EE) { constexpr int k = 2 * decltype(SS)::v + decltype(EE)::v; if constexpr (k < 28) xf[k] = lds_ld<bf16x8>(L, oX[k & 3] + (k >> 2) * 16384); };
    auto rdcs = [&](int half) {
#pragma unroll
      for (int i = 0; i < 2; ++i) ch[i] = lds_ld<u32x2>(L, L_CSH + (half * 64 + i * 32 + (l & 31)) * 8);
    };
    auto hpack = [&](int half, int c) {
      const float rs2 = rs * rs;
      u32x4 hp;
#pragma unroll
      for (int j = 0; j < 4; ++j) {
        const float t0 = aH[c >> 1][8 * (c & 1) + 2 * j], t1 = aH[c >> 1][8 * (c & 1) + 2 * j + 1];
        hp[j] = pk(t0 * rs2 * fmx(t0, 0.f), t1 * rs2 * fmx(t1, 0.f));
      }
      hA[half * 4 + c] = hp;
    };
    auto hstore = [&](int m) { st16<16>(hA[m], rAC, l * 16 + ((rb * 4 + w) * 144 + cb * 8 + m) * 1024); };
    auto qpack = [&](int c) {
      const int i = c >> 1, e = c & 1, tq = 6 * cb + 2 * ((i + rq) % 3) + e;
      const float m = tq < 28 ? rs * (0.18898223650461361f * 1.4426950408889634f) : rs;
      const auto s0 = __builtin_amdgcn_permlane16_swap(pk(aQ[i][8 * e] * m, aQ[i][8 * e + 1] * m), pk(aQ[i][8 * e + 4] * m, aQ[i][8 * e + 5] * m), false, false);
      const auto s1 = __builtin_amdgcn_permlane16_swap(pk(aQ[i][8 * e + 2] * m, aQ[i][8 * e + 3] * m), pk(aQ[i][8 * e + 6] * m, aQ[i][8 * e + 7] * m), false, false);
      qp[c] = u32x4{s0[0], s1[0], s0[1], s1[1]};
    };
    auto qstore = [&](int c) {
      const int fl = ((g & 1) << 1) | (g >> 1), tq = 6 * cb + 2 * (((c >> 1) + rq) % 3) + (c & 1);
      u32x4 v = qp[c];
      u32 vo = fl * 256 + l15 * 16;
      if (tq >= 56) {
        asm volatile("" ::: "memory");
        if (fl & 1) v = u32x4{v[2], v[3], v[0], v[1]};
        vo ^= (2 * (tq & 1) + (fl >> 1)) << 6;
      }
      st16<16>(v, rQKV, vo + ((rb * 4 + w) * 336 + 4 * tq) * 256);
    };
    bool sigq = false, siga = false;
    const bool giver = (w >> 1) != kh;
    bar();
    STAMP(1);
    rdX(IC<0>{}, IC<0>{}); rdX(IC<0>{}, IC<1>{});
    sfor<0, PF>([&](auto I) { rdW(IC<0>{}, I); });
    sfor<0, 21>([&](auto SS) {
      constexpr int s = decltype(SS)::v, NT = s < 14 ? 10 : 8;
      if constexpr (s == 14) wait_bar<63>();
      else if constexpr (s < 14) bar();
      if constexpr (s == 13) lanes();
      if constexpr (s == 14) {
        STAMP(2);
        u32 cq[3];
#pragma unroll
        for (int i = 0; i < 3; ++i) cq[i] = lds_ld<u32>(L, L_CSQ + (((i + rq) % 3) * 32 + (l & 31)) * 4);
        const u32 rdy = cnt_ld(L, C_ST);
        u32x2 st = lds_ld<u32x2>(L, L_ST + (w * 32 + (l & 31)) * 8);
        rdcs(0);
        if (!__builtin_amdgcn_readfirstlane(rdy)) { cnt_wait(L, C_ST, 1); st = lds_ld<u32x2>(L, L_ST + (w * 32 + (l & 31)) * 8); rdcs(0); }
        SB();
        lnb = __builtin_bit_cast(bf16x8, u32x4{l < 32 ? (st[0] & 0xffffu) * 0x10001u : 0u, l < 32 ? st[0] >> 16 : 0u, 0u, 0u});
        rs = __uint_as_float(st[1]);
#pragma unroll
        for (int i = 0; i < 3; ++i) aQ[i] = mfma32(__builtin_bit_cast(bf16x8, u32x4{cq[i], 0u, 0u, 0u}), lnb, aQ[i]);
#pragma unroll
        for (int i = 0; i < 2; ++i) aH[i] = mfma32(__builtin_bit_cast(bf16x8, u32x4{ch[i][0], ch[i][1], 0u, 0u}), lnb, aH[i]);
        rdcs(1);
        SB();
#pragma unroll
        for (int c = 0; c < 6; ++c) { qpack(c); if (c % (6 / QNOW) == 6 / QNOW - 1) qstore(c / (6 / QNOW)); SB(); }
#pragma unroll
        for (int c = 0; c < 4; ++c) hpack(0, c);
        SB();
        STAMP(3);
      }
      u32 land;
      sfor<0, NT>([&](auto II) {
        constexpr int i = decltype(II)::v, slot = (s < 14 ? s * 10 + i : 140 + i) % 8;
        if constexpr ((s == 15 || s == 18) && i == 1) land = land_ld(L);
        if constexpr ((s == 15 || s == 18) && i == NT - PF) {
          if ((u32)__builtin_amdgcn_readfirstlane(land) < (s == 15 ? 1u : 2u)) prog_wait<false>(L, s == 15 ? 1 : 2);
          asm volatile("" ::: "memory");
        }
        if constexpr (i + PF < NT) rdW(SS, IC<i + PF>{});
        else rdW(IC<s + 1>{}, IC<i + PF - NT>{});
        if constexpr (i < 2) rdX(IC<s + 1>{}, II);
        if constexpr (s < 14) {
          if constexpr (i % 5 < 3) aQ[i % 5] = mfma32(wf[slot], xf[2 * s + i / 5], aQ[i % 5]);
          else aH[i % 5 - 3] = mfma32(wf[slot], xf[2 * s + i / 5], aH[i % 5 - 3]);
          if constexpr (i % 5 == 0) cs = mfma(wf[slot], sel, cs);
          if constexpr (s == 13 && i == 7) {
            u32x4 v;
#pragma unroll
            for (int j = 0; j < 4; ++j) { const float hi = rbf(cs[j]); v[j] = pk(hi, cs[j] - hi); }
            if (l15 < 2) lds_st<u32x4>(L, L_CSQ + w * 128 + (l15 * 16 + 4 * g) * 4, v);
          }
        } else {
          constexpr int k = 4 * (s - 14) + (i >> 1), m = QNOW + 2 * (s - 14) + (i >> 2);
          if constexpr (k == 0) aH[i] = mfma32(__builtin_bit_cast(bf16x8, u32x4{ch[i][0], ch[i][1], 0u, 0u}), lnb, f32x16{});
          aH[i & 1] = mfma32(wf[slot], xf[k], aH[i & 1]);
          if constexpr ((i & 3) == 1 && m < 6) qstore(m);
          if constexpr ((i & 3) == 1 && m >= 6 && m < 10) hstore(m - 6);
        }
        SB();
      });
      if constexpr (s == 18 || s == 20) prog_st(L, C_DONE + w, s);
      if constexpr (s >= 14) {
        constexpr int ns = QNOW + 2 * (s - 13), nh = ns < 6 ? 0 : (ns > 10 ? 4 : ns - 6);
        if constexpr (ns > 6 && s <= S_FQ) { if (!sigq && (s == S_FQ || vm_le<nh>())) { vm<nh>(); cnt_add(lb, C_FQ); sigq = true; } }
        if constexpr (ns > 10) { if (!siga && vm_le<0>()) { vm<0>(); cnt_add(lb, C_FA); siga = true; } }
      }
    });
    STAMP(4);
    lanes();
    const u32 vFG = WS_FG + (rb * 16 + 7 * kh + (l < 7 ? l : 0)) * FS;
#pragma unroll
    for (int c = 0; c < 4; ++c) hpack(1, c);
    SB();
    if (!siga) { vm<0>(); cnt_add(lb, C_FA); }
#pragma unroll
    for (int m = 4; m < 8; ++m) hstore(m);
    STAMP(5);
    u32x2 polled;
    u32 vW[2];
    vW2(vW);
    const u32 vA = l * 16, sA = (rb * 4 + w) * 144, sO = ((rb * 4 + (giver ? w ^ 2 : w)) * 144 + (giver ? 128 : 112)) * 1024;
    u32 sN[6];
#pragma unroll
    for (int j = 0; j < 6; ++j) { sN[j] = (sA + kstep(8 + 4 * j)) * 1024; asm volatile("" : "+s"(sN[j])); }
    const u32 vB = vA + 4096;
    auto ldA = [&](auto M) { constexpr int m = decltype(M)::v; hA[m] = ld16_async<true, (m & 3) * 1024>(m >= 32 ? vB : vA, rAC, sN[((m - 8) % 24) / 4]); };
    auto ldO = [&](auto M) { constexpr int m = decltype(M)::v; hA[m] = ld16_async<true, (m & 3) * 1024>(vA, rAC, sO + ((m - 56) & ~3) * 1024); };
    u32 oW[2], oO[2];
#pragma unroll
    for (int b = 0; b < 2; ++b) { oW[b] = (4 * (g >> 1) + q4) * 128 + ((b ^ (q4 >> 1)) << 6) + (g & 1) * 32 + p4 * 8; oO[b] = oW[b] + (giver ? 8 * 3584 : 0); }
    bf16x8 wc[2][2];
    auto rdC = [&](int m) {
#pragma unroll
      for (int b = 0; b < 2; ++b) {
        const u32 o = m < 56 ? oW[b] + L_WC + m * 2048 : oO[b] + ((m - 56) >> 1) * 3584 + (m & 1) * 2048 + (m >= 66 && giver ? L_WC - 364 * 128 : 0);
        wc[m & 1][b] = trfrag(L, o, o + 1024);
      }
    };
    f32x16 acc[2];
    u32x4 xres[2], bres, pp[4];
    auto step = [&](auto N) {
      constexpr int n = decltype(N)::v;
      if constexpr (n + 1 < 72) rdC(n + 1);
      SB();
      acc[0] = mfma32(wc[n & 1][0], __builtin_bit_cast(bf16x8, hA[n]), n == 0 ? f32x16{} : acc[0]);
      SB();
      if constexpr (n >= 8 && n < 56) sfor<SC.a0[n], SC.a1[n]>(ldA);
      if constexpr (n >= 8 && n <= 16 && !(n & 1)) {
        const int t = 5 * w + (n - 8) / 2;
        dman<2>(lb + L_WC + (32 + t) * 2048, vW, rW2, ((8 * (7 * kh + (cc + 1 + (t >> 2)) % 7) + 4 + (t & 3)) * 16) * 896 + cc * 128);
      }
      acc[1] = mfma32(wc[n & 1][1], __builtin_bit_cast(bf16x8, hA[n]), n == 0 ? f32x16{} : acc[1]);
      SB();
    };
    cnt_wait(L, C_WLO, 2);
    rdC(0);
    sfor<0, OWN1>(step);
    cnt_wait(L, C_FH, 1);
    cnt_wait(L, C_KV, 2);
    STAMP(6);
    __builtin_amdgcn_s_setprio(PRIO_C);
    sfor<8, 8 + DA>(ldA);
    prog_wait<true>(L, 20);
    sfor<OWN1, 8>(step);
    sfor<8, 56>([&](auto N) {
      constexpr int n = decltype(N)::v;
      asm volatile("s_waitcnt vmcnt(%1)" : "+v"(hA[n]) : "n"(SC.wC[n]) : "memory");
      if constexpr (n == 33) cnt_add(lb, C_P33);
      if constexpr (n == 8) { cnt_add(lb, C_P6); if (last_of4(L, C_FG) && l == 0) st_flag(rCTL, WS_FG + (rb * 16 + cb) * FS, tag); }
      if constexpr (n == T_WH) cnt_add(lb, C_WHI);
      if constexpr (n == TB - 3) polled = ld8c_async(vFG, rCTL);
      if constexpr (n == TB) { PIN(polled); if (!tagged(polled, tag)) spin<SLP>(rCTL, vFG, tag); }
      if constexpr (n == 31) cnt_wait(L, C_WHI, 4);
      if constexpr (n == 51) cnt_wait(L, C_LATE, 4);
      step(N);
      if constexpr (n >= T_O && n < T_O + 8) { ldO(IC<56 + 2 * (n - T_O)>{}); ldO(IC<57 + 2 * (n - T_O)>{}); }
    });
    asm volatile("s_waitcnt vmcnt(0)" : "+v"(hA[56]), "+v"(hA[57]), "+v"(hA[58]), "+v"(hA[59]), "+v"(hA[60]), "+v"(hA[61]), "+v"(hA[62]), "+v"(hA[63]),
                 "+v"(hA[64]), "+v"(hA[65]), "+v"(hA[66]), "+v"(hA[67]), "+v"(hA[68]), "+v"(hA[69]), "+v"(hA[70]), "+v"(hA[71])::"memory");
    if (giver) {
#pragma unroll
      for (int k = 0; k < 4; ++k) {
        const f32x16& a = acc[k >> 1];
        const int o = 8 * (k & 1);
        st16<16>(u32x4{pk(a[o], a[o + 1]), pk(a[o + 2], a[o + 3]), pk(a[o + 4], a[o + 5]), pk(a[o + 6], a[o + 7])}, rAC, PX + ((rb * 7 + cc) * 4 + w) * 4096 + k * 1024 + l * 16);
      }
      acc[0] = f32x16{}; acc[1] = f32x16{};
    }
    auto fresh = [&](const u32x4* v, int n) {
      u32 m = 0;
#pragma unroll
      for (int k = 0; k < n; ++k) m = max(max(m, max(v[k][0], v[k][1])), max(v[k][2], v[k][3]));
      return __all(m != ~0u);
    };
#pragma unroll 1
    for (u32 n = 0; n < (1u << 17) && !fresh(&hA[56], 16); ++n) {
#pragma unroll 1
      for (; n < (1u << 17); ++n) {
        asm volatile("" ::: "memory");
        if (__all(__builtin_amdgcn_raw_buffer_load_b32(rAC, sO + (l & 7) * 2048 + 1024, 0, 16) != ~0u)) break;
        __builtin_amdgcn_s_sleep(SLP);
      }
#pragma unroll
      for (int k = 0; k < 16; ++k) hA[56 + k] = __builtin_amdgcn_raw_buffer_load_b128(rAC, vA + sO + k * 1024, 0, 16);
    }
    const int tile = giver ? w ^ 2 : w, r0 = (giver ? 16 : 0) + (l >> 3);
#pragma unroll
    for (int k = 0; k < 2; ++k) xres[k] = ld16_async<false>((r0 + 8 * k) * 896 + (l & 7) * 16, rX, (rb * 128 + tile * 32) * 896 + cc * 128);
    bres = ld16_async<false>((l & 7) * 16, mkrs(b2, 896), cc * 128);
    sfor<56, 66>(step);
    sfor<0, 4>([&](auto K) { constexpr int k = decltype(K)::v; pp[k] = ld16_async<true, k * 1024>(vA, rAC, giver ? 0xfffff000u : PX + ((rb * 7 + cc) * 4 + w) * 4096); });
    sfor<66, 72>(step);
    asm volatile("s_waitcnt vmcnt(0)" : "+v"(xres[0]), "+v"(xres[1]), "+v"(bres), "+v"(pp[0]), "+v"(pp[1]), "+v"(pp[2]), "+v"(pp[3])::"memory");
    STAMP(10);
#pragma unroll 1
    for (u32 n = 0; n < (1u << 17) && !fresh(pp, 4); ++n) {
      asm volatile("" ::: "memory");
#pragma unroll
      for (int k = 0; k < 4; ++k) pp[k] = __builtin_amdgcn_raw_buffer_load_b128(rAC, PX + ((rb * 7 + cc) * 4 + w) * 4096 + k * 1024 + vA, 0, 16);
    }
#pragma unroll
    for (int k = 0; k < 4; ++k)
#pragma unroll
      for (int j = 0; j < 4; ++j) { acc[k >> 1][8 * (k & 1) + 2 * j] += bflo(pp[k][j]); acc[k >> 1][8 * (k & 1) + 2 * j + 1] += bfhi(pp[k][j]); }
    cnt_wait(L, C_P33, 4);
#pragma unroll
    for (int b = 0; b < 2; ++b)
#pragma unroll
      for (int k = 0; k < 4; ++k)
        lds_st<f32x4>(L, L_RED + w * 8192 + (l & 31) * 256 + (((b * 8 + k * 2 + (g >> 1)) ^ (l & 7)) << 4), f32x4{acc[b][4 * k], acc[b][4 * k + 1], acc[b][4 * k + 2], acc[b][4 * k + 3]});
    cnt_add(lb, C_PAIR0 + (w & 1));
    cnt_wait(L, C_PAIR0 + (w & 1), 2);
    const rsrc_t rO = mkrs(out, 2048 * 896);
#pragma unroll
    for (int k = 0; k < 2; ++k) {
      const int row = r0 + 8 * k;
      f32x4 a = {bflo(xres[k][0]) + bflo(bres[0]), bfhi(xres[k][0]) + bfhi(bres[0]), bflo(xres[k][1]) + bflo(bres[1]), bfhi(xres[k][1]) + bfhi(bres[1])};
      f32x4 b = {bflo(xres[k][2]) + bflo(bres[2]), bfhi(xres[k][2]) + bfhi(bres[2]), bflo(xres[k][3]) + bflo(bres[3]), bfhi(xres[k][3]) + bfhi(bres[3])};
#pragma unroll
      for (int j = 0; j < 2; ++j) {
        a += lds_ld<f32x4>(L, L_RED + ((w & 1) + 2 * j) * 8192 + row * 256 + (((2 * (l & 7)) ^ (row & 7)) << 4));
        b += lds_ld<f32x4>(L, L_RED + ((w & 1) + 2 * j) * 8192 + row * 256 + (((2 * (l & 7) + 1) ^ (row & 7)) << 4));
      }
      st16<16>(u32x4{pk(a[0], a[1]), pk(a[2], a[3]), pk(b[0], b[1]), pk(b[2], b[3])}, rO, (rb * 128 + tile * 32 + row) * 896 + cc * 128 + (l & 7) * 16);
    }
    if (w == 0) STAMP(11);
    FLUSH();
    return;
  }

  const int li = w & 1;
  auto clear_o = [&] {
#pragma unroll
    for (int k = 0; k < 8; ++k) st16<16>(u32x4{~0u, ~0u, ~0u, ~0u}, rAC, ((ai * 4 + (k >> 1)) * 144 + 112 + 2 * ah + (k & 1)) * 1024 + l * 16);
  };
  if (!busy) {
    const int blk = (rk - 28) * 8 + x8;
    u32x4 d[14];
#pragma unroll
    for (int k = 0; k < 14; ++k) d[k] = __builtin_amdgcn_raw_buffer_load_b128(mkrs(w1, 448 * 3584), (112 * (w - 4) + (l >> 3)) * 3584 + (l & 7) * 16 + (blk < 28 ? blk * 128 : 0x7fffffff), k * 8 * 3584, 0);
    const u32 bias = __builtin_amdgcn_raw_buffer_load_b16(mkrs(b1, 3584), (blk * 64 + l) * 2, 0, 0);
    SB();
    epoch();
    if (w == 6 && l < C_N) lds_st<u32>(L, L_CNT + l * 4, l < 4 ? 64u : 0u);
    wait_bar<63>();
    if (w == 4) { vm<0>(); STAMP(38); }
    float a[8] = {};
#pragma unroll
    for (int k = 0; k < 14; ++k)
#pragma unroll
      for (int j = 0; j < 4; ++j) { a[2 * j] += bflo(d[k][j]); a[2 * j + 1] += bfhi(d[k][j]); }
    lds_st<f32x4>(L, RING + ((w - 4) * 64 + l) * 32, f32x4{a[0], a[1], a[2], a[3]});
    lds_st<f32x4>(L, RING + ((w - 4) * 64 + l) * 32 + 16, f32x4{a[4], a[5], a[6], a[7]});
    if (last_of4(L, C_CS0) && blk < 28) {
      float t = 0.f;
#pragma unroll
      for (int k = 0; k < 32; ++k) t += lds_ld<float>(L, RING + k * 256 + l * 4);
      const float hi = rbf(t);
      __builtin_amdgcn_raw_buffer_store_b64(u32x2{pk(hi, t - hi), bias}, rCTL, WS_CS + (blk * 64 + l) * 8, 0, 16);
      vm<0>();
      if (l == 0) st_flag(rCTL, WS_FC + blk * FS, tag);
      STAMP(39);
    }
    if (w == 7) { clear_o(); vm<0>(); if (l == 0) st_flag(rCTL, WS_FZ + (ai * 16 + ah) * FS, tag); }
  } else if (w < 6) {
    const rsrc_t rW1 = mkrs(w1, 448 * 3584), rWq = mkrs(wqkv, 448 * 2688);
    u32 vX[4], vH[4], vQ[3], vC[2], so7[7];
#pragma unroll
    for (int j = 0; j < 4; ++j) vX[j] = (8 * j + (l >> 3)) * 896 + (((l & 7) ^ ((j & 1) * 4 + g)) << 4) - 1024 * j;
    so7[0] = 0;
    const u32 sX = (rb * 128 + 64 * li) * 896, sH = cb * 256 + 16 * li * 3584, sQ = cb * 192 + 16 * li * 2688;
    auto issue = [&](auto O) {
      constexpr int op = decltype(O)::v, k = op / 256, a = (op % 256) / 2, e = op & 1;
      if constexpr (k == K_X) dman<4>(lb + (a * 16 + 8 * li + 4 * e) * 1024, vX, rX, sX + so7[a] * 128 + e * 28672);
      if constexpr (k == K_WA && a < 14) {
        dman<3>(lb + stage_addr(a) + li * 3072, vQ, rWq, sQ + so7[a >> 1] * (64 * 2688) + (a & 1) * (32 * 2688));
        dman<2>(lb + stage_addr(a) + 6144 + li * 2048, vH, rW1, sH + so7[a >> 1] * (64 * 3584) + (a & 1) * (32 * 3584));
      }
      if constexpr (k == K_WA && a >= 14) dman<4>(lb + stage_addr(a) + li * 4096, vH, rW1, sH + 16 * li * 3584 + so7[a - 14] * (64 * 3584) + 128);
      if constexpr (k == K_WCP) dman<2>(lb + L_WC + (2 * a + li) * 2048, vC, rW2, (kstep(2 * a) + li) * (16 * 896) + cc * 128);
    };
    sfor<0, 2>([&](auto K) { issue(IC<SC.pro[decltype(K)::v]>{}); });
    lanes();
#pragma unroll
    for (int j = 0; j < 3; ++j) { const u32 n = 64 * j + l; vQ[j] = (n / 12) * 2688 + (n % 12) * 16 - 1024 * j; }
#pragma unroll
    for (int j = 0; j < 4; ++j) vH[j] = (8 * j + (l >> 3)) * 3584 + (((l & 7) ^ ((g & 1) << 2)) << 4) - 1024 * j;
    sfor<2, 4>([&](auto K) { issue(IC<SC.pro[decltype(K)::v]>{}); });
#pragma unroll
    for (int j = 1; j < 7; ++j) so7[j] = rotl(j);
    sfor<4, NPRO>([&](auto K) { issue(IC<SC.pro[decltype(K)::v]>{}); });
    epoch();
    vW2(vC);
    vm_bar<SC.wA[0]>();
    sfor<0, 15>([&](auto SS) {
      constexpr int s = decltype(SS)::v;
      vm_bar<SC.wA[s + 1]>();
      sfor<0, (s == 14 ? 2 : MAXO)>([&](auto K) { issue(IC<SC.opA[s][decltype(K)::v]>{}); });
    });
    vm<SC.wL[0]>();
    prog_st(L, C_LAND + li, 1);
    sfor<2, 8>([&](auto K) { issue(IC<SC.opA[14][decltype(K)::v]>{}); });
    vm<SC.wL[1]>();
    prog_st(L, C_LAND + li, 2);
    sfor<8, MAXO>([&](auto K) { issue(IC<SC.opA[14][decltype(K)::v]>{}); });
    vm<0>();
    cnt_add(lb, C_WLO);
    if (w == 4) { STAMP(13); cnt_wait(L, C_FA, 5); STAMP(14); if (l == 0) st_flag(rCTL, WS_FH + (rb * 16 + cb) * FS, tag); }
    else { cnt_wait(L, C_FA, 5); spin<SLP>(rCTL, l >= 16 && l < 32 ? WS_FZ + (rb * 16 + l - 16) * FS : WS_FH + (rb * 16 + (l < 7 ? 7 * kh + l : 7 * (1 - kh) + cc)) * FS, tag); STAMP(15); cnt_add(lb, C_FH); }
  } else {
    if (w == 6 && cb < 8) {
      u32x4 d[14];
#pragma unroll
      for (int k = 0; k < 14; ++k) d[k] = __builtin_amdgcn_raw_buffer_load_b128(rX, (rb * 128 + cb * 16 + (l >> 3)) * 896 + (l & 7) * 16, (k / 7) * 7168 + (k % 7) * 128, 0);
      SB();
      epoch();
      bar(); bar();
#pragma unroll
      for (int r = 0; r < 2; ++r) {
        bar();
        float s1 = 0.f, s2 = 0.f;
#pragma unroll
        for (int k = 0; k < 7; ++k)
#pragma unroll
          for (int j = 0; j < 4; ++j) { s1 = dot2(d[7 * r + k][j], 0x3f803f80u, s1); s2 = dot2(d[7 * r + k][j], d[7 * r + k][j], s2); }
        const float mean = sum8(s1) * (1.f / 448.f);
        const float var = fmaxf(sum8(s2) * (1.f / 448.f) - mean * mean, 0.f);
        const float sd = rbf(__builtin_amdgcn_sqrtf(rbf(rbf(var) + 1e-5f)));
        if ((l & 7) == 0) __builtin_amdgcn_raw_buffer_store_b64(u32x2{pk(-mean, sd), __float_as_uint(__builtin_amdgcn_rcpf(sd))}, rCTL, WS_ST + (rb * 128 + cb * 16 + r * 8 + (l >> 3)) * 8, 0, 16);
        SB();
      }
    } else { epoch(); bar(); bar(); bar(); bar(); }
    if (w == 6) {
      const u32 fo = l < 8 ? WS_FS + (rb * 8 + l) * FS : WS_FC + (2 * cb + (l & 1)) * FS;
      const u32 fp = (l < 16 ? WS_FS + (rb * 8 + (l >> 1)) * FS : WS_FC + (2 * cb + ((l >> 1) & 1)) * FS) + (l & 1) * 4;
      auto fetch = [&] {
        asm volatile("s_mov_b32 m0, %0\n\ts_nop 0\n\tbuffer_load_dwordx4 %1, %3, 0 offen sc1 lds\n\tbuffer_load_dwordx4 %2, %3, 0 offen offset:1024 sc1 lds"
                     ::"s"(lb + L_CSH), "v"(l * 16 + WS_CS + cb * 1024), "v"(l * 16 + WS_ST + rb * 1024 - 1024), "s"(rCTL) : "memory");
      };
      int state = -1;
#pragma unroll 1
      for (int s = 3; s <= 14; ++s) {
        wait_bar<63>();
        if (s == 4 && cb < 8) { vm<0>(); if (l == 0) st_flag(rCTL, WS_FS + (rb * 8 + cb) * FS, tag); }
        if (s == 5 && l < C_N) lds_st<u32>(L, L_CNT + l * 4, 0u);
        if (s >= 7 && (s & 1) && state < 2) {
          vm<0>();
          if (state == 1) { cnt_add(lb, C_ST); state = 2; STAMP(33); }
          else if (state == 0 && __all(lds_ld<u32>(L, L_POLL + l * 4) == (u32)(l & 1 ? tag >> 32 : tag))) { fetch(); state = 1; }
          else { asm volatile("s_mov_b32 m0, %0\n\ts_nop 0\n\tbuffer_load_dword %1, %2, 0 offen sc1 lds" ::"s"(lb + L_POLL), "v"(fp), "s"(rCTL) : "memory"); state = 0; }
        }
      }
      vm<0>();
      if (state <= 0) { spin<SLP>(rCTL, fo, tag); fetch(); vm<0>(); }
      if (state < 2) { cnt_add(lb, C_ST); STAMP(33); }
    } else {
#pragma unroll 1
      for (int s = 3; s <= 14; ++s) {
        bar();
        const int k = s - CLR0;
        if (k >= 0 && k < 8) {
          st16<16>(u32x4{~0u, ~0u, ~0u, ~0u}, rAC, ((ai * 4 + (k >> 1)) * 144 + 112 + 2 * ah + (k & 1)) * 1024 + l * 16);
          st16<16>(u32x4{~0u, ~0u, ~0u, ~0u}, rAC, PX + ((rb * 7 + cc) * 4 + 2 * kh) * 4096 + k * 1024 + l * 16);
        }
      }
      vm<0>();
      cnt_add(lb, C_FA);
      if (l == 0) st_flag(rCTL, WS_FZ + (ai * 16 + ah) * FS, tag);
    }
  }
  lanes();
  const int aw = w - 4, hh = g >> 1;
  if (w >= 6) {
    const int e = l & 1, k = l < 14 ? l >> 1 : 0;
    const int ty = k == 0 ? 0 : (k < 4 ? 1 : 2), ro = k == 0 ? 0 : (k < 4 ? k - 1 : k - 4);
    const int rr = ai - ro < 0 ? 0 : ai - ro;
    if (!busy) { for (int k = 0; k < 12; ++k) __builtin_amdgcn_s_sleep(15); }
    if (busy) { cnt_wait(L, C_FQ, 4); if (w == 6) STAMP(12); if (w == 6 && l == 0) st_flag(rCTL, WS_FQ + (rb * 16 + cb) * FS, tag); }
    prog_wait<true>(L, 18);
#pragma unroll
    for (int k = 0; k < 3; ++k) lds_st<u32x2>(L, L_V + (6 * li + 2 * k + (l >> 5)) * 2048 + 1792 + (l & 31) * 8, u32x2{0x3f80u, 0u});
    if (w == 6) { spin<SLP>(rCTL, WS_FQ + (rr * 16 + (ty * 448 + 28 * ah + 27 * e) / 96) * FS, tag); cnt_add(lb, C_SQ); }
    else cnt_wait(L, C_SQ, 1);
    if (w == 6) STAMP(7);
#pragma unroll
    for (int e = 0; e < 2; ++e) {
#pragma unroll
      for (int j = 0; j < 6; ++j) {
        const int b = 2 * j + li, tb = 4 * ai - 8 + b < 0 ? 0 : 4 * ai - 8 + b;
        dma1792c(lb + (e ? L_V + b * 2048 : L_K + b * 1792), l * 16, rQKV, (tb * 336 + 112 + 112 * e + 7 * ah) * 256);
      }
      if (e == 0) {
#pragma unroll
        for (int j = 0; j < 2; ++j) dma1792c(lb + L_Q + (2 * li + j) * 2048, l * 16, rQKV, ((4 * ai + 2 * li + j) * 336 + 7 * ah) * 256);
      }
    }
    vm<12>();
    cnt_add(lb, C_KV);
  }
  cnt_wait(L, C_KV, 2);
  __builtin_amdgcn_s_setprio(PRIO_B);
  if (w == 4) STAMP(8);
  u32x2 qv[4];
#pragma unroll
  for (int k = 0; k < 4; ++k) qv[k] = lds_ld<u32x2>(L, L_Q + aw * 2048 + (k == 3 ? 6 : 2 * k + hh) * 256 + l15 * 16 + (g & 1) * 8);
  if (hh) qv[3] = u32x2{0u, 0u};
  const bf16x8 qf[2] = {__builtin_bit_cast(bf16x8, u32x4{qv[0][0], qv[0][1], qv[1][0], qv[1][1]}), __builtin_bit_cast(bf16x8, u32x4{qv[2][0], qv[2][1], qv[3][0], qv[3][1]})};
  const u32 oK = L_K + aw * 1792 + hh * 256 + l15 * 16 + (g & 1) * 8, oK3 = hh ? oK + 1280 : oK + 1536;
  const int F = 7 * ah + 4 * (g & 1) + p4;
  u32 oV[2][2];
#pragma unroll
  for (int t = 0; t < 2; ++t)
#pragma unroll
    for (int e = 0; e < 2; ++e) oV[t][e] = L_V + aw * 2048 + (4 * (g & 1) + p4) * 256 + (((8 * e + 4 * hh + q4) ^ ((F & 6) << 1)) << 4) + ((t ^ (F & 1)) << 3);
  f32x16 sc[9];
  bf16x8 kf[9][2], vf[9][2];
  auto rdK = [&](int m) {
    const u32x2 a = lds_ld<u32x2>(L, oK + m * 1792), b = lds_ld<u32x2>(L, oK + m * 1792 + 512), c = lds_ld<u32x2>(L, oK + m * 1792 + 1024), d = lds_ld<u32x2>(L, oK3 + m * 1792);
    kf[m][0] = __builtin_bit_cast(bf16x8, u32x4{a[0], a[1], b[0], b[1]}); kf[m][1] = __builtin_bit_cast(bf16x8, u32x4{c[0], c[1], d[0], d[1]});
  };
  auto rdV = [&](int m) { vf[m][0] = trfrag(L, oV[0][0] + m * 2048, oV[0][1] + m * 2048); vf[m][1] = trfrag(L, oV[1][0] + m * 2048, oV[1][1] + m * 2048); };
  rdK(0); SB(); rdK(8); SB(); rdK(1); SB();
#pragma unroll
  for (int i = 0; i < 9; ++i) {
    const int m = i == 0 ? 0 : (i == 1 ? 8 : i - 1);
    if (i + 2 < 8) rdK(i + 2);
    sc[m] = mfma32(kf[m][1], qf[1], mfma32(kf[m][0], qf[0], f32x16{}));
    SB();
  }
  if (w >= 6) { vm<0>(); cnt_add(lb, C_V); }
  constexpr float NEG = -1e30f;
  if (ai < 2) {
    asm volatile("" ::: "memory");
    const int m0 = 8 - 4 * ai - aw;
#pragma unroll
    for (int m = 0; m < 8; ++m)
      if (m < m0) {
#pragma unroll
        for (int r = 0; r < 16; ++r) sc[m][r] = NEG;
      }
  }
  const int dq = (l & 31) - 4 * hh;
  u32 mk[8];
#pragma unroll
  for (int r = 0; r < 16; r += 2) {
    const bool c0 = (r & 3) + 8 * (r >> 2) <= dq, c1 = (r & 3) + 1 + 8 * (r >> 2) <= dq;
    sc[0][r] = c0 ? sc[8][r] : sc[0][r]; sc[0][r + 1] = c1 ? sc[8][r + 1] : sc[0][r + 1];
    mk[r >> 1] = c1 ? 0xffffffffu : (c0 ? 0xffffu : 0u);
  }
  cnt_wait(L, C_V, 2);
  rdV(1); rdV(2);
  SB();
  float mx0 = fmx(sc[0][0], sc[0][1]), mx1 = fmx(sc[0][2], sc[0][3]);
#pragma unroll
  for (int k = 1; k < 32; ++k) { mx0 = fmx(fmx(mx0, sc[k >> 2][4 * (k & 3)]), sc[k >> 2][4 * (k & 3) + 1]); mx1 = fmx(fmx(mx1, sc[k >> 2][4 * (k & 3) + 2]), sc[k >> 2][4 * (k & 3) + 3]); }
  mx0 = fmx(mx0, mx1);
  const auto ms = __builtin_amdgcn_permlane32_swap(__float_as_uint(mx0), __float_as_uint(mx0), false, false);
  const float mx = fmx(__uint_as_float(ms[0]), __uint_as_float(ms[1]));
  SB();
  f32x16 o = {};
#pragma unroll
  for (int i = 0; i < 8; ++i) {
    const int m = (i + 1) & 7;
    if (i < 5) rdV(i + 3); else if (i == 5) rdV(0); else if (i == 6) rdV(8);
    u32x4 p[2];
#pragma unroll
    for (int j = 0; j < 8; ++j) {
      f32x2 d = f32x2{sc[m][2 * j], sc[m][2 * j + 1]} - f32x2{mx, mx};
      PIN(d);
      p[j >> 2][j & 3] = pk(__builtin_amdgcn_exp2f(d[0]), __builtin_amdgcn_exp2f(d[1]));
    }
#pragma unroll
    for (int s = 0; s < 2; ++s) {
      if (m) o = mfma32(vf[m][s], __builtin_bit_cast(bf16x8, p[s]), o);
      else {
        const u32x4 k4 = {mk[4 * s], mk[4 * s + 1], mk[4 * s + 2], mk[4 * s + 3]};
        o = mfma32(vf[0][s], __builtin_bit_cast(bf16x8, p[s] & ~k4), o);
        o = mfma32(vf[8][s], __builtin_bit_cast(bf16x8, p[s] & k4), o);
      }
    }
    SB();
  }
  cnt_add(lb, C_ATT);
  const auto dn = __builtin_amdgcn_permlane32_swap(__float_as_uint(o[12]), __float_as_uint(o[12]), false, false);
  const float inv = __builtin_amdgcn_rcpf(__uint_as_float(dn[1]));
  const float in2 = hh ? 0.f : inv;
  const u32 so = ((ai * 4 + aw) * 144 + 112 + 2 * ah) * 1024;
  st16<16>(u32x4{pk(o[0] * inv, o[1] * inv), pk(o[2] * inv, o[3] * inv), pk(o[4] * inv, o[5] * inv), pk(o[6] * inv, o[7] * inv)}, rAC, l * 16 + so);
  st16<16>(u32x4{pk(o[8] * inv, o[9] * inv), pk(o[10] * inv, o[11] * inv), pk(o[12] * in2, o[13] * in2), pk(o[14] * in2, o[15] * in2)}, rAC, l * 16 + so + 1024);
  if (w == 4) STAMP(9);
  if (busy) {
    u32 vW[2];
    vW2(vW);
    dman<2>(lb + L_Q + aw * 2048, vW, rW2, (kstep(52) + aw) * (16 * 896) + cc * 128);
    vm<2>();
    if (last_of4(L, C_FB) && l == 0) st_flag(rCTL, WS_FB + (ai * 16 + ah) * FS, tag);
    cnt_wait(L, C_ATT, 4);
    cnt_wait(L, C_P6, 4);
    const rsrc_t rWo = mkrs(wo, 448 * 896);
#pragma unroll
    for (int k = 0; k < 6; ++k) if (k < 5 || aw < 3) dman<2>(lb + (4 * k + aw) * 2048, vW, rWo, (4 * k + aw) * (16 * 896) + cc * 128);
#pragma unroll
    for (int k = 0; k < 2; ++k) if (k == 0 || aw < 2) dman<2>(lb + L_WC + (4 * k + aw) * 2048, vW, rWo, (364 + 16 * (4 * k + aw)) * 896 + cc * 128);
    vm<0>();
    cnt_add(lb, C_LATE);
  } else {
    vm<0>();
    if (last_of4(L, C_FB)) {
      if (l == 0) st_flag(rCTL, WS_FB + (ai * 16 + ah) * FS, tag);
      if (blockIdx.x == 255) {
#pragma unroll 1
        for (int k = 0; k < 4; ++k) spin<32>(rCTL, WS_FB + (k * 64 + l) * FS, tag);
        if (l == 0) st_flag(rCTL, 0, tag);
      }
    }
  }
  FLUSH();
}

extern "C" void kernel_launch(void* const* d_in, const int* in_sizes, int n_in, void* d_out, int out_size, void* d_ws, size_t ws_size, hipStream_t stream) {
  fused_block<<<256, 512, 0, stream>>>((const char*)d_in[0], (const char*)d_in[1], (const char*)d_in[2], (const char*)d_in[4], (const char*)d_in[5],
                                       (const char*)d_in[6], (const char*)d_in[7], (char*)d_out, (char*)d_ws + WS_OFF);
}
```

```cpp
#include <hip/hip_runtime.h>
#include <hip/hip_bf16.h>
#include <stdint.h>

#define DI __device__ __forceinline__
#define LAS __attribute__((address_space(3)))
typedef unsigned u32;
typedef unsigned long long u64;
typedef __bf16 bf16x8 __attribute__((ext_vector_type(8)));
typedef __bf16 bf16x2 __attribute__((ext_vector_type(2)));
typedef short s16x4 __attribute__((ext_vector_type(4)));
typedef float f32x4 __attribute__((ext_vector_type(4)));
typedef float f32x2 __attribute__((ext_vector_type(2)));
typedef float f32x16 __attribute__((ext_vector_type(16)));
typedef unsigned u32x4 __attribute__((ext_vector_type(4)));
typedef unsigned u32x2 __attribute__((ext_vector_type(2)));
typedef __amdgpu_buffer_rsrc_t rsrc_t;

template <int N> struct IC { static constexpr int v = N; };
template <int B, int E, class F> DI void sfor(F&& f) { if constexpr (B < E) { f(IC<B>{}); sfor<B + 1, E>(f); } }

constexpr u32 FS = 128;
constexpr u32 WS_FH = FS, WS_FQ = WS_FH + 256 * FS, WS_FB = WS_FQ + 256 * FS, WS_FG = WS_FB + 256 * FS, WS_FP = WS_FG + 256 * FS, WS_FC = WS_FP + 448 * FS;
constexpr u32 WS_FS = WS_FC + 32 * FS, WS_FZ = WS_FS + 128 * FS, WS_CS = WS_FZ + 287 * FS, WS_ST = WS_CS + 16384, CTL_BYTES = WS_ST + 32768;
constexpr u32 WS_QKV = CTL_BYTES, QKV_BYTES = 64u * 336u * 256u;
constexpr u32 WS_AC = WS_QKV + QKV_BYTES, AC_BYTES = 64u * 144u * 1024u + 448u * 4096u;
constexpr u32 PX = 64u * 144u * 1024u;
constexpr u32 RING = 7 * 16384;
constexpr u32 L_ST = 40960, L_POLL = 45056, L_CSQ = 46080, L_CSH = 46848, L_CNT = 48896;
constexpr u32 L_K = 0, L_V = 21504, L_Q = RING + 5 * 8192;
constexpr u32 L_WC = 49152, L_RED = 0;
enum { C_DONE, C_LAND = 4, C_FQ = 6, C_FA, C_FG, C_KV, C_FB, C_WLO, C_WHI, C_P30, C_WO, C_ATT, C_LATE, C_PAIR0, C_PAIR1, C_CSH, C_FH, C_SQ, C_V, C_ST, C_CS0, C_N };
constexpr u32 stage_addr(int s) {
  return s < 14 ? RING + (s % 6) * 6144u : s < 19 ? (s - 14) * 8192u : s < 21 ? RING + (s - 19) * 8192u : s < 23 ? 6 * 16384u + (s - 21) * 8192u : s < 26 ? RING + (s - 21) * 8192u : RING + (s - 26) * 8192u;
}
constexpr int NWP = 24;

enum { K_NONE = 0, K_X, K_WA, K_BIAS, K_WCP };
constexpr int OP(int k, int a, int e = 0) { return k * 256 + 2 * a + e; }
constexpr int opn(int op) { int k = op / 256, a = (op % 256) / 2; return k == K_NONE ? 0 : k == K_X ? 4 : k == K_WA ? (a < 14 ? 3 : 4) : k == K_WCP ? 2 : 1; }
#define CAP_1 8
#define P_NPRO 8
#define P_DA 12
#define P_TB 16
#define P_TO 44
#define P_FQ 18
#define PRIO_C 0
#define PRIO_B 0
#define ROTA 0
#define WS_OFF 0
#define CODE_KB 39
#define PF_DELAY 20
#define P_TFP 66
#define SLP 2
#define LSLP 1
#define GATE_K 0
#define OWN1 4
#define CLR0 4
#define TOUCH 1
#define HNOW 2
#define CLRQ 0
#define HOLD 4
#define IDLE_SLP 9
#define T_PP 62
#define QNOW 6
#define P_NPRE 6
constexpr int NPRE = P_NPRE;
constexpr int MAXO = 24, NPRO = P_NPRO, DA = P_DA, TB = P_TB, S_FQ = P_FQ, T_WH = 20, T_WO = 33, T_O = P_TO;
struct Sched {
  int pro[NPRO], opA[15][MAXO];
  int wA[16];
  int wL[2];
  int wC[56], a0[56], a1[56];
  int ok;
};
constexpr int cl(int n) { return n > 63 ? 63 : n; }
constexpr Sched make_sched() {
  Sched r{};
  r.ok = 1;
  int n = 0, iW[26] = {}, iX[7] = {};
  int lo[128] = {}, le[128] = {}, nl = 0;
  for (int s = 0; s < 26; ++s) {
    if (s < 14 && !(s & 1)) { lo[nl] = OP(K_X, s >> 1, 0); le[nl++] = -1; lo[nl] = OP(K_X, s >> 1, 1); le[nl++] = -1; }
    lo[nl] = OP(K_WA, s); le[nl++] = s < 14 ? s - 5 : (s < 19 ? 5 : (s == 21 || s == 22 ? 13 : 14));
  }
  for (int k = 0; k < NWP / 2; ++k) { lo[nl] = OP(K_WCP, k); le[nl++] = 7 + 2 * (k >> 2); }
  bool done[128] = {};
  auto note = [&](int op) { n += opn(op); int k = op / 256, a = (op % 256) / 2; if (k == K_X) iX[a] = (op & 1) ? n : 0; if (k == K_WA) iW[a] = n; };
  for (int k = 0; k < NPRO; ++k) { if (k == NPRE) r.wA[0] = cl(n - iW[0]); r.pro[k] = lo[k]; done[k] = true; note(lo[k]); }
  for (int s = 0; s < 15; ++s) {
    int need = iW[s + 1]; if (!need) r.ok = 0;
    if (s + 1 <= 13) { int v = iX[(s + 1) >> 1]; if (!v) r.ok = 0; if (v > need) need = v; }
    r.wA[s + 1] = cl(n - need);
    int cap = s == 14 ? 64 : CAP_1, c = 0, used = 0;
    for (int k = 0; k < nl; ++k) if (!done[k] && le[k] <= s && used + opn(lo[k]) <= cap && c < MAXO) { done[k] = true; r.opA[s][c++] = lo[k]; used += opn(lo[k]); note(lo[k]); if (s == 14 && c == 2) r.wL[0] = cl(n - iW[18]); }
  }
  r.wL[1] = cl(n - iW[25]);
  if (r.opA[14][0] != OP(K_WA, 19)) r.ok = 0;
  for (int k = 0; k < nl; ++k) if (!done[k]) r.ok = 0;
  n = 0;
  int iA[56] = {}, iP = 0, iH = 0, iO = 0, nA = 8 + DA;
  for (int m = 8; m < 8 + DA; ++m) { n += 1; iA[m] = n; }
  for (int s = 8; s < 56; ++s) {
    int need = iA[s]; if (!need) r.ok = 0;
    if (s == TB && iP > need) need = iP;
    if (s == T_WH && iH > need) need = iH;
    if (s == T_WO + 12 && iO > need) need = iO;
    r.wC[s] = cl(n - need);
    if (s == TB - 3) { n += 1; iP = n; }
    r.a0[s] = nA;
    for (int c = 0; c < 2 && nA < 56 && nA <= s + DA && (nA < 32 || s >= TB); ++c) { n += 1; iA[nA++] = n; }
    r.a1[s] = nA;
    if (s < 15) { n += 2; iH = n; }
    if (s >= T_WO && s < T_WO + 7) { n += 2; iO = n; }
    if (s >= T_O && s < T_O + 8) n += 2;
  }
  return r;
}
constexpr Sched SC = make_sched();
static_assert(SC.ok, "schedule");

DI rsrc_t mkrs(const void* p, u32 bytes) { return __builtin_amdgcn_make_buffer_rsrc((void*)p, 0, bytes, 0x00020000); }
template <int N> DI void dman(u32 lds, const u32* v, rsrc_t rs, u32 soff) {
  if constexpr (N == 4) asm volatile("s_mov_b32 m0, %0\n\ts_nop 0\n\tbuffer_load_dwordx4 %1, %5, %6 offen lds\n\tbuffer_load_dwordx4 %2, %5, %6 offen offset:1024 lds\n\t"
    "buffer_load_dwordx4 %3, %5, %6 offen offset:2048 lds\n\tbuffer_load_dwordx4 %4, %5, %6 offen offset:3072 lds" ::"s"(lds), "v"(v[0]), "v"(v[1]), "v"(v[2]), "v"(v[3]), "s"(rs), "s"(soff) : "memory");
  if constexpr (N == 3) asm volatile("s_mov_b32 m0, %0\n\ts_nop 0\n\tbuffer_load_dwordx4 %1, %4, %5 offen lds\n\tbuffer_load_dwordx4 %2, %4, %5 offen offset:1024 lds\n\t"
    "buffer_load_dwordx4 %3, %4, %5 offen offset:2048 lds" ::"s"(lds), "v"(v[0]), "v"(v[1]), "v"(v[2]), "s"(rs), "s"(soff) : "memory");
  if constexpr (N == 2) asm volatile("s_mov_b32 m0, %0\n\ts_nop 0\n\tbuffer_load_dwordx4 %1, %3, %4 offen lds\n\tbuffer_load_dwordx4 %2, %3, %4 offen offset:1024 lds" ::"s"(lds), "v"(v[0]), "v"(v[1]), "s"(rs), "s"(soff) : "memory");
  if constexpr (N == 1) asm volatile("s_mov_b32 m0, %0\n\ts_nop 0\n\tbuffer_load_dwordx4 %1, %2, %3 offen lds" ::"s"(lds), "v"(v[0]), "s"(rs), "s"(soff) : "memory");
}
DI void dma1792c(u32 lds, u32 voff, rsrc_t rs, u32 soff) {
  asm volatile("s_mov_b32 m0, %0\n\ts_nop 0\n\tbuffer_load_dwordx4 %1, %2, %3 offen sc1 lds\n\ts_mov_b32 exec_hi, 0xffff\n\t"
               "buffer_load_dwordx4 %1, %2, %3 offen offset:1024 sc1 lds\n\ts_mov_b32 exec_hi, -1" ::"s"(lds), "v"(voff), "s"(rs), "s"(soff) : "memory");
}
DI void dma4(u32 lds, u32 voff, rsrc_t rs, u32 soff) {
  asm volatile("s_mov_b32 m0, %0\n\ts_nop 0\n\tbuffer_load_dword %1, %2, %3 offen lds" ::"s"(lds), "v"(voff), "s"(rs), "s"(soff) : "memory");
}
template <bool C, int IMM = 0> DI u32x4 ld16_async(u32 voff, rsrc_t rs, u32 soff) {
  u32x4 r;
  if constexpr (C) asm volatile("buffer_load_dwordx4 %0, %1, %2, %3 offen offset:%4 sc1" : "=v"(r) : "v"(voff), "s"(rs), "s"(soff), "n"(IMM) : "memory");
  else asm volatile("buffer_load_dwordx4 %0, %1, %2, %3 offen" : "=v"(r) : "v"(voff), "s"(rs), "s"(soff) : "memory");
  return r;
}
DI u32x2 ld8c_async(u32 voff, rsrc_t rs) {
  u32x2 r; asm volatile("buffer_load_dwordx2 %0, %1, %2, 0 offen sc1" : "=v"(r) : "v"(voff), "s"(rs) : "memory"); return r;
}
template <int N> DI void wait_bar() { asm volatile("s_waitcnt vmcnt(%0) lgkmcnt(0)\n\ts_barrier" ::"n"(N) : "memory"); }
template <int N> DI void vm_bar() { asm volatile("s_waitcnt vmcnt(%0)\n\ts_barrier" ::"n"(N) : "memory"); }
template <int N> DI void vm() { asm volatile("s_waitcnt vmcnt(%0)" ::"n"(N) : "memory"); }
DI void bar() { asm volatile("s_barrier" ::: "memory"); }
#define PIN(x) asm volatile("" : "+v"(x))
#define SB() __builtin_amdgcn_sched_barrier(0)
DI f32x4 mfma(bf16x8 a, bf16x8 b, f32x4 c) { return __builtin_amdgcn_mfma_f32_16x16x32_bf16(a, b, c, 0, 0, 0); }
DI f32x16 mfma32(bf16x8 a, bf16x8 b, f32x16 c) { return __builtin_amdgcn_mfma_f32_32x32x16_bf16(a, b, c, 0, 0, 0); }
DI u32 pk(float a, float b) { bf16x2 v = {(__bf16)a, (__bf16)b}; return __builtin_bit_cast(u32, v); }
DI float fmx(float a, float b) { return __builtin_elementwise_maximum(a, b); }
DI float rbf(float a) { return (float)(__bf16)a; }
DI float bflo(u32 v) { return __uint_as_float(v << 16); }
DI float bfhi(u32 v) { return __uint_as_float(v & 0xffff0000u); }
DI float dot2(u32 a, u32 b, float c) { return __builtin_amdgcn_fdot2_f32_bf16(__builtin_bit_cast(bf16x2, a), __builtin_bit_cast(bf16x2, b), c, false); }
DI float sum8(float v) {
  v += __builtin_bit_cast(float, __builtin_amdgcn_update_dpp(0, __builtin_bit_cast(int, v), 0xb1, 0xf, 0xf, true));
  v += __builtin_bit_cast(float, __builtin_amdgcn_update_dpp(0, __builtin_bit_cast(int, v), 0x4e, 0xf, 0xf, true));
  v += __builtin_bit_cast(float, __builtin_amdgcn_update_dpp(0, __builtin_bit_cast(int, v), 0x141, 0xf, 0xf, true));
  return v;
}
template <class T> DI T lds_ld(const LAS char* base, u32 off) { return *(const LAS T*)(base + off); }
template <class T> DI void lds_st(LAS char* base, u32 off, T v) { *(LAS T*)(base + off) = v; }
DI bf16x8 trfrag(LAS char* base, u32 off0, u32 off1) {
  s16x4 lo = __builtin_amdgcn_ds_read_tr16_b64_v4i16((LAS s16x4*)(base + off0));
  s16x4 hi = __builtin_amdgcn_ds_read_tr16_b64_v4i16((LAS s16x4*)(base + off1));
  return __builtin_bit_cast(bf16x8, __builtin_shufflevector(lo, hi, 0, 1, 2, 3, 4, 5, 6, 7));
}
#define ST_AUX 16
template <int AUX> DI void st16(u32x4 v, rsrc_t rs, u32 voff) { __builtin_amdgcn_raw_buffer_store_b128(v, rs, voff, 0, AUX ? ST_AUX : 0); }
DI void st_flag(rsrc_t rc, u32 off, u64 v) { __builtin_amdgcn_raw_buffer_store_b64(u32x2{(u32)v, (u32)(v >> 32)}, rc, off, 0, 16); }
DI bool tagged(u32x2 v, u64 tag) { return __all(v[0] == (u32)tag && v[1] == (u32)(tag >> 32)); }
template <int SLEEP> DI void spin(rsrc_t rc, u32 off, u64 tag) {
#pragma unroll 1
  for (u32 n = 0; n < (1u << 17); ++n) {
    asm volatile("" ::: "memory");
    if (tagged(__builtin_amdgcn_raw_buffer_load_b64(rc, off, 0, 16), tag)) break;
    __builtin_amdgcn_s_sleep(SLEEP);
  }
  asm volatile("" ::: "memory");
}
DI u32 cnt_ld(LAS char* L, int k) { return __hip_atomic_load((LAS u32*)(L + L_CNT + 4 * k), __ATOMIC_RELAXED, __HIP_MEMORY_SCOPE_WORKGROUP); }
DI void cnt_wait(LAS char* L, int k, u32 v) {
#pragma unroll 1
  for (u32 n = 0; n < (1u << 20); ++n) {
    if ((u32)__builtin_amdgcn_readfirstlane(cnt_ld(L, k)) >= v) break;
    __builtin_amdgcn_s_sleep(LSLP);
  }
  asm volatile("" ::: "memory");
}
DI void prog_st(LAS char* L, int k, u32 v) { asm volatile("" ::: "memory"); __hip_atomic_store((LAS u32*)(L + L_CNT + 4 * k), v, __ATOMIC_RELAXED, __HIP_MEMORY_SCOPE_WORKGROUP); asm volatile("" ::: "memory"); }
DI u32 land_ld(LAS char* L) { const u64 v = __hip_atomic_load((LAS u64*)(L + L_CNT + 4 * C_LAND), __ATOMIC_RELAXED, __HIP_MEMORY_SCOPE_WORKGROUP); return min((u32)v, (u32)(v >> 32)); }
DI u32 done_ld(LAS char* L) {
  const u64 a = __hip_atomic_load((LAS u64*)(L + L_CNT), __ATOMIC_RELAXED, __HIP_MEMORY_SCOPE_WORKGROUP), b = __hip_atomic_load((LAS u64*)(L + L_CNT + 8), __ATOMIC_RELAXED, __HIP_MEMORY_SCOPE_WORKGROUP);
  return min(min((u32)a, (u32)(a >> 32)), min((u32)b, (u32)(b >> 32)));
}
template <bool DONE> DI void prog_wait(LAS char* L, u32 v) {
#pragma unroll 1
  for (u32 n = 0; n < (1u << 20); ++n) {
    if ((u32)__builtin_amdgcn_readfirstlane(DONE ? done_ld(L) : land_ld(L)) >= v) break;
    __builtin_amdgcn_s_sleep(LSLP);
  }
  asm volatile("" ::: "memory");
}
DI void cnt_add(u32 lb, int k) {
  asm volatile("s_mov_b64 exec, 1\n\tds_add_u32 %0, %1\n\ts_mov_b64 exec, -1" ::"v"(lb + L_CNT + 4 * k), "v"(1u) : "memory");
}
template <int N> DI bool vm_le() {
  u32 r; asm volatile("s_getreg_b32 %0, hwreg(HW_REG_IB_STS)" : "=s"(r)::"memory");
  return (r & 0xc0000fu) <= (u32)N;
}
DI bool last_of4(LAS char* L, int k) {
  u32 old = 0;
  asm volatile("" ::: "memory");
  if (__builtin_amdgcn_mbcnt_hi(~0u, __builtin_amdgcn_mbcnt_lo(~0u, 0u)) == 0) old = __hip_atomic_fetch_add((LAS u32*)(L + L_CNT + 4 * k), 1u, __ATOMIC_RELAXED, __HIP_MEMORY_SCOPE_WORKGROUP);
  asm volatile("" ::: "memory");
  return __builtin_amdgcn_readfirstlane(old) == 3;
}
#define STAMP(k)
#define FLUSH()

__global__ __launch_bounds__(512, 2) void fused_block(const char* __restrict__ x, const char* __restrict__ wqkv, const char* __restrict__ wo,
                                                      const char* __restrict__ w1, const char* __restrict__ b1, const char* __restrict__ w2,
                                                      const char* __restrict__ b2, char* __restrict__ out, char* ws) {
  __shared__ __attribute__((aligned(1024))) char smem[163840];
  asm volatile("" : "+s"(x), "+s"(wqkv), "+s"(wo), "+s"(w1), "+s"(b1), "+s"(w2), "+s"(b2), "+s"(out), "+s"(ws));
  u64 pc; asm volatile("s_getpc_b64 %0" : "=s"(pc));
  LAS char* const L = (LAS char*)smem;
  const u32 lb = (u32)(uintptr_t)smem;
  int l = threadIdx.x & 63, g, q4, p4, l15;
  auto lanes = [&] { PIN(l); g = l >> 4; q4 = (l >> 2) & 3; p4 = l & 3; l15 = l & 15; };
  lanes();
  int w = __builtin_amdgcn_readfirstlane(threadIdx.x >> 6);
  asm volatile("s_nop 4" : "+s"(w));
  const int x8 = blockIdx.x & 7, rk = blockIdx.x >> 3;
  const bool busy = rk < 28;
  if (w == 0) STAMP(0);
  const rsrc_t rQKV = mkrs(ws + WS_QKV, QKV_BYTES), rAC = mkrs(ws + WS_AC, AC_BYTES), rCTL = mkrs(ws, CTL_BYTES);
  const rsrc_t rX = mkrs(x, 2048 * 896), rW2 = mkrs(w2, 1792 * 896);
  const int kh = x8 >> 2, cc = rk % 7, rb = (x8 & 3) * 4 + rk / 7, cb = kh * 7 + cc;
  const int ai = rk & 15, ah = 2 * x8 + (rk >> 4);
  const int ro6 = ROTA ? (cc + ((3 * (rk / 7)) >> 1)) % 6 : 0;
  auto rotl = [&](int j) { const int t = j + ro6; return j == 0 ? 0 : (t > 6 ? t - 6 : t); };
  auto kstep = [&](int p) {
    if (p < 8) return 8 * cb + p;
    const int t = cc + 1 + ((p - 8) % 24) / 4;
    return 8 * (7 * kh + (t >= 7 ? t - 7 : t)) + (p & 3) + (p >= 32 ? 4 : 0);
  };
  if (busy && (w == 4 || w == 5)) {
    u32 v[4];
#pragma unroll
    for (int j = 0; j < 4; ++j) v[j] = (8 * j + (l >> 3)) * 896 + (((l & 7) ^ ((j & 1) * 4 + g)) << 4) - 1024 * j;
    dman<4>(lb + (w & 1) * 8192, v, rX, (rb * 128 + 64 * (w & 1)) * 896);
    dman<4>(lb + (w & 1) * 8192 + 4096, v, rX, (rb * 128 + 64 * (w & 1) + 32) * 896);
    u32 vq[3];
#pragma unroll
    for (int j = 0; j < 3; ++j) { const u32 n = 64 * j + l; vq[j] = (n / 12) * 2688 + (n % 12) * 16 - 1024 * j; }
#pragma unroll
    for (int a = 0; a < 2; ++a) {
      dman<3>(lb + stage_addr(a) + (w & 1) * 3072, vq, mkrs(wqkv, 448 * 2688), cb * 192 + (16 * (w & 1) + 32 * a) * 2688);
    }
  }
  if (w == 6) dma4(lb + L_Q + 512, (rk * 32 + (l & 31)) * 64, mkrs((const void*)(pc & ~255ull), CODE_KB * 1024), 0);
  u32x4 d[14];
  if (busy && w == 6 && cb < 8) {
#pragma unroll
    for (int k = 0; k < 14; ++k) d[k] = ld16_async<false>((rb * 128 + cb * 16 + (l >> 3)) * 896 + (l & 7) * 16, rX, (k / 7) * 7168 + (k % 7) * 128);
  }
  if (busy && w == 7) {
    const int r = rb * 28 + (l >> 1);
    if (l < 56) dma4(lb + L_Q, r * 2688 + cb * 192 + (l & 1) * 188, mkrs(wqkv, 448 * 2688), 0);
  }
  SB();
  u64 tag;
  auto epoch = [&] { asm volatile("s_load_dwordx2 %0, %1, 0x0\n\ts_waitcnt lgkmcnt(0)" : "=s"(tag) : "s"(ws) : "memory"); tag += 1; };
  auto vW2 = [&](u32* v) { v[0] = (l >> 3) * 896 + (((l & 7) ^ ((g & 1) << 2)) << 4); v[1] = v[0] + 7168 - 1024; };

  if (w < 4) {
    if (!busy) {
      const int q = (rk - 28) * 4 + w;
      bar();
      for (int k = 0; k < PF_DELAY; ++k) __builtin_amdgcn_s_sleep(2);
      const rsrc_t rW1 = mkrs(w1, 448 * 3584), rWo = mkrs(wo, 448 * 896);
      const u32 v = l * 16;
#pragma unroll 1
      for (int k = 0; k < 49; ++k) {
        int r = k * 16 + q + (x8 & 3) * 196; r = r >= 784 ? r - 784 : r;
        dman<1>(lb + L_WC + w * 16384 + (k & 15) * 1024, &v, rW2, kh * 802816 + r * 1024);
        vm<10>();
      }
#pragma unroll 1
      for (int k = 0; k < 25; ++k) { dman<1>(lb + L_WC + w * 16384 + (k & 15) * 1024, &v, rWo, (k * 16 + q) * 1024); vm<10>(); }
      vm<0>();
      FLUSH();
      return;
    }
    constexpr int PF = 4;
    u32 oX[4], oH[2], oQ[3];
#pragma unroll
    for (int p = 0; p < 4; ++p) oX[p] = (4 * w + ((l & 31) >> 3)) * 1024 + (l & 7) * 128 + (((2 * p + (g >> 1)) ^ (l15 >> 1)) << 4);
    const int rq = w == 3 ? 0 : w;
#pragma unroll
    for (int i = 0; i < 3; ++i) oQ[i] = RING + (8 * (g >> 1) + q4) * 192 + ((i + rq) % 3) * 64 + (g & 1) * 32 + p4 * 8;
#pragma unroll
    for (int i = 0; i < 2; ++i) oH[i] = RING + (g >> 1) * 1024 + q4 * 128 + ((i ^ (q4 >> 1)) << 6) + (g & 1) * 32 + p4 * 8;
    const u32 selv = l15 == (g & 1) ? 0x3f803f80u : 0u;
    const bf16x8 sel = __builtin_bit_cast(bf16x8, u32x4{selv, selv, selv, selv});
    bf16x8 xf[28], wf[8];
    f32x16 aH[2], aQ[3] = {};
    f32x4 cs = {};
    u32x2 ch[2];
    float rs;
    bf16x8 lnb;
    u32x4 hA[72], qp[6];
    auto rdW = [&](auto SS, auto II) {
      constexpr int s = decltype(SS)::v, i = decltype(II)::v;
      constexpr int slot = (s < 14 ? s * 6 + i : 84 + i) % 8;
      if constexpr (s < 14) { const u32 o = oQ[i % 3] + (stage_addr(s) - RING) + (i / 3) * 3072; wf[slot] = trfrag(L, o, o + 768); }
      else if constexpr (s < 28) { const u32 o = oH[i & 1] + (stage_addr(s) - RING) + (i >> 1) * 2048; wf[slot] = trfrag(L, o, o + 512); }
    };
    auto rdX = [&](auto SS, auto EE) { constexpr int k = 2 * decltype(SS)::v + decltype(EE)::v; if constexpr (k < 28) xf[k] = lds_ld<bf16x8>(L, oX[k & 3] + (k >> 2) * 16384); };
    auto rdcs = [&](int half) {
#pragma unroll
      for (int i = 0; i < 2; ++i) ch[i] = lds_ld<u32x2>(L, L_CSH + (half * 64 + i * 32 + (l & 31)) * 8);
    };
    auto hpack = [&](int half, int c) {
      u32x4 hp;
#pragma unroll
      for (int j = 0; j < 4; ++j) {
        const float t0 = aH[c >> 1][8 * (c & 1) + 2 * j], t1 = aH[c >> 1][8 * (c & 1) + 2 * j + 1];
        hp[j] = pk(t0 * fmx(t0, 0.f), t1 * fmx(t1, 0.f));
      }
      hA[half * 4 + c] = hp;
    };
    auto hstore = [&](int m) { st16<16>(hA[m], rAC, l * 16 + ((rb * 4 + w) * 144 + cb * 8 + m) * 1024); };
    auto qpack = [&](int c) {
      const int i = c >> 1, e = c & 1, tq = 6 * cb + 2 * ((i + rq) % 3) + e;
      const float m = tq < 28 ? rs * (0.18898223650461361f * 1.4426950408889634f) : rs;
      const auto s0 = __builtin_amdgcn_permlane16_swap(pk(aQ[i][8 * e] * m, aQ[i][8 * e + 1] * m), pk(aQ[i][8 * e + 4] * m, aQ[i][8 * e + 5] * m), false, false);
      const auto s1 = __builtin_amdgcn_permlane16_swap(pk(aQ[i][8 * e + 2] * m, aQ[i][8 * e + 3] * m), pk(aQ[i][8 * e + 6] * m, aQ[i][8 * e + 7] * m), false, false);
      qp[c] = u32x4{s0[0], s1[0], s0[1], s1[1]};
    };
    auto qstore = [&](int c) {
      const int fl = ((g & 1) << 1) | (g >> 1), tq = 6 * cb + 2 * (((c >> 1) + rq) % 3) + (c & 1);
      u32x4 v = qp[c];
      u32 vo = fl * 256 + l15 * 16;
      if (tq >= 56) {
        asm volatile("" ::: "memory");
        if (fl & 1) v = u32x4{v[2], v[3], v[0], v[1]};
        vo ^= (2 * (tq & 1) + (fl >> 1)) << 6;
      }
      st16<16>(v, rQKV, vo + ((rb * 4 + w) * 336 + 4 * tq) * 256);
    };
    bool sigq = false, siga = false;
    const bool giver = (w >> 1) != kh;
    bar();
    STAMP(1);
    rdX(IC<0>{}, IC<0>{}); rdX(IC<0>{}, IC<1>{});
    sfor<0, PF>([&](auto I) { rdW(IC<0>{}, I); });
    sfor<0, 28>([&](auto SS) {
      constexpr int s = decltype(SS)::v, NT = s < 14 ? 6 : 8;
      if constexpr (s == 14) wait_bar<63>();
      else if constexpr (s < 14) bar();
      if constexpr (s == 13) lanes();
      if constexpr (s == 14) {
        STAMP(2);
        u32 cq[3];
#pragma unroll
        for (int i = 0; i < 3; ++i) cq[i] = lds_ld<u32>(L, L_CSQ + (((i + rq) % 3) * 32 + (l & 31)) * 4);
        const u32 rdy = cnt_ld(L, C_ST);
        u32 st = lds_ld<u32>(L, L_ST + (w * 32 + (l & 31)) * 16);
        if (!__builtin_amdgcn_readfirstlane(rdy)) { cnt_wait(L, C_ST, 1); st = lds_ld<u32>(L, L_ST + (w * 32 + (l & 31)) * 16); }
        SB();
        lnb = __builtin_bit_cast(bf16x8, u32x4{l < 32 ? (st & 0xffffu) * 0x10001u : 0u, l < 32 ? st >> 16 : 0u, 0u, 0u});
        rs = __builtin_amdgcn_rcpf(bfhi(st));
#pragma unroll
        for (int i = 0; i < 3; ++i) aQ[i] = mfma32(__builtin_bit_cast(bf16x8, u32x4{cq[i], 0u, 0u, 0u}), lnb, aQ[i]);
        SB();
#pragma unroll
        for (int c = 0; c < 6; ++c) { qpack(c); if (c % (6 / QNOW) == 6 / QNOW - 1) qstore(c / (6 / QNOW)); SB(); }
        STAMP(3);
      }
      if constexpr (s == 21) {
        rdcs(1);
        SB();
#pragma unroll
        for (int c = 0; c < 4; ++c) { hpack(0, c); if (c < HNOW) hstore(c); SB(); }
      }
      u32 land;
      sfor<0, NT>([&](auto II) {
        constexpr int i = decltype(II)::v, slot = (s < 14 ? s * 6 + i : 84 + i) % 8;
        constexpr u32 need = s == 15 ? 1 : s == 18 ? 2 : s == 22 ? 3 : s == 25 ? 4 : 0;
        if constexpr (need && i == 1) land = land_ld(L);
        if constexpr (need && i == NT - PF) {
          if ((u32)__builtin_amdgcn_readfirstlane(land) < need) prog_wait<false>(L, need);
          asm volatile("" ::: "memory");
        }
        if constexpr (i + PF < NT) rdW(SS, IC<i + PF>{});
        else rdW(IC<s + 1>{}, IC<i + PF - NT>{});
        if constexpr (i < 2) rdX(IC<s + 1>{}, II);
        if constexpr (s < 14) {
          aQ[i % 3] = mfma32(wf[slot], xf[2 * s + i / 3], aQ[i % 3]);
          if constexpr (i % 3 == 0) cs = mfma(wf[slot], sel, cs);
          if constexpr (s == 13 && i == 4) {
            u32x4 v;
#pragma unroll
            for (int j = 0; j < 4; ++j) { const float hi = rbf(cs[j]); v[j] = pk(hi, cs[j] - hi); }
            if (l15 < 2) lds_st<u32x4>(L, L_CSQ + w * 128 + (l15 * 16 + 4 * g) * 4, v);
          }
        } else {
          constexpr int k = 4 * ((s - 14) % 7) + (i >> 1), m = (s < 21 ? QNOW : HNOW) + 2 * ((s - 14) % 7) + (i >> 2);
          if constexpr (k == 0 && s == 21) aH[i] = mfma32(__builtin_bit_cast(bf16x8, u32x4{ch[i][0], ch[i][1], 0u, 0u}), lnb, f32x16{});
          aH[i & 1] = mfma32(wf[slot], xf[k], s == 14 && k == 0 ? f32x16{} : aH[i & 1]);
          if constexpr (s == 19 && i == 7) { cnt_wait(L, C_CSH, 1); rdcs(0); }
          if constexpr (s == 20 && (i == 2 || i == 3)) aH[i & 1] = mfma32(__builtin_bit_cast(bf16x8, u32x4{ch[i & 1][0], ch[i & 1][1], 0u, 0u}), lnb, aH[i & 1]);
          if constexpr ((i & 3) == 1 && s < 21 && m < 6) qstore(m);
          if constexpr ((i & 3) == 1 && s >= 21 && m < 4) hstore(m);
        }
        SB();
      });
      if constexpr (s == 18 || s == 20 || s == 27) prog_st(L, C_DONE + w, s);
      if constexpr (s >= 14 && s <= S_FQ && QNOW + 2 * (s - 14) >= 6) { if (!sigq && (s == S_FQ || vm_le<0>())) { vm<0>(); cnt_add(lb, C_FQ); sigq = true; } }
      if constexpr (s >= 21 && HNOW + 2 * (s - 21) >= 4) { if (!siga && vm_le<0>()) { vm<0>(); cnt_add(lb, C_FA); siga = true; } }
    });
    STAMP(4);
    lanes();
    const u32 vFG = WS_FG + (rb * 16 + 7 * kh + (l < 7 ? l : 0)) * FS;
#pragma unroll
    for (int c = 0; c < 4; ++c) hpack(1, c);
    SB();
    if (!siga) { vm<0>(); cnt_add(lb, C_FA); }
#pragma unroll
    for (int m = 4; m < 8; ++m) hstore(m);
    STAMP(5);
    { const u32x2 t = lds_ld<u32x2>(L, L_CNT + 112); tag = (u64)(u32)__builtin_amdgcn_readfirstlane(t[0]) | (u64)(u32)__builtin_amdgcn_readfirstlane(t[1]) << 32; }
    u32x2 polled;
    u32 vW[2];
    vW2(vW);
    const u32 vA = l * 16, sA = (rb * 4 + w) * 144, sO = ((rb * 4 + (giver ? w ^ 2 : w)) * 144 + (giver ? 128 : 112)) * 1024;
    u32 sN[6];
#pragma unroll
    for (int j = 0; j < 6; ++j) { sN[j] = (sA + kstep(8 + 4 * j)) * 1024; asm volatile("" : "+s"(sN[j])); }
    const u32 vB = vA + 4096;
    auto ldA = [&](auto M) { constexpr int m = decltype(M)::v; hA[m] = ld16_async<true, (m & 3) * 1024>(m >= 32 ? vB : vA, rAC, sN[((m - 8) % 24) / 4]); };
    auto ldO = [&](auto M) { constexpr int m = decltype(M)::v; hA[m] = ld16_async<true, (m & 3) * 1024>(vA, rAC, sO + ((m - 56) & ~3) * 1024); };
    const rsrc_t rWo = mkrs(wo, 448 * 896);
    u32 oW[2], oO[2];
#pragma unroll
    for (int b = 0; b < 2; ++b) { oW[b] = (4 * (g >> 1) + q4) * 128 + ((b ^ (q4 >> 1)) << 6) + (g & 1) * 32 + p4 * 8; oO[b] = oW[b] + L_WC + (giver ? 8 * 3584 : 0); }
    bf16x8 wc[2][2];
    auto rdC = [&](int m) {
#pragma unroll
      for (int b = 0; b < 2; ++b) {
        const u32 o = m < 56 ? oW[b] + L_WC + m * 2048 : oO[b] + ((m - 56) >> 1) * 3584 + (m & 1) * 2048;
        wc[m & 1][b] = trfrag(L, o, o + 1024);
      }
    };
    f32x16 acc[2];
    u32x4 xres[2], bres, pp[4];
    auto step = [&](auto N) {
      constexpr int n = decltype(N)::v;
      if constexpr (n + 1 < 72) rdC(n + 1);
      SB();
      acc[0] = mfma32(wc[n & 1][0], __builtin_bit_cast(bf16x8, hA[n]), n == 0 ? f32x16{} : acc[0]);
      SB();
      if constexpr (n >= 8 && n < 56) sfor<SC.a0[n], SC.a1[n]>(ldA);
      if constexpr (n >= 8 && n < 15) dman<2>(lb + L_WC + (NWP + 4 * (n - 8) + w) * 2048, vW, rW2, (kstep(NWP + 4 * (n - 8)) + w) * (16 * 896) + cc * 128);
      if constexpr (n >= T_WO && n < T_WO + 7) dman<2>(lb + L_WC + (4 * (n - T_WO) + w) * 2048, vW, rWo, (4 * (n - T_WO) + w) * (16 * 896) + cc * 128);
      if constexpr (n == T_WO) { if (w == 0) dman<2>(lb + L_WC + 28 * 2048, vW, rWo, 28 * (16 * 896) + cc * 128); }
      acc[1] = mfma32(wc[n & 1][1], __builtin_bit_cast(bf16x8, hA[n]), n == 0 ? f32x16{} : acc[1]);
      SB();
    };
    cnt_wait(L, C_WLO, 2);
    rdC(0);
    sfor<0, OWN1>(step);
    cnt_wait(L, C_FH, 1);
    STAMP(6);
    __builtin_amdgcn_s_setprio(PRIO_C);
    sfor<8, 8 + DA>(ldA);
    prog_wait<true>(L, 27);
    sfor<OWN1, 8>(step);
    sfor<8, 56>([&](auto N) {
      constexpr int n = decltype(N)::v;
      asm volatile("s_waitcnt vmcnt(%1)" : "+v"(hA[n]) : "n"(SC.wC[n]) : "memory");
      if constexpr (n == 30) cnt_add(lb, C_P30);
      if constexpr (n == T_WO) cnt_wait(L, C_P30, 4);
      if constexpr (n == T_WO + 12) cnt_add(lb, C_WO);
      if constexpr (n == 55) cnt_wait(L, C_WO, 4);
      if constexpr (n == 8) { if (last_of4(L, C_FG) && l == 0) st_flag(rCTL, WS_FG + (rb * 16 + cb) * FS, tag); }
      if constexpr (n == T_WH) cnt_add(lb, C_WHI);
      if constexpr (n == TB - 3) polled = ld8c_async(vFG, rCTL);
      if constexpr (n == TB) { PIN(polled); if (!tagged(polled, tag)) spin<SLP>(rCTL, vFG, tag); }
      if constexpr (n == NWP - 1) cnt_wait(L, C_WHI, 4);
      if constexpr (n == 51) cnt_wait(L, C_LATE, 4);
      step(N);
      if constexpr (n >= T_O && n < T_O + 8) { ldO(IC<56 + 2 * (n - T_O)>{}); ldO(IC<57 + 2 * (n - T_O)>{}); }
    });
    acc[0] *= rs * rs; acc[1] *= rs * rs;
#pragma unroll
    for (int k = 0; k < 4; ++k) {
      const f32x16& a = acc[k >> 1];
      const int o = 8 * (k & 1);
      st16<16>(u32x4{pk(a[o], a[o + 1]), pk(a[o + 2], a[o + 3]), pk(a[o + 4], a[o + 5]), pk(a[o + 6], a[o + 7])}, rAC, (giver ? PX + ((rb * 7 + cc) * 4 + w) * 4096 : 0xffff0000u) + k * 1024 + l * 16);
    }
    if (giver) { acc[0] = f32x16{}; acc[1] = f32x16{}; }
    asm volatile("s_waitcnt vmcnt(4)" : "+v"(hA[56]), "+v"(hA[57]), "+v"(hA[58]), "+v"(hA[59]), "+v"(hA[60]), "+v"(hA[61]), "+v"(hA[62]), "+v"(hA[63]),
                 "+v"(hA[64]), "+v"(hA[65]), "+v"(hA[66]), "+v"(hA[67]), "+v"(hA[68]), "+v"(hA[69]), "+v"(hA[70]), "+v"(hA[71])::"memory");
    auto fresh = [&](const u32x4* v, int n) {
      u32 m = 0;
#pragma unroll
      for (int k = 0; k < n; ++k) m = max(max(m, max(v[k][0], v[k][1])), max(v[k][2], v[k][3]));
      return __all(m != ~0u);
    };
#pragma unroll 1
    for (u32 n = 0; n < (1u << 17) && !fresh(&hA[56], 16); ++n) {
#pragma unroll 1
      for (; n < (1u << 17); ++n) {
        asm volatile("" ::: "memory");
        if (__all(__builtin_amdgcn_raw_buffer_load_b32(rAC, sO + (l & 7) * 2048 + 1024, 0, 16) != ~0u)) break;
        __builtin_amdgcn_s_sleep(SLP);
      }
#pragma unroll
      for (int k = 0; k < 16; ++k) hA[56 + k] = __builtin_amdgcn_raw_buffer_load_b128(rAC, vA + sO + k * 1024, 0, 16);
    }
    const int tile = giver ? w ^ 2 : w, r0 = (giver ? 16 : 0) + (l >> 3);
#pragma unroll
    for (int k = 0; k < 2; ++k) xres[k] = ld16_async<false>((r0 + 8 * k) * 896 + (l & 7) * 16, rX, (rb * 128 + tile * 32) * 896 + cc * 128);
    bres = ld16_async<false>((l & 7) * 16, mkrs(b2, 896), cc * 128);
    sfor<56, T_PP>(step);
    sfor<0, 4>([&](auto K) { constexpr int k = decltype(K)::v; pp[k] = ld16_async<true, k * 1024>(vA, rAC, giver ? 0xfffff000u : PX + ((rb * 7 + cc) * 4 + w) * 4096); });
    sfor<T_PP, 72>(step);
    asm volatile("s_waitcnt vmcnt(0)" : "+v"(xres[0]), "+v"(xres[1]), "+v"(bres), "+v"(pp[0]), "+v"(pp[1]), "+v"(pp[2]), "+v"(pp[3])::"memory");
    STAMP(10);
#pragma unroll 1
    for (u32 n = 0; n < (1u << 17) && !fresh(pp, 4); ++n) {
      asm volatile("" ::: "memory");
#pragma unroll
      for (int k = 0; k < 4; ++k) pp[k] = __builtin_amdgcn_raw_buffer_load_b128(rAC, PX + ((rb * 7 + cc) * 4 + w) * 4096 + k * 1024 + vA, 0, 16);
    }
#pragma unroll
    for (int k = 0; k < 4; ++k)
#pragma unroll
      for (int j = 0; j < 4; ++j) { acc[k >> 1][8 * (k & 1) + 2 * j] += bflo(pp[k][j]); acc[k >> 1][8 * (k & 1) + 2 * j + 1] += bfhi(pp[k][j]); }
    cnt_wait(L, C_ATT, 4);
#pragma unroll
    for (int b = 0; b < 2; ++b)
#pragma unroll
      for (int k = 0; k < 4; ++k)
        lds_st<f32x4>(L, L_RED + w * 8192 + (l & 31) * 256 + (((b * 8 + k * 2 + (g >> 1)) ^ (l & 7)) << 4), f32x4{acc[b][4 * k], acc[b][4 * k + 1], acc[b][4 * k + 2], acc[b][4 * k + 3]});
    cnt_add(lb, C_PAIR0 + (w & 1));
    cnt_wait(L, C_PAIR0 + (w & 1), 2);
    const rsrc_t rO = mkrs(out, 2048 * 896);
#pragma unroll
    for (int k = 0; k < 2; ++k) {
      const int row = r0 + 8 * k;
      f32x4 a = {bflo(xres[k][0]) + bflo(bres[0]), bfhi(xres[k][0]) + bfhi(bres[0]), bflo(xres[k][1]) + bflo(bres[1]), bfhi(xres[k][1]) + bfhi(bres[1])};
      f32x4 b = {bflo(xres[k][2]) + bflo(bres[2]), bfhi(xres[k][2]) + bfhi(bres[2]), bflo(xres[k][3]) + bflo(bres[3]), bfhi(xres[k][3]) + bfhi(bres[3])};
#pragma unroll
      for (int j = 0; j < 2; ++j) {
        a += lds_ld<f32x4>(L, L_RED + ((w & 1) + 2 * j) * 8192 + row * 256 + (((2 * (l & 7)) ^ (row & 7)) << 4));
        b += lds_ld<f32x4>(L, L_RED + ((w & 1) + 2 * j) * 8192 + row * 256 + (((2 * (l & 7) + 1) ^ (row & 7)) << 4));
      }
      st16<16>(u32x4{pk(a[0], a[1]), pk(a[2], a[3]), pk(b[0], b[1]), pk(b[2], b[3])}, rO, (rb * 128 + tile * 32 + row) * 896 + cc * 128 + (l & 7) * 16);
    }
    if (w == 0) STAMP(11);
    FLUSH();
    return;
  }

  const int li = w & 1;
  auto clear_o = [&] {
#pragma unroll
    for (int k = 0; k < 8; ++k) st16<16>(u32x4{~0u, ~0u, ~0u, ~0u}, rAC, ((ai * 4 + (k >> 1)) * 144 + 112 + 2 * ah + (k & 1)) * 1024 + l * 16);
  };
  if (!busy) {
    const int blk = (rk - 28) * 8 + x8;
#pragma unroll
    for (int k = 0; k < 14; ++k) d[k] = __builtin_amdgcn_raw_buffer_load_b128(mkrs(w1, 448 * 3584), (112 * (w - 4) + (l >> 3)) * 3584 + (l & 7) * 16 + (blk < 28 ? blk * 128 : 0x7fffffff), k * 8 * 3584, 0);
    const u32 bias = __builtin_amdgcn_raw_buffer_load_b16(mkrs(b1, 3584), (blk * 64 + l) * 2, 0, 0);
    SB();
    epoch();
    if (w == 6 && l < C_N) lds_st<u32>(L, L_CNT + l * 4, l < 4 ? 64u : 0u);
    wait_bar<63>();
    if (w == 4) { vm<0>(); STAMP(38); }
    float a[8] = {};
#pragma unroll
    for (int k = 0; k < 14; ++k)
#pragma unroll
      for (int j = 0; j < 4; ++j) { a[2 * j] += bflo(d[k][j]); a[2 * j + 1] += bfhi(d[k][j]); }
    lds_st<f32x4>(L, RING + ((w - 4) * 64 + l) * 32, f32x4{a[0], a[1], a[2], a[3]});
    lds_st<f32x4>(L, RING + ((w - 4) * 64 + l) * 32 + 16, f32x4{a[4], a[5], a[6], a[7]});
    if (last_of4(L, C_CS0) && blk < 28) {
      float t = 0.f;
#pragma unroll
      for (int k = 0; k < 32; ++k) t += lds_ld<float>(L, RING + k * 256 + l * 4);
      const float hi = rbf(t);
      __builtin_amdgcn_raw_buffer_store_b64(u32x2{pk(hi, t - hi), bias}, rCTL, WS_CS + (blk * 64 + l) * 8, 0, 16);
      vm<0>();
      if (l == 0) st_flag(rCTL, WS_FC + blk * FS, tag);
      STAMP(39);
    }
    if (w == 7) { clear_o(); vm<0>(); if (l == 0) st_flag(rCTL, WS_FZ + (ai * 16 + ah) * FS, tag); }
  } else if (w < 6) {
    const rsrc_t rW1 = mkrs(w1, 448 * 3584), rWq = mkrs(wqkv, 448 * 2688);
    u32 vX[4], vH[4], vQ[3], vC[2], so7[7];
#pragma unroll
    for (int j = 0; j < 4; ++j) vX[j] = (8 * j + (l >> 3)) * 896 + (((l & 7) ^ ((j & 1) * 4 + g)) << 4) - 1024 * j;
    so7[0] = 0;
    const u32 sX = (rb * 128 + 64 * li) * 896, sH = cb * 256 + 32 * li * 3584, sQ = cb * 192 + 16 * li * 2688;
    auto issue = [&](auto O) {
      constexpr int op = decltype(O)::v, k = op / 256, a = (op % 256) / 2, e = op & 1;
      if constexpr (k == K_X) dman<4>(lb + (a * 16 + 8 * li + 4 * e) * 1024, vX, rX, sX + so7[a] * 128 + e * 28672);
      if constexpr (k == K_WA && a < 14) dman<3>(lb + stage_addr(a) + li * 3072, vQ, rWq, sQ + so7[a >> 1] * (64 * 2688) + (a & 1) * (32 * 2688));
      if constexpr (k == K_WA && a >= 14) dman<4>(lb + stage_addr(a) + li * 4096, vH, rW1, sH + so7[(a - 14) % 7] * (64 * 3584) + (a >= 21 ? 128 : 0));
      if constexpr (k == K_WCP) dman<2>(lb + L_WC + (2 * a + li) * 2048, vC, rW2, (kstep(2 * a) + li) * (16 * 896) + cc * 128);
    };
    lanes();
#pragma unroll
    for (int j = 0; j < 3; ++j) { const u32 n = 64 * j + l; vQ[j] = (n / 12) * 2688 + (n % 12) * 16 - 1024 * j; }
#pragma unroll
    for (int j = 0; j < 4; ++j) vH[j] = (8 * j + (l >> 3)) * 3584 + (((l & 7) ^ ((g & 1) << 2)) << 4) - 1024 * j;
#pragma unroll
    for (int j = 1; j < 7; ++j) so7[j] = rotl(j);
    sfor<4, NPRE>([&](auto K) { issue(IC<SC.pro[decltype(K)::v]>{}); });
    vW2(vC);
    vm_bar<SC.wA[0]>();
    sfor<NPRE, NPRO>([&](auto K) { issue(IC<SC.pro[decltype(K)::v]>{}); });
    sfor<0, 15>([&](auto SS) {
      constexpr int s = decltype(SS)::v;
      vm_bar<SC.wA[s + 1]>();
      sfor<0, (s == 14 ? 2 : MAXO)>([&](auto K) { issue(IC<SC.opA[s][decltype(K)::v]>{}); });
    });
    vm<SC.wL[0]>();
    prog_st(L, C_LAND + li, 1);
    vm<0>();
    prog_st(L, C_LAND + li, 2);
    cnt_wait(L, C_FQ, HOLD);
    sfor<2, MAXO>([&](auto K) { issue(IC<SC.opA[14][decltype(K)::v]>{}); });
    vm<SC.wL[1]>();
    prog_st(L, C_LAND + li, 3);
    epoch();
    prog_wait<true>(L, 20);
    issue(IC<OP(K_WA, 26)>{}); issue(IC<OP(K_WA, 27)>{});
    vm<0>();
    prog_st(L, C_LAND + li, 4);
    cnt_add(lb, C_WLO);
    if (w == 4) { STAMP(13); cnt_wait(L, C_FA, 5); STAMP(14); if (l == 0) st_flag(rCTL, WS_FH + (rb * 16 + cb) * FS, tag); }
    else { cnt_wait(L, C_FA, 5); spin<SLP>(rCTL, l >= 16 && l < 32 ? WS_FZ + (rb * 16 + l - 16) * FS : WS_FH + (rb * 16 + (l < 7 ? 7 * kh + l : 7 * (1 - kh) + cc)) * FS, tag); STAMP(15); cnt_add(lb, C_FH); }
  } else {
    if (w == 6 && cb < 8) {
      epoch();
      bar();
      asm volatile("s_waitcnt vmcnt(0)" : "+v"(d[0]), "+v"(d[1]), "+v"(d[2]), "+v"(d[3]), "+v"(d[4]), "+v"(d[5]), "+v"(d[6]), "+v"(d[7]), "+v"(d[8]), "+v"(d[9]), "+v"(d[10]), "+v"(d[11]), "+v"(d[12]), "+v"(d[13])::"memory");
      float s1[2] = {}, s2[2] = {};
#pragma unroll
      for (int r = 0; r < 2; ++r) {
        bar();
#pragma unroll
        for (int k = 0; k < 28; ++k) { const u32 v = d[7 * r + (k >> 2)][k & 3]; s1[r] = dot2(v, 0x3f803f80u, s1[r]); s2[r] = dot2(v, v, s2[r]); }
        PIN(s1[r]); PIN(s2[r]);
        SB();
      }
#pragma unroll
      for (int r = 0; r < 2; ++r) {
        bar();
        const float mean = sum8(s1[r]) * (1.f / 448.f);
        const float var = fmaxf(sum8(s2[r]) * (1.f / 448.f) - mean * mean, 0.f);
        const float sd = rbf(__builtin_amdgcn_sqrtf(rbf(rbf(var) + 1e-5f)));
        if ((l & 7) == 0) st16<16>(u32x4{pk(-mean, sd), (u32)tag, pk(-mean, sd), (u32)(tag >> 32)}, rCTL, WS_ST + (rb * 128 + cb * 16 + r * 8 + (l >> 3)) * 16);
        SB();
      }
    } else { bar(); bar(); epoch(); bar(); bar(); bar(); }
    if (w == 6) {
      const u32 fp = WS_FC + (2 * cb + ((l >> 1) & 1)) * FS + (l & 1) * 4;
      int ss = 0, cs = 0;
      auto look = [&] {
        vm<0>();
        if (ss == 1) {
          const u32x4 a = lds_ld<u32x4>(L, L_ST + l * 16), b = lds_ld<u32x4>(L, L_ST + 1024 + l * 16);
          if (__all(a[1] == (u32)tag && a[3] == (u32)(tag >> 32) && b[1] == (u32)tag && b[3] == (u32)(tag >> 32))) { cnt_add(lb, C_ST); ss = 3; STAMP(33); }
        }
        if (ss < 3) {
          asm volatile("s_mov_b32 m0, %0\n\ts_nop 0\n\tbuffer_load_dwordx4 %1, %2, 0 offen sc1 lds\n\tbuffer_load_dwordx4 %1, %2, 0 offen offset:1024 sc1 lds" ::"s"(lb + L_ST), "v"(l * 16 + WS_ST + rb * 2048), "s"(rCTL) : "memory");
          ss = 1;
        }
        if (cs == 2) { cnt_add(lb, C_CSH); cs = 3; STAMP(37); }
        if (cs == 1 && __all(lds_ld<u32>(L, L_POLL + l * 4) == (u32)(l & 1 ? tag >> 32 : tag))) {
          asm volatile("s_mov_b32 m0, %0\n\ts_nop 0\n\tbuffer_load_dwordx4 %1, %2, 0 offen sc1 lds" ::"s"(lb + L_CSH), "v"(l * 16 + WS_CS + cb * 1024), "s"(rCTL) : "memory");
          cs = 2;
        }
        if (cs < 2) { asm volatile("s_mov_b32 m0, %0\n\ts_nop 0\n\tbuffer_load_dword %1, %2, 0 offen sc1 lds" ::"s"(lb + L_POLL), "v"(fp), "s"(rCTL) : "memory"); cs = 1; }
      };
#pragma unroll 1
      for (int s = 4; s <= 14; ++s) {
        wait_bar<63>();
        if (s == 6 && l < 30) lds_st<u32>(L, L_CNT + l * 4, l < 28 ? 0u : (u32)(l & 1 ? tag >> 32 : tag));
        if (s >= 7 && ss + cs != 6 && vm_le<0>()) look();
      }
#pragma unroll 1
      for (u32 n = 0; n < (1u << 17) && ss + cs != 6; ++n) look();
    } else {
      vm<0>();
      int k = 0;
      auto clear = [&] {
        st16<16>(u32x4{~0u, ~0u, ~0u, ~0u}, rAC, ((ai * 4 + (k >> 1)) * 144 + 112 + 2 * ah + (k & 1)) * 1024 + l * 16);
        st16<16>(u32x4{~0u, ~0u, ~0u, ~0u}, rAC, PX + ((rb * 7 + cc) * 4 + 2 * kh) * 4096 + k * 1024 + l * 16);
        ++k;
      };
#pragma unroll 1
      for (int s = 4; s <= 14; ++s) {
        bar();
        if (s >= CLR0 && k < 8 && vm_le<CLRQ>()) clear();
      }
      if (k < 8) cnt_wait(L, C_FQ, 4);
#pragma unroll 1
      while (k < 8) clear();
      vm<0>();
      cnt_add(lb, C_FA);
      if (l == 0) st_flag(rCTL, WS_FZ + (ai * 16 + ah) * FS, tag);
    }
  }
  lanes();
  const int aw = w - 4, hh = g >> 1;
  if (w >= 6) {
    const int e = l & 1, k = l < 14 ? l >> 1 : 0;
    const int ty = k == 0 ? 0 : (k < 4 ? 1 : 2), ro = k == 0 ? 0 : (k < 4 ? k - 1 : k - 4);
    const int rr = ai - ro < 0 ? 0 : ai - ro;
    if (!busy) { for (int k = 0; k < IDLE_SLP; ++k) __builtin_amdgcn_s_sleep(15); }
    if (busy) { cnt_wait(L, C_FQ, 4); if (w == 6) STAMP(12); if (w == 6 && l == 0) st_flag(rCTL, WS_FQ + (rb * 16 + cb) * FS, tag); }
    prog_wait<true>(L, 18);
#pragma unroll
    for (int k = 0; k < 3; ++k) lds_st<u32x2>(L, L_V + (6 * li + 2 * k + (l >> 5)) * 2048 + 1792 + (l & 31) * 8, u32x2{0x3f80u, 0u});
    if (w == 6) { spin<SLP>(rCTL, WS_FQ + (rr * 16 + (ty * 448 + 28 * ah + 27 * e) / 96) * FS, tag); cnt_add(lb, C_SQ); }
    else cnt_wait(L, C_SQ, 1);
    if (w == 6) STAMP(7);
#pragma unroll
    for (int e = 0; e < 2; ++e) {
#pragma unroll
      for (int j = 0; j < 6; ++j) {
        const int b = 2 * j + li, tb = 4 * ai - 8 + b < 0 ? 0 : 4 * ai - 8 + b;
        dma1792c(lb + (e ? L_V + b * 2048 : L_K + b * 1792), l * 16, rQKV, (tb * 336 + 112 + 112 * e + 7 * ah) * 256);
      }
      if (e == 0) {
#pragma unroll
        for (int j = 0; j < 2; ++j) dma1792c(lb + L_Q + (2 * li + j) * 2048, l * 16, rQKV, ((4 * ai + 2 * li + j) * 336 + 7 * ah) * 256);
      }
    }
    vm<12>();
    cnt_add(lb, C_KV);
  }
  cnt_wait(L, C_KV, 2);
  __builtin_amdgcn_s_setprio(PRIO_B);
  if (w == 4) STAMP(8);
  u32x2 qv[4];
#pragma unroll
  for (int k = 0; k < 4; ++k) qv[k] = lds_ld<u32x2>(L, L_Q + aw * 2048 + (k == 3 ? 6 : 2 * k + hh) * 256 + l15 * 16 + (g & 1) * 8);
  if (hh) qv[3] = u32x2{0u, 0u};
  const bf16x8 qf[2] = {__builtin_bit_cast(bf16x8, u32x4{qv[0][0], qv[0][1], qv[1][0], qv[1][1]}), __builtin_bit_cast(bf16x8, u32x4{qv[2][0], qv[2][1], qv[3][0], qv[3][1]})};
  const u32 oK = L_K + aw * 1792 + hh * 256 + l15 * 16 + (g & 1) * 8, oK3 = hh ? oK + 1280 : oK + 1536;
  const int F = 7 * ah + 4 * (g & 1) + p4;
  u32 oV[2][2];
#pragma unroll
  for (int t = 0; t < 2; ++t)
#pragma unroll
    for (int e = 0; e < 2; ++e) oV[t][e] = L_V + aw * 2048 + (4 * (g & 1) + p4) * 256 + (((8 * e + 4 * hh + q4) ^ ((F & 6) << 1)) << 4) + ((t ^ (F & 1)) << 3);
  f32x16 sc[9];
  bf16x8 kf[9][2], vf[9][2];
  auto rdK = [&](int m) {
    const u32x2 a = lds_ld<u32x2>(L, oK + m * 1792), b = lds_ld<u32x2>(L, oK + m * 1792 + 512), c = lds_ld<u32x2>(L, oK + m * 1792 + 1024), d = lds_ld<u32x2>(L, oK3 + m * 1792);
    kf[m][0] = __builtin_bit_cast(bf16x8, u32x4{a[0], a[1], b[0], b[1]}); kf[m][1] = __builtin_bit_cast(bf16x8, u32x4{c[0], c[1], d[0], d[1]});
  };
  auto rdV = [&](int m) { vf[m][0] = trfrag(L, oV[0][0] + m * 2048, oV[0][1] + m * 2048); vf[m][1] = trfrag(L, oV[1][0] + m * 2048, oV[1][1] + m * 2048); };
  rdK(0); SB(); rdK(8); SB(); rdK(1); SB();
#pragma unroll
  for (int i = 0; i < 9; ++i) {
    const int m = i == 0 ? 0 : (i == 1 ? 8 : i - 1);
    if (i + 2 < 8) rdK(i + 2);
    sc[m] = mfma32(kf[m][1], qf[1], mfma32(kf[m][0], qf[0], f32x16{}));
    SB();
  }
  if (w >= 6) { vm<0>(); cnt_add(lb, C_V); }
  if (busy) {
    u32 vW[2];
    vW2(vW);
    dman<2>(lb + L_Q + aw * 2048, vW, rW2, (kstep(52) + aw) * (16 * 896) + cc * 128);
  }
  constexpr float NEG = -1e30f;
  if (ai < 2) {
    asm volatile("" ::: "memory");
    const int m0 = 8 - 4 * ai - aw;
#pragma unroll
    for (int m = 0; m < 8; ++m)
      if (m < m0) {
#pragma unroll
        for (int r = 0; r < 16; ++r) sc[m][r] = NEG;
      }
  }
  const int dq = (l & 31) - 4 * hh;
  u32 mk[8];
#pragma unroll
  for (int r = 0; r < 16; r += 2) {
    const bool c0 = (r & 3) + 8 * (r >> 2) <= dq, c1 = (r & 3) + 1 + 8 * (r >> 2) <= dq;
    sc[0][r] = c0 ? sc[8][r] : sc[0][r]; sc[0][r + 1] = c1 ? sc[8][r + 1] : sc[0][r + 1];
    mk[r >> 1] = c1 ? 0xffffffffu : (c0 ? 0xffffu : 0u);
  }
  cnt_wait(L, C_V, 2);
  rdV(1); rdV(2);
  SB();
  float mx0 = fmx(sc[0][0], sc[0][1]), mx1 = fmx(sc[0][2], sc[0][3]);
#pragma unroll
  for (int k = 1; k < 32; ++k) { mx0 = fmx(fmx(mx0, sc[k >> 2][4 * (k & 3)]), sc[k >> 2][4 * (k & 3) + 1]); mx1 = fmx(fmx(mx1, sc[k >> 2][4 * (k & 3) + 2]), sc[k >> 2][4 * (k & 3) + 3]); }
  mx0 = fmx(mx0, mx1);
  const auto ms = __builtin_amdgcn_permlane32_swap(__float_as_uint(mx0), __float_as_uint(mx0), false, false);
  const float mx = fmx(__uint_as_float(ms[0]), __uint_as_float(ms[1]));
  vm<0>();
  cnt_add(lb, C_LATE);
  SB();
  f32x16 o = {};
#pragma unroll
  for (int i = 0; i < 8; ++i) {
    const int m = (i + 1) & 7;
    if (i < 5) rdV(i + 3); else if (i == 5) rdV(0); else if (i == 6) rdV(8);
    u32x4 p[2];
#pragma unroll
    for (int j = 0; j < 8; ++j) {
      f32x2 d = f32x2{sc[m][2 * j], sc[m][2 * j + 1]} - f32x2{mx, mx};
      PIN(d);
      p[j >> 2][j & 3] = pk(__builtin_amdgcn_exp2f(d[0]), __builtin_amdgcn_exp2f(d[1]));
    }
#pragma unroll
    for (int s = 0; s < 2; ++s) {
      if (m) o = mfma32(vf[m][s], __builtin_bit_cast(bf16x8, p[s]), o);
      else {
        const u32x4 k4 = {mk[4 * s], mk[4 * s + 1], mk[4 * s + 2], mk[4 * s + 3]};
        o = mfma32(vf[0][s], __builtin_bit_cast(bf16x8, p[s] & ~k4), o);
        o = mfma32(vf[8][s], __builtin_bit_cast(bf16x8, p[s] & k4), o);
      }
    }
    SB();
  }
  cnt_add(lb, C_ATT);
  const auto dn = __builtin_amdgcn_permlane32_swap(__float_as_uint(o[12]), __float_as_uint(o[12]), false, false);
  const float inv = __builtin_amdgcn_rcpf(__uint_as_float(dn[1]));
  const float in2 = hh ? 0.f : inv;
  const u32 so = ((ai * 4 + aw) * 144 + 112 + 2 * ah) * 1024;
  st16<16>(u32x4{pk(o[0] * inv, o[1] * inv), pk(o[2] * inv, o[3] * inv), pk(o[4] * inv, o[5] * inv), pk(o[6] * inv, o[7] * inv)}, rAC, l * 16 + so);
  st16<16>(u32x4{pk(o[8] * inv, o[9] * inv), pk(o[10] * inv, o[11] * inv), pk(o[12] * in2, o[13] * in2), pk(o[14] * in2, o[15] * in2)}, rAC, l * 16 + so + 1024);
  if (w == 4) STAMP(9);
  vm<0>();
  if (last_of4(L, C_FB)) {
    if (l == 0) st_flag(rCTL, WS_FB + (ai * 16 + ah) * FS, tag);
    if (blockIdx.x == 255) {
#pragma unroll 1
      for (int k = 0; k < 4; ++k) spin<32>(rCTL, WS_FB + (k * 64 + l) * FS, tag);
      if (l == 0) st_flag(rCTL, 0, tag);
    }
  }
  FLUSH();
}

extern "C" void kernel_launch(void* const* d_in, const int* in_sizes, int n_in, void* d_out, int out_size, void* d_ws, size_t ws_size, hipStream_t stream) {
  fused_block<<<256, 512, 0, stream>>>((const char*)d_in[0], (const char*)d_in[1], (const char*)d_in[2], (const char*)d_in[4], (const char*)d_in[5],
                                       (const char*)d_in[6], (const char*)d_in[7], (char*)d_out, (char*)d_ws + WS_OFF);
}
```

```cpp
#define PROBE 1
#include <hip/hip_runtime.h>
#include <hip/hip_bf16.h>
#include <stdint.h>

#define DI __device__ __forceinline__
#define LAS __attribute__((address_space(3)))
typedef unsigned u32;
typedef unsigned long long u64;
typedef __bf16 bf16x8 __attribute__((ext_vector_type(8)));
typedef __bf16 bf16x2 __attribute__((ext_vector_type(2)));
typedef short s16x4 __attribute__((ext_vector_type(4)));
typedef float f32x4 __attribute__((ext_vector_type(4)));
typedef float f32x2 __attribute__((ext_vector_type(2)));
typedef float f32x16 __attribute__((ext_vector_type(16)));
typedef unsigned u32x4 __attribute__((ext_vector_type(4)));
typedef unsigned u32x2 __attribute__((ext_vector_type(2)));
typedef __amdgpu_buffer_rsrc_t rsrc_t;

template <int N> struct IC { static constexpr int v = N; };
template <int B, int E, class F> DI void sfor(F&& f) { if constexpr (B < E) { f(IC<B>{}); sfor<B + 1, E>(f); } }

constexpr u32 FS = 128;
constexpr u32 WS_FH = FS, WS_FQ = WS_FH + 256 * FS, WS_FB = WS_FQ + 256 * FS, WS_FG = WS_FB + 256 * FS, WS_FP = WS_FG + 256 * FS, WS_FC = WS_FP + 448 * FS;
constexpr u32 WS_FS = WS_FC + 32 * FS, WS_FZ = WS_FS + 128 * FS, WS_CS = WS_FZ + 287 * FS, WS_ST = WS_CS + 16384, CTL_BYTES = WS_ST + 32768;
constexpr u32 WS_QKV = CTL_BYTES, QKV_BYTES = 64u * 336u * 256u;
constexpr u32 WS_AC = WS_QKV + QKV_BYTES, AC_BYTES = 64u * 144u * 1024u + 448u * 4096u;
constexpr u32 PX = 64u * 144u * 1024u;
constexpr u32 RING = 7 * 16384;
constexpr u32 L_ST = 40960, L_POLL = 45056, L_CSQ = 46080, L_CSH = 46848, L_CNT = 48896;
constexpr u32 L_K = 0, L_V = 21504, L_Q = RING + 5 * 8192;
constexpr u32 L_WC = 49152, L_RED = 0;
enum { C_DONE, C_LAND = 4, C_FQ = 6, C_FA, C_FG, C_KV, C_FB, C_WLO, C_WHI, C_P30, C_WO, C_ATT, C_LATE, C_PAIR0, C_PAIR1, C_CSH, C_FH, C_SQ, C_V, C_ST, C_CS0, C_N };
constexpr u32 stage_addr(int s) {
  return s < 14 ? RING + (s % 6) * 6144u : s < 19 ? (s - 14) * 8192u : s < 21 ? RING + (s - 19) * 8192u : s < 23 ? 6 * 16384u + (s - 21) * 8192u : s < 26 ? RING + (s - 21) * 8192u : RING + (s - 26) * 8192u;
}
constexpr int NWP = 24;

enum { K_NONE = 0, K_X, K_WA, K_BIAS, K_WCP };
constexpr int OP(int k, int a, int e = 0) { return k * 256 + 2 * a + e; }
constexpr int opn(int op) { int k = op / 256, a = (op % 256) / 2; return k == K_NONE ? 0 : k == K_X ? 4 : k == K_WA ? (a < 14 ? 3 : 4) : k == K_WCP ? 2 : 1; }
#define CAP_1 8
#define P_NPRO 8
#define P_DA 12
#define P_TB 16
#define P_TO 44
#define P_FQ 18
#define PRIO_C 0
#define PRIO_B 0
#define ROTA 0
#define WS_OFF 0
#define CODE_KB 39
#define PF_DELAY 20
#define P_TFP 66
#define SLP 2
#define LSLP 1
#define GATE_K 0
#define OWN1 4
#define CLR0 4
#define TOUCH 1
#define HNOW 2
#define CLRQ 0
#define HOLD 4
#define IDLE_SLP 9
#define T_PP 62
#define QNOW 6
#define P_NPRE 6
constexpr int NPRE = P_NPRE;
constexpr int MAXO = 24, NPRO = P_NPRO, DA = P_DA, TB = P_TB, S_FQ = P_FQ, T_WH = 20, T_WO = 33, T_O = P_TO;
struct Sched {
  int pro[NPRO], opA[15][MAXO];
  int wA[16];
  int wL[2];
  int wC[56], a0[56], a1[56];
  int ok;
};
constexpr int cl(int n) { return n > 63 ? 63 : n; }
constexpr Sched make_sched() {
  Sched r{};
  r.ok = 1;
  int n = 0, iW[26] = {}, iX[7] = {};
  int lo[128] = {}, le[128] = {}, nl = 0;
  for (int s = 0; s < 26; ++s) {
    if (s < 14 && !(s & 1)) { lo[nl] = OP(K_X, s >> 1, 0); le[nl++] = -1; lo[nl] = OP(K_X, s >> 1, 1); le[nl++] = -1; }
    lo[nl] = OP(K_WA, s); le[nl++] = s < 14 ? s - 5 : (s < 19 ? 5 : (s == 21 || s == 22 ? 13 : 14));
  }
  for (int k = 0; k < NWP / 2; ++k) { lo[nl] = OP(K_WCP, k); le[nl++] = 7 + 2 * (k >> 2); }
  bool done[128] = {};
  auto note = [&](int op) { n += opn(op); int k = op / 256, a = (op % 256) / 2; if (k == K_X) iX[a] = (op & 1) ? n : 0; if (k == K_WA) iW[a] = n; };
  for (int k = 0; k < NPRO; ++k) { if (k == NPRE) r.wA[0] = cl(n - iW[0]); r.pro[k] = lo[k]; done[k] = true; note(lo[k]); }
  for (int s = 0; s < 15; ++s) {
    int need = iW[s + 1]; if (!need) r.ok = 0;
    if (s + 1 <= 13) { int v = iX[(s + 1) >> 1]; if (!v) r.ok = 0; if (v > need) need = v; }
    r.wA[s + 1] = cl(n - need);
    int cap = s == 14 ? 64 : CAP_1, c = 0, used = 0;
    for (int k = 0; k < nl; ++k) if (!done[k] && le[k] <= s && used + opn(lo[k]) <= cap && c < MAXO) { done[k] = true; r.opA[s][c++] = lo[k]; used += opn(lo[k]); note(lo[k]); if (s == 14 && c == 2) r.wL[0] = cl(n - iW[18]); }
  }
  r.wL[1] = cl(n - iW[25]);
  if (r.opA[14][0] != OP(K_WA, 19)) r.ok = 0;
  for (int k = 0; k < nl; ++k) if (!done[k]) r.ok = 0;
  n = 0;
  int iA[56] = {}, iP = 0, iH = 0, iO = 0, nA = 8 + DA;
  for (int m = 8; m < 8 + DA; ++m) { n += 1; iA[m] = n; }
  for (int s = 8; s < 56; ++s) {
    int need = iA[s]; if (!need) r.ok = 0;
    if (s == TB && iP > need) need = iP;
    if (s == T_WH && iH > need) need = iH;
    if (s == T_WO + 12 && iO > need) need = iO;
    r.wC[s] = cl(n - need);
    if (s == TB - 3) { n += 1; iP = n; }
    r.a0[s] = nA;
    for (int c = 0; c < 2 && nA < 56 && nA <= s + DA && (nA < 32 || s >= TB); ++c) { n += 1; iA[nA++] = n; }
    r.a1[s] = nA;
    if (s < 15) { n += 2; iH = n; }
    if (s >= T_WO && s < T_WO + 7) { n += 2; iO = n; }
    if (s >= T_O && s < T_O + 8) n += 2;
  }
  return r;
}
constexpr Sched SC = make_sched();
static_assert(SC.ok, "schedule");

DI rsrc_t mkrs(const void* p, u32 bytes) { return __builtin_amdgcn_make_buffer_rsrc((void*)p, 0, bytes, 0x00020000); }
template <int N> DI void dman(u32 lds, const u32* v, rsrc_t rs, u32 soff) {
  if constexpr (N == 4) asm volatile("s_mov_b32 m0, %0\n\ts_nop 0\n\tbuffer_load_dwordx4 %1, %5, %6 offen lds\n\tbuffer_load_dwordx4 %2, %5, %6 offen offset:1024 lds\n\t"
    "buffer_load_dwordx4 %3, %5, %6 offen offset:2048 lds\n\tbuffer_load_dwordx4 %4, %5, %6 offen offset:3072 lds" ::"s"(lds), "v"(v[0]), "v"(v[1]), "v"(v[2]), "v"(v[3]), "s"(rs), "s"(soff) : "memory");
  if constexpr (N == 3) asm volatile("s_mov_b32 m0, %0\n\ts_nop 0\n\tbuffer_load_dwordx4 %1, %4, %5 offen lds\n\tbuffer_load_dwordx4 %2, %4, %5 offen offset:1024 lds\n\t"
    "buffer_load_dwordx4 %3, %4, %5 offen offset:2048 lds" ::"s"(lds), "v"(v[0]), "v"(v[1]), "v"(v[2]), "s"(rs), "s"(soff) : "memory");
  if constexpr (N == 2) asm volatile("s_mov_b32 m0, %0\n\ts_nop 0\n\tbuffer_load_dwordx4 %1, %3, %4 offen lds\n\tbuffer_load_dwordx4 %2, %3, %4 offen offset:1024 lds" ::"s"(lds), "v"(v[0]), "v"(v[1]), "s"(rs), "s"(soff) : "memory");
  if constexpr (N == 1) asm volatile("s_mov_b32 m0, %0\n\ts_nop 0\n\tbuffer_load_dwordx4 %1, %2, %3 offen lds" ::"s"(lds), "v"(v[0]), "s"(rs), "s"(soff) : "memory");
}
DI void dma1792c(u32 lds, u32 voff, rsrc_t rs, u32 soff) {
  asm volatile("s_mov_b32 m0, %0\n\ts_nop 0\n\tbuffer_load_dwordx4 %1, %2, %3 offen sc1 lds\n\ts_mov_b32 exec_hi, 0xffff\n\t"
               "buffer_load_dwordx4 %1, %2, %3 offen offset:1024 sc1 lds\n\ts_mov_b32 exec_hi, -1" ::"s"(lds), "v"(voff), "s"(rs), "s"(soff) : "memory");
}
DI void dma4(u32 lds, u32 voff, rsrc_t rs, u32 soff) {
  asm volatile("s_mov_b32 m0, %0\n\ts_nop 0\n\tbuffer_load_dword %1, %2, %3 offen lds" ::"s"(lds), "v"(voff), "s"(rs), "s"(soff) : "memory");
}
template <bool C, int IMM = 0> DI u32x4 ld16_async(u32 voff, rsrc_t rs, u32 soff) {
  u32x4 r;
  if constexpr (C) asm volatile("buffer_load_dwordx4 %0, %1, %2, %3 offen offset:%4 sc1" : "=v"(r) : "v"(voff), "s"(rs), "s"(soff), "n"(IMM) : "memory");
  else asm volatile("buffer_load_dwordx4 %0, %1, %2, %3 offen" : "=v"(r) : "v"(voff), "s"(rs), "s"(soff) : "memory");
  return r;
}
DI u32x2 ld8c_async(u32 voff, rsrc_t rs) {
  u32x2 r; asm volatile("buffer_load_dwordx2 %0, %1, %2, 0 offen sc1" : "=v"(r) : "v"(voff), "s"(rs) : "memory"); return r;
}
template <int N> DI void wait_bar() { asm volatile("s_waitcnt vmcnt(%0) lgkmcnt(0)\n\ts_barrier" ::"n"(N) : "memory"); }
template <int N> DI void vm_bar() { asm volatile("s_waitcnt vmcnt(%0)\n\ts_barrier" ::"n"(N) : "memory"); }
template <int N> DI void vm() { asm volatile("s_waitcnt vmcnt(%0)" ::"n"(N) : "memory"); }
DI void bar() { asm volatile("s_barrier" ::: "memory"); }
#define PIN(x) asm volatile("" : "+v"(x))
#define SB() __builtin_amdgcn_sched_barrier(0)
DI f32x4 mfma(bf16x8 a, bf16x8 b, f32x4 c) { return __builtin_amdgcn_mfma_f32_16x16x32_bf16(a, b, c, 0, 0, 0); }
DI f32x16 mfma32(bf16x8 a, bf16x8 b, f32x16 c) { return __builtin_amdgcn_mfma_f32_32x32x16_bf16(a, b, c, 0, 0, 0); }
DI u32 pk(float a, float b) { bf16x2 v = {(__bf16)a, (__bf16)b}; return __builtin_bit_cast(u32, v); }
DI float fmx(float a, float b) { return __builtin_elementwise_maximum(a, b); }
DI float rbf(float a) { return (float)(__bf16)a; }
DI float bflo(u32 v) { return __uint_as_float(v << 16); }
DI float bfhi(u32 v) { return __uint_as_float(v & 0xffff0000u); }
DI float dot2(u32 a, u32 b, float c) { return __builtin_amdgcn_fdot2_f32_bf16(__builtin_bit_cast(bf16x2, a), __builtin_bit_cast(bf16x2, b), c, false); }
DI float sum8(float v) {
  v += __builtin_bit_cast(float, __builtin_amdgcn_update_dpp(0, __builtin_bit_cast(int, v), 0xb1, 0xf, 0xf, true));
  v += __builtin_bit_cast(float, __builtin_amdgcn_update_dpp(0, __builtin_bit_cast(int, v), 0x4e, 0xf, 0xf, true));
  v += __builtin_bit_cast(float, __builtin_amdgcn_update_dpp(0, __builtin_bit_cast(int, v), 0x141, 0xf, 0xf, true));
  return v;
}
template <class T> DI T lds_ld(const LAS char* base, u32 off) { return *(const LAS T*)(base + off); }
template <class T> DI void lds_st(LAS char* base, u32 off, T v) { *(LAS T*)(base + off) = v; }
DI bf16x8 trfrag(LAS char* base, u32 off0, u32 off1) {
  s16x4 lo = __builtin_amdgcn_ds_read_tr16_b64_v4i16((LAS s16x4*)(base + off0));
  s16x4 hi = __builtin_amdgcn_ds_read_tr16_b64_v4i16((LAS s16x4*)(base + off1));
  return __builtin_bit_cast(bf16x8, __builtin_shufflevector(lo, hi, 0, 1, 2, 3, 4, 5, 6, 7));
}
#define ST_AUX 16
template <int AUX> DI void st16(u32x4 v, rsrc_t rs, u32 voff) { __builtin_amdgcn_raw_buffer_store_b128(v, rs, voff, 0, AUX ? ST_AUX : 0); }
DI void st_flag(rsrc_t rc, u32 off, u64 v) { __builtin_amdgcn_raw_buffer_store_b64(u32x2{(u32)v, (u32)(v >> 32)}, rc, off, 0, 16); }
DI bool tagged(u32x2 v, u64 tag) { return __all(v[0] == (u32)tag && v[1] == (u32)(tag >> 32)); }
template <int SLEEP> DI void spin(rsrc_t rc, u32 off, u64 tag) {
#pragma unroll 1
  for (u32 n = 0; n < (1u << 17); ++n) {
    asm volatile("" ::: "memory");
    if (tagged(__builtin_amdgcn_raw_buffer_load_b64(rc, off, 0, 16), tag)) break;
    __builtin_amdgcn_s_sleep(SLEEP);
  }
  asm volatile("" ::: "memory");
}
DI u32 cnt_ld(LAS char* L, int k) { return __hip_atomic_load((LAS u32*)(L + L_CNT + 4 * k), __ATOMIC_RELAXED, __HIP_MEMORY_SCOPE_WORKGROUP); }
DI void cnt_wait(LAS char* L, int k, u32 v) {
#pragma unroll 1
  for (u32 n = 0; n < (1u << 20); ++n) {
    if ((u32)__builtin_amdgcn_readfirstlane(cnt_ld(L, k)) >= v) break;
    __builtin_amdgcn_s_sleep(LSLP);
  }
  asm volatile("" ::: "memory");
}
DI void prog_st(LAS char* L, int k, u32 v) { asm volatile("" ::: "memory"); __hip_atomic_store((LAS u32*)(L + L_CNT + 4 * k), v, __ATOMIC_RELAXED, __HIP_MEMORY_SCOPE_WORKGROUP); asm volatile("" ::: "memory"); }
DI u32 land_ld(LAS char* L) { const u64 v = __hip_atomic_load((LAS u64*)(L + L_CNT + 4 * C_LAND), __ATOMIC_RELAXED, __HIP_MEMORY_SCOPE_WORKGROUP); return min((u32)v, (u32)(v >> 32)); }
DI u32 done_ld(LAS char* L) {
  const u64 a = __hip_atomic_load((LAS u64*)(L + L_CNT), __ATOMIC_RELAXED, __HIP_MEMORY_SCOPE_WORKGROUP), b = __hip_atomic_load((LAS u64*)(L + L_CNT + 8), __ATOMIC_RELAXED, __HIP_MEMORY_SCOPE_WORKGROUP);
  return min(min((u32)a, (u32)(a >> 32)), min((u32)b, (u32)(b >> 32)));
}
template <bool DONE> DI void prog_wait(LAS char* L, u32 v) {
#pragma unroll 1
  for (u32 n = 0; n < (1u << 20); ++n) {
    if ((u32)__builtin_amdgcn_readfirstlane(DONE ? done_ld(L) : land_ld(L)) >= v) break;
    __builtin_amdgcn_s_sleep(LSLP);
  }
  asm volatile("" ::: "memory");
}
DI void cnt_add(u32 lb, int k) {
  asm volatile("s_mov_b64 exec, 1\n\tds_add_u32 %0, %1\n\ts_mov_b64 exec, -1" ::"v"(lb + L_CNT + 4 * k), "v"(1u) : "memory");
}
template <int N> DI bool vm_le() {
  u32 r; asm volatile("s_getreg_b32 %0, hwreg(HW_REG_IB_STS)" : "=s"(r)::"memory");
  return (r & 0xc0000fu) <= (u32)N;
}
DI bool last_of4(LAS char* L, int k) {
  u32 old = 0;
  asm volatile("" ::: "memory");
  if (__builtin_amdgcn_mbcnt_hi(~0u, __builtin_amdgcn_mbcnt_lo(~0u, 0u)) == 0) old = __hip_atomic_fetch_add((LAS u32*)(L + L_CNT + 4 * k), 1u, __ATOMIC_RELAXED, __HIP_MEMORY_SCOPE_WORKGROUP);
  asm volatile("" ::: "memory");
  return __builtin_amdgcn_readfirstlane(old) == 3;
}
#define STAMP(k) do { if ((k) == PROBE) { SB(); asm volatile("s_memrealtime %0\n\ts_waitcnt lgkmcnt(0)" : "=s"(mark)::"memory"); SB(); } } while (0)
#define FLUSH() do { if (mark) { u32 n_ = 0; while (__builtin_amdgcn_s_memrealtime() - mark < 4000 && ++n_ < 1000000) __builtin_amdgcn_s_sleep(4); } } while (0)

__global__ __launch_bounds__(512, 2) void fused_block(const char* __restrict__ x, const char* __restrict__ wqkv, const char* __restrict__ wo,
                                                      const char* __restrict__ w1, const char* __restrict__ b1, const char* __restrict__ w2,
                                                      const char* __restrict__ b2, char* __restrict__ out, char* ws) {
  __shared__ __attribute__((aligned(1024))) char smem[163840];
  asm volatile("" : "+s"(x), "+s"(wqkv), "+s"(wo), "+s"(w1), "+s"(b1), "+s"(w2), "+s"(b2), "+s"(out), "+s"(ws));
  u64 pc; asm volatile("s_getpc_b64 %0" : "=s"(pc));
  LAS char* const L = (LAS char*)smem;
  const u32 lb = (u32)(uintptr_t)smem;
  int l = threadIdx.x & 63, g, q4, p4, l15;
  auto lanes = [&] { PIN(l); g = l >> 4; q4 = (l >> 2) & 3; p4 = l & 3; l15 = l & 15; };
  lanes();
  int w = __builtin_amdgcn_readfirstlane(threadIdx.x >> 6);
  asm volatile("s_nop 4" : "+s"(w));
  const int x8 = blockIdx.x & 7, rk = blockIdx.x >> 3;
  const bool busy = rk < 28;
  u64 mark = 0;
  if (w == 0) STAMP(0);
  const rsrc_t rQKV = mkrs(ws + WS_QKV, QKV_BYTES), rAC = mkrs(ws + WS_AC, AC_BYTES), rCTL = mkrs(ws, CTL_BYTES);
  const rsrc_t rX = mkrs(x, 2048 * 896), rW2 = mkrs(w2, 1792 * 896);
  const int kh = x8 >> 2, cc = rk % 7, rb = (x8 & 3) * 4 + rk / 7, cb = kh * 7 + cc;
  const int ai = rk & 15, ah = 2 * x8 + (rk >> 4);
  const int ro6 = ROTA ? (cc + ((3 * (rk / 7)) >> 1)) % 6 : 0;
  auto rotl = [&](int j) { const int t = j + ro6; return j == 0 ? 0 : (t > 6 ? t - 6 : t); };
  auto kstep = [&](int p) {
    if (p < 8) return 8 * cb + p;
    const int t = cc + 1 + ((p - 8) % 24) / 4;
    return 8 * (7 * kh + (t >= 7 ? t - 7 : t)) + (p & 3) + (p >= 32 ? 4 : 0);
  };
  if (busy && (w == 4 || w == 5)) {
    u32 v[4];
#pragma unroll
    for (int j = 0; j < 4; ++j) v[j] = (8 * j + (l >> 3)) * 896 + (((l & 7) ^ ((j & 1) * 4 + g)) << 4) - 1024 * j;
    dman<4>(lb + (w & 1) * 8192, v, rX, (rb * 128 + 64 * (w & 1)) * 896);
    dman<4>(lb + (w & 1) * 8192 + 4096, v, rX, (rb * 128 + 64 * (w & 1) + 32) * 896);
    u32 vq[3];
#pragma unroll
    for (int j = 0; j < 3; ++j) { const u32 n = 64 * j + l; vq[j] = (n / 12) * 2688 + (n % 12) * 16 - 1024 * j; }
#pragma unroll
    for (int a = 0; a < 2; ++a) {
      dman<3>(lb + stage_addr(a) + (w & 1) * 3072, vq, mkrs(wqkv, 448 * 2688), cb * 192 + (16 * (w & 1) + 32 * a) * 2688);
    }
  }
  if (w == 6) dma4(lb + L_Q + 512, (rk * 32 + (l & 31)) * 64, mkrs((const void*)(pc & ~255ull), CODE_KB * 1024), 0);
  u32x4 d[14];
  if (busy && w == 6 && cb < 8) {
#pragma unroll
    for (int k = 0; k < 14; ++k) d[k] = ld16_async<false>((rb * 128 + cb * 16 + (l >> 3)) * 896 + (l & 7) * 16, rX, (k / 7) * 7168 + (k % 7) * 128);
  }
  if (busy && w == 7) {
    const int r = rb * 28 + (l >> 1);
    if (l < 56) dma4(lb + L_Q, r * 2688 + cb * 192 + (l & 1) * 188, mkrs(wqkv, 448 * 2688), 0);
  }
  SB();
  u64 tag;
  auto epoch = [&] { asm volatile("s_load_dwordx2 %0, %1, 0x0\n\ts_waitcnt lgkmcnt(0)" : "=s"(tag) : "s"(ws) : "memory"); tag += 1; };
  auto vW2 = [&](u32* v) { v[0] = (l >> 3) * 896 + (((l & 7) ^ ((g & 1) << 2)) << 4); v[1] = v[0] + 7168 - 1024; };

  if (w < 4) {
    if (!busy) {
      const int q = (rk - 28) * 4 + w;
      bar();
      for (int k = 0; k < PF_DELAY; ++k) __builtin_amdgcn_s_sleep(2);
      const rsrc_t rW1 = mkrs(w1, 448 * 3584), rWo = mkrs(wo, 448 * 896);
      const u32 v = l * 16;
#pragma unroll 1
      for (int k = 0; k < 49; ++k) {
        int r = k * 16 + q + (x8 & 3) * 196; r = r >= 784 ? r - 784 : r;
        dman<1>(lb + L_WC + w * 16384 + (k & 15) * 1024, &v, rW2, kh * 802816 + r * 1024);
        vm<10>();
      }
#pragma unroll 1
      for (int k = 0; k < 25; ++k) { dman<1>(lb + L_WC + w * 16384 + (k & 15) * 1024, &v, rWo, (k * 16 + q) * 1024); vm<10>(); }
      vm<0>();
      FLUSH();
      return;
    }
    constexpr int PF = 4;
    u32 oX[4], oH[2], oQ[3];
#pragma unroll
    for (int p = 0; p < 4; ++p) oX[p] = (4 * w + ((l & 31) >> 3)) * 1024 + (l & 7) * 128 + (((2 * p + (g >> 1)) ^ (l15 >> 1)) << 4);
    const int rq = w == 3 ? 0 : w;
#pragma unroll
    for (int i = 0; i < 3; ++i) oQ[i] = RING + (8 * (g >> 1) + q4) * 192 + ((i + rq) % 3) * 64 + (g & 1) * 32 + p4 * 8;
#pragma unroll
    for (int i = 0; i < 2; ++i) oH[i] = RING + (g >> 1) * 1024 + q4 * 128 + ((i ^ (q4 >> 1)) << 6) + (g & 1) * 32 + p4 * 8;
    const u32 selv = l15 == (g & 1) ? 0x3f803f80u : 0u;
    const bf16x8 sel = __builtin_bit_cast(bf16x8, u32x4{selv, selv, selv, selv});
    bf16x8 xf[28], wf[8];
    f32x16 aH[2], aQ[3] = {};
    f32x4 cs = {};
    u32x2 ch[2];
    float rs;
    bf16x8 lnb;
    u32x4 hA[72], qp[6];
    auto rdW = [&](auto SS, auto II) {
      constexpr int s = decltype(SS)::v, i = decltype(II)::v;
      constexpr int slot = (s < 14 ? s * 6 + i : 84 + i) % 8;
      if constexpr (s < 14) { const u32 o = oQ[i % 3] + (stage_addr(s) - RING) + (i / 3) * 3072; wf[slot] = trfrag(L, o, o + 768); }
      else if constexpr (s < 28) { const u32 o = oH[i & 1] + (stage_addr(s) - RING) + (i >> 1) * 2048; wf[slot] = trfrag(L, o, o + 512); }
    };
    auto rdX = [&](auto SS, auto EE) { constexpr int k = 2 * decltype(SS)::v + decltype(EE)::v; if constexpr (k < 28) xf[k] = lds_ld<bf16x8>(L, oX[k & 3] + (k >> 2) * 16384); };
    auto rdcs = [&](int half) {
#pragma unroll
      for (int i = 0; i < 2; ++i) ch[i] = lds_ld<u32x2>(L, L_CSH + (half * 64 + i * 32 + (l & 31)) * 8);
    };
    auto hpack = [&](int half, int c) {
      u32x4 hp;
#pragma unroll
      for (int j = 0; j < 4; ++j) {
        const float t0 = aH[c >> 1][8 * (c & 1) + 2 * j], t1 = aH[c >> 1][8 * (c & 1) + 2 * j + 1];
        hp[j] = pk(t0 * fmx(t0, 0.f), t1 * fmx(t1, 0.f));
      }
      hA[half * 4 + c] = hp;
    };
    auto hstore = [&](int m) { st16<16>(hA[m], rAC, l * 16 + ((rb * 4 + w) * 144 + cb * 8 + m) * 1024); };
    auto qpack = [&](int c) {
      const int i = c >> 1, e = c & 1, tq = 6 * cb + 2 * ((i + rq) % 3) + e;
      const float m = tq < 28 ? rs * (0.18898223650461361f * 1.4426950408889634f) : rs;
      const auto s0 = __builtin_amdgcn_permlane16_swap(pk(aQ[i][8 * e] * m, aQ[i][8 * e + 1] * m), pk(aQ[i][8 * e + 4] * m, aQ[i][8 * e + 5] * m), false, false);
      const auto s1 = __builtin_amdgcn_permlane16_swap(pk(aQ[i][8 * e + 2] * m, aQ[i][8 * e + 3] * m), pk(aQ[i][8 * e + 6] * m, aQ[i][8 * e + 7] * m), false, false);
      qp[c] = u32x4{s0[0], s1[0], s0[1], s1[1]};
    };
    auto qstore = [&](int c) {
      const int fl = ((g & 1) << 1) | (g >> 1), tq = 6 * cb + 2 * (((c >> 1) + rq) % 3) + (c & 1);
      u32x4 v = qp[c];
      u32 vo = fl * 256 + l15 * 16;
      if (tq >= 56) {
        asm volatile("" ::: "memory");
        if (fl & 1) v = u32x4{v[2], v[3], v[0], v[1]};
        vo ^= (2 * (tq & 1) + (fl >> 1)) << 6;
      }
      st16<16>(v, rQKV, vo + ((rb * 4 + w) * 336 + 4 * tq) * 256);
    };
    bool sigq = false, siga = false;
    const bool giver = (w >> 1) != kh;
    bar();
    STAMP(1);
    rdX(IC<0>{}, IC<0>{}); rdX(IC<0>{}, IC<1>{});
    sfor<0, PF>([&](auto I) { rdW(IC<0>{}, I); });
    sfor<0, 28>([&](auto SS) {
      constexpr int s = decltype(SS)::v, NT = s < 14 ? 6 : 8;
      if constexpr (s == 14) wait_bar<63>();
      else if constexpr (s < 14) bar();
      if constexpr (s == 13) lanes();
      if constexpr (s == 14) {
        STAMP(2);
        u32 cq[3];
#pragma unroll
        for (int i = 0; i < 3; ++i) cq[i] = lds_ld<u32>(L, L_CSQ + (((i + rq) % 3) * 32 + (l & 31)) * 4);
        const u32 rdy = cnt_ld(L, C_ST);
        u32 st = lds_ld<u32>(L, L_ST + (w * 32 + (l & 31)) * 16);
        if (!__builtin_amdgcn_readfirstlane(rdy)) { cnt_wait(L, C_ST, 1); st = lds_ld<u32>(L, L_ST + (w * 32 + (l & 31)) * 16); }
        SB();
        lnb = __builtin_bit_cast(bf16x8, u32x4{l < 32 ? (st & 0xffffu) * 0x10001u : 0u, l < 32 ? st >> 16 : 0u, 0u, 0u});
        rs = __builtin_amdgcn_rcpf(bfhi(st));
#pragma unroll
        for (int i = 0; i < 3; ++i) aQ[i] = mfma32(__builtin_bit_cast(bf16x8, u32x4{cq[i], 0u, 0u, 0u}), lnb, aQ[i]);
        SB();
#pragma unroll
        for (int c = 0; c < 6; ++c) { qpack(c); if (c % (6 / QNOW) == 6 / QNOW - 1) qstore(c / (6 / QNOW)); SB(); }
        STAMP(3);
      }
      if constexpr (s == 21) {
        rdcs(1);
        SB();
#pragma unroll
        for (int c = 0; c < 4; ++c) { hpack(0, c); if (c < HNOW) hstore(c); SB(); }
      }
      u32 land;
      sfor<0, NT>([&](auto II) {
        constexpr int i = decltype(II)::v, slot = (s < 14 ? s * 6 + i : 84 + i) % 8;
        constexpr u32 need = s == 15 ? 1 : s == 18 ? 2 : s == 22 ? 3 : s == 25 ? 4 : 0;
        if constexpr (need && i == 1) land = land_ld(L);
        if constexpr (need && i == NT - PF) {
          if ((u32)__builtin_amdgcn_readfirstlane(land) < need) prog_wait<false>(L, need);
          asm volatile("" ::: "memory");
        }
        if constexpr (i + PF < NT) rdW(SS, IC<i + PF>{});
        else rdW(IC<s + 1>{}, IC<i + PF - NT>{});
        if constexpr (i < 2) rdX(IC<s + 1>{}, II);
        if constexpr (s < 14) {
          aQ[i % 3] = mfma32(wf[slot], xf[2 * s + i / 3], aQ[i % 3]);
          if constexpr (i % 3 == 0) cs = mfma(wf[slot], sel, cs);
          if constexpr (s == 13 && i == 4) {
            u32x4 v;
#pragma unroll
            for (int j = 0; j < 4; ++j) { const float hi = rbf(cs[j]); v[j] = pk(hi, cs[j] - hi); }
            if (l15 < 2) lds_st<u32x4>(L, L_CSQ + w * 128 + (l15 * 16 + 4 * g) * 4, v);
          }
        } else {
          constexpr int k = 4 * ((s - 14) % 7) + (i >> 1), m = (s < 21 ? QNOW : HNOW) + 2 * ((s - 14) % 7) + (i >> 2);
          if constexpr (k == 0 && s == 21) aH[i] = mfma32(__builtin_bit_cast(bf16x8, u32x4{ch[i][0], ch[i][1], 0u, 0u}), lnb, f32x16{});
          aH[i & 1] = mfma32(wf[slot], xf[k], s == 14 && k == 0 ? f32x16{} : aH[i & 1]);
          if constexpr (s == 19 && i == 7) { cnt_wait(L, C_CSH, 1); rdcs(0); }
          if constexpr (s == 20 && (i == 2 || i == 3)) aH[i & 1] = mfma32(__builtin_bit_cast(bf16x8, u32x4{ch[i & 1][0], ch[i & 1][1], 0u, 0u}), lnb, aH[i & 1]);
          if constexpr ((i & 3) == 1 && s < 21 && m < 6) qstore(m);
          if constexpr ((i & 3) == 1 && s >= 21 && m < 4) hstore(m);
        }
        SB();
      });
      if constexpr (s == 18 || s == 20 || s == 27) prog_st(L, C_DONE + w, s);
      if constexpr (s >= 14 && s <= S_FQ && QNOW + 2 * (s - 14) >= 6) { if (!sigq && (s == S_FQ || vm_le<0>())) { vm<0>(); cnt_add(lb, C_FQ); sigq = true; } }
      if constexpr (s >= 21 && HNOW + 2 * (s - 21) >= 4) { if (!siga && vm_le<0>()) { vm<0>(); cnt_add(lb, C_FA); siga = true; } }
    });
    STAMP(4);
    lanes();
    const u32 vFG = WS_FG + (rb * 16 + 7 * kh + (l < 7 ? l : 0)) * FS;
#pragma unroll
    for (int c = 0; c < 4; ++c) hpack(1, c);
    SB();
    if (!siga) { vm<0>(); cnt_add(lb, C_FA); }
#pragma unroll
    for (int m = 4; m < 8; ++m) hstore(m);
    STAMP(5);
    { const u32x2 t = lds_ld<u32x2>(L, L_CNT + 112); tag = (u64)(u32)__builtin_amdgcn_readfirstlane(t[0]) | (u64)(u32)__builtin_amdgcn_readfirstlane(t[1]) << 32; }
    u32x2 polled;
    u32 vW[2];
    vW2(vW);
    const u32 vA = l * 16, sA = (rb * 4 + w) * 144, sO = ((rb * 4 + (giver ? w ^ 2 : w)) * 144 + (giver ? 128 : 112)) * 1024;
    u32 sN[6];
#pragma unroll
    for (int j = 0; j < 6; ++j) { sN[j] = (sA + kstep(8 + 4 * j)) * 1024; asm volatile("" : "+s"(sN[j])); }
    const u32 vB = vA + 4096;
    auto ldA = [&](auto M) { constexpr int m = decltype(M)::v; hA[m] = ld16_async<true, (m & 3) * 1024>(m >= 32 ? vB : vA, rAC, sN[((m - 8) % 24) / 4]); };
    auto ldO = [&](auto M) { constexpr int m = decltype(M)::v; hA[m] = ld16_async<true, (m & 3) * 1024>(vA, rAC, sO + ((m - 56) & ~3) * 1024); };
    const rsrc_t rWo = mkrs(wo, 448 * 896);
    u32 oW[2], oO[2];
#pragma unroll
    for (int b = 0; b < 2; ++b) { oW[b] = (4 * (g >> 1) + q4) * 128 + ((b ^ (q4 >> 1)) << 6) + (g & 1) * 32 + p4 * 8; oO[b] = oW[b] + L_WC + (giver ? 8 * 3584 : 0); }
    bf16x8 wc[2][2];
    auto rdC = [&](int m) {
#pragma unroll
      for (int b = 0; b < 2; ++b) {
        const u32 o = m < 56 ? oW[b] + L_WC + m * 2048 : oO[b] + ((m - 56) >> 1) * 3584 + (m & 1) * 2048;
        wc[m & 1][b] = trfrag(L, o, o + 1024);
      }
    };
    f32x16 acc[2];
    u32x4 xres[2], bres, pp[4];
    auto step = [&](auto N) {
      constexpr int n = decltype(N)::v;
      if constexpr (n + 1 < 72) rdC(n + 1);
      SB();
      acc[0] = mfma32(wc[n & 1][0], __builtin_bit_cast(bf16x8, hA[n]), n == 0 ? f32x16{} : acc[0]);
      SB();
      if constexpr (n >= 8 && n < 56) sfor<SC.a0[n], SC.a1[n]>(ldA);
      if constexpr (n >= 8 && n < 15) dman<2>(lb + L_WC + (NWP + 4 * (n - 8) + w) * 2048, vW, rW2, (kstep(NWP + 4 * (n - 8)) + w) * (16 * 896) + cc * 128);
      if constexpr (n >= T_WO && n < T_WO + 7) dman<2>(lb + L_WC + (4 * (n - T_WO) + w) * 2048, vW, rWo, (4 * (n - T_WO) + w) * (16 * 896) + cc * 128);
      if constexpr (n == T_WO) { if (w == 0) dman<2>(lb + L_WC + 28 * 2048, vW, rWo, 28 * (16 * 896) + cc * 128); }
      acc[1] = mfma32(wc[n & 1][1], __builtin_bit_cast(bf16x8, hA[n]), n == 0 ? f32x16{} : acc[1]);
      SB();
    };
    cnt_wait(L, C_WLO, 2);
    rdC(0);
    sfor<0, OWN1>(step);
    cnt_wait(L, C_FH, 1);
    STAMP(6);
    __builtin_amdgcn_s_setprio(PRIO_C);
    sfor<8, 8 + DA>(ldA);
    prog_wait<true>(L, 27);
    sfor<OWN1, 8>(step);
    sfor<8, 56>([&](auto N) {
      constexpr int n = decltype(N)::v;
      asm volatile("s_waitcnt vmcnt(%1)" : "+v"(hA[n]) : "n"(SC.wC[n]) : "memory");
      if constexpr (n == 30) cnt_add(lb, C_P30);
      if constexpr (n == T_WO) cnt_wait(L, C_P30, 4);
      if constexpr (n == T_WO + 12) cnt_add(lb, C_WO);
      if constexpr (n == 55) cnt_wait(L, C_WO, 4);
      if constexpr (n == 8) { if (last_of4(L, C_FG) && l == 0) st_flag(rCTL, WS_FG + (rb * 16 + cb) * FS, tag); }
      if constexpr (n == T_WH) cnt_add(lb, C_WHI);
      if constexpr (n == TB - 3) polled = ld8c_async(vFG, rCTL);
      if constexpr (n == TB) { PIN(polled); if (!tagged(polled, tag)) spin<SLP>(rCTL, vFG, tag); }
      if constexpr (n == NWP - 1) cnt_wait(L, C_WHI, 4);
      if constexpr (n == 51) cnt_wait(L, C_LATE, 4);
      step(N);
      if constexpr (n >= T_O && n < T_O + 8) { ldO(IC<56 + 2 * (n - T_O)>{}); ldO(IC<57 + 2 * (n - T_O)>{}); }
    });
    acc[0] *= rs * rs; acc[1] *= rs * rs;
#pragma unroll
    for (int k = 0; k < 4; ++k) {
      const f32x16& a = acc[k >> 1];
      const int o = 8 * (k & 1);
      st16<16>(u32x4{pk(a[o], a[o + 1]), pk(a[o + 2], a[o + 3]), pk(a[o + 4], a[o + 5]), pk(a[o + 6], a[o + 7])}, rAC, (giver ? PX + ((rb * 7 + cc) * 4 + w) * 4096 : 0xffff0000u) + k * 1024 + l * 16);
    }
    if (giver) { acc[0] = f32x16{}; acc[1] = f32x16{}; }
    asm volatile("s_waitcnt vmcnt(4)" : "+v"(hA[56]), "+v"(hA[57]), "+v"(hA[58]), "+v"(hA[59]), "+v"(hA[60]), "+v"(hA[61]), "+v"(hA[62]), "+v"(hA[63]),
                 "+v"(hA[64]), "+v"(hA[65]), "+v"(hA[66]), "+v"(hA[67]), "+v"(hA[68]), "+v"(hA[69]), "+v"(hA[70]), "+v"(hA[71])::"memory");
    auto fresh = [&](const u32x4* v, int n) {
      u32 m = 0;
#pragma unroll
      for (int k = 0; k < n; ++k) m = max(max(m, max(v[k][0], v[k][1])), max(v[k][2], v[k][3]));
      return __all(m != ~0u);
    };
#pragma unroll 1
    for (u32 n = 0; n < (1u << 17) && !fresh(&hA[56], 16); ++n) {
#pragma unroll 1
      for (; n < (1u << 17); ++n) {
        asm volatile("" ::: "memory");
        if (__all(__builtin_amdgcn_raw_buffer_load_b32(rAC, sO + (l & 7) * 2048 + 1024, 0, 16) != ~0u)) break;
        __builtin_amdgcn_s_sleep(SLP);
      }
#pragma unroll
      for (int k = 0; k < 16; ++k) hA[56 + k] = __builtin_amdgcn_raw_buffer_load_b128(rAC, vA + sO + k * 1024, 0, 16);
    }
    const int tile = giver ? w ^ 2 : w, r0 = (giver ? 16 : 0) + (l >> 3);
#pragma unroll
    for (int k = 0; k < 2; ++k) xres[k] = ld16_async<false>((r0 + 8 * k) * 896 + (l & 7) * 16, rX, (rb * 128 + tile * 32) * 896 + cc * 128);
    bres = ld16_async<false>((l & 7) * 16, mkrs(b2, 896), cc * 128);
    sfor<56, T_PP>(step);
    sfor<0, 4>([&](auto K) { constexpr int k = decltype(K)::v; pp[k] = ld16_async<true, k * 1024>(vA, rAC, giver ? 0xfffff000u : PX + ((rb * 7 + cc) * 4 + w) * 4096); });
    sfor<T_PP, 72>(step);
    asm volatile("s_waitcnt vmcnt(0)" : "+v"(xres[0]), "+v"(xres[1]), "+v"(bres), "+v"(pp[0]), "+v"(pp[1]), "+v"(pp[2]), "+v"(pp[3])::"memory");
    STAMP(10);
#pragma unroll 1
    for (u32 n = 0; n < (1u << 17) && !fresh(pp, 4); ++n) {
      asm volatile("" ::: "memory");
#pragma unroll
      for (int k = 0; k < 4; ++k) pp[k] = __builtin_amdgcn_raw_buffer_load_b128(rAC, PX + ((rb * 7 + cc) * 4 + w) * 4096 + k * 1024 + vA, 0, 16);
    }
#pragma unroll
    for (int k = 0; k < 4; ++k)
#pragma unroll
      for (int j = 0; j < 4; ++j) { acc[k >> 1][8 * (k & 1) + 2 * j] += bflo(pp[k][j]); acc[k >> 1][8 * (k & 1) + 2 * j + 1] += bfhi(pp[k][j]); }
    cnt_wait(L, C_ATT, 4);
#pragma unroll
    for (int b = 0; b < 2; ++b)
#pragma unroll
      for (int k = 0; k < 4; ++k)
        lds_st<f32x4>(L, L_RED + w * 8192 + (l & 31) * 256 + (((b * 8 + k * 2 + (g >> 1)) ^ (l & 7)) << 4), f32x4{acc[b][4 * k], acc[b][4 * k + 1], acc[b][4 * k + 2], acc[b][4 * k + 3]});
    cnt_add(lb, C_PAIR0 + (w & 1));
    cnt_wait(L, C_PAIR0 + (w & 1), 2);
    const rsrc_t rO = mkrs(out, 2048 * 896);
#pragma unroll
    for (int k = 0; k < 2; ++k) {
      const int row = r0 + 8 * k;
      f32x4 a = {bflo(xres[k][0]) + bflo(bres[0]), bfhi(xres[k][0]) + bfhi(bres[0]), bflo(xres[k][1]) + bflo(bres[1]), bfhi(xres[k][1]) + bfhi(bres[1])};
      f32x4 b = {bflo(xres[k][2]) + bflo(bres[2]), bfhi(xres[k][2]) + bfhi(bres[2]), bflo(xres[k][3]) + bflo(bres[3]), bfhi(xres[k][3]) + bfhi(bres[3])};
#pragma unroll
      for (int j = 0; j < 2; ++j) {
        a += lds_ld<f32x4>(L, L_RED + ((w & 1) + 2 * j) * 8192 + row * 256 + (((2 * (l & 7)) ^ (row & 7)) << 4));
        b += lds_ld<f32x4>(L, L_RED + ((w & 1) + 2 * j) * 8192 + row * 256 + (((2 * (l & 7) + 1) ^ (row & 7)) << 4));
      }
      st16<16>(u32x4{pk(a[0], a[1]), pk(a[2], a[3]), pk(b[0], b[1]), pk(b[2], b[3])}, rO, (rb * 128 + tile * 32 + row) * 896 + cc * 128 + (l & 7) * 16);
    }
    if (w == 0) STAMP(11);
    FLUSH();
    return;
  }

  const int li = w & 1;
  auto clear_o = [&] {
#pragma unroll
    for (int k = 0; k < 8; ++k) st16<16>(u32x4{~0u, ~0u, ~0u, ~0u}, rAC, ((ai * 4 + (k >> 1)) * 144 + 112 + 2 * ah + (k & 1)) * 1024 + l * 16);
  };
  if (!busy) {
    const int blk = (rk - 28) * 8 + x8;
#pragma unroll
    for (int k = 0; k < 14; ++k) d[k] = __builtin_amdgcn_raw_buffer_load_b128(mkrs(w1, 448 * 3584), (112 * (w - 4) + (l >> 3)) * 3584 + (l & 7) * 16 + (blk < 28 ? blk * 128 : 0x7fffffff), k * 8 * 3584, 0);
    const u32 bias = __builtin_amdgcn_raw_buffer_load_b16(mkrs(b1, 3584), (blk * 64 + l) * 2, 0, 0);
    SB();
    epoch();
    if (w == 6 && l < C_N) lds_st<u32>(L, L_CNT + l * 4, l < 4 ? 64u : 0u);
    wait_bar<63>();
    if (w == 4) { vm<0>(); STAMP(38); }
    float a[8] = {};
#pragma unroll
    for (int k = 0; k < 14; ++k)
#pragma unroll
      for (int j = 0; j < 4; ++j) { a[2 * j] += bflo(d[k][j]); a[2 * j + 1] += bfhi(d[k][j]); }
    lds_st<f32x4>(L, RING + ((w - 4) * 64 + l) * 32, f32x4{a[0], a[1], a[2], a[3]});
    lds_st<f32x4>(L, RING + ((w - 4) * 64 + l) * 32 + 16, f32x4{a[4], a[5], a[6], a[7]});
    if (last_of4(L, C_CS0) && blk < 28) {
      float t = 0.f;
#pragma unroll
      for (int k = 0; k < 32; ++k) t += lds_ld<float>(L, RING + k * 256 + l * 4);
      const float hi = rbf(t);
      __builtin_amdgcn_raw_buffer_store_b64(u32x2{pk(hi, t - hi), bias}, rCTL, WS_CS + (blk * 64 + l) * 8, 0, 16);
      vm<0>();
      if (l == 0) st_flag(rCTL, WS_FC + blk * FS, tag);
      STAMP(39);
    }
    if (w == 7) { clear_o(); vm<0>(); if (l == 0) st_flag(rCTL, WS_FZ + (ai * 16 + ah) * FS, tag); }
  } else if (w < 6) {
    const rsrc_t rW1 = mkrs(w1, 448 * 3584), rWq = mkrs(wqkv, 448 * 2688);
    u32 vX[4], vH[4], vQ[3], vC[2], so7[7];
#pragma unroll
    for (int j = 0; j < 4; ++j) vX[j] = (8 * j + (l >> 3)) * 896 + (((l & 7) ^ ((j & 1) * 4 + g)) << 4) - 1024 * j;
    so7[0] = 0;
    const u32 sX = (rb * 128 + 64 * li) * 896, sH = cb * 256 + 32 * li * 3584, sQ = cb * 192 + 16 * li * 2688;
    auto issue = [&](auto O) {
      constexpr int op = decltype(O)::v, k = op / 256, a = (op % 256) / 2, e = op & 1;
      if constexpr (k == K_X) dman<4>(lb + (a * 16 + 8 * li + 4 * e) * 1024, vX, rX, sX + so7[a] * 128 + e * 28672);
      if constexpr (k == K_WA && a < 14) dman<3>(lb + stage_addr(a) + li * 3072, vQ, rWq, sQ + so7[a >> 1] * (64 * 2688) + (a & 1) * (32 * 2688));
      if constexpr (k == K_WA && a >= 14) dman<4>(lb + stage_addr(a) + li * 4096, vH, rW1, sH + so7[(a - 14) % 7] * (64 * 3584) + (a >= 21 ? 128 : 0));
      if constexpr (k == K_WCP) dman<2>(lb + L_WC + (2 * a + li) * 2048, vC, rW2, (kstep(2 * a) + li) * (16 * 896) + cc * 128);
    };
    lanes();
#pragma unroll
    for (int j = 0; j < 3; ++j) { const u32 n = 64 * j + l; vQ[j] = (n / 12) * 2688 + (n % 12) * 16 - 1024 * j; }
#pragma unroll
    for (int j = 0; j < 4; ++j) vH[j] = (8 * j + (l >> 3)) * 3584 + (((l & 7) ^ ((g & 1) << 2)) << 4) - 1024 * j;
#pragma unroll
    for (int j = 1; j < 7; ++j) so7[j] = rotl(j);
    sfor<4, NPRE>([&](auto K) { issue(IC<SC.pro[decltype(K)::v]>{}); });
    vW2(vC);
    vm_bar<SC.wA[0]>();
    sfor<NPRE, NPRO>([&](auto K) { issue(IC<SC.pro[decltype(K)::v]>{}); });
    sfor<0, 15>([&](auto SS) {
      constexpr int s = decltype(SS)::v;
      vm_bar<SC.wA[s + 1]>();
      sfor<0, (s == 14 ? 2 : MAXO)>([&](auto K) { issue(IC<SC.opA[s][decltype(K)::v]>{}); });
    });
    vm<SC.wL[0]>();
    prog_st(L, C_LAND + li, 1);
    vm<0>();
    prog_st(L, C_LAND + li, 2);
    cnt_wait(L, C_FQ, HOLD);
    sfor<2, MAXO>([&](auto K) { issue(IC<SC.opA[14][decltype(K)::v]>{}); });
    vm<SC.wL[1]>();
    prog_st(L, C_LAND + li, 3);
    epoch();
    prog_wait<true>(L, 20);
    issue(IC<OP(K_WA, 26)>{}); issue(IC<OP(K_WA, 27)>{});
    vm<0>();
    prog_st(L, C_LAND + li, 4);
    cnt_add(lb, C_WLO);
    if (w == 4) { STAMP(13); cnt_wait(L, C_FA, 5); STAMP(14); if (l == 0) st_flag(rCTL, WS_FH + (rb * 16 + cb) * FS, tag); }
    else { cnt_wait(L, C_FA, 5); spin<SLP>(rCTL, l >= 16 && l < 32 ? WS_FZ + (rb * 16 + l - 16) * FS : WS_FH + (rb * 16 + (l < 7 ? 7 * kh + l : 7 * (1 - kh) + cc)) * FS, tag); STAMP(15); cnt_add(lb, C_FH); }
  } else {
    if (w == 6 && cb < 8) {
      epoch();
      bar();
      asm volatile("s_waitcnt vmcnt(0)" : "+v"(d[0]), "+v"(d[1]), "+v"(d[2]), "+v"(d[3]), "+v"(d[4]), "+v"(d[5]), "+v"(d[6]), "+v"(d[7]), "+v"(d[8]), "+v"(d[9]), "+v"(d[10]), "+v"(d[11]), "+v"(d[12]), "+v"(d[13])::"memory");
      float s1[2] = {}, s2[2] = {};
#pragma unroll
      for (int r = 0; r < 2; ++r) {
        bar();
#pragma unroll
        for (int k = 0; k < 28; ++k) { const u32 v = d[7 * r + (k >> 2)][k & 3]; s1[r] = dot2(v, 0x3f803f80u, s1[r]); s2[r] = dot2(v, v, s2[r]); }
        PIN(s1[r]); PIN(s2[r]);
        SB();
      }
#pragma unroll
      for (int r = 0; r < 2; ++r) {
        bar();
        const float mean = sum8(s1[r]) * (1.f / 448.f);
        const float var = fmaxf(sum8(s2[r]) * (1.f / 448.f) - mean * mean, 0.f);
        const float sd = rbf(__builtin_amdgcn_sqrtf(rbf(rbf(var) + 1e-5f)));
        if ((l & 7) == 0) st16<16>(u32x4{pk(-mean, sd), (u32)tag, pk(-mean, sd), (u32)(tag >> 32)}, rCTL, WS_ST + (rb * 128 + cb * 16 + r * 8 + (l >> 3)) * 16);
        SB();
      }
    } else { bar(); bar(); epoch(); bar(); bar(); bar(); }
    if (w == 6) {
      const u32 fp = WS_FC + (2 * cb + ((l >> 1) & 1)) * FS + (l & 1) * 4;
      int ss = 0, cs = 0;
      auto look = [&] {
        vm<0>();
        if (ss == 1) {
          const u32x4 a = lds_ld<u32x4>(L, L_ST + l * 16), b = lds_ld<u32x4>(L, L_ST + 1024 + l * 16);
          if (__all(a[1] == (u32)tag && a[3] == (u32)(tag >> 32) && b[1] == (u32)tag && b[3] == (u32)(tag >> 32))) { cnt_add(lb, C_ST); ss = 3; STAMP(33); }
        }
        if (ss < 3) {
          asm volatile("s_mov_b32 m0, %0\n\ts_nop 0\n\tbuffer_load_dwordx4 %1, %2, 0 offen sc1 lds\n\tbuffer_load_dwordx4 %1, %2, 0 offen offset:1024 sc1 lds" ::"s"(lb + L_ST), "v"(l * 16 + WS_ST + rb * 2048), "s"(rCTL) : "memory");
          ss = 1;
        }
        if (cs == 2) { cnt_add(lb, C_CSH); cs = 3; STAMP(37); }
        if (cs == 1 && __all(lds_ld<u32>(L, L_POLL + l * 4) == (u32)(l & 1 ? tag >> 32 : tag))) {
          asm volatile("s_mov_b32 m0, %0\n\ts_nop 0\n\tbuffer_load_dwordx4 %1, %2, 0 offen sc1 lds" ::"s"(lb + L_CSH), "v"(l * 16 + WS_CS + cb * 1024), "s"(rCTL) : "memory");
          cs = 2;
        }
        if (cs < 2) { asm volatile("s_mov_b32 m0, %0\n\ts_nop 0\n\tbuffer_load_dword %1, %2, 0 offen sc1 lds" ::"s"(lb + L_POLL), "v"(fp), "s"(rCTL) : "memory"); cs = 1; }
      };
#pragma unroll 1
      for (int s = 4; s <= 14; ++s) {
        wait_bar<63>();
        if (s == 6 && l < 30) lds_st<u32>(L, L_CNT + l * 4, l < 28 ? 0u : (u32)(l & 1 ? tag >> 32 : tag));
        if (s >= 7 && ss + cs != 6 && vm_le<0>()) look();
      }
#pragma unroll 1
      for (u32 n = 0; n < (1u << 17) && ss + cs != 6; ++n) look();
    } else {
      vm<0>();
      int k = 0;
      auto clear = [&] {
        st16<16>(u32x4{~0u, ~0u, ~0u, ~0u}, rAC, ((ai * 4 + (k >> 1)) * 144 + 112 + 2 * ah + (k & 1)) * 1024 + l * 16);
        st16<16>(u32x4{~0u, ~0u, ~0u, ~0u}, rAC, PX + ((rb * 7 + cc) * 4 + 2 * kh) * 4096 + k * 1024 + l * 16);
        ++k;
      };
#pragma unroll 1
      for (int s = 4; s <= 14; ++s) {
        bar();
        if (s >= CLR0 && k < 8 && vm_le<CLRQ>()) clear();
      }
      if (k < 8) cnt_wait(L, C_FQ, 4);
#pragma unroll 1
      while (k < 8) clear();
      vm<0>();
      cnt_add(lb, C_FA);
      if (l == 0) st_flag(rCTL, WS_FZ + (ai * 16 + ah) * FS, tag);
    }
  }
  lanes();
  const int aw = w - 4, hh = g >> 1;
  if (w >= 6) {
    const int e = l & 1, k = l < 14 ? l >> 1 : 0;
    const int ty = k == 0 ? 0 : (k < 4 ? 1 : 2), ro = k == 0 ? 0 : (k < 4 ? k - 1 : k - 4);
    const int rr = ai - ro < 0 ? 0 : ai - ro;
    if (!busy) { for (int k = 0; k < IDLE_SLP; ++k) __builtin_amdgcn_s_sleep(15); }
    if (busy) { cnt_wait(L, C_FQ, 4); if (w == 6) STAMP(12); if (w == 6 && l == 0) st_flag(rCTL, WS_FQ + (rb * 16 + cb) * FS, tag); }
    prog_wait<true>(L, 18);
#pragma unroll
    for (int k = 0; k < 3; ++k) lds_st<u32x2>(L, L_V + (6 * li + 2 * k + (l >> 5)) * 2048 + 1792 + (l & 31) * 8, u32x2{0x3f80u, 0u});
    if (w == 6) { spin<SLP>(rCTL, WS_FQ + (rr * 16 + (ty * 448 + 28 * ah + 27 * e) / 96) * FS, tag); cnt_add(lb, C_SQ); }
    else cnt_wait(L, C_SQ, 1);
    if (w == 6) STAMP(7);
#pragma unroll
    for (int e = 0; e < 2; ++e) {
#pragma unroll
      for (int j = 0; j < 6; ++j) {
        const int b = 2 * j + li, tb = 4 * ai - 8 + b < 0 ? 0 : 4 * ai - 8 + b;
        dma1792c(lb + (e ? L_V + b * 2048 : L_K + b * 1792), l * 16, rQKV, (tb * 336 + 112 + 112 * e + 7 * ah) * 256);
      }
      if (e == 0) {
#pragma unroll
        for (int j = 0; j < 2; ++j) dma1792c(lb + L_Q + (2 * li + j) * 2048, l * 16, rQKV, ((4 * ai + 2 * li + j) * 336 + 7 * ah) * 256);
      }
    }
    vm<12>();
    cnt_add(lb, C_KV);
  }
  cnt_wait(L, C_KV, 2);
  __builtin_amdgcn_s_setprio(PRIO_B);
  if (w == 4) STAMP(8);
  u32x2 qv[4];
#pragma unroll
  for (int k = 0; k < 4; ++k) qv[k] = lds_ld<u32x2>(L, L_Q + aw * 2048 + (k == 3 ? 6 : 2 * k + hh) * 256 + l15 * 16 + (g & 1) * 8);
  if (hh) qv[3] = u32x2{0u, 0u};
  const bf16x8 qf[2] = {__builtin_bit_cast(bf16x8, u32x4{qv[0][0], qv[0][1], qv[1][0], qv[1][1]}), __builtin_bit_cast(bf16x8, u32x4{qv[2][0], qv[2][1], qv[3][0], qv[3][1]})};
  const u32 oK = L_K + aw * 1792 + hh * 256 + l15 * 16 + (g & 1) * 8, oK3 = hh ? oK + 1280 : oK + 1536;
  const int F = 7 * ah + 4 * (g & 1) + p4;
  u32 oV[2][2];
#pragma unroll
  for (int t = 0; t < 2; ++t)
#pragma unroll
    for (int e = 0; e < 2; ++e) oV[t][e] = L_V + aw * 2048 + (4 * (g & 1) + p4) * 256 + (((8 * e + 4 * hh + q4) ^ ((F & 6) << 1)) << 4) + ((t ^ (F & 1)) << 3);
  f32x16 sc[9];
  bf16x8 kf[9][2], vf[9][2];
  auto rdK = [&](int m) {
    const u32x2 a = lds_ld<u32x2>(L, oK + m * 1792), b = lds_ld<u32x2>(L, oK + m * 1792 + 512), c = lds_ld<u32x2>(L, oK + m * 1792 + 1024), d = lds_ld<u32x2>(L, oK3 + m * 1792);
    kf[m][0] = __builtin_bit_cast(bf16x8, u32x4{a[0], a[1], b[0], b[1]}); kf[m][1] = __builtin_bit_cast(bf16x8, u32x4{c[0], c[1], d[0], d[1]});
  };
  auto rdV = [&](int m) { vf[m][0] = trfrag(L, oV[0][0] + m * 2048, oV[0][1] + m * 2048); vf[m][1] = trfrag(L, oV[1][0] + m * 2048, oV[1][1] + m * 2048); };
  rdK(0); SB(); rdK(8); SB(); rdK(1); SB();
#pragma unroll
  for (int i = 0; i < 9; ++i) {
    const int m = i == 0 ? 0 : (i == 1 ? 8 : i - 1);
    if (i + 2 < 8) rdK(i + 2);
    sc[m] = mfma32(kf[m][1], qf[1], mfma32(kf[m][0], qf[0], f32x16{}));
    SB();
  }
  if (w >= 6) { vm<0>(); cnt_add(lb, C_V); }
  if (busy) {
    u32 vW[2];
    vW2(vW);
    dman<2>(lb + L_Q + aw * 2048, vW, rW2, (kstep(52) + aw) * (16 * 896) + cc * 128);
  }
  constexpr float NEG = -1e30f;
  if (ai < 2) {
    asm volatile("" ::: "memory");
    const int m0 = 8 - 4 * ai - aw;
#pragma unroll
    for (int m = 0; m < 8; ++m)
      if (m < m0) {
#pragma unroll
        for (int r = 0; r < 16; ++r) sc[m][r] = NEG;
      }
  }
  const int dq = (l & 31) - 4 * hh;
  u32 mk[8];
#pragma unroll
  for (int r = 0; r < 16; r += 2) {
    const bool c0 = (r & 3) + 8 * (r >> 2) <= dq, c1 = (r & 3) + 1 + 8 * (r >> 2) <= dq;
    sc[0][r] = c0 ? sc[8][r] : sc[0][r]; sc[0][r + 1] = c1 ? sc[8][r + 1] : sc[0][r + 1];
    mk[r >> 1] = c1 ? 0xffffffffu : (c0 ? 0xffffu : 0u);
  }
  cnt_wait(L, C_V, 2);
  rdV(1); rdV(2);
  SB();
  float mx0 = fmx(sc[0][0], sc[0][1]), mx1 = fmx(sc[0][2], sc[0][3]);
#pragma unroll
  for (int k = 1; k < 32; ++k) { mx0 = fmx(fmx(mx0, sc[k >> 2][4 * (k & 3)]), sc[k >> 2][4 * (k & 3) + 1]); mx1 = fmx(fmx(mx1, sc[k >> 2][4 * (k & 3) + 2]), sc[k >> 2][4 * (k & 3) + 3]); }
  mx0 = fmx(mx0, mx1);
  const auto ms = __builtin_amdgcn_permlane32_swap(__float_as_uint(mx0), __float_as_uint(mx0), false, false);
  const float mx = fmx(__uint_as_float(ms[0]), __uint_as_float(ms[1]));
  vm<0>();
  cnt_add(lb, C_LATE);
  SB();
  f32x16 o = {};
#pragma unroll
  for (int i = 0; i < 8; ++i) {
    const int m = (i + 1) & 7;
    if (i < 5) rdV(i + 3); else if (i == 5) rdV(0); else if (i == 6) rdV(8);
    u32x4 p[2];
#pragma unroll
    for (int j = 0; j < 8; ++j) {
      f32x2 d = f32x2{sc[m][2 * j], sc[m][2 * j + 1]} - f32x2{mx, mx};
      PIN(d);
      p[j >> 2][j & 3] = pk(__builtin_amdgcn_exp2f(d[0]), __builtin_amdgcn_exp2f(d[1]));
    }
#pragma unroll
    for (int s = 0; s < 2; ++s) {
      if (m) o = mfma32(vf[m][s], __builtin_bit_cast(bf16x8, p[s]), o);
      else {
        const u32x4 k4 = {mk[4 * s], mk[4 * s + 1], mk[4 * s + 2], mk[4 * s + 3]};
        o = mfma32(vf[0][s], __builtin_bit_cast(bf16x8, p[s] & ~k4), o);
        o = mfma32(vf[8][s], __builtin_bit_cast(bf16x8, p[s] & k4), o);
      }
    }
    SB();
  }
  cnt_add(lb, C_ATT);
  const auto dn = __builtin_amdgcn_permlane32_swap(__float_as_uint(o[12]), __float_as_uint(o[12]), false, false);
  const float inv = __builtin_amdgcn_rcpf(__uint_as_float(dn[1]));
  const float in2 = hh ? 0.f : inv;
  const u32 so = ((ai * 4 + aw) * 144 + 112 + 2 * ah) * 1024;
  st16<16>(u32x4{pk(o[0] * inv, o[1] * inv), pk(o[2] * inv, o[3] * inv), pk(o[4] * inv, o[5] * inv), pk(o[6] * inv, o[7] * inv)}, rAC, l * 16 + so);
  st16<16>(u32x4{pk(o[8] * inv, o[9] * inv), pk(o[10] * inv, o[11] * inv), pk(o[12] * in2, o[13] * in2), pk(o[14] * in2, o[15] * in2)}, rAC, l * 16 + so + 1024);
  if (w == 4) STAMP(9);
  vm<0>();
  if (last_of4(L, C_FB)) {
    if (l == 0) st_flag(rCTL, WS_FB + (ai * 16 + ah) * FS, tag);
    if (blockIdx.x == 255) {
#pragma unroll 1
      for (int k = 0; k < 4; ++k) spin<32>(rCTL, WS_FB + (k * 64 + l) * FS, tag);
      if (l == 0) st_flag(rCTL, 0, tag);
    }
  }
  FLUSH();
}

extern "C" void kernel_launch(void* const* d_in, const int* in_sizes, int n_in, void* d_out, int out_size, void* d_ws, size_t ws_size, hipStream_t stream) {
  fused_block<<<256, 512, 0, stream>>>((const char*)d_in[0], (const char*)d_in[1], (const char*)d_in[2], (const char*)d_in[4], (const char*)d_in[5],
                                       (const char*)d_in[6], (const char*)d_in[7], (char*)d_out, (char*)d_ws + WS_OFF);
}
```

```cpp
#include <hip/hip_runtime.h>
#include <hip/hip_bf16.h>
#include <stdint.h>

#define DI __device__ __forceinline__
#define LAS __attribute__((address_space(3)))
typedef unsigned u32;
typedef unsigned long long u64;
typedef __bf16 bf16x8 __attribute__((ext_vector_type(8)));
typedef __bf16 bf16x2 __attribute__((ext_vector_type(2)));
typedef short s16x4 __attribute__((ext_vector_type(4)));
typedef float f32x4 __attribute__((ext_vector_type(4)));
typedef float f32x2 __attribute__((ext_vector_type(2)));
typedef float f32x16 __attribute__((ext_vector_type(16)));
typedef unsigned u32x4 __attribute__((ext_vector_type(4)));
typedef unsigned u32x2 __attribute__((ext_vector_type(2)));
typedef __amdgpu_buffer_rsrc_t rsrc_t;

template <int N> struct IC { static constexpr int v = N; };
template <int B, int E, class F> DI void sfor(F&& f) { if constexpr (B < E) { f(IC<B>{}); sfor<B + 1, E>(f); } }

constexpr u32 FS = 128;
constexpr u32 WS_FH = FS, WS_FQ = WS_FH + 256 * FS, WS_FB = WS_FQ + 256 * FS, WS_FG = WS_FB + 256 * FS, WS_FP = WS_FG + 256 * FS, WS_FC = WS_FP + 448 * FS;
constexpr u32 WS_FS = WS_FC + 32 * FS, WS_FZ = WS_FS + 128 * FS, WS_CS = WS_FZ + 287 * FS, WS_ST = WS_CS + 16384, CTL_BYTES = WS_ST + 32768;
constexpr u32 WS_QKV = CTL_BYTES, QKV_BYTES = 64u * 336u * 256u;
constexpr u32 WS_AC = WS_QKV + QKV_BYTES, AC_BYTES = 64u * 144u * 1024u + 448u * 4096u;
constexpr u32 PX = 64u * 144u * 1024u;
constexpr u32 RING = 7 * 16384;
constexpr u32 L_ST = 40960, L_POLL = 45056, L_CSQ = 46080, L_CSH = 46848, L_CNT = 48896;
constexpr u32 L_K = 0, L_V = 21504, L_Q = RING + 5 * 8192;
constexpr u32 L_WC = 49152, L_RED = 0;
enum { C_DONE, C_LAND = 4, C_FQ = 6, C_FA, C_FG, C_KV, C_FB, C_WLO, C_WHI, C_P30, C_WO, C_ATT, C_LATE, C_PAIR0, C_PAIR1, C_CSH, C_FH, C_SQ, C_V, C_ST, C_CS0, C_N };
constexpr u32 stage_addr(int s) {
  return s < 14 ? RING + (s % 6) * 6144u : s < 19 ? (s - 14) * 8192u : s < 21 ? RING + (s - 19) * 8192u : s < 23 ? 6 * 16384u + (s - 21) * 8192u : s < 26 ? RING + (s - 21) * 8192u : RING + (s - 26) * 8192u;
}
constexpr int NWP = 24;

enum { K_NONE = 0, K_X, K_WA, K_BIAS, K_WCP };
constexpr int OP(int k, int a, int e = 0) { return k * 256 + 2 * a + e; }
constexpr int opn(int op) { int k = op / 256, a = (op % 256) / 2; return k == K_NONE ? 0 : k == K_X ? 4 : k == K_WA ? (a < 14 ? 3 : 4) : k == K_WCP ? 2 : 1; }
#define CAP_1 8
#define P_NPRO 8
#define P_DA 12
#define P_TB 16
#define P_TO 44
#define P_FQ 18
#define PRIO_C 0
#define PRIO_B 1
#define ROTA 0
#define WS_OFF 0
#define CODE_KB 39
#define PF_DELAY 20
#define P_TFP 66
#define SLP 2
#define LSLP 1
#define GATE_K 0
#define OWN1 4
#define CLR0 4
#define TOUCH 1
#define HNOW 2
#define CLRQ 0
#define HOLD 4
#define IDLE_SLP 9
#define T_PP 62
#define P_PF 4
#define WCP_LATE 0
#define TLBW 1
#define QNOW 6
#define P_NPRE 6
constexpr int NPRE = P_NPRE;
constexpr int MAXO = 24, NPRO = P_NPRO, DA = P_DA, TB = P_TB, S_FQ = P_FQ, T_WH = 20, T_WO = 33, T_O = P_TO;
struct Sched {
  int pro[NPRO], opA[15][MAXO];
  int wA[16];
  int wL[2];
  int wC[56], a0[56], a1[56];
  int ok;
};
constexpr int cl(int n) { return n > 63 ? 63 : n; }
constexpr Sched make_sched() {
  Sched r{};
  r.ok = 1;
  int n = 0, iW[26] = {}, iX[7] = {};
  int lo[128] = {}, le[128] = {}, nl = 0;
  for (int s = 0; s < 26; ++s) {
    if (s < 14 && !(s & 1)) { lo[nl] = OP(K_X, s >> 1, 0); le[nl++] = -1; lo[nl] = OP(K_X, s >> 1, 1); le[nl++] = -1; }
    lo[nl] = OP(K_WA, s); le[nl++] = s < 14 ? s - 5 : (s < 19 ? 5 : (s == 21 || s == 22 ? 13 : 14));
  }
  for (int k = 0; k < NWP / 2; ++k) { lo[nl] = OP(K_WCP, k); le[nl++] = 7 + 2 * (k >> 2); }
  bool done[128] = {};
  auto note = [&](int op) { n += opn(op); int k = op / 256, a = (op % 256) / 2; if (k == K_X) iX[a] = (op & 1) ? n : 0; if (k == K_WA) iW[a] = n; };
  for (int k = 0; k < NPRO; ++k) { if (k == NPRE) r.wA[0] = cl(n - iW[0]); r.pro[k] = lo[k]; done[k] = true; note(lo[k]); }
  for (int s = 0; s < 15; ++s) {
    int need = iW[s + 1]; if (!need) r.ok = 0;
    if (s + 1 <= 13) { int v = iX[(s + 1) >> 1]; if (!v) r.ok = 0; if (v > need) need = v; }
    r.wA[s + 1] = cl(n - need);
    int cap = s == 14 ? 64 : CAP_1, c = 0, used = 0;
    for (int k = 0; k < nl; ++k) if (!done[k] && le[k] <= s && used + opn(lo[k]) <= cap && c < MAXO) { done[k] = true; r.opA[s][c++] = lo[k]; used += opn(lo[k]); note(lo[k]); if (s == 14 && c == 2) r.wL[0] = cl(n - iW[18]); }
  }
  r.wL[1] = cl(n - iW[25]);
  if (r.opA[14][0] != OP(K_WA, 19) || r.opA[14][4] != OP(K_WA, 25) || r.opA[14][12] != OP(K_WCP, 11) || r.opA[14][13] != 0) r.ok = 0;
  for (int k = 0; k < nl; ++k) if (!done[k]) r.ok = 0;
  n = 0;
  int iA[56] = {}, iP = 0, iH = 0, iO = 0, nA = 8 + DA;
  for (int m = 8; m < 8 + DA; ++m) { n += 1; iA[m] = n; }
  for (int s = 8; s < 56; ++s) {
    int need = iA[s]; if (!need) r.ok = 0;
    if (s == TB && iP > need) need = iP;
    if (s == T_WH && iH > need) need = iH;
    if (s == T_WO + 12 && iO > need) need = iO;
    r.wC[s] = cl(n - need);
    if (s == TB - 3) { n += 1; iP = n; }
    r.a0[s] = nA;
    for (int c = 0; c < 2 && nA < 56 && nA <= s + DA && (nA < 32 || s >= TB); ++c) { n += 1; iA[nA++] = n; }
    r.a1[s] = nA;
    if (s < 15) { n += 2; iH = n; }
    if (s >= T_WO && s < T_WO + 7) { n += 2; iO = n; }
    if (s >= T_O && s < T_O + 8) n += 2;
  }
  return r;
}
constexpr Sched SC = make_sched();
static_assert(SC.ok, "schedule");

DI rsrc_t mkrs(const void* p, u32 bytes) { return __builtin_amdgcn_make_buffer_rsrc((void*)p, 0, bytes, 0x00020000); }
template <int N> DI void dman(u32 lds, const u32* v, rsrc_t rs, u32 soff) {
  if constexpr (N == 4) asm volatile("s_mov_b32 m0, %0\n\ts_nop 0\n\tbuffer_load_dwordx4 %1, %5, %6 offen lds\n\tbuffer_load_dwordx4 %2, %5, %6 offen offset:1024 lds\n\t"
    "buffer_load_dwordx4 %3, %5, %6 offen offset:2048 lds\n\tbuffer_load_dwordx4 %4, %5, %6 offen offset:3072 lds" ::"s"(lds), "v"(v[0]), "v"(v[1]), "v"(v[2]), "v"(v[3]), "s"(rs), "s"(soff) : "memory");
  if constexpr (N == 3) asm volatile("s_mov_b32 m0, %0\n\ts_nop 0\n\tbuffer_load_dwordx4 %1, %4, %5 offen lds\n\tbuffer_load_dwordx4 %2, %4, %5 offen offset:1024 lds\n\t"
    "buffer_load_dwordx4 %3, %4, %5 offen offset:2048 lds" ::"s"(lds), "v"(v[0]), "v"(v[1]), "v"(v[2]), "s"(rs), "s"(soff) : "memory");
  if constexpr (N == 2) asm volatile("s_mov_b32 m0, %0\n\ts_nop 0\n\tbuffer_load_dwordx4 %1, %3, %4 offen lds\n\tbuffer_load_dwordx4 %2, %3, %4 offen offset:1024 lds" ::"s"(lds), "v"(v[0]), "v"(v[1]), "s"(rs), "s"(soff) : "memory");
  if constexpr (N == 1) asm volatile("s_mov_b32 m0, %0\n\ts_nop 0\n\tbuffer_load_dwordx4 %1, %2, %3 offen lds" ::"s"(lds), "v"(v[0]), "s"(rs), "s"(soff) : "memory");
}
DI void dma1792c(u32 lds, u32 voff, rsrc_t rs, u32 soff) {
  asm volatile("s_mov_b32 m0, %0\n\ts_nop 0\n\tbuffer_load_dwordx4 %1, %2, %3 offen sc1 lds\n\ts_mov_b32 exec_hi, 0xffff\n\t"
               "buffer_load_dwordx4 %1, %2, %3 offen offset:1024 sc1 lds\n\ts_mov_b32 exec_hi, -1" ::"s"(lds), "v"(voff), "s"(rs), "s"(soff) : "memory");
}
DI void dma4(u32 lds, u32 voff, rsrc_t rs, u32 soff) {
  asm volatile("s_mov_b32 m0, %0\n\ts_nop 0\n\tbuffer_load_dword %1, %2, %3 offen lds" ::"s"(lds), "v"(voff), "s"(rs), "s"(soff) : "memory");
}
template <bool C, int IMM = 0> DI u32x4 ld16_async(u32 voff, rsrc_t rs, u32 soff) {
  u32x4 r;
  if constexpr (C) asm volatile("buffer_load_dwordx4 %0, %1, %2, %3 offen offset:%4 sc1" : "=v"(r) : "v"(voff), "s"(rs), "s"(soff), "n"(IMM) : "memory");
  else asm volatile("buffer_load_dwordx4 %0, %1, %2, %3 offen" : "=v"(r) : "v"(voff), "s"(rs), "s"(soff) : "memory");
  return r;
}
DI u32x2 ld8c_async(u32 voff, rsrc_t rs) {
  u32x2 r; asm volatile("buffer_load_dwordx2 %0, %1, %2, 0 offen sc1" : "=v"(r) : "v"(voff), "s"(rs) : "memory"); return r;
}
template <int N> DI void wait_bar() { asm volatile("s_waitcnt vmcnt(%0) lgkmcnt(0)\n\ts_barrier" ::"n"(N) : "memory"); }
template <int N> DI void vm_bar() { asm volatile("s_waitcnt vmcnt(%0)\n\ts_barrier" ::"n"(N) : "memory"); }
template <int N> DI void vm() { asm volatile("s_waitcnt vmcnt(%0)" ::"n"(N) : "memory"); }
DI void bar() { asm volatile("s_barrier" ::: "memory"); }
#define PIN(x) asm volatile("" : "+v"(x))
#define SB() __builtin_amdgcn_sched_barrier(0)
DI f32x4 mfma(bf16x8 a, bf16x8 b, f32x4 c) { return __builtin_amdgcn_mfma_f32_16x16x32_bf16(a, b, c, 0, 0, 0); }
DI f32x16 mfma32(bf16x8 a, bf16x8 b, f32x16 c) { return __builtin_amdgcn_mfma_f32_32x32x16_bf16(a, b, c, 0, 0, 0); }
DI u32 pk(float a, float b) { bf16x2 v = {(__bf16)a, (__bf16)b}; return __builtin_bit_cast(u32, v); }
DI float fmx(float a, float b) { return __builtin_elementwise_maximum(a, b); }
DI float rbf(float a) { return (float)(__bf16)a; }
DI float bflo(u32 v) { return __uint_as_float(v << 16); }
DI float bfhi(u32 v) { return __uint_as_float(v & 0xffff0000u); }
DI float dot2(u32 a, u32 b, float c) { return __builtin_amdgcn_fdot2_f32_bf16(__builtin_bit_cast(bf16x2, a), __builtin_bit_cast(bf16x2, b), c, false); }
DI float sum8(float v) {
  v += __builtin_bit_cast(float, __builtin_amdgcn_update_dpp(0, __builtin_bit_cast(int, v), 0xb1, 0xf, 0xf, true));
  v += __builtin_bit_cast(float, __builtin_amdgcn_update_dpp(0, __builtin_bit_cast(int, v), 0x4e, 0xf, 0xf, true));
  v += __builtin_bit_cast(float, __builtin_amdgcn_update_dpp(0, __builtin_bit_cast(int, v), 0x141, 0xf, 0xf, true));
  return v;
}
template <class T> DI T lds_ld(const LAS char* base, u32 off) { return *(const LAS T*)(base + off); }
template <class T> DI void lds_st(LAS char* base, u32 off, T v) { *(LAS T*)(base + off) = v; }
DI bf16x8 trfrag(LAS char* base, u32 off0, u32 off1) {
  s16x4 lo = __builtin_amdgcn_ds_read_tr16_b64_v4i16((LAS s16x4*)(base + off0));
  s16x4 hi = __builtin_amdgcn_ds_read_tr16_b64_v4i16((LAS s16x4*)(base + off1));
  return __builtin_bit_cast(bf16x8, __builtin_shufflevector(lo, hi, 0, 1, 2, 3, 4, 5, 6, 7));
}
#define ST_AUX 16
template <int AUX> DI void st16(u32x4 v, rsrc_t rs, u32 voff) { __builtin_amdgcn_raw_buffer_store_b128(v, rs, voff, 0, AUX ? ST_AUX : 0); }
DI void st_flag(rsrc_t rc, u32 off, u64 v) { __builtin_amdgcn_raw_buffer_store_b64(u32x2{(u32)v, (u32)(v >> 32)}, rc, off, 0, 16); }
DI bool tagged(u32x2 v, u64 tag) { return __all(v[0] == (u32)tag && v[1] == (u32)(tag >> 32)); }
template <int SLEEP> DI void spin(rsrc_t rc, u32 off, u64 tag) {
#pragma unroll 1
  for (u32 n = 0; n < (1u << 17); ++n) {
    asm volatile("" ::: "memory");
    if (tagged(__builtin_amdgcn_raw_buffer_load_b64(rc, off, 0, 16), tag)) break;
    __builtin_amdgcn_s_sleep(SLEEP);
  }
  asm volatile("" ::: "memory");
}
DI u32 cnt_ld(LAS char* L, int k) { return __hip_atomic_load((LAS u32*)(L + L_CNT + 4 * k), __ATOMIC_RELAXED, __HIP_MEMORY_SCOPE_WORKGROUP); }
DI void cnt_wait(LAS char* L, int k, u32 v) {
#pragma unroll 1
  for (u32 n = 0; n < (1u << 20); ++n) {
    if ((u32)__builtin_amdgcn_readfirstlane(cnt_ld(L, k)) >= v) break;
    __builtin_amdgcn_s_sleep(LSLP);
  }
  asm volatile("" ::: "memory");
}
DI void prog_st(LAS char* L, int k, u32 v) { asm volatile("" ::: "memory"); __hip_atomic_store((LAS u32*)(L + L_CNT + 4 * k), v, __ATOMIC_RELAXED, __HIP_MEMORY_SCOPE_WORKGROUP); asm volatile("" ::: "memory"); }
DI u32 land_ld(LAS char* L) { const u64 v = __hip_atomic_load((LAS u64*)(L + L_CNT + 4 * C_LAND), __ATOMIC_RELAXED, __HIP_MEMORY_SCOPE_WORKGROUP); return min((u32)v, (u32)(v >> 32)); }
DI u32 done_ld(LAS char* L) {
  const u64 a = __hip_atomic_load((LAS u64*)(L + L_CNT), __ATOMIC_RELAXED, __HIP_MEMORY_SCOPE_WORKGROUP), b = __hip_atomic_load((LAS u64*)(L + L_CNT + 8), __ATOMIC_RELAXED, __HIP_MEMORY_SCOPE_WORKGROUP);
  return min(min((u32)a, (u32)(a >> 32)), min((u32)b, (u32)(b >> 32)));
}
template <bool DONE> DI void prog_wait(LAS char* L, u32 v) {
#pragma unroll 1
  for (u32 n = 0; n < (1u << 20); ++n) {
    if ((u32)__builtin_amdgcn_readfirstlane(DONE ? done_ld(L) : land_ld(L)) >= v) break;
    __builtin_amdgcn_s_sleep(LSLP);
  }
  asm volatile("" ::: "memory");
}
DI void cnt_add(u32 lb, int k) {
  asm volatile("s_mov_b64 exec, 1\n\tds_add_u32 %0, %1\n\ts_mov_b64 exec, -1" ::"v"(lb + L_CNT + 4 * k), "v"(1u) : "memory");
}
template <int N> DI bool vm_le() {
  u32 r; asm volatile("s_getreg_b32 %0, hwreg(HW_REG_IB_STS)" : "=s"(r)::"memory");
  return (r & 0xc0000fu) <= (u32)N;
}
DI bool last_of4(LAS char* L, int k) {
  u32 old = 0;
  asm volatile("" ::: "memory");
  if (__builtin_amdgcn_mbcnt_hi(~0u, __builtin_amdgcn_mbcnt_lo(~0u, 0u)) == 0) old = __hip_atomic_fetch_add((LAS u32*)(L + L_CNT + 4 * k), 1u, __ATOMIC_RELAXED, __HIP_MEMORY_SCOPE_WORKGROUP);
  asm volatile("" ::: "memory");
  return __builtin_amdgcn_readfirstlane(old) == 3;
}
#define STAMP(k)
#define FLUSH()

__global__ __launch_bounds__(512, 2) void fused_block(const char* __restrict__ x, const char* __restrict__ wqkv, const char* __restrict__ wo,
                                                      const char* __restrict__ w1, const char* __restrict__ b1, const char* __restrict__ w2,
                                                      const char* __restrict__ b2, char* __restrict__ out, char* ws) {
  __shared__ __attribute__((aligned(1024))) char smem[163840];
  asm volatile("" : "+s"(x), "+s"(wqkv), "+s"(wo), "+s"(w1), "+s"(b1), "+s"(w2), "+s"(b2), "+s"(out), "+s"(ws));
  u64 pc; asm volatile("s_getpc_b64 %0" : "=s"(pc));
  LAS char* const L = (LAS char*)smem;
  const u32 lb = (u32)(uintptr_t)smem;
  int l = threadIdx.x & 63, g, q4, p4, l15;
  auto lanes = [&] { PIN(l); g = l >> 4; q4 = (l >> 2) & 3; p4 = l & 3; l15 = l & 15; };
  lanes();
  int w = __builtin_amdgcn_readfirstlane(threadIdx.x >> 6);
  asm volatile("s_nop 4" : "+s"(w));
  const int x8 = blockIdx.x & 7, rk = blockIdx.x >> 3;
  const bool busy = rk < 28;
  if (w == 0) STAMP(0);
  const rsrc_t rQKV = mkrs(ws + WS_QKV, QKV_BYTES), rAC = mkrs(ws + WS_AC, AC_BYTES), rCTL = mkrs(ws, CTL_BYTES);
  const rsrc_t rX = mkrs(x, 2048 * 896), rW2 = mkrs(w2, 1792 * 896);
  const int kh = x8 >> 2, cc = rk % 7, rb = (x8 & 3) * 4 + rk / 7, cb = kh * 7 + cc;
  const int ai = rk & 15, ah = 2 * x8 + (rk >> 4);
  const int ro6 = ROTA ? (cc + ((3 * (rk / 7)) >> 1)) % 6 : 0;
  auto rotl = [&](int j) { const int t = j + ro6; return j == 0 ? 0 : (t > 6 ? t - 6 : t); };
  auto kstep = [&](int p) {
    if (p < 8) return 8 * cb + p;
    const int t = cc + 1 + ((p - 8) % 24) / 4;
    return 8 * (7 * kh + (t >= 7 ? t - 7 : t)) + (p & 3) + (p >= 32 ? 4 : 0);
  };
  if (busy && (w == 4 || w == 5)) {
    u32 v[4];
#pragma unroll
    for (int j = 0; j < 4; ++j) v[j] = (8 * j + (l >> 3)) * 896 + (((l & 7) ^ ((j & 1) * 4 + g)) << 4) - 1024 * j;
    dman<4>(lb + (w & 1) * 8192, v, rX, (rb * 128 + 64 * (w & 1)) * 896);
    dman<4>(lb + (w & 1) * 8192 + 4096, v, rX, (rb * 128 + 64 * (w & 1) + 32) * 896);
    u32 vq[3];
#pragma unroll
    for (int j = 0; j < 3; ++j) { const u32 n = 64 * j + l; vq[j] = (n / 12) * 2688 + (n % 12) * 16 - 1024 * j; }
#pragma unroll
    for (int a = 0; a < 2; ++a) {
      dman<3>(lb + stage_addr(a) + (w & 1) * 3072, vq, mkrs(wqkv, 448 * 2688), cb * 192 + (16 * (w & 1) + 32 * a) * 2688);
    }
  }
  if (w == 6) dma4(lb + L_Q + 512, (rk * 32 + (l & 31)) * 64, mkrs((const void*)(pc & ~255ull), CODE_KB * 1024), 0);
  u32x4 d[14];
  if (busy && w == 6 && cb < 8) {
#pragma unroll
    for (int k = 0; k < 14; ++k) d[k] = ld16_async<false>((rb * 128 + cb * 16 + (l >> 3)) * 896 + (l & 7) * 16, rX, (k / 7) * 7168 + (k % 7) * 128);
  }
  if (w == 7) {
    const char* p = ws + min((u32)l, 8u) * 0x200000u;
    p = l == 9 ? w1 : l == 10 ? w1 + 448 * 3584 - 4 : l == 11 ? w2 : l == 12 ? w2 + 1792 * 896 - 4 : l == 13 ? wo : l == 14 ? wo + 448 * 896 - 4 : p;
    p = l == 15 ? out : l == 16 ? out + 2048 * 896 - 4 : l == 17 ? b2 : l == 18 ? b1 : p;
    asm volatile("s_mov_b32 m0, %0\n\ts_nop 0\n\tglobal_load_lds_dword %1, off" ::"s"(lb + L_Q + 768), "v"(p) : "memory");
  }
  if (busy && w == 7) {
    const int r = rb * 28 + (l >> 1);
    if (l < 56) dma4(lb + L_Q, r * 2688 + cb * 192 + (l & 1) * 188, mkrs(wqkv, 448 * 2688), 0);
  }
  SB();
  u64 tag;
  auto epoch = [&] { asm volatile("s_load_dwordx2 %0, %1, 0x0\n\ts_waitcnt lgkmcnt(0)" : "=s"(tag) : "s"(ws) : "memory"); tag += 1; };
  auto vW2 = [&](u32* v) { v[0] = (l >> 3) * 896 + (((l & 7) ^ ((g & 1) << 2)) << 4); v[1] = v[0] + 7168 - 1024; };

  if (w < 4) {
    if (!busy) {
      const int q = (rk - 28) * 4 + w;
      bar();
      for (int k = 0; k < PF_DELAY; ++k) __builtin_amdgcn_s_sleep(2);
      const rsrc_t rW1 = mkrs(w1, 448 * 3584), rWo = mkrs(wo, 448 * 896);
      const u32 v = l * 16;
#pragma unroll 1
      for (int k = 0; k < 49; ++k) {
        int r = k * 16 + q + (x8 & 3) * 196; r = r >= 784 ? r - 784 : r;
        dman<1>(lb + L_WC + w * 16384 + (k & 15) * 1024, &v, rW2, kh * 802816 + r * 1024);
        vm<10>();
      }
#pragma unroll 1
      for (int k = 0; k < 25; ++k) { dman<1>(lb + L_WC + w * 16384 + (k & 15) * 1024, &v, rWo, (k * 16 + q) * 1024); vm<10>(); }
      vm<0>();
      FLUSH();
      return;
    }
    constexpr int PF = P_PF;
    u32 oX[4], oH[2], oQ[3];
#pragma unroll
    for (int p = 0; p < 4; ++p) oX[p] = (4 * w + ((l & 31) >> 3)) * 1024 + (l & 7) * 128 + (((2 * p + (g >> 1)) ^ (l15 >> 1)) << 4);
    const int rq = w == 3 ? 0 : w;
#pragma unroll
    for (int i = 0; i < 3; ++i) oQ[i] = RING + (8 * (g >> 1) + q4) * 192 + ((i + rq) % 3) * 64 + (g & 1) * 32 + p4 * 8;
#pragma unroll
    for (int i = 0; i < 2; ++i) oH[i] = RING + (g >> 1) * 1024 + q4 * 128 + ((i ^ (q4 >> 1)) << 6) + (g & 1) * 32 + p4 * 8;
    const u32 selv = l15 == (g & 1) ? 0x3f803f80u : 0u;
    const bf16x8 sel = __builtin_bit_cast(bf16x8, u32x4{selv, selv, selv, selv});
    bf16x8 xf[28], wf[8];
    f32x16 aH[2], aQ[3] = {};
    f32x4 cs = {};
    u32x2 ch[2];
    float rs;
    bf16x8 lnb;
    u32x4 hA[72], qp[6];
    auto rdW = [&](auto SS, auto II) {
      constexpr int s = decltype(SS)::v, i = decltype(II)::v;
      constexpr int slot = (s < 14 ? s * 6 + i : 84 + i) % 8;
      if constexpr (s < 14) { const u32 o = oQ[i % 3] + (stage_addr(s) - RING) + (i / 3) * 3072; wf[slot] = trfrag(L, o, o + 768); }
      else if constexpr (s < 28) { const u32 o = oH[i & 1] + (stage_addr(s) - RING) + (i >> 1) * 2048; wf[slot] = trfrag(L, o, o + 512); }
    };
    auto rdX = [&](auto SS, auto EE) { constexpr int k = 2 * decltype(SS)::v + decltype(EE)::v; if constexpr (k < 28) xf[k] = lds_ld<bf16x8>(L, oX[k & 3] + (k >> 2) * 16384); };
    auto rdcs = [&](int half) {
#pragma unroll
      for (int i = 0; i < 2; ++i) ch[i] = lds_ld<u32x2>(L, L_CSH + (half * 64 + i * 32 + (l & 31)) * 8);
    };
    auto hpack = [&](int half, int c) {
      u32x4 hp;
#pragma unroll
      for (int j = 0; j < 4; ++j) {
        const float t0 = aH[c >> 1][8 * (c & 1) + 2 * j], t1 = aH[c >> 1][8 * (c & 1) + 2 * j + 1];
        hp[j] = pk(t0 * fmx(t0, 0.f), t1 * fmx(t1, 0.f));
      }
      hA[half * 4 + c] = hp;
    };
    auto hstore = [&](int m) { st16<16>(hA[m], rAC, l * 16 + ((rb * 4 + w) * 144 + cb * 8 + m) * 1024); };
    auto qpack = [&](int c) {
      const int i = c >> 1, e = c & 1, tq = 6 * cb + 2 * ((i + rq) % 3) + e;
      const float m = tq < 28 ? rs * (0.18898223650461361f * 1.4426950408889634f) : rs;
      const auto s0 = __builtin_amdgcn_permlane16_swap(pk(aQ[i][8 * e] * m, aQ[i][8 * e + 1] * m), pk(aQ[i][8 * e + 4] * m, aQ[i][8 * e + 5] * m), false, false);
      const auto s1 = __builtin_amdgcn_permlane16_swap(pk(aQ[i][8 * e + 2] * m, aQ[i][8 * e + 3] * m), pk(aQ[i][8 * e + 6] * m, aQ[i][8 * e + 7] * m), false, false);
      qp[c] = u32x4{s0[0], s1[0], s0[1], s1[1]};
    };
    auto qstore = [&](int c) {
      const int fl = ((g & 1) << 1) | (g >> 1), tq = 6 * cb + 2 * (((c >> 1) + rq) % 3) + (c & 1);
      u32x4 v = qp[c];
      u32 vo = fl * 256 + l15 * 16;
      if (tq >= 56) {
        asm volatile("" ::: "memory");
        if (fl & 1) v = u32x4{v[2], v[3], v[0], v[1]};
        vo ^= (2 * (tq & 1) + (fl >> 1)) << 6;
      }
      st16<16>(v, rQKV, vo + ((rb * 4 + w) * 336 + 4 * tq) * 256);
    };
    bool sigq = false, siga = false;
    const bool giver = (w >> 1) != kh;
    bar();
    STAMP(1);
    rdX(IC<0>{}, IC<0>{}); rdX(IC<0>{}, IC<1>{});
    sfor<0, PF>([&](auto I) { rdW(IC<0>{}, I); });
    sfor<0, 28>([&](auto SS) {
      constexpr int s = decltype(SS)::v, NT = s < 14 ? 6 : 8;
      if constexpr (s == 14) wait_bar<63>();
      else if constexpr (s < 14) bar();
      if constexpr (s == 13) lanes();
      if constexpr (s == 14) {
        STAMP(2);
        u32 cq[3];
#pragma unroll
        for (int i = 0; i < 3; ++i) cq[i] = lds_ld<u32>(L, L_CSQ + (((i + rq) % 3) * 32 + (l & 31)) * 4);
        const u32 rdy = cnt_ld(L, C_ST);
        u32 st = lds_ld<u32>(L, L_ST + (w * 32 + (l & 31)) * 16);
        if (!__builtin_amdgcn_readfirstlane(rdy)) { cnt_wait(L, C_ST, 1); st = lds_ld<u32>(L, L_ST + (w * 32 + (l & 31)) * 16); }
        SB();
        lnb = __builtin_bit_cast(bf16x8, u32x4{l < 32 ? (st & 0xffffu) * 0x10001u : 0u, l < 32 ? st >> 16 : 0u, 0u, 0u});
        rs = __builtin_amdgcn_rcpf(bfhi(st));
#pragma unroll
        for (int i = 0; i < 3; ++i) aQ[i] = mfma32(__builtin_bit_cast(bf16x8, u32x4{cq[i], 0u, 0u, 0u}), lnb, aQ[i]);
        SB();
#pragma unroll
        for (int c = 0; c < 6; ++c) { qpack(c); if (c % (6 / QNOW) == 6 / QNOW - 1) qstore(c / (6 / QNOW)); SB(); }
        STAMP(3);
      }
      if constexpr (s == 21) {
        rdcs(1);
        SB();
#pragma unroll
        for (int c = 0; c < 4; ++c) { hpack(0, c); if (c < HNOW) hstore(c); SB(); }
      }
      u32 land;
      sfor<0, NT>([&](auto II) {
        constexpr int i = decltype(II)::v, slot = (s < 14 ? s * 6 + i : 84 + i) % 8;
        constexpr u32 need = s == 15 ? 1 : s == 18 ? 2 : s == 22 ? 3 : s == 25 ? 4 : 0;
        if constexpr (need && i == 0) land = land_ld(L);
        if constexpr (need && i == NT - PF) {
          if ((u32)__builtin_amdgcn_readfirstlane(land) < need) prog_wait<false>(L, need);
          asm volatile("" ::: "memory");
        }
        if constexpr (i + PF < NT) rdW(SS, IC<i + PF>{});
        else rdW(IC<s + 1>{}, IC<i + PF - NT>{});
        if constexpr (i < 2) rdX(IC<s + 1>{}, II);
        if constexpr (s < 14) {
          aQ[i % 3] = mfma32(wf[slot], xf[2 * s + i / 3], aQ[i % 3]);
          if constexpr (i % 3 == 0) cs = mfma(wf[slot], sel, cs);
          if constexpr (s == 13 && i == 4) {
            u32x4 v;
#pragma unroll
            for (int j = 0; j < 4; ++j) { const float hi = rbf(cs[j]); v[j] = pk(hi, cs[j] - hi); }
            if (l15 < 2) lds_st<u32x4>(L, L_CSQ + w * 128 + (l15 * 16 + 4 * g) * 4, v);
          }
        } else {
          constexpr int k = 4 * ((s - 14) % 7) + (i >> 1), m = (s < 21 ? QNOW : HNOW) + 2 * ((s - 14) % 7) + (i >> 2);
          if constexpr (k == 0 && s == 21) aH[i] = mfma32(__builtin_bit_cast(bf16x8, u32x4{ch[i][0], ch[i][1], 0u, 0u}), lnb, f32x16{});
          aH[i & 1] = mfma32(wf[slot], xf[k], s == 14 && k == 0 ? f32x16{} : aH[i & 1]);
          if constexpr (s == 19 && i == 7) { cnt_wait(L, C_CSH, 1); rdcs(0); }
          if constexpr (s == 20 && (i == 2 || i == 3)) aH[i & 1] = mfma32(__builtin_bit_cast(bf16x8, u32x4{ch[i & 1][0], ch[i & 1][1], 0u, 0u}), lnb, aH[i & 1]);
          if constexpr ((i & 3) == 1 && s < 21 && m < 6) qstore(m);
          if constexpr ((i & 3) == 1 && s >= 21 && m < 4) hstore(m);
        }
        SB();
      });
      if constexpr (s == 18 || s == 20 || s == 27) prog_st(L, C_DONE + w, s);
      if constexpr (s >= 14 && s <= S_FQ && QNOW + 2 * (s - 14) >= 6) { if (!sigq && (s == S_FQ || vm_le<0>())) { vm<0>(); cnt_add(lb, C_FQ); sigq = true; } }
      if constexpr (s >= 21 && HNOW + 2 * (s - 21) >= 4) { if (!siga && vm_le<0>()) { vm<0>(); cnt_add(lb, C_FA); siga = true; } }
    });
    STAMP(4);
    lanes();
    const u32 vFG = WS_FG + (rb * 16 + 7 * kh + (l < 7 ? l : 0)) * FS;
#pragma unroll
    for (int c = 0; c < 4; ++c) hpack(1, c);
    SB();
    if (!siga) { vm<0>(); cnt_add(lb, C_FA); }
#pragma unroll
    for (int m = 4; m < 8; ++m) hstore(m);
    STAMP(5);
    { const u32x2 t = lds_ld<u32x2>(L, L_CNT + 112); tag = (u64)(u32)__builtin_amdgcn_readfirstlane(t[0]) | (u64)(u32)__builtin_amdgcn_readfirstlane(t[1]) << 32; }
    u32x2 polled;
    u32 vW[2];
    vW2(vW);
    const u32 vA = l * 16, sA = (rb * 4 + w) * 144, sO = ((rb * 4 + (giver ? w ^ 2 : w)) * 144 + (giver ? 128 : 112)) * 1024;
    u32 sN[6];
#pragma unroll
    for (int j = 0; j < 6; ++j) { sN[j] = (sA + kstep(8 + 4 * j)) * 1024; asm volatile("" : "+s"(sN[j])); }
    const u32 vB = vA + 4096;
    auto ldA = [&](auto M) { constexpr int m = decltype(M)::v; hA[m] = ld16_async<true, (m & 3) * 1024>(m >= 32 ? vB : vA, rAC, sN[((m - 8) % 24) / 4]); };
    auto ldO = [&](auto M) { constexpr int m = decltype(M)::v; hA[m] = ld16_async<true, (m & 3) * 1024>(vA, rAC, sO + ((m - 56) & ~3) * 1024); };
    const rsrc_t rWo = mkrs(wo, 448 * 896);
    u32 oW[2], oO[2];
#pragma unroll
    for (int b = 0; b < 2; ++b) { oW[b] = (4 * (g >> 1) + q4) * 128 + ((b ^ (q4 >> 1)) << 6) + (g & 1) * 32 + p4 * 8; oO[b] = oW[b] + L_WC + (giver ? 8 * 3584 : 0); }
    bf16x8 wc[2][2];
    auto rdC = [&](int m) {
#pragma unroll
      for (int b = 0; b < 2; ++b) {
        const u32 o = m < 56 ? oW[b] + L_WC + m * 2048 : oO[b] + ((m - 56) >> 1) * 3584 + (m & 1) * 2048;
        wc[m & 1][b] = trfrag(L, o, o + 1024);
      }
    };
    f32x16 acc[2];
    u32x4 xres[2], bres, pp[4];
    auto step = [&](auto N) {
      constexpr int n = decltype(N)::v;
      if constexpr (n + 1 < 72) rdC(n + 1);
      SB();
      acc[0] = mfma32(wc[n & 1][0], __builtin_bit_cast(bf16x8, hA[n]), n == 0 ? f32x16{} : acc[0]);
      SB();
      if constexpr (n >= 8 && n < 56) sfor<SC.a0[n], SC.a1[n]>(ldA);
      if constexpr (n >= 8 && n < 15) dman<2>(lb + L_WC + (NWP + 4 * (n - 8) + w) * 2048, vW, rW2, (kstep(NWP + 4 * (n - 8)) + w) * (16 * 896) + cc * 128);
      if constexpr (n >= T_WO && n < T_WO + 7) dman<2>(lb + L_WC + (4 * (n - T_WO) + w) * 2048, vW, rWo, (4 * (n - T_WO) + w) * (16 * 896) + cc * 128);
      if constexpr (n == T_WO) { if (w == 0) dman<2>(lb + L_WC + 28 * 2048, vW, rWo, 28 * (16 * 896) + cc * 128); }
      acc[1] = mfma32(wc[n & 1][1], __builtin_bit_cast(bf16x8, hA[n]), n == 0 ? f32x16{} : acc[1]);
      SB();
    };
    cnt_wait(L, C_WLO, 2);
    rdC(0);
    sfor<0, OWN1>(step);
    bool sigg = false;
    auto pubg = [&] { if (last_of4(L, C_FG) && l == 0) st_flag(rCTL, WS_FG + (rb * 16 + cb) * FS, tag); sigg = true; };
#pragma unroll 1
    for (u32 n = 0; n < (1u << 20); ++n) {
      if (!sigg && vm_le<0>()) { vm<0>(); pubg(); }
      if (__builtin_amdgcn_readfirstlane(cnt_ld(L, C_FH))) break;
      __builtin_amdgcn_s_sleep(LSLP);
    }
    asm volatile("" ::: "memory");
    STAMP(6);
    __builtin_amdgcn_s_setprio(PRIO_C);
    sfor<8, 8 + DA>(ldA);
    prog_wait<true>(L, 27);
    sfor<OWN1, 8>(step);
    sfor<8, 56>([&](auto N) {
      constexpr int n = decltype(N)::v;
      asm volatile("s_waitcnt vmcnt(%1)" : "+v"(hA[n]) : "n"(SC.wC[n]) : "memory");
      if constexpr (n == 30) cnt_add(lb, C_P30);
      if constexpr (n == T_WO) cnt_wait(L, C_P30, 4);
      if constexpr (n == T_WO + 12) cnt_add(lb, C_WO);
      if constexpr (n == 55) cnt_wait(L, C_WO, 4);
      if constexpr (n == 8) { if (!sigg) pubg(); }
      if constexpr (n == T_WH) cnt_add(lb, C_WHI);
      if constexpr (n == TB - 3) polled = ld8c_async(vFG, rCTL);
      if constexpr (n == TB) { PIN(polled); if (!tagged(polled, tag)) spin<SLP>(rCTL, vFG, tag); }
      if constexpr (n == NWP - 1) cnt_wait(L, C_WHI, 4);
      if constexpr (n == 51) cnt_wait(L, C_LATE, 4);
      step(N);
      if constexpr (n >= T_O && n < T_O + 8) { ldO(IC<56 + 2 * (n - T_O)>{}); ldO(IC<57 + 2 * (n - T_O)>{}); }
    });
    acc[0] *= rs * rs; acc[1] *= rs * rs;
#pragma unroll
    for (int k = 0; k < 4; ++k) {
      const f32x16& a = acc[k >> 1];
      const int o = 8 * (k & 1);
      st16<16>(u32x4{pk(a[o], a[o + 1]), pk(a[o + 2], a[o + 3]), pk(a[o + 4], a[o + 5]), pk(a[o + 6], a[o + 7])}, rAC, (giver ? PX + ((rb * 7 + cc) * 4 + w) * 4096 : 0xffff0000u) + k * 1024 + l * 16);
    }
    if (giver) { acc[0] = f32x16{}; acc[1] = f32x16{}; }
    asm volatile("s_waitcnt vmcnt(4)" : "+v"(hA[56]), "+v"(hA[57]), "+v"(hA[58]), "+v"(hA[59]), "+v"(hA[60]), "+v"(hA[61]), "+v"(hA[62]), "+v"(hA[63]),
                 "+v"(hA[64]), "+v"(hA[65]), "+v"(hA[66]), "+v"(hA[67]), "+v"(hA[68]), "+v"(hA[69]), "+v"(hA[70]), "+v"(hA[71])::"memory");
    auto fresh = [&](const u32x4* v, int n) {
      u32 m = 0;
#pragma unroll
      for (int k = 0; k < n; ++k) m = max(max(m, max(v[k][0], v[k][1])), max(v[k][2], v[k][3]));
      return __all(m != ~0u);
    };
#pragma unroll 1
    for (u32 n = 0; n < (1u << 17) && !fresh(&hA[56], 16); ++n) {
#pragma unroll 1
      for (; n < (1u << 17); ++n) {
        asm volatile("" ::: "memory");
        if (__all(__builtin_amdgcn_raw_buffer_load_b32(rAC, sO + (l & 7) * 2048 + 1024, 0, 16) != ~0u)) break;
        __builtin_amdgcn_s_sleep(SLP);
      }
#pragma unroll
      for (int k = 0; k < 16; ++k) hA[56 + k] = __builtin_amdgcn_raw_buffer_load_b128(rAC, vA + sO + k * 1024, 0, 16);
    }
    const int tile = giver ? w ^ 2 : w, r0 = (giver ? 16 : 0) + (l >> 3);
#pragma unroll
    for (int k = 0; k < 2; ++k) xres[k] = ld16_async<false>((r0 + 8 * k) * 896 + (l & 7) * 16, rX, (rb * 128 + tile * 32) * 896 + cc * 128);
    bres = ld16_async<false>((l & 7) * 16, mkrs(b2, 896), cc * 128);
    sfor<56, T_PP>(step);
    sfor<0, 4>([&](auto K) { constexpr int k = decltype(K)::v; pp[k] = ld16_async<true, k * 1024>(vA, rAC, giver ? 0xfffff000u : PX + ((rb * 7 + cc) * 4 + w) * 4096); });
    sfor<T_PP, 72>(step);
    asm volatile("s_waitcnt vmcnt(0)" : "+v"(xres[0]), "+v"(xres[1]), "+v"(bres), "+v"(pp[0]), "+v"(pp[1]), "+v"(pp[2]), "+v"(pp[3])::"memory");
    STAMP(10);
#pragma unroll 1
    for (u32 n = 0; n < (1u << 17) && !fresh(pp, 4); ++n) {
      asm volatile("" ::: "memory");
#pragma unroll
      for (int k = 0; k < 4; ++k) pp[k] = __builtin_amdgcn_raw_buffer_load_b128(rAC, PX + ((rb * 7 + cc) * 4 + w) * 4096 + k * 1024 + vA, 0, 16);
    }
#pragma unroll
    for (int k = 0; k < 4; ++k)
#pragma unroll
      for (int j = 0; j < 4; ++j) { acc[k >> 1][8 * (k & 1) + 2 * j] += bflo(pp[k][j]); acc[k >> 1][8 * (k & 1) + 2 * j + 1] += bfhi(pp[k][j]); }
    cnt_wait(L, C_ATT, 4);
#pragma unroll
    for (int b = 0; b < 2; ++b)
#pragma unroll
      for (int k = 0; k < 4; ++k)
        lds_st<f32x4>(L, L_RED + w * 8192 + (l & 31) * 256 + (((b * 8 + k * 2 + (g >> 1)) ^ (l & 7)) << 4), f32x4{acc[b][4 * k], acc[b][4 * k + 1], acc[b][4 * k + 2], acc[b][4 * k + 3]});
    cnt_add(lb, C_PAIR0 + (w & 1));
    cnt_wait(L, C_PAIR0 + (w & 1), 2);
    const rsrc_t rO = mkrs(out, 2048 * 896);
#pragma unroll
    for (int k = 0; k < 2; ++k) {
      const int row = r0 + 8 * k;
      f32x4 a = {bflo(xres[k][0]) + bflo(bres[0]), bfhi(xres[k][0]) + bfhi(bres[0]), bflo(xres[k][1]) + bflo(bres[1]), bfhi(xres[k][1]) + bfhi(bres[1])};
      f32x4 b = {bflo(xres[k][2]) + bflo(bres[2]), bfhi(xres[k][2]) + bfhi(bres[2]), bflo(xres[k][3]) + bflo(bres[3]), bfhi(xres[k][3]) + bfhi(bres[3])};
#pragma unroll
      for (int j = 0; j < 2; ++j) {
        a += lds_ld<f32x4>(L, L_RED + ((w & 1) + 2 * j) * 8192 + row * 256 + (((2 * (l & 7)) ^ (row & 7)) << 4));
        b += lds_ld<f32x4>(L, L_RED + ((w & 1) + 2 * j) * 8192 + row * 256 + (((2 * (l & 7) + 1) ^ (row & 7)) << 4));
      }
      st16<16>(u32x4{pk(a[0], a[1]), pk(a[2], a[3]), pk(b[0], b[1]), pk(b[2], b[3])}, rO, (rb * 128 + tile * 32 + row) * 896 + cc * 128 + (l & 7) * 16);
    }
    if (w == 0) STAMP(11);
    FLUSH();
    return;
  }

  const int li = w & 1;
  auto clear_o = [&] {
#pragma unroll
    for (int k = 0; k < 8; ++k) st16<16>(u32x4{~0u, ~0u, ~0u, ~0u}, rAC, ((ai * 4 + (k >> 1)) * 144 + 112 + 2 * ah + (k & 1)) * 1024 + l * 16);
  };
  if (!busy) {
    const int blk = (rk - 28) * 8 + x8;
#pragma unroll
    for (int k = 0; k < 14; ++k) d[k] = __builtin_amdgcn_raw_buffer_load_b128(mkrs(w1, 448 * 3584), (112 * (w - 4) + (l >> 3)) * 3584 + (l & 7) * 16 + (blk < 28 ? blk * 128 : 0x7fffffff), k * 8 * 3584, 0);
    const u32 bias = __builtin_amdgcn_raw_buffer_load_b16(mkrs(b1, 3584), (blk * 64 + l) * 2, 0, 0);
    SB();
    epoch();
    if (w == 6 && l < C_N) lds_st<u32>(L, L_CNT + l * 4, l < 4 ? 64u : 0u);
    wait_bar<63>();
    float a[8] = {};
#pragma unroll
    for (int k = 0; k < 14; ++k)
#pragma unroll
      for (int j = 0; j < 4; ++j) { a[2 * j] += bflo(d[k][j]); a[2 * j + 1] += bfhi(d[k][j]); }
    lds_st<f32x4>(L, RING + ((w - 4) * 64 + l) * 32, f32x4{a[0], a[1], a[2], a[3]});
    lds_st<f32x4>(L, RING + ((w - 4) * 64 + l) * 32 + 16, f32x4{a[4], a[5], a[6], a[7]});
    if (last_of4(L, C_CS0) && blk < 28) {
      float t = 0.f;
#pragma unroll
      for (int k = 0; k < 32; ++k) t += lds_ld<float>(L, RING + k * 256 + l * 4);
      const float hi = rbf(t);
      __builtin_amdgcn_raw_buffer_store_b64(u32x2{pk(hi, t - hi), bias}, rCTL, WS_CS + (blk * 64 + l) * 8, 0, 16);
      vm<0>();
      if (l == 0) st_flag(rCTL, WS_FC + blk * FS, tag);
    }
    if (w == 7) { clear_o(); vm<0>(); if (l == 0) st_flag(rCTL, WS_FZ + (ai * 16 + ah) * FS, tag); }
  } else if (w < 6) {
    const rsrc_t rW1 = mkrs(w1, 448 * 3584), rWq = mkrs(wqkv, 448 * 2688);
    u32 vX[4], vH[4], vQ[3], vC[2], so7[7];
#pragma unroll
    for (int j = 0; j < 4; ++j) vX[j] = (8 * j + (l >> 3)) * 896 + (((l & 7) ^ ((j & 1) * 4 + g)) << 4) - 1024 * j;
    so7[0] = 0;
    const u32 sX = (rb * 128 + 64 * li) * 896, sH = cb * 256 + 32 * li * 3584, sQ = cb * 192 + 16 * li * 2688;
    auto issue = [&](auto O) {
      constexpr int op = decltype(O)::v, k = op / 256, a = (op % 256) / 2, e = op & 1;
      if constexpr (k == K_X) dman<4>(lb + (a * 16 + 8 * li + 4 * e) * 1024, vX, rX, sX + so7[a] * 128 + e * 28672);
      if constexpr (k == K_WA && a < 14) dman<3>(lb + stage_addr(a) + li * 3072, vQ, rWq, sQ + so7[a >> 1] * (64 * 2688) + (a & 1) * (32 * 2688));
      if constexpr (k == K_WA && a >= 14) dman<4>(lb + stage_addr(a) + li * 4096, vH, rW1, sH + so7[(a - 14) % 7] * (64 * 3584) + (a >= 21 ? 128 : 0));
      if constexpr (k == K_WCP) dman<2>(lb + L_WC + (2 * a + li) * 2048, vC, rW2, (kstep(2 * a) + li) * (16 * 896) + cc * 128);
    };
    lanes();
#pragma unroll
    for (int j = 0; j < 3; ++j) { const u32 n = 64 * j + l; vQ[j] = (n / 12) * 2688 + (n % 12) * 16 - 1024 * j; }
#pragma unroll
    for (int j = 0; j < 4; ++j) vH[j] = (8 * j + (l >> 3)) * 3584 + (((l & 7) ^ ((g & 1) << 2)) << 4) - 1024 * j;
#pragma unroll
    for (int j = 1; j < 7; ++j) so7[j] = rotl(j);
    sfor<4, NPRE>([&](auto K) { issue(IC<SC.pro[decltype(K)::v]>{}); });
    vW2(vC);
    vm_bar<SC.wA[0]>();
    sfor<NPRE, NPRO>([&](auto K) { issue(IC<SC.pro[decltype(K)::v]>{}); });
    sfor<0, 15>([&](auto SS) {
      constexpr int s = decltype(SS)::v;
      vm_bar<SC.wA[s + 1]>();
      sfor<0, (s == 14 ? 2 : MAXO)>([&](auto K) { issue(IC<SC.opA[s][decltype(K)::v]>{}); });
    });
    vm<SC.wL[0]>();
    prog_st(L, C_LAND + li, 1);
    vm<0>();
    prog_st(L, C_LAND + li, 2);
    cnt_wait(L, C_FQ, HOLD);
    sfor<2, MAXO>([&](auto K) { issue(IC<SC.opA[14][decltype(K)::v]>{}); });
    vm<SC.wL[1]>();
    prog_st(L, C_LAND + li, 3);
    epoch();
    prog_wait<true>(L, 20);
    issue(IC<OP(K_WA, 26)>{}); issue(IC<OP(K_WA, 27)>{});
    vm<0>();
    prog_st(L, C_LAND + li, 4);
    cnt_add(lb, C_WLO);
    if (w == 4) { STAMP(13); cnt_wait(L, C_FA, 5); STAMP(14); if (l == 0) st_flag(rCTL, WS_FH + (rb * 16 + cb) * FS, tag); }
    else { cnt_wait(L, C_FA, 5); spin<SLP>(rCTL, l >= 16 && l < 32 ? WS_FZ + (rb * 16 + l - 16) * FS : WS_FH + (rb * 16 + (l < 7 ? 7 * kh + l : 7 * (1 - kh) + cc)) * FS, tag); STAMP(15); cnt_add(lb, C_FH); }
  } else {
    if (w == 6 && cb < 8) {
      epoch();
      bar();
      asm volatile("s_waitcnt vmcnt(0)" : "+v"(d[0]), "+v"(d[1]), "+v"(d[2]), "+v"(d[3]), "+v"(d[4]), "+v"(d[5]), "+v"(d[6]), "+v"(d[7]), "+v"(d[8]), "+v"(d[9]), "+v"(d[10]), "+v"(d[11]), "+v"(d[12]), "+v"(d[13])::"memory");
      float s1[2] = {}, s2[2] = {};
#pragma unroll
      for (int r = 0; r < 2; ++r) {
        bar();
#pragma unroll
        for (int k = 0; k < 28; ++k) { const u32 v = d[7 * r + (k >> 2)][k & 3]; s1[r] = dot2(v, 0x3f803f80u, s1[r]); s2[r] = dot2(v, v, s2[r]); }
        PIN(s1[r]); PIN(s2[r]);
        SB();
      }
#pragma unroll
      for (int r = 0; r < 2; ++r) {
        bar();
        const float mean = sum8(s1[r]) * (1.f / 448.f);
        const float var = fmaxf(sum8(s2[r]) * (1.f / 448.f) - mean * mean, 0.f);
        const float sd = rbf(__builtin_amdgcn_sqrtf(rbf(rbf(var) + 1e-5f)));
        if ((l & 7) == 0) st16<16>(u32x4{pk(-mean, sd), (u32)tag, pk(-mean, sd), (u32)(tag >> 32)}, rCTL, WS_ST + (rb * 128 + cb * 16 + r * 8 + (l >> 3)) * 16);
        SB();
      }
    } else { bar(); bar(); epoch(); bar(); bar(); bar(); }
    if (w == 6) {
      const u32 fp = WS_FC + (2 * cb + ((l >> 1) & 1)) * FS + (l & 1) * 4;
      int ss = 0, cs = 0;
      auto look = [&] {
        vm<0>();
        if (ss == 1) {
          const u32x4 a = lds_ld<u32x4>(L, L_ST + l * 16), b = lds_ld<u32x4>(L, L_ST + 1024 + l * 16);
          if (__all(a[1] == (u32)tag && a[3] == (u32)(tag >> 32) && b[1] == (u32)tag && b[3] == (u32)(tag >> 32))) { cnt_add(lb, C_ST); ss = 3; STAMP(33); }
        }
        if (ss < 3) {
          asm volatile("s_mov_b32 m0, %0\n\ts_nop 0\n\tbuffer_load_dwordx4 %1, %2, 0 offen sc1 lds\n\tbuffer_load_dwordx4 %1, %2, 0 offen offset:1024 sc1 lds" ::"s"(lb + L_ST), "v"(l * 16 + WS_ST + rb * 2048), "s"(rCTL) : "memory");
          ss = 1;
        }
        if (cs == 2) { cnt_add(lb, C_CSH); cs = 3; STAMP(37); }
        if (cs == 1 && __all(lds_ld<u32>(L, L_POLL + l * 4) == (u32)(l & 1 ? tag >> 32 : tag))) {
          asm volatile("s_mov_b32 m0, %0\n\ts_nop 0\n\tbuffer_load_dwordx4 %1, %2, 0 offen sc1 lds" ::"s"(lb + L_CSH), "v"(l * 16 + WS_CS + cb * 1024), "s"(rCTL) : "memory");
          cs = 2;
        }
        if (cs < 2) { asm volatile("s_mov_b32 m0, %0\n\ts_nop 0\n\tbuffer_load_dword %1, %2, 0 offen sc1 lds" ::"s"(lb + L_POLL), "v"(fp), "s"(rCTL) : "memory"); cs = 1; }
      };
#pragma unroll 1
      for (int s = 4; s <= 14; ++s) {
        wait_bar<63>();
        if (s == 6 && l < 30) lds_st<u32>(L, L_CNT + l * 4, l < 28 ? 0u : (u32)(l & 1 ? tag >> 32 : tag));
        if (s >= 7 && ss + cs != 6 && vm_le<0>()) look();
      }
#pragma unroll 1
      for (u32 n = 0; n < (1u << 17) && ss + cs != 6; ++n) look();
    } else {
      vm<0>();
      int k = 0;
      auto clear = [&] {
        st16<16>(u32x4{~0u, ~0u, ~0u, ~0u}, rAC, ((ai * 4 + (k >> 1)) * 144 + 112 + 2 * ah + (k & 1)) * 1024 + l * 16);
        st16<16>(u32x4{~0u, ~0u, ~0u, ~0u}, rAC, PX + ((rb * 7 + cc) * 4 + 2 * kh) * 4096 + k * 1024 + l * 16);
        ++k;
      };
#pragma unroll 1
      for (int s = 4; s <= 14; ++s) {
        bar();
        if (s >= CLR0 && k < 8 && vm_le<CLRQ>()) clear();
      }
      if (k < 8) cnt_wait(L, C_FQ, 4);
#pragma unroll 1
      while (k < 8) clear();
      vm<0>();
      cnt_add(lb, C_FA);
      if (l == 0) st_flag(rCTL, WS_FZ + (ai * 16 + ah) * FS, tag);
    }
  }
  lanes();
  const int aw = w - 4, hh = g >> 1;
  if (w == 7) dma4(lb + L_POLL, (rk * 32 + (l & 31)) * 64, mkrs((const void*)(pc & ~255ull), CODE_KB * 1024), 0);
  if (w >= 6) {
    const int e = l & 1, k = l < 14 ? l >> 1 : 0;
    const int ty = k == 0 ? 0 : (k < 4 ? 1 : 2), ro = k == 0 ? 0 : (k < 4 ? k - 1 : k - 4);
    const int rr = ai - ro < 0 ? 0 : ai - ro;
    if (!busy) { for (int k = 0; k < IDLE_SLP; ++k) __builtin_amdgcn_s_sleep(15); }
    if (busy) { cnt_wait(L, C_FQ, 4); if (w == 6) STAMP(12); if (w == 6 && l == 0) st_flag(rCTL, WS_FQ + (rb * 16 + cb) * FS, tag); }
    prog_wait<true>(L, 18);
#pragma unroll
    for (int k = 0; k < 3; ++k) lds_st<u32x2>(L, L_V + (6 * li + 2 * k + (l >> 5)) * 2048 + 1792 + (l & 31) * 8, u32x2{0x3f80u, 0u});
    if (w == 6) { spin<SLP>(rCTL, WS_FQ + (rr * 16 + (ty * 448 + 28 * ah + 27 * e) / 96) * FS, tag); cnt_add(lb, C_SQ); }
    else cnt_wait(L, C_SQ, 1);
    if (w == 6) STAMP(7);
#pragma unroll
    for (int e = 0; e < 2; ++e) {
#pragma unroll
      for (int j = 0; j < 6; ++j) {
        const int b = 2 * j + li, tb = 4 * ai - 8 + b < 0 ? 0 : 4 * ai - 8 + b;
        dma1792c(lb + (e ? L_V + b * 2048 : L_K + b * 1792), l * 16, rQKV, (tb * 336 + 112 + 112 * e + 7 * ah) * 256);
      }
      if (e == 0) {
#pragma unroll
        for (int j = 0; j < 2; ++j) dma1792c(lb + L_Q + (2 * li + j) * 2048, l * 16, rQKV, ((4 * ai + 2 * li + j) * 336 + 7 * ah) * 256);
      }
    }
    vm<12>();
    cnt_add(lb, C_KV);
  }
  u32 oK = L_K + aw * 1792 + hh * 256 + l15 * 16 + (g & 1) * 8, oK3 = hh ? oK + 1280 : oK + 1536;
  u32 oQv = L_Q + aw * 2048 + hh * 256 + l15 * 16 + (g & 1) * 8;
  const int F = 7 * ah + 4 * (g & 1) + p4;
  u32 oV[2][2];
#pragma unroll
  for (int t = 0; t < 2; ++t)
#pragma unroll
    for (int e = 0; e < 2; ++e) { oV[t][e] = L_V + aw * 2048 + (4 * (g & 1) + p4) * 256 + (((8 * e + 4 * hh + q4) ^ ((F & 6) << 1)) << 4) + ((t ^ (F & 1)) << 3); PIN(oV[t][e]); }
  const int dq = (l & 31) - 4 * hh;
  u32 mk[8], vW[2];
#pragma unroll
  for (int r = 0; r < 16; r += 2) { mk[r >> 1] = (r & 3) + 1 + 8 * (r >> 2) <= dq ? 0xffffffffu : ((r & 3) + 8 * (r >> 2) <= dq ? 0xffffu : 0u); PIN(mk[r >> 1]); }
  vW2(vW);
  PIN(oK); PIN(oK3); PIN(oQv); PIN(vW[0]); PIN(vW[1]);
  cnt_wait(L, C_KV, 2);
  __builtin_amdgcn_s_setprio(PRIO_B);
  if (w == 4) STAMP(8);
  u32x2 qv[4];
#pragma unroll
  for (int k = 0; k < 4; ++k) qv[k] = lds_ld<u32x2>(L, oQv + k * 512);
  f32x16 sc[9];
  bf16x8 kf[9][2], vf[9][2];
  auto rdK = [&](int m) {
    const u32x2 a = lds_ld<u32x2>(L, oK + m * 1792), b = lds_ld<u32x2>(L, oK + m * 1792 + 512), c = lds_ld<u32x2>(L, oK + m * 1792 + 1024), d = lds_ld<u32x2>(L, oK3 + m * 1792);
    kf[m][0] = __builtin_bit_cast(bf16x8, u32x4{a[0], a[1], b[0], b[1]}); kf[m][1] = __builtin_bit_cast(bf16x8, u32x4{c[0], c[1], d[0], d[1]});
  };
  auto rdV = [&](int m) { vf[m][0] = trfrag(L, oV[0][0] + m * 2048, oV[0][1] + m * 2048); vf[m][1] = trfrag(L, oV[1][0] + m * 2048, oV[1][1] + m * 2048); };
  SB(); rdK(0); SB(); rdK(8); SB(); rdK(1); SB();
  if (hh) qv[3] = u32x2{0u, 0u};
  const bf16x8 qf[2] = {__builtin_bit_cast(bf16x8, u32x4{qv[0][0], qv[0][1], qv[1][0], qv[1][1]}), __builtin_bit_cast(bf16x8, u32x4{qv[2][0], qv[2][1], qv[3][0], qv[3][1]})};
  constexpr float NEG = -1e30f;
  const int m0 = 8 - 4 * ai - aw;
  float mx4[4];
  auto rowmax = [&](int m, bool first) {
#pragma unroll
    for (int k = 0; k < 4; ++k) {
      mx4[k] = first ? fmx(sc[m][4 * k], sc[m][4 * k + 1]) : fmx(fmx(mx4[k], sc[m][4 * k]), sc[m][4 * k + 1]);
      mx4[k] = fmx(fmx(mx4[k], sc[m][4 * k + 2]), sc[m][4 * k + 3]);
    }
    PIN(mx4[0]); PIN(mx4[1]); PIN(mx4[2]); PIN(mx4[3]);
  };
#pragma unroll
  for (int i = 0; i < 9; ++i) {
    const int m = i == 0 ? 0 : (i == 1 ? 8 : i - 1);
    if (i + 2 < 8) rdK(i + 2);
    sc[m] = mfma32(kf[m][0], qf[0], f32x16{});
    if (i == 3) {
      if (m0 > 0) {
        asm volatile("" ::: "memory");
#pragma unroll
        for (int r = 0; r < 16; ++r) sc[0][r] = NEG;
      }
#pragma unroll
      for (int r = 0; r < 16; ++r) sc[0][r] = (r & 3) + 8 * (r >> 2) <= dq ? sc[8][r] : sc[0][r];
    }
    if (i == 4) rowmax(0, true);
    sc[m] = mfma32(kf[m][1], qf[1], sc[m]);
    if (i >= 4 && i - 3 >= m0) rowmax(i - 3, false);
    SB();
  }
  if (w >= 6) { vm<0>(); cnt_add(lb, C_V); }
  if (busy) dman<2>(lb + L_Q + aw * 2048, vW, rW2, (kstep(52) + aw) * (16 * 896) + cc * 128);
  if (6 >= m0) rowmax(6, false);
  cnt_wait(L, C_V, 2);
  rdV(1); rdV(2);
  SB();
  if (7 >= m0) rowmax(7, false);
  const float mx0 = fmx(fmx(mx4[0], mx4[1]), fmx(mx4[2], mx4[3]));
  const auto ms = __builtin_amdgcn_permlane32_swap(__float_as_uint(mx0), __float_as_uint(mx0), false, false);
  const float mx = fmx(__uint_as_float(ms[0]), __uint_as_float(ms[1]));
  SB();
  f32x16 o;
  u32x4 pq[2];
#pragma unroll
  for (int i = 0; i < 16; ++i) {
    const int m = ((i >> 1) + 1) & 7, s = i & 1;
    if (!s) { if (i < 10) rdV((i >> 1) + 3); else if (i == 10) rdV(0); else if (i == 12) rdV(8); }
    if (i == 6) { vm<0>(); cnt_add(lb, C_LATE); }
    const float mm = m != 0 && m < m0 ? -NEG : mx;
    f32x2 d[4];
#pragma unroll
    for (int j = 0; j < 4; ++j) { d[j] = f32x2{sc[m][8 * s + 2 * j], sc[m][8 * s + 2 * j + 1]} - f32x2{mm, mm}; PIN(d[j]); }
#pragma unroll
    for (int j = 0; j < 2; ++j) { d[j][0] = __builtin_amdgcn_exp2f(d[j][0]); d[j][1] = __builtin_amdgcn_exp2f(d[j][1]); PIN(d[j]); }
    SB();
    if (i >= 1 && i < 15) o = mfma32(vf[((i - 1) >> 1) + 1][s ^ 1], __builtin_bit_cast(bf16x8, pq[s ^ 1]), i == 1 ? f32x16{} : o);
    if (i == 15) {
      const u32x4 k4 = {mk[0], mk[1], mk[2], mk[3]};
      o = mfma32(vf[0][0], __builtin_bit_cast(bf16x8, pq[0] & ~k4), o);
      o = mfma32(vf[8][0], __builtin_bit_cast(bf16x8, pq[0] & k4), o);
    }
    SB();
#pragma unroll
    for (int j = 2; j < 4; ++j) { d[j][0] = __builtin_amdgcn_exp2f(d[j][0]); d[j][1] = __builtin_amdgcn_exp2f(d[j][1]); }
#pragma unroll
    for (int j = 0; j < 4; ++j) pq[s][j] = pk(d[j][0], d[j][1]);
    PIN(pq[s]);
    SB();
  }
  {
    const u32x4 k4 = {mk[4], mk[5], mk[6], mk[7]};
    o = mfma32(vf[0][1], __builtin_bit_cast(bf16x8, pq[1] & ~k4), o);
    o = mfma32(vf[8][1], __builtin_bit_cast(bf16x8, pq[1] & k4), o);
  }
  SB();
  cnt_add(lb, C_ATT);
  const auto dn = __builtin_amdgcn_permlane32_swap(__float_as_uint(o[12]), __float_as_uint(o[12]), false, false);
  const float inv = __builtin_amdgcn_rcpf(__uint_as_float(dn[1]));
  const float in2 = hh ? 0.f : inv;
  const u32 so = ((ai * 4 + aw) * 144 + 112 + 2 * ah) * 1024;
  st16<16>(u32x4{pk(o[0] * inv, o[1] * inv), pk(o[2] * inv, o[3] * inv), pk(o[4] * inv, o[5] * inv), pk(o[6] * inv, o[7] * inv)}, rAC, l * 16 + so);
  st16<16>(u32x4{pk(o[8] * inv, o[9] * inv), pk(o[10] * inv, o[11] * inv), pk(o[12] * in2, o[13] * in2), pk(o[14] * in2, o[15] * in2)}, rAC, l * 16 + so + 1024);
  if (w == 4) STAMP(9);
  vm<0>();
  if (last_of4(L, C_FB)) {
    if (l == 0) st_flag(rCTL, WS_FB + (ai * 16 + ah) * FS, tag);
    if (blockIdx.x == 255) {
#pragma unroll 1
      for (int k = 0; k < 4; ++k) spin<32>(rCTL, WS_FB + (k * 64 + l) * FS, tag);
      if (l == 0) st_flag(rCTL, 0, tag);
    }
  }
  FLUSH();
}

extern "C" void kernel_launch(void* const* d_in, const int* in_sizes, int n_in, void* d_out, int out_size, void* d_ws, size_t ws_size, hipStream_t stream) {
  fused_block<<<256, 512, 0, stream>>>((const char*)d_in[0], (const char*)d_in[1], (const char*)d_in[2], (const char*)d_in[4], (const char*)d_in[5],
                                       (const char*)d_in[6], (const char*)d_in[7], (char*)d_out, (char*)d_ws + WS_OFF);
}
```

```cpp
#define TLBW 0
#include <hip/hip_runtime.h>
#include <hip/hip_bf16.h>
#include <stdint.h>

#define DI __device__ __forceinline__
#define LAS __attribute__((address_space(3)))
typedef unsigned u32;
typedef unsigned long long u64;
typedef __bf16 bf16x8 __attribute__((ext_vector_type(8)));
typedef __bf16 bf16x2 __attribute__((ext_vector_type(2)));
typedef short s16x4 __attribute__((ext_vector_type(4)));
typedef float f32x4 __attribute__((ext_vector_type(4)));
typedef float f32x2 __attribute__((ext_vector_type(2)));
typedef float f32x16 __attribute__((ext_vector_type(16)));
typedef unsigned u32x4 __attribute__((ext_vector_type(4)));
typedef unsigned u32x2 __attribute__((ext_vector_type(2)));
typedef __amdgpu_buffer_rsrc_t rsrc_t;

template <int N> struct IC { static constexpr int v = N; };
template <int B, int E, class F> DI void sfor(F&& f) { if constexpr (B < E) { f(IC<B>{}); sfor<B + 1, E>(f); } }

constexpr u32 FS = 128;
constexpr u32 WS_FH = FS, WS_FQ = WS_FH + 256 * FS, WS_FB = WS_FQ + 256 * FS, WS_FG = WS_FB + 256 * FS, WS_FP = WS_FG + 256 * FS, WS_FC = WS_FP + 448 * FS;
constexpr u32 WS_FS = WS_FC + 32 * FS, WS_FZ = WS_FS + 128 * FS, WS_CS = WS_FZ + 287 * FS, WS_ST = WS_CS + 16384, CTL_BYTES = WS_ST + 32768;
constexpr u32 WS_QKV = CTL_BYTES, QKV_BYTES = 64u * 336u * 256u;
constexpr u32 WS_AC = WS_QKV + QKV_BYTES, AC_BYTES = 64u * 144u * 1024u + 448u * 4096u;
constexpr u32 PX = 64u * 144u * 1024u;
constexpr u32 RING = 7 * 16384;
constexpr u32 L_ST = 40960, L_POLL = 45056, L_CSQ = 46080, L_CSH = 46848, L_CNT = 48896;
constexpr u32 L_K = 0, L_V = 21504, L_Q = RING + 5 * 8192;
constexpr u32 L_WC = 49152, L_RED = 0;
enum { C_DONE, C_LAND = 4, C_FQ = 6, C_FA, C_FG, C_KV, C_FB, C_WLO, C_WHI, C_P30, C_WO, C_ATT, C_LATE, C_PAIR0, C_PAIR1, C_CSH, C_FH, C_SQ, C_V, C_ST, C_CS0, C_N };
constexpr u32 stage_addr(int s) {
  return s < 14 ? RING + (s % 6) * 6144u : s < 19 ? (s - 14) * 8192u : s < 21 ? RING + (s - 19) * 8192u : s < 23 ? 6 * 16384u + (s - 21) * 8192u : s < 26 ? RING + (s - 21) * 8192u : RING + (s - 26) * 8192u;
}
constexpr int NWP = 24;

enum { K_NONE = 0, K_X, K_WA, K_BIAS, K_WCP };
constexpr int OP(int k, int a, int e = 0) { return k * 256 + 2 * a + e; }
constexpr int opn(int op) { int k = op / 256, a = (op % 256) / 2; return k == K_NONE ? 0 : k == K_X ? 4 : k == K_WA ? (a < 14 ? 3 : 4) : k == K_WCP ? 2 : 1; }
#define CAP_1 8
#define P_NPRO 8
#define P_DA 12
#define P_TB 16
#define P_TO 44
#define P_FQ 18
#define PRIO_C 0
#define PRIO_B 1
#define ROTA 0
#define WS_OFF 0
#define CODE_KB 40
#define PF_DELAY 20
#define P_TFP 66
#define SLP 2
#define LSLP 1
#define GATE_K 0
#define OWN1 4
#define CLR0 4
#define TOUCH 1
#define HNOW 2
#define CLRQ 0
#define HOLD 4
#define IDLE_SLP 9
#define T_PP 62
#define P_PF 4
#define WCP_LATE 0
#define WCP_EARLY 0
#define HA_EARLY 19
#define PRIO_L 0
#define PRIO_A 0
#define QNOW 6
#define P_NPRE 6
constexpr int NPRE = P_NPRE;
constexpr int MAXO = 24, NPRO = P_NPRO, DA = P_DA, TB = P_TB, S_FQ = P_FQ, T_WH = 20, T_WO = 33, T_O = P_TO;
struct Sched {
  int pro[NPRO], opA[15][MAXO];
  int wA[16];
  int wL[2];
  int wC[56], a0[56], a1[56];
  int ok;
};
constexpr int cl(int n) { return n > 63 ? 63 : n; }
constexpr Sched make_sched() {
  Sched r{};
  r.ok = 1;
  int n = 0, iW[26] = {}, iX[7] = {};
  int lo[128] = {}, le[128] = {}, nl = 0;
  for (int s = 0; s < 26; ++s) {
    if (s < 14 && !(s & 1)) { lo[nl] = OP(K_X, s >> 1, 0); le[nl++] = -1; lo[nl] = OP(K_X, s >> 1, 1); le[nl++] = -1; }
    lo[nl] = OP(K_WA, s); le[nl++] = s < 14 ? s - 5 : (s < 19 ? 5 : (s == 21 || s == 22 ? 13 : 14));
  }
  for (int k = 0; k < NWP / 2; ++k) { lo[nl] = OP(K_WCP, k); le[nl++] = k < WCP_EARLY ? 7 + 2 * (k >> 2) : 14; }
  bool done[128] = {};
  auto note = [&](int op) { n += opn(op); int k = op / 256, a = (op % 256) / 2; if (k == K_X) iX[a] = (op & 1) ? n : 0; if (k == K_WA) iW[a] = n; };
  for (int k = 0; k < NPRO; ++k) { if (k == NPRE) r.wA[0] = cl(n - iW[0]); r.pro[k] = lo[k]; done[k] = true; note(lo[k]); }
  for (int s = 0; s < 15; ++s) {
    int need = iW[s + 1]; if (!need) r.ok = 0;
    if (s + 1 <= 13) { int v = iX[(s + 1) >> 1]; if (!v) r.ok = 0; if (v > need) need = v; }
    r.wA[s + 1] = cl(n - need);
    int cap = s == 14 ? 64 : CAP_1, c = 0, used = 0;
    for (int k = 0; k < nl; ++k) if (!done[k] && le[k] <= s && used + opn(lo[k]) <= cap && c < MAXO) { done[k] = true; r.opA[s][c++] = lo[k]; used += opn(lo[k]); note(lo[k]); if (s == 14 && c == 2) r.wL[0] = cl(n - iW[18]); }
  }
  r.wL[1] = cl(n - iW[25]);
  if (r.opA[14][0] != OP(K_WA, 19)) r.ok = 0;
  for (int k = 0; k < nl; ++k) if (!done[k]) r.ok = 0;
  n = 0;
  int iA[56] = {}, iP = 0, iH = 0, iO = 0, nA = 8 + DA;
  for (int m = 8; m < 8 + DA; ++m) { n += 1; iA[m] = n; }
  for (int s = 8; s < 56; ++s) {
    int need = iA[s]; if (!need) r.ok = 0;
    if (s == TB && iP > need) need = iP;
    if (s == T_WH && iH > need) need = iH;
    if (s == T_WO + 12 && iO > need) need = iO;
    r.wC[s] = cl(n - need);
    if (s == TB - 3) { n += 1; iP = n; }
    r.a0[s] = nA;
    for (int c = 0; c < 2 && nA < 56 && nA <= s + DA && (nA < 32 || s >= TB); ++c) { n += 1; iA[nA++] = n; }
    r.a1[s] = nA;
    if (s < 15) { n += 2; iH = n; }
    if (s >= T_WO && s < T_WO + 7) { n += 2; iO = n; }
    if (s >= T_O && s < T_O + 8) n += 2;
    if (s == T_O + 8) n += 3;
  }
  return r;
}
constexpr Sched SC = make_sched();
static_assert(SC.ok, "schedule");

DI rsrc_t mkrs(const void* p, u32 bytes) { return __builtin_amdgcn_make_buffer_rsrc((void*)p, 0, bytes, 0x00020000); }
template <int N> DI void dman(u32 lds, const u32* v, rsrc_t rs, u32 soff) {
  if constexpr (N == 4) asm volatile("s_mov_b32 m0, %0\n\ts_nop 0\n\tbuffer_load_dwordx4 %1, %5, %6 offen lds\n\tbuffer_load_dwordx4 %2, %5, %6 offen offset:1024 lds\n\t"
    "buffer_load_dwordx4 %3, %5, %6 offen offset:2048 lds\n\tbuffer_load_dwordx4 %4, %5, %6 offen offset:3072 lds" ::"s"(lds), "v"(v[0]), "v"(v[1]), "v"(v[2]), "v"(v[3]), "s"(rs), "s"(soff) : "memory");
  if constexpr (N == 3) asm volatile("s_mov_b32 m0, %0\n\ts_nop 0\n\tbuffer_load_dwordx4 %1, %4, %5 offen lds\n\tbuffer_load_dwordx4 %2, %4, %5 offen offset:1024 lds\n\t"
    "buffer_load_dwordx4 %3, %4, %5 offen offset:2048 lds" ::"s"(lds), "v"(v[0]), "v"(v[1]), "v"(v[2]), "s"(rs), "s"(soff) : "memory");
  if constexpr (N == 2) asm volatile("s_mov_b32 m0, %0\n\ts_nop 0\n\tbuffer_load_dwordx4 %1, %3, %4 offen lds\n\tbuffer_load_dwordx4 %2, %3, %4 offen offset:1024 lds" ::"s"(lds), "v"(v[0]), "v"(v[1]), "s"(rs), "s"(soff) : "memory");
  if constexpr (N == 1) asm volatile("s_mov_b32 m0, %0\n\ts_nop 0\n\tbuffer_load_dwordx4 %1, %2, %3 offen lds" ::"s"(lds), "v"(v[0]), "s"(rs), "s"(soff) : "memory");
}
DI void dma1792c(u32 lds, u32 voff, rsrc_t rs, u32 soff) {
  asm volatile("s_mov_b32 m0, %0\n\ts_nop 0\n\tbuffer_load_dwordx4 %1, %2, %3 offen sc1 lds\n\ts_mov_b32 exec_hi, 0xffff\n\t"
               "buffer_load_dwordx4 %1, %2, %3 offen offset:1024 sc1 lds\n\ts_mov_b32 exec_hi, -1" ::"s"(lds), "v"(voff), "s"(rs), "s"(soff) : "memory");
}
DI void dma4(u32 lds, u32 voff, rsrc_t rs, u32 soff) {
  asm volatile("s_mov_b32 m0, %0\n\ts_nop 0\n\tbuffer_load_dword %1, %2, %3 offen lds" ::"s"(lds), "v"(voff), "s"(rs), "s"(soff) : "memory");
}
template <bool C, int IMM = 0> DI u32x4 ld16_async(u32 voff, rsrc_t rs, u32 soff) {
  u32x4 r;
  if constexpr (C) asm volatile("buffer_load_dwordx4 %0, %1, %2, %3 offen offset:%4 sc1" : "=v"(r) : "v"(voff), "s"(rs), "s"(soff), "n"(IMM) : "memory");
  else asm volatile("buffer_load_dwordx4 %0, %1, %2, %3 offen" : "=v"(r) : "v"(voff), "s"(rs), "s"(soff) : "memory");
  return r;
}
DI u32x2 ld8c_async(u32 voff, rsrc_t rs) {
  u32x2 r; asm volatile("buffer_load_dwordx2 %0, %1, %2, 0 offen sc1" : "=v"(r) : "v"(voff), "s"(rs) : "memory"); return r;
}
template <int N> DI void wait_bar() { asm volatile("s_waitcnt vmcnt(%0) lgkmcnt(0)\n\ts_barrier" ::"n"(N) : "memory"); }
template <int N> DI void vm_bar() { asm volatile("s_waitcnt vmcnt(%0)\n\ts_barrier" ::"n"(N) : "memory"); }
template <int N> DI void vm() { asm volatile("s_waitcnt vmcnt(%0)" ::"n"(N) : "memory"); }
DI void bar() { asm volatile("s_barrier" ::: "memory"); }
#define PIN(x) asm volatile("" : "+v"(x))
#define SB() __builtin_amdgcn_sched_barrier(0)
DI f32x4 mfma(bf16x8 a, bf16x8 b, f32x4 c) { return __builtin_amdgcn_mfma_f32_16x16x32_bf16(a, b, c, 0, 0, 0); }
DI f32x16 mfma32(bf16x8 a, bf16x8 b, f32x16 c) { return __builtin_amdgcn_mfma_f32_32x32x16_bf16(a, b, c, 0, 0, 0); }
DI u32 pk(float a, float b) { bf16x2 v = {(__bf16)a, (__bf16)b}; return __builtin_bit_cast(u32, v); }
DI float fmx(float a, float b) { return __builtin_elementwise_maximum(a, b); }
DI float rbf(float a) { return (float)(__bf16)a; }
DI float bflo(u32 v) { return __uint_as_float(v << 16); }
DI float bfhi(u32 v) { return __uint_as_float(v & 0xffff0000u); }
DI float dot2(u32 a, u32 b, float c) { return __builtin_amdgcn_fdot2_f32_bf16(__builtin_bit_cast(bf16x2, a), __builtin_bit_cast(bf16x2, b), c, false); }
DI float sum8(float v) {
  v += __builtin_bit_cast(float, __builtin_amdgcn_update_dpp(0, __builtin_bit_cast(int, v), 0xb1, 0xf, 0xf, true));
  v += __builtin_bit_cast(float, __builtin_amdgcn_update_dpp(0, __builtin_bit_cast(int, v), 0x4e, 0xf, 0xf, true));
  v += __builtin_bit_cast(float, __builtin_amdgcn_update_dpp(0, __builtin_bit_cast(int, v), 0x141, 0xf, 0xf, true));
  return v;
}
template <class T> DI T lds_ld(const LAS char* base, u32 off) { return *(const LAS T*)(base + off); }
template <class T> DI void lds_st(LAS char* base, u32 off, T v) { *(LAS T*)(base + off) = v; }
DI bf16x8 trfrag(LAS char* base, u32 off0, u32 off1) {
  s16x4 lo = __builtin_amdgcn_ds_read_tr16_b64_v4i16((LAS s16x4*)(base + off0));
  s16x4 hi = __builtin_amdgcn_ds_read_tr16_b64_v4i16((LAS s16x4*)(base + off1));
  return __builtin_bit_cast(bf16x8, __builtin_shufflevector(lo, hi, 0, 1, 2, 3, 4, 5, 6, 7));
}
#define ST_AUX 16
template <int AUX> DI void st16(u32x4 v, rsrc_t rs, u32 voff) { __builtin_amdgcn_raw_buffer_store_b128(v, rs, voff, 0, AUX ? ST_AUX : 0); }
DI void st_flag(rsrc_t rc, u32 off, u64 v) { __builtin_amdgcn_raw_buffer_store_b64(u32x2{(u32)v, (u32)(v >> 32)}, rc, off, 0, 16); }
DI bool tagged(u32x2 v, u64 tag) { return __all(v[0] == (u32)tag && v[1] == (u32)(tag >> 32)); }
template <int SLEEP> DI void spin(rsrc_t rc, u32 off, u64 tag) {
#pragma unroll 1
  for (u32 n = 0; n < (1u << 17); ++n) {
    asm volatile("" ::: "memory");
    if (tagged(__builtin_amdgcn_raw_buffer_load_b64(rc, off, 0, 16), tag)) break;
    __builtin_amdgcn_s_sleep(SLEEP);
  }
  asm volatile("" ::: "memory");
}
DI u32 cnt_ld(LAS char* L, int k) { return __hip_atomic_load((LAS u32*)(L + L_CNT + 4 * k), __ATOMIC_RELAXED, __HIP_MEMORY_SCOPE_WORKGROUP); }
DI void cnt_wait(LAS char* L, int k, u32 v) {
#pragma unroll 1
  for (u32 n = 0; n < (1u << 20); ++n) {
    if ((u32)__builtin_amdgcn_readfirstlane(cnt_ld(L, k)) >= v) break;
    __builtin_amdgcn_s_sleep(LSLP);
  }
  asm volatile("" ::: "memory");
}
DI void prog_st(LAS char* L, int k, u32 v) { asm volatile("" ::: "memory"); __hip_atomic_store((LAS u32*)(L + L_CNT + 4 * k), v, __ATOMIC_RELAXED, __HIP_MEMORY_SCOPE_WORKGROUP); asm volatile("" ::: "memory"); }
DI u32 land_ld(LAS char* L) { const u64 v = __hip_atomic_load((LAS u64*)(L + L_CNT + 4 * C_LAND), __ATOMIC_RELAXED, __HIP_MEMORY_SCOPE_WORKGROUP); return min((u32)v, (u32)(v >> 32)); }
DI u32 done_ld(LAS char* L) {
  const u64 a = __hip_atomic_load((LAS u64*)(L + L_CNT), __ATOMIC_RELAXED, __HIP_MEMORY_SCOPE_WORKGROUP), b = __hip_atomic_load((LAS u64*)(L + L_CNT + 8), __ATOMIC_RELAXED, __HIP_MEMORY_SCOPE_WORKGROUP);
  return min(min((u32)a, (u32)(a >> 32)), min((u32)b, (u32)(b >> 32)));
}
template <bool DONE> DI void prog_wait(LAS char* L, u32 v) {
#pragma unroll 1
  for (u32 n = 0; n < (1u << 20); ++n) {
    if ((u32)__builtin_amdgcn_readfirstlane(DONE ? done_ld(L) : land_ld(L)) >= v) break;
    __builtin_amdgcn_s_sleep(LSLP);
  }
  asm volatile("" ::: "memory");
}
DI void cnt_add(u32 lb, int k) {
  asm volatile("s_mov_b64 exec, 1\n\tds_add_u32 %0, %1\n\ts_mov_b64 exec, -1" ::"v"(lb + L_CNT + 4 * k), "v"(1u) : "memory");
}
template <int N> DI bool vm_le() {
  u32 r; asm volatile("s_getreg_b32 %0, hwreg(HW_REG_IB_STS)" : "=s"(r)::"memory");
  return (r & 0xc0000fu) <= (u32)N;
}
DI bool last_of4(LAS char* L, int k) {
  u32 old = 0;
  asm volatile("" ::: "memory");
  if (__builtin_amdgcn_mbcnt_hi(~0u, __builtin_amdgcn_mbcnt_lo(~0u, 0u)) == 0) old = __hip_atomic_fetch_add((LAS u32*)(L + L_CNT + 4 * k), 1u, __ATOMIC_RELAXED, __HIP_MEMORY_SCOPE_WORKGROUP);
  asm volatile("" ::: "memory");
  return __builtin_amdgcn_readfirstlane(old) == 3;
}
#define STAMP(k)
#define FLUSH()

__global__ __launch_bounds__(512, 2) void fused_block(const char* __restrict__ x, const char* __restrict__ wqkv, const char* __restrict__ wo,
                                                      const char* __restrict__ w1, const char* __restrict__ b1, const char* __restrict__ w2,
                                                      const char* __restrict__ b2, char* __restrict__ out, char* ws) {
  __shared__ __attribute__((aligned(1024))) char smem[163840];
  asm volatile("" : "+s"(x), "+s"(wqkv), "+s"(wo), "+s"(w1), "+s"(b1), "+s"(w2), "+s"(b2), "+s"(out), "+s"(ws));
  u64 pc; asm volatile("s_getpc_b64 %0" : "=s"(pc));
  LAS char* const L = (LAS char*)smem;
  const u32 lb = (u32)(uintptr_t)smem;
  int l = threadIdx.x & 63, g, q4, p4, l15;
  auto lanes = [&] { PIN(l); g = l >> 4; q4 = (l >> 2) & 3; p4 = l & 3; l15 = l & 15; };
  lanes();
  int w = __builtin_amdgcn_readfirstlane(threadIdx.x >> 6);
  asm volatile("s_nop 4" : "+s"(w));
  const int x8 = blockIdx.x & 7, rk = blockIdx.x >> 3;
  const bool busy = rk < 28;
  if (w == 0) STAMP(0);
  const rsrc_t rQKV = mkrs(ws + WS_QKV, QKV_BYTES), rAC = mkrs(ws + WS_AC, AC_BYTES), rCTL = mkrs(ws, CTL_BYTES);
  const rsrc_t rX = mkrs(x, 2048 * 896), rW2 = mkrs(w2, 1792 * 896);
  const int kh = x8 >> 2, cc = rk % 7, rb = (x8 & 3) * 4 + rk / 7, cb = kh * 7 + cc;
  const int ai = rk & 15, ah = 2 * x8 + (rk >> 4);
  const int ro6 = ROTA ? (cc + ((3 * (rk / 7)) >> 1)) % 6 : 0;
  auto rotl = [&](int j) { const int t = j + ro6; return j == 0 ? 0 : (t > 6 ? t - 6 : t); };
  auto kstep = [&](int p) {
    if (p < 8) return 8 * cb + p;
    const int t = cc + 1 + ((p - 8) % 24) / 4;
    return 8 * (7 * kh + (t >= 7 ? t - 7 : t)) + (p & 3) + (p >= 32 ? 4 : 0);
  };
  if (busy && (w == 4 || w == 5)) {
    u32 v[4];
#pragma unroll
    for (int j = 0; j < 4; ++j) v[j] = (8 * j + (l >> 3)) * 896 + (((l & 7) ^ ((j & 1) * 4 + g)) << 4) - 1024 * j;
    dman<4>(lb + (w & 1) * 8192, v, rX, (rb * 128 + 64 * (w & 1)) * 896);
    dman<4>(lb + (w & 1) * 8192 + 4096, v, rX, (rb * 128 + 64 * (w & 1) + 32) * 896);
    u32 vq[3];
#pragma unroll
    for (int j = 0; j < 3; ++j) { const u32 n = 64 * j + l; vq[j] = (n / 12) * 2688 + (n % 12) * 16 - 1024 * j; }
#pragma unroll
    for (int a = 0; a < 2; ++a) {
      dman<3>(lb + stage_addr(a) + (w & 1) * 3072, vq, mkrs(wqkv, 448 * 2688), cb * 192 + (16 * (w & 1) + 32 * a) * 2688);
    }
  }
  if (w == 6) dma4(lb + L_Q + 512, (rk * 32 + (l & 31)) * 64, mkrs((const void*)(pc & ~255ull), CODE_KB * 1024), 0);
  u32x4 d[14];
  const bool stat = busy && (w == 6 || (w == 7 && cb < 2));
  const int srow = rb * 128 + (w == 6 ? cb : 14 + cb) * 8 + (l >> 3);
  if (stat) {
#pragma unroll
    for (int k = 0; k < 7; ++k) d[k] = ld16_async<false>(srow * 896 + (l & 7) * 16, rX, k * 128);
  }
  if (busy && w == 7) {
    const int r = rb * 28 + (l >> 1);
    if (l < 56) dma4(lb + L_Q, r * 2688 + cb * 192 + (l & 1) * 188, mkrs(wqkv, 448 * 2688), 0);
  }
  SB();
  u64 tag;
  auto epoch = [&] { asm volatile("s_load_dwordx2 %0, %1, 0x0\n\ts_waitcnt lgkmcnt(0)" : "=s"(tag) : "s"(ws) : "memory"); tag += 1; };
  auto vW2 = [&](u32* v) { v[0] = (l >> 3) * 896 + (((l & 7) ^ ((g & 1) << 2)) << 4); v[1] = v[0] + 7168 - 1024; };

  if (w < 4) {
    if (!busy) {
      const int q = (rk - 28) * 4 + w;
      bar();
      for (int k = 0; k < PF_DELAY; ++k) __builtin_amdgcn_s_sleep(2);
      const rsrc_t rW1 = mkrs(w1, 448 * 3584), rWo = mkrs(wo, 448 * 896);
      const u32 v = l * 16;
#pragma unroll 1
      for (int k = 0; k < 49; ++k) {
        int r = k * 16 + q + (x8 & 3) * 196; r = r >= 784 ? r - 784 : r;
        dman<1>(lb + L_WC + w * 16384 + (k & 15) * 1024, &v, rW2, kh * 802816 + r * 1024);
        vm<10>();
      }
#pragma unroll 1
      for (int k = 0; k < 25; ++k) { dman<1>(lb + L_WC + w * 16384 + (k & 15) * 1024, &v, rWo, (k * 16 + q) * 1024); vm<10>(); }
      vm<0>();
      FLUSH();
      return;
    }
    __builtin_amdgcn_s_setprio(PRIO_A);
    constexpr int PF = P_PF;
    u32 oX[4], oH[2], oQ[3];
#pragma unroll
    for (int p = 0; p < 4; ++p) oX[p] = (4 * w + ((l & 31) >> 3)) * 1024 + (l & 7) * 128 + (((2 * p + (g >> 1)) ^ (l15 >> 1)) << 4);
    const int rq = w == 3 ? 0 : w;
#pragma unroll
    for (int i = 0; i < 3; ++i) oQ[i] = RING + (8 * (g >> 1) + q4) * 192 + ((i + rq) % 3) * 64 + (g & 1) * 32 + p4 * 8;
#pragma unroll
    for (int i = 0; i < 2; ++i) oH[i] = RING + (g >> 1) * 1024 + q4 * 128 + ((i ^ (q4 >> 1)) << 6) + (g & 1) * 32 + p4 * 8;
    const u32 selv = l15 == (g & 1) ? 0x3f803f80u : 0u;
    const bf16x8 sel = __builtin_bit_cast(bf16x8, u32x4{selv, selv, selv, selv});
    bf16x8 xf[28], wf[8];
    f32x16 aH[2], aQ[3] = {};
    f32x4 cs = {};
    u32x2 ch[2];
    float rs;
    bf16x8 lnb;
    u32x4 hA[72], qp[6];
    auto rdW = [&](auto SS, auto II) {
      constexpr int s = decltype(SS)::v, i = decltype(II)::v;
      constexpr int slot = (s < 14 ? s * 6 + i : 84 + i) % 8;
      if constexpr (s < 14) { const u32 o = oQ[i % 3] + (stage_addr(s) - RING) + (i / 3) * 3072; wf[slot] = trfrag(L, o, o + 768); }
      else if constexpr (s < 28) { const u32 o = oH[i & 1] + (stage_addr(s) - RING) + (i >> 1) * 2048; wf[slot] = trfrag(L, o, o + 512); }
    };
    auto rdX = [&](auto SS, auto EE) { constexpr int k = 2 * decltype(SS)::v + decltype(EE)::v; if constexpr (k < 28) xf[k] = lds_ld<bf16x8>(L, oX[k & 3] + (k >> 2) * 16384); };
    auto rdcs = [&](int half) {
#pragma unroll
      for (int i = 0; i < 2; ++i) ch[i] = lds_ld<u32x2>(L, L_CSH + (half * 64 + i * 32 + (l & 31)) * 8);
    };
    auto hpack = [&](int half, int c) {
      u32x4 hp;
#pragma unroll
      for (int j = 0; j < 4; ++j) {
        const float t0 = aH[c >> 1][8 * (c & 1) + 2 * j], t1 = aH[c >> 1][8 * (c & 1) + 2 * j + 1];
        hp[j] = pk(t0 * fmx(t0, 0.f), t1 * fmx(t1, 0.f));
      }
      hA[half * 4 + c] = hp;
    };
    auto hstore = [&](int m) { st16<16>(hA[m], rAC, l * 16 + ((rb * 4 + w) * 144 + cb * 8 + m) * 1024); };
    auto qpack = [&](int c) {
      const int i = c >> 1, e = c & 1, tq = 6 * cb + 2 * ((i + rq) % 3) + e;
      const float m = tq < 28 ? rs * (0.18898223650461361f * 1.4426950408889634f) : rs;
      const auto s0 = __builtin_amdgcn_permlane16_swap(pk(aQ[i][8 * e] * m, aQ[i][8 * e + 1] * m), pk(aQ[i][8 * e + 4] * m, aQ[i][8 * e + 5] * m), false, false);
      const auto s1 = __builtin_amdgcn_permlane16_swap(pk(aQ[i][8 * e + 2] * m, aQ[i][8 * e + 3] * m), pk(aQ[i][8 * e + 6] * m, aQ[i][8 * e + 7] * m), false, false);
      qp[c] = u32x4{s0[0], s1[0], s0[1], s1[1]};
    };
    auto qstore = [&](int c) {
      const int fl = ((g & 1) << 1) | (g >> 1), tq = 6 * cb + 2 * (((c >> 1) + rq) % 3) + (c & 1);
      u32x4 v = qp[c];
      u32 vo = fl * 256 + l15 * 16;
      if (tq >= 56) {
        asm volatile("" ::: "memory");
        if (fl & 1) v = u32x4{v[2], v[3], v[0], v[1]};
        vo ^= (2 * (tq & 1) + (fl >> 1)) << 6;
      }
      st16<16>(v, rQKV, vo + ((rb * 4 + w) * 336 + 4 * tq) * 256);
    };
    bool sigq = false, siga = false;
    const bool giver = (w >> 1) != kh;
    bar();
    STAMP(1);
    rdX(IC<0>{}, IC<0>{}); rdX(IC<0>{}, IC<1>{});
    sfor<0, PF>([&](auto I) { rdW(IC<0>{}, I); });
    sfor<0, 28>([&](auto SS) {
      constexpr int s = decltype(SS)::v, NT = s < 14 ? 6 : 8;
      if constexpr (s == 14) wait_bar<63>();
      else if constexpr (s < 14) bar();
      if constexpr (s == 13) lanes();
      if constexpr (s == 14) {
        STAMP(2);
        u32 cq[3];
#pragma unroll
        for (int i = 0; i < 3; ++i) cq[i] = lds_ld<u32>(L, L_CSQ + (((i + rq) % 3) * 32 + (l & 31)) * 4);
        const u32 rdy = cnt_ld(L, C_ST);
        u32 st = lds_ld<u32>(L, L_ST + (w * 32 + (l & 31)) * 16);
        if (!__builtin_amdgcn_readfirstlane(rdy)) { cnt_wait(L, C_ST, 1); st = lds_ld<u32>(L, L_ST + (w * 32 + (l & 31)) * 16); }
        SB();
        lnb = __builtin_bit_cast(bf16x8, u32x4{l < 32 ? (st & 0xffffu) * 0x10001u : 0u, l < 32 ? st >> 16 : 0u, 0u, 0u});
        rs = __builtin_amdgcn_rcpf(bfhi(st));
#pragma unroll
        for (int i = 0; i < 3; ++i) aQ[i] = mfma32(__builtin_bit_cast(bf16x8, u32x4{cq[i], 0u, 0u, 0u}), lnb, aQ[i]);
        SB();
#pragma unroll
        for (int c = 0; c < 6; ++c) { qpack(c); if (c % (6 / QNOW) == 6 / QNOW - 1) qstore(c / (6 / QNOW)); SB(); }
        STAMP(3);
      }
      if constexpr (s == 21) {
        rdcs(1);
        SB();
#pragma unroll
        for (int c = 0; c < 4; ++c) { hpack(0, c); if (c < HNOW) hstore(c); SB(); }
      }
      u32 land;
      sfor<0, NT>([&](auto II) {
        constexpr int i = decltype(II)::v, slot = (s < 14 ? s * 6 + i : 84 + i) % 8;
        constexpr u32 need = s == 15 ? 1 : s == 18 ? 2 : s == 22 ? 3 : s == 25 ? 4 : 0;
        if constexpr (need && i == 0) land = land_ld(L);
        if constexpr (need && i == NT - PF) {
          if ((u32)__builtin_amdgcn_readfirstlane(land) < need) prog_wait<false>(L, need);
          asm volatile("" ::: "memory");
        }
        if constexpr (i + PF < NT) rdW(SS, IC<i + PF>{});
        else rdW(IC<s + 1>{}, IC<i + PF - NT>{});
        if constexpr (i < 2) rdX(IC<s + 1>{}, II);
        if constexpr (s < 14) {
          aQ[i % 3] = mfma32(wf[slot], xf[2 * s + i / 3], aQ[i % 3]);
          if constexpr (i % 3 == 0) cs = mfma(wf[slot], sel, cs);
          if constexpr (s == 13 && i == 4) {
            u32x4 v;
#pragma unroll
            for (int j = 0; j < 4; ++j) { const float hi = rbf(cs[j]); v[j] = pk(hi, cs[j] - hi); }
            if (l15 < 2) lds_st<u32x4>(L, L_CSQ + w * 128 + (l15 * 16 + 4 * g) * 4, v);
          }
        } else {
          constexpr int k = 4 * ((s - 14) % 7) + (i >> 1), m = (s < 21 ? QNOW : HNOW) + 2 * ((s - 14) % 7) + (i >> 2);
          if constexpr (k == 0 && s == 21) aH[i] = mfma32(__builtin_bit_cast(bf16x8, u32x4{ch[i][0], ch[i][1], 0u, 0u}), lnb, f32x16{});
          aH[i & 1] = mfma32(wf[slot], xf[k], s == 14 && k == 0 ? f32x16{} : aH[i & 1]);
          if constexpr (s == 19 && i == 7) { cnt_wait(L, C_CSH, 1); rdcs(0); }
          if constexpr (s == 20 && (i == 2 || i == 3)) aH[i & 1] = mfma32(__builtin_bit_cast(bf16x8, u32x4{ch[i & 1][0], ch[i & 1][1], 0u, 0u}), lnb, aH[i & 1]);
          if constexpr ((i & 3) == 1 && s < 21 && m < 6) qstore(m);
          if constexpr ((i & 3) == 1 && s >= 21 && m < 4) hstore(m);
        }
        SB();
      });
      if constexpr (s == 18 || s == 20 || s == 27) prog_st(L, C_DONE + w, s);
      if constexpr (s >= 14 && s <= S_FQ && QNOW + 2 * (s - 14) >= 6) { if (!sigq && (s == S_FQ || vm_le<0>())) { vm<0>(); cnt_add(lb, C_FQ); sigq = true; } }
      if constexpr (s >= 21 && HNOW + 2 * (s - 21) >= 4) { if (!siga && vm_le<0>()) { vm<0>(); cnt_add(lb, C_FA); siga = true; } }
    });
    STAMP(4);
    lanes();
    const u32 vFG = WS_FG + (rb * 16 + 7 * kh + (l < 7 ? l : 0)) * FS;
#pragma unroll
    for (int c = 0; c < 4; ++c) hpack(1, c);
    SB();
    if (!siga) { vm<0>(); cnt_add(lb, C_FA); }
#pragma unroll
    for (int m = 4; m < 8; ++m) hstore(m);
    STAMP(5);
    { const u32x2 t = lds_ld<u32x2>(L, L_CNT + 112); tag = (u64)(u32)__builtin_amdgcn_readfirstlane(t[0]) | (u64)(u32)__builtin_amdgcn_readfirstlane(t[1]) << 32; }
    u32x2 polled;
    u32 vW[2];
    vW2(vW);
    const u32 vA = l * 16, sA = (rb * 4 + w) * 144, sO = ((rb * 4 + (giver ? w ^ 2 : w)) * 144 + (giver ? 128 : 112)) * 1024;
    u32 sN[6];
#pragma unroll
    for (int j = 0; j < 6; ++j) { sN[j] = (sA + kstep(8 + 4 * j)) * 1024; asm volatile("" : "+s"(sN[j])); }
    const u32 vB = vA + 4096;
    auto ldA = [&](auto M) { constexpr int m = decltype(M)::v; hA[m] = ld16_async<true, (m & 3) * 1024>(m >= 32 ? vB : vA, rAC, sN[((m - 8) % 24) / 4]); };
    auto ldO = [&](auto M) { constexpr int m = decltype(M)::v; hA[m] = ld16_async<true, (m & 3) * 1024>(vA, rAC, sO + ((m - 56) & ~3) * 1024); };
    const rsrc_t rWo = mkrs(wo, 448 * 896);
    u32 oW[2], oO[2];
#pragma unroll
    for (int b = 0; b < 2; ++b) { oW[b] = (4 * (g >> 1) + q4) * 128 + ((b ^ (q4 >> 1)) << 6) + (g & 1) * 32 + p4 * 8; oO[b] = oW[b] + L_WC + (giver ? 8 * 3584 : 0); }
    bf16x8 wc[2][2];
    auto rdC = [&](int m) {
#pragma unroll
      for (int b = 0; b < 2; ++b) {
        const u32 o = m < 56 ? oW[b] + L_WC + m * 2048 : oO[b] + ((m - 56) >> 1) * 3584 + (m & 1) * 2048;
        wc[m & 1][b] = trfrag(L, o, o + 1024);
      }
    };
    f32x16 acc[2];
    f32x16 aco[2];
    u32 top, pre;
    u32x4 xres[2], bres;
    u32x2 pp[4];
    const int tile = giver ? w ^ 2 : w, r0 = (giver ? 16 : 0) + (l >> 3);
    auto step = [&](auto N) {
      constexpr int n = decltype(N)::v;
      if constexpr (n + 1 < 72) rdC(n + 1);
      SB();
      f32x16* a = n < 56 ? acc : aco;
      a[0] = mfma32(wc[n & 1][0], __builtin_bit_cast(bf16x8, hA[n]), n == 0 || n == 56 ? f32x16{} : a[0]);
      SB();
      if constexpr (n >= 8 && n < 56) sfor<SC.a0[n], SC.a1[n]>(ldA);
      if constexpr (n >= 8 && n < 15) dman<2>(lb + L_WC + (NWP + 4 * (n - 8) + w) * 2048, vW, rW2, (kstep(NWP + 4 * (n - 8)) + w) * (16 * 896) + cc * 128);
      if constexpr (n >= T_WO && n < T_WO + 7) dman<2>(lb + L_WC + (4 * (n - T_WO) + w) * 2048, vW, rWo, (4 * (n - T_WO) + w) * (16 * 896) + cc * 128);
      if constexpr (n == T_WO) { if (w == 0) dman<2>(lb + L_WC + 28 * 2048, vW, rWo, 28 * (16 * 896) + cc * 128); }
      if constexpr (n >= 56) { top = max(max(n == 56 ? 0u : top, hA[n][0]), hA[n][1]); top = max(max(top, hA[n][2]), hA[n][3]); PIN(top); }
      a[1] = mfma32(wc[n & 1][1], __builtin_bit_cast(bf16x8, hA[n]), n == 0 || n == 56 ? f32x16{} : a[1]);
      SB();
    };
    cnt_wait(L, C_WLO, 2);
    rdC(0);
    sfor<0, OWN1>(step);
    bool sigg = false;
    auto pubg = [&] { if (last_of4(L, C_FG) && l == 0) st_flag(rCTL, WS_FG + (rb * 16 + cb) * FS, tag); sigg = true; };
#pragma unroll 1
    for (u32 n = 0; n < (1u << 20); ++n) {
      if (!sigg && vm_le<0>()) { vm<0>(); pubg(); }
      if (__builtin_amdgcn_readfirstlane(cnt_ld(L, C_FH))) break;
      __builtin_amdgcn_s_sleep(LSLP);
    }
    asm volatile("" ::: "memory");
    STAMP(6);
    __builtin_amdgcn_s_setprio(PRIO_C);
    sfor<8, 8 + DA>(ldA);
    prog_wait<true>(L, 27);
    sfor<OWN1, 8>(step);
    sfor<8, 56>([&](auto N) {
      constexpr int n = decltype(N)::v;
      asm volatile("s_waitcnt vmcnt(%1)" : "+v"(hA[n]) : "n"(SC.wC[n]) : "memory");
      if constexpr (n == 30) cnt_add(lb, C_P30);
      constexpr auto gate = [](int m) { return m == T_WO ? C_P30 : m == 55 ? C_WO : m == NWP - 1 ? C_WHI : m == 51 ? C_LATE : -1; };
      if constexpr (gate(n) >= 0) { if ((u32)__builtin_amdgcn_readfirstlane(pre) < 4u) cnt_wait(L, gate(n), 4); asm volatile("" ::: "memory"); }
      if constexpr (gate(n + 2) >= 0) pre = cnt_ld(L, gate(n + 2));
      if constexpr (n == T_WO + 12) cnt_add(lb, C_WO);
      if constexpr (n == 8) { if (!sigg) pubg(); }
      if constexpr (n == T_WH) cnt_add(lb, C_WHI);
      if constexpr (n == TB - 3) polled = ld8c_async(vFG, rCTL);
      if constexpr (n == TB) { PIN(polled); if (!tagged(polled, tag)) spin<SLP>(rCTL, vFG, tag); }
      step(N);
      if constexpr (n >= T_O && n < T_O + 8) { ldO(IC<56 + 2 * (n - T_O)>{}); ldO(IC<57 + 2 * (n - T_O)>{}); }
      if constexpr (n == T_O + 8) {
#pragma unroll
        for (int k = 0; k < 2; ++k) xres[k] = ld16_async<false>((r0 + 8 * k) * 896 + (l & 7) * 16, rX, (rb * 128 + tile * 32) * 896 + cc * 128);
        bres = ld16_async<false>((l & 7) * 16, mkrs(b2, 896), cc * 128);
      }
    });
    asm volatile("s_waitcnt vmcnt(0)" : "+v"(hA[56]), "+v"(hA[57]), "+v"(hA[58]), "+v"(hA[59]), "+v"(hA[60]), "+v"(hA[61]), "+v"(hA[62]), "+v"(hA[63]), "+v"(xres[0]), "+v"(xres[1]),
                 "+v"(hA[64]), "+v"(hA[65]), "+v"(hA[66]), "+v"(hA[67]), "+v"(hA[68]), "+v"(hA[69]), "+v"(hA[70]), "+v"(hA[71]), "+v"(bres)::"memory");
    const float rs2 = rs * rs;
    if (giver) {
#pragma unroll
      for (int k = 0; k < 4; ++k) {
        const f32x16& a = acc[k >> 1];
        const int o = 8 * (k & 1);
        st16<16>(u32x4{pk(a[o] * rs2, a[o + 1] * rs2), pk(a[o + 2] * rs2, a[o + 3] * rs2), pk(a[o + 4] * rs2, a[o + 5] * rs2), pk(a[o + 6] * rs2, a[o + 7] * rs2)}, rAC,
                 PX + ((rb * 7 + cc) * 4 + w) * 4096 + k * 1024 + l * 16);
      }
    }
    const u32 vP = PX + ((rb * 7 + cc) * 4 + tile) * 4096 + ((l & 7) >> 1) * 1024 + r0 * 16 + (l & 1) * 8;
    sfor<56, T_PP>(step);
#pragma unroll
    for (int k = 0; k < 4; ++k) pp[k] = ld8c_async(vP + (k >> 1) * 128 + (k & 1) * 512, rAC);
    pre = cnt_ld(L, C_ATT);
    sfor<T_PP, 72>(step);
    asm volatile("s_waitcnt vmcnt(0)" : "+v"(pp[0]), "+v"(pp[1]), "+v"(pp[2]), "+v"(pp[3])::"memory");
#pragma unroll 1
    for (u32 n = 0; n < (1u << 17) && !__all(top != ~0u); ++n) {
#pragma unroll 1
      for (; n < (1u << 17); ++n) {
        asm volatile("" ::: "memory");
        if (__all(__builtin_amdgcn_raw_buffer_load_b32(rAC, sO + (l & 7) * 2048 + 1024, 0, 16) != ~0u)) break;
        __builtin_amdgcn_s_sleep(SLP);
      }
#pragma unroll
      for (int k = 0; k < 16; ++k) hA[56 + k] = __builtin_amdgcn_raw_buffer_load_b128(rAC, vA + sO + k * 1024, 0, 16);
      rdC(56);
      sfor<56, 72>(step);
    }
    STAMP(10);
    if (!giver) { aco[0] += acc[0] * rs2; aco[1] += acc[1] * rs2; }
    if ((u32)__builtin_amdgcn_readfirstlane(pre) < 4u) cnt_wait(L, C_ATT, 4);
    asm volatile("" ::: "memory");
#pragma unroll
    for (int b = 0; b < 2; ++b)
#pragma unroll
      for (int k = 0; k < 4; ++k)
        lds_st<f32x4>(L, L_RED + w * 8192 + (l & 31) * 256 + (((b * 8 + k * 2 + (g >> 1)) ^ (l & 7)) << 4), f32x4{aco[b][4 * k], aco[b][4 * k + 1], aco[b][4 * k + 2], aco[b][4 * k + 3]});
    cnt_add(lb, C_PAIR0 + (w & 1));
#pragma unroll 1
    for (u32 n = 0; n < (1u << 17) && !__all(max(max(max(pp[0][0], pp[0][1]), max(pp[1][0], pp[1][1])), max(max(pp[2][0], pp[2][1]), max(pp[3][0], pp[3][1]))) != ~0u); ++n) {
      asm volatile("" ::: "memory");
#pragma unroll
      for (int k = 0; k < 4; ++k) pp[k] = __builtin_amdgcn_raw_buffer_load_b64(rAC, vP + (k >> 1) * 128 + (k & 1) * 512, 0, 16);
    }
    f32x4 ra[2], rc[2];
#pragma unroll
    for (int k = 0; k < 2; ++k) {
      ra[k] = f32x4{bflo(xres[k][0]) + bflo(bres[0]), bfhi(xres[k][0]) + bfhi(bres[0]), bflo(xres[k][1]) + bflo(bres[1]), bfhi(xres[k][1]) + bfhi(bres[1])} +
              f32x4{bflo(pp[2 * k][0]), bfhi(pp[2 * k][0]), bflo(pp[2 * k][1]), bfhi(pp[2 * k][1])};
      rc[k] = f32x4{bflo(xres[k][2]) + bflo(bres[2]), bfhi(xres[k][2]) + bfhi(bres[2]), bflo(xres[k][3]) + bflo(bres[3]), bfhi(xres[k][3]) + bfhi(bres[3])} +
              f32x4{bflo(pp[2 * k + 1][0]), bfhi(pp[2 * k + 1][0]), bflo(pp[2 * k + 1][1]), bfhi(pp[2 * k + 1][1])};
      PIN(ra[k]); PIN(rc[k]);
    }
    cnt_wait(L, C_PAIR0 + (w & 1), 2);
    const rsrc_t rO = mkrs(out, 2048 * 896);
#pragma unroll
    for (int k = 0; k < 2; ++k) {
      const int row = r0 + 8 * k;
      f32x4 a = ra[k], b = rc[k];
#pragma unroll
      for (int j = 0; j < 2; ++j) {
        a += lds_ld<f32x4>(L, L_RED + ((w & 1) + 2 * j) * 8192 + row * 256 + (((2 * (l & 7)) ^ (row & 7)) << 4));
        b += lds_ld<f32x4>(L, L_RED + ((w & 1) + 2 * j) * 8192 + row * 256 + (((2 * (l & 7) + 1) ^ (row & 7)) << 4));
      }
      st16<16>(u32x4{pk(a[0], a[1]), pk(a[2], a[3]), pk(b[0], b[1]), pk(b[2], b[3])}, rO, (rb * 128 + tile * 32 + row) * 896 + cc * 128 + (l & 7) * 16);
    }
    if (w == 0) STAMP(11);
    FLUSH();
    return;
  }

  const int li = w & 1;
  auto clear_o = [&] {
#pragma unroll
    for (int k = 0; k < 8; ++k) st16<16>(u32x4{~0u, ~0u, ~0u, ~0u}, rAC, ((ai * 4 + (k >> 1)) * 144 + 112 + 2 * ah + (k & 1)) * 1024 + l * 16);
  };
  if (!busy) {
    const int blk = (rk - 28) * 8 + x8;
#pragma unroll
    for (int k = 0; k < 14; ++k) d[k] = __builtin_amdgcn_raw_buffer_load_b128(mkrs(w1, 448 * 3584), (112 * (w - 4) + (l >> 3)) * 3584 + (l & 7) * 16 + (blk < 28 ? blk * 128 : 0x7fffffff), k * 8 * 3584, 0);
    const u32 bias = __builtin_amdgcn_raw_buffer_load_b16(mkrs(b1, 3584), (blk * 64 + l) * 2, 0, 0);
    SB();
    epoch();
    if (w == 6 && l < C_N) lds_st<u32>(L, L_CNT + l * 4, l < 4 ? 64u : 0u);
    wait_bar<63>();
    float a[8] = {};
#pragma unroll
    for (int k = 0; k < 14; ++k)
#pragma unroll
      for (int j = 0; j < 4; ++j) { a[2 * j] += bflo(d[k][j]); a[2 * j + 1] += bfhi(d[k][j]); }
    lds_st<f32x4>(L, RING + ((w - 4) * 64 + l) * 32, f32x4{a[0], a[1], a[2], a[3]});
    lds_st<f32x4>(L, RING + ((w - 4) * 64 + l) * 32 + 16, f32x4{a[4], a[5], a[6], a[7]});
    if (last_of4(L, C_CS0) && blk < 28) {
      float t = 0.f;
#pragma unroll
      for (int k = 0; k < 32; ++k) t += lds_ld<float>(L, RING + k * 256 + l * 4);
      const float hi = rbf(t);
      __builtin_amdgcn_raw_buffer_store_b64(u32x2{pk(hi, t - hi), bias}, rCTL, WS_CS + (blk * 64 + l) * 8, 0, 16);
      vm<0>();
      if (l == 0) st_flag(rCTL, WS_FC + blk * FS, tag);
    }
    if (w == 7) { clear_o(); vm<0>(); if (l == 0) st_flag(rCTL, WS_FZ + (ai * 16 + ah) * FS, tag); }
  } else if (w < 6) {
    __builtin_amdgcn_s_setprio(PRIO_L);
    const rsrc_t rW1 = mkrs(w1, 448 * 3584), rWq = mkrs(wqkv, 448 * 2688);
    u32 vX[4], vH[4], vQ[3], vC[2], so7[7];
#pragma unroll
    for (int j = 0; j < 4; ++j) vX[j] = (8 * j + (l >> 3)) * 896 + (((l & 7) ^ ((j & 1) * 4 + g)) << 4) - 1024 * j;
    so7[0] = 0;
    const u32 sX = (rb * 128 + 64 * li) * 896, sH = cb * 256 + 32 * li * 3584, sQ = cb * 192 + 16 * li * 2688;
    auto issue = [&](auto O) {
      constexpr int op = decltype(O)::v, k = op / 256, a = (op % 256) / 2, e = op & 1;
      if constexpr (k == K_X) dman<4>(lb + (a * 16 + 8 * li + 4 * e) * 1024, vX, rX, sX + so7[a] * 128 + e * 28672);
      if constexpr (k == K_WA && a < 14) dman<3>(lb + stage_addr(a) + li * 3072, vQ, rWq, sQ + so7[a >> 1] * (64 * 2688) + (a & 1) * (32 * 2688));
      if constexpr (k == K_WA && a >= 14) dman<4>(lb + stage_addr(a) + li * 4096, vH, rW1, sH + so7[(a - 14) % 7] * (64 * 3584) + (a >= 21 ? 128 : 0));
      if constexpr (k == K_WCP) dman<2>(lb + L_WC + (2 * a + li) * 2048, vC, rW2, (kstep(2 * a) + li) * (16 * 896) + cc * 128);
    };
    lanes();
#pragma unroll
    for (int j = 0; j < 3; ++j) { const u32 n = 64 * j + l; vQ[j] = (n / 12) * 2688 + (n % 12) * 16 - 1024 * j; }
#pragma unroll
    for (int j = 0; j < 4; ++j) vH[j] = (8 * j + (l >> 3)) * 3584 + (((l & 7) ^ ((g & 1) << 2)) << 4) - 1024 * j;
#pragma unroll
    for (int j = 1; j < 7; ++j) so7[j] = rotl(j);
    sfor<4, NPRE>([&](auto K) { issue(IC<SC.pro[decltype(K)::v]>{}); });
    vW2(vC);
    vm_bar<SC.wA[0]>();
    sfor<NPRE, NPRO>([&](auto K) { issue(IC<SC.pro[decltype(K)::v]>{}); });
    sfor<0, 15>([&](auto SS) {
      constexpr int s = decltype(SS)::v;
      vm_bar<SC.wA[s + 1]>();
      sfor<0, (s == 14 ? 2 : MAXO)>([&](auto K) { issue(IC<SC.opA[s][decltype(K)::v]>{}); });
    });
    vm<SC.wL[0]>();
    prog_st(L, C_LAND + li, 1);
    vm<0>();
    prog_st(L, C_LAND + li, 2);
    cnt_wait(L, C_FQ, HOLD);
    sfor<2, MAXO>([&](auto K) { issue(IC<SC.opA[14][decltype(K)::v]>{}); });
    vm<SC.wL[1]>();
    prog_st(L, C_LAND + li, 3);
    epoch();
    prog_wait<true>(L, 20);
    issue(IC<OP(K_WA, 26)>{}); issue(IC<OP(K_WA, 27)>{});
    vm<0>();
    prog_st(L, C_LAND + li, 4);
    cnt_add(lb, C_WLO);
    if (w == 4) { STAMP(13); cnt_wait(L, C_FA, 5); STAMP(14); if (l == 0) st_flag(rCTL, WS_FH + (rb * 16 + cb) * FS, tag); }
    else { cnt_wait(L, C_FA, 5); spin<SLP>(rCTL, l >= 16 && l < 32 ? WS_FZ + (rb * 16 + l - 16) * FS : WS_FH + (rb * 16 + (l < 7 ? 7 * kh + l : 7 * (1 - kh) + cc)) * FS, tag); STAMP(15); cnt_add(lb, C_FH); }
  } else {
    if (stat) {
      epoch();
      bar();
      asm volatile("s_waitcnt vmcnt(0)" : "+v"(d[0]), "+v"(d[1]), "+v"(d[2]), "+v"(d[3]), "+v"(d[4]), "+v"(d[5]), "+v"(d[6])::"memory");
      float s1 = 0.f, s2 = 0.f;
#pragma unroll
      for (int r = 0; r < 2; ++r) {
        bar();
#pragma unroll
        for (int k = 14 * r; k < 14 * r + 14; ++k) { const u32 v = d[k >> 2][k & 3]; s1 = dot2(v, 0x3f803f80u, s1); s2 = dot2(v, v, s2); }
        PIN(s1); PIN(s2);
        SB();
      }
      bar();
      const float mean = sum8(s1) * (1.f / 448.f);
      const float var = fmaxf(sum8(s2) * (1.f / 448.f) - mean * mean, 0.f);
      const float sd = rbf(__builtin_amdgcn_sqrtf(rbf(rbf(var) + 1e-5f)));
      if ((l & 7) == 0) st16<16>(u32x4{pk(-mean, sd), (u32)tag, pk(-mean, sd), (u32)(tag >> 32)}, rCTL, WS_ST + srow * 16);
      bar();
    } else { bar(); bar(); epoch(); bar(); bar(); bar(); }
    if (w == 6) {
      const u32 fp = WS_FC + (2 * cb + ((l >> 1) & 1)) * FS + (l & 1) * 4;
      int ss = 0, cs = 0;
      auto look = [&] {
        vm<0>();
        if (ss == 1) {
          const u32x4 a = lds_ld<u32x4>(L, L_ST + l * 16), b = lds_ld<u32x4>(L, L_ST + 1024 + l * 16);
          if (__all(a[1] == (u32)tag && a[3] == (u32)(tag >> 32) && b[1] == (u32)tag && b[3] == (u32)(tag >> 32))) { cnt_add(lb, C_ST); ss = 3; STAMP(33); }
        }
        if (ss < 3) {
          asm volatile("s_mov_b32 m0, %0\n\ts_nop 0\n\tbuffer_load_dwordx4 %1, %2, 0 offen sc1 lds\n\tbuffer_load_dwordx4 %1, %2, 0 offen offset:1024 sc1 lds" ::"s"(lb + L_ST), "v"(l * 16 + WS_ST + rb * 2048), "s"(rCTL) : "memory");
          ss = 1;
        }
        if (cs == 2) { cnt_add(lb, C_CSH); cs = 3; STAMP(37); }
        if (cs == 1 && __all(lds_ld<u32>(L, L_POLL + l * 4) == (u32)(l & 1 ? tag >> 32 : tag))) {
          asm volatile("s_mov_b32 m0, %0\n\ts_nop 0\n\tbuffer_load_dwordx4 %1, %2, 0 offen sc1 lds" ::"s"(lb + L_CSH), "v"(l * 16 + WS_CS + cb * 1024), "s"(rCTL) : "memory");
          cs = 2;
        }
        if (cs < 2) { asm volatile("s_mov_b32 m0, %0\n\ts_nop 0\n\tbuffer_load_dword %1, %2, 0 offen sc1 lds" ::"s"(lb + L_POLL), "v"(fp), "s"(rCTL) : "memory"); cs = 1; }
      };
#pragma unroll 1
      for (int s = 4; s <= 14; ++s) {
        wait_bar<63>();
        if (s == 6 && l < 30) lds_st<u32>(L, L_CNT + l * 4, l < 28 ? 0u : (u32)(l & 1 ? tag >> 32 : tag));
        if (s >= 7 && ss + cs != 6 && vm_le<0>()) look();
      }
#pragma unroll 1
      for (u32 n = 0; n < (1u << 17) && ss + cs != 6; ++n) look();
    } else {
      vm<0>();
      int k = 0;
      auto clear = [&] {
        st16<16>(u32x4{~0u, ~0u, ~0u, ~0u}, rAC, ((ai * 4 + (k >> 1)) * 144 + 112 + 2 * ah + (k & 1)) * 1024 + l * 16);
        st16<16>(u32x4{~0u, ~0u, ~0u, ~0u}, rAC, PX + ((rb * 7 + cc) * 4 + 2 * kh) * 4096 + k * 1024 + l * 16);
        ++k;
      };
#pragma unroll 1
      for (int s = 4; s <= 14; ++s) {
        bar();
        if (s >= CLR0 && k < 8 && vm_le<CLRQ>()) clear();
      }
      if (k < 8) cnt_wait(L, C_FQ, 4);
#pragma unroll 1
      while (k < 8) clear();
      vm<0>();
      cnt_add(lb, C_FA);
      if (l == 0) st_flag(rCTL, WS_FZ + (ai * 16 + ah) * FS, tag);
    }
  }
  lanes();
  const int aw = w - 4, hh = g >> 1;
  if (w >= 6) {
    const int e = l & 1, k = l < 14 ? l >> 1 : 0;
    const int ty = k == 0 ? 0 : (k < 4 ? 1 : 2), ro = k == 0 ? 0 : (k < 4 ? k - 1 : k - 4);
    const int rr = ai - ro < 0 ? 0 : ai - ro;
    if (!busy) { for (int k = 0; k < IDLE_SLP; ++k) __builtin_amdgcn_s_sleep(15); }
    if (busy) { cnt_wait(L, C_FQ, 4); if (w == 6) STAMP(12); if (w == 6 && l == 0) st_flag(rCTL, WS_FQ + (rb * 16 + cb) * FS, tag); }
    prog_wait<true>(L, 18);
#pragma unroll
    for (int k = 0; k < 3; ++k) lds_st<u32x2>(L, L_V + (6 * li + 2 * k + (l >> 5)) * 2048 + 1792 + (l & 31) * 8, u32x2{0x3f80u, 0u});
    if (w == 6) { spin<SLP>(rCTL, WS_FQ + (rr * 16 + (ty * 448 + 28 * ah + 27 * e) / 96) * FS, tag); cnt_add(lb, C_SQ); }
    else cnt_wait(L, C_SQ, 1);
    if (w == 6) STAMP(7);
#pragma unroll
    for (int e = 0; e < 2; ++e) {
#pragma unroll
      for (int j = 0; j < 6; ++j) {
        const int b = 2 * j + li, tb = 4 * ai - 8 + b < 0 ? 0 : 4 * ai - 8 + b;
        dma1792c(lb + (e ? L_V + b * 2048 : L_K + b * 1792), l * 16, rQKV, (tb * 336 + 112 + 112 * e + 7 * ah) * 256);
      }
      if (e == 0) {
#pragma unroll
        for (int j = 0; j < 2; ++j) dma1792c(lb + L_Q + (2 * li + j) * 2048, l * 16, rQKV, ((4 * ai + 2 * li + j) * 336 + 7 * ah) * 256);
      }
    }
    vm<12>();
    cnt_add(lb, C_KV);
  }
  u32 oK = L_K + aw * 1792 + hh * 256 + l15 * 16 + (g & 1) * 8, oK3 = hh ? oK + 1280 : oK + 1536;
  u32 oQv = L_Q + aw * 2048 + hh * 256 + l15 * 16 + (g & 1) * 8;
  const int F = 7 * ah + 4 * (g & 1) + p4;
  u32 oV[2][2];
#pragma unroll
  for (int t = 0; t < 2; ++t)
#pragma unroll
    for (int e = 0; e < 2; ++e) { oV[t][e] = L_V + aw * 2048 + (4 * (g & 1) + p4) * 256 + (((8 * e + 4 * hh + q4) ^ ((F & 6) << 1)) << 4) + ((t ^ (F & 1)) << 3); PIN(oV[t][e]); }
  const int dq = (l & 31) - 4 * hh;
  u32 mk[8], vW[2];
#pragma unroll
  for (int r = 0; r < 16; r += 2) { mk[r >> 1] = (r & 3) + 1 + 8 * (r >> 2) <= dq ? 0xffffffffu : ((r & 3) + 8 * (r >> 2) <= dq ? 0xffffu : 0u); PIN(mk[r >> 1]); }
  vW2(vW);
  PIN(oK); PIN(oK3); PIN(oQv); PIN(vW[0]); PIN(vW[1]);
  cnt_wait(L, C_KV, 2);
  __builtin_amdgcn_s_setprio(PRIO_B);
  if (w == 4) STAMP(8);
  u32x2 qv[4];
#pragma unroll
  for (int k = 0; k < 4; ++k) qv[k] = lds_ld<u32x2>(L, oQv + k * 512);
  f32x16 sc[9];
  bf16x8 kf[9][2], vf[9][2];
  auto rdK = [&](int m) {
    const u32x2 a = lds_ld<u32x2>(L, oK + m * 1792), b = lds_ld<u32x2>(L, oK + m * 1792 + 512), c = lds_ld<u32x2>(L, oK + m * 1792 + 1024), d = lds_ld<u32x2>(L, oK3 + m * 1792);
    kf[m][0] = __builtin_bit_cast(bf16x8, u32x4{a[0], a[1], b[0], b[1]}); kf[m][1] = __builtin_bit_cast(bf16x8, u32x4{c[0], c[1], d[0], d[1]});
  };
  auto rdV = [&](int m) { vf[m][0] = trfrag(L, oV[0][0] + m * 2048, oV[0][1] + m * 2048); vf[m][1] = trfrag(L, oV[1][0] + m * 2048, oV[1][1] + m * 2048); };
  SB(); rdK(0); SB(); rdK(8); SB(); rdK(1); SB();
  if (hh) qv[3] = u32x2{0u, 0u};
  const bf16x8 qf[2] = {__builtin_bit_cast(bf16x8, u32x4{qv[0][0], qv[0][1], qv[1][0], qv[1][1]}), __builtin_bit_cast(bf16x8, u32x4{qv[2][0], qv[2][1], qv[3][0], qv[3][1]})};
  constexpr float NEG = -1e30f;
  const int m0 = 8 - 4 * ai - aw;
  float mx4[4];
  auto rowmax = [&](int m, bool first) {
#pragma unroll
    for (int k = 0; k < 4; ++k) {
      mx4[k] = first ? fmx(sc[m][4 * k], sc[m][4 * k + 1]) : fmx(fmx(mx4[k], sc[m][4 * k]), sc[m][4 * k + 1]);
      mx4[k] = fmx(fmx(mx4[k], sc[m][4 * k + 2]), sc[m][4 * k + 3]);
    }
    PIN(mx4[0]); PIN(mx4[1]); PIN(mx4[2]); PIN(mx4[3]);
  };
#pragma unroll
  for (int i = 0; i < 9; ++i) {
    const int m = i == 0 ? 0 : (i == 1 ? 8 : i - 1);
    if (i + 2 < 8) rdK(i + 2);
    sc[m] = mfma32(kf[m][0], qf[0], f32x16{});
    if (i == 3) {
      if (m0 > 0) {
        asm volatile("" ::: "memory");
#pragma unroll
        for (int r = 0; r < 16; ++r) sc[0][r] = NEG;
      }
#pragma unroll
      for (int r = 0; r < 16; ++r) sc[0][r] = (r & 3) + 8 * (r >> 2) <= dq ? sc[8][r] : sc[0][r];
    }
    if (i == 4) rowmax(0, true);
    sc[m] = mfma32(kf[m][1], qf[1], sc[m]);
    if (i >= 4 && i - 3 >= m0) rowmax(i - 3, false);
    SB();
  }
  if (w >= 6) { vm<0>(); cnt_add(lb, C_V); }
  if (busy) dman<2>(lb + L_Q + aw * 2048, vW, rW2, (kstep(52) + aw) * (16 * 896) + cc * 128);
  if (6 >= m0) rowmax(6, false);
  cnt_wait(L, C_V, 2);
  rdV(1); rdV(2);
  SB();
  if (7 >= m0) rowmax(7, false);
  const float mx0 = fmx(fmx(mx4[0], mx4[1]), fmx(mx4[2], mx4[3]));
  const auto ms = __builtin_amdgcn_permlane32_swap(__float_as_uint(mx0), __float_as_uint(mx0), false, false);
  const float mx = fmx(__uint_as_float(ms[0]), __uint_as_float(ms[1]));
  SB();
  f32x16 o;
  u32x4 pq[2];
#pragma unroll
  for (int i = 0; i < 16; ++i) {
    const int m = ((i >> 1) + 1) & 7, s = i & 1;
    if (!s) { if (i < 10) rdV((i >> 1) + 3); else if (i == 10) rdV(0); else if (i == 12) rdV(8); }
    if (i == 6) { vm<0>(); cnt_add(lb, C_LATE); }
    const float mm = m != 0 && m < m0 ? -NEG : mx;
    f32x2 d[4];
#pragma unroll
    for (int j = 0; j < 4; ++j) { d[j] = f32x2{sc[m][8 * s + 2 * j], sc[m][8 * s + 2 * j + 1]} - f32x2{mm, mm}; PIN(d[j]); }
#pragma unroll
    for (int j = 0; j < 2; ++j) { d[j][0] = __builtin_amdgcn_exp2f(d[j][0]); d[j][1] = __builtin_amdgcn_exp2f(d[j][1]); PIN(d[j]); }
    SB();
    if (i >= 1 && i < 15) o = mfma32(vf[((i - 1) >> 1) + 1][s ^ 1], __builtin_bit_cast(bf16x8, pq[s ^ 1]), i == 1 ? f32x16{} : o);
    if (i == 15) {
      const u32x4 k4 = {mk[0], mk[1], mk[2], mk[3]};
      o = mfma32(vf[0][0], __builtin_bit_cast(bf16x8, pq[0] & ~k4), o);
      o = mfma32(vf[8][0], __builtin_bit_cast(bf16x8, pq[0] & k4), o);
    }
    SB();
#pragma unroll
    for (int j = 2; j < 4; ++j) { d[j][0] = __builtin_amdgcn_exp2f(d[j][0]); d[j][1] = __builtin_amdgcn_exp2f(d[j][1]); }
#pragma unroll
    for (int j = 0; j < 4; ++j) pq[s][j] = pk(d[j][0], d[j][1]);
    PIN(pq[s]);
    SB();
  }
  {
    const u32x4 k4 = {mk[4], mk[5], mk[6], mk[7]};
    o = mfma32(vf[0][1], __builtin_bit_cast(bf16x8, pq[1] & ~k4), o);
    o = mfma32(vf[8][1], __builtin_bit_cast(bf16x8, pq[1] & k4), o);
  }
  SB();
  cnt_add(lb, C_ATT);
  const auto dn = __builtin_amdgcn_permlane32_swap(__float_as_uint(o[12]), __float_as_uint(o[12]), false, false);
  const float inv = __builtin_amdgcn_rcpf(__uint_as_float(dn[1]));
  const float in2 = hh ? 0.f : inv;
  const u32 so = ((ai * 4 + aw) * 144 + 112 + 2 * ah) * 1024;
  st16<16>(u32x4{pk(o[0] * inv, o[1] * inv), pk(o[2] * inv, o[3] * inv), pk(o[4] * inv, o[5] * inv), pk(o[6] * inv, o[7] * inv)}, rAC, l * 16 + so);
  st16<16>(u32x4{pk(o[8] * inv, o[9] * inv), pk(o[10] * inv, o[11] * inv), pk(o[12] * in2, o[13] * in2), pk(o[14] * in2, o[15] * in2)}, rAC, l * 16 + so + 1024);
  if (w == 4) STAMP(9);
  vm<0>();
  if (last_of4(L, C_FB)) {
    if (l == 0) st_flag(rCTL, WS_FB + (ai * 16 + ah) * FS, tag);
    if (blockIdx.x == 255) {
#pragma unroll 1
      for (int k = 0; k < 4; ++k) spin<32>(rCTL, WS_FB + (k * 64 + l) * FS, tag);
      if (l == 0) st_flag(rCTL, 0, tag);
    }
  }
  FLUSH();
}

extern "C" void kernel_launch(void* const* d_in, const int* in_sizes, int n_in, void* d_out, int out_size, void* d_ws, size_t ws_size, hipStream_t stream) {
  fused_block<<<256, 512, 0, stream>>>((const char*)d_in[0], (const char*)d_in[1], (const char*)d_in[2], (const char*)d_in[4], (const char*)d_in[5],
                                       (const char*)d_in[6], (const char*)d_in[7], (char*)d_out, (char*)d_ws + WS_OFF);
}
```

```cpp
#define PRIO_B 3
#include <hip/hip_runtime.h>
#include <hip/hip_bf16.h>
#include <stdint.h>

#define DI __device__ __forceinline__
#define LAS __attribute__((address_space(3)))
typedef unsigned u32;
typedef unsigned long long u64;
typedef __bf16 bf16x8 __attribute__((ext_vector_type(8)));
typedef __bf16 bf16x2 __attribute__((ext_vector_type(2)));
typedef short s16x4 __attribute__((ext_vector_type(4)));
typedef float f32x4 __attribute__((ext_vector_type(4)));
typedef float f32x2 __attribute__((ext_vector_type(2)));
typedef float f32x16 __attribute__((ext_vector_type(16)));
typedef unsigned u32x4 __attribute__((ext_vector_type(4)));
typedef unsigned u32x2 __attribute__((ext_vector_type(2)));
typedef __amdgpu_buffer_rsrc_t rsrc_t;

template <int N> struct IC { static constexpr int v = N; };
template <int B, int E, class F> DI void sfor(F&& f) { if constexpr (B < E) { f(IC<B>{}); sfor<B + 1, E>(f); } }

constexpr u32 FS = 128;
constexpr u32 WS_FH = FS, WS_FQ = WS_FH + 256 * FS, WS_FB = WS_FQ + 256 * FS, WS_FG = WS_FB + 256 * FS, WS_FP = WS_FG + 256 * FS, WS_FC = WS_FP + 448 * FS;
constexpr u32 WS_FS = WS_FC + 32 * FS, WS_FZ = WS_FS + 128 * FS, WS_CS = WS_FZ + 287 * FS, WS_ST = WS_CS + 16384, CTL_BYTES = WS_ST + 32768;
constexpr u32 WS_QKV = CTL_BYTES, QKV_BYTES = 64u * 336u * 256u;
constexpr u32 WS_AC = WS_QKV + QKV_BYTES, AC_BYTES = 64u * 144u * 1024u + 448u * 4096u;
constexpr u32 PX = 64u * 144u * 1024u;
constexpr u32 RING = 7 * 16384;
constexpr u32 L_ST = 40960, L_POLL = 45056, L_CSQ = 46080, L_CSH = 46848, L_CNT = 48896;
constexpr u32 L_K = 0, L_V = 21504, L_Q = RING + 5 * 8192;
constexpr u32 L_WC = 49152, L_RED = 0;
enum { C_DONE, C_LAND = 4, C_FQ = 6, C_FA, C_FG, C_KV, C_FB, C_WLO, C_WHI, C_P30, C_WO, C_ATT, C_LATE, C_PAIR0, C_PAIR1, C_CSH, C_FH, C_SQ, C_V, C_ST, C_CS0, C_N };
constexpr u32 stage_addr(int s) {
  return s < 14 ? RING + (s % 6) * 6144u : s < 19 ? (s - 14) * 8192u : s < 21 ? RING + (s - 19) * 8192u : s < 23 ? 6 * 16384u + (s - 21) * 8192u : s < 26 ? RING + (s - 21) * 8192u : RING + (s - 26) * 8192u;
}
constexpr int NWP = 24;

enum { K_NONE = 0, K_X, K_WA, K_BIAS, K_WCP };
constexpr int OP(int k, int a, int e = 0) { return k * 256 + 2 * a + e; }
constexpr int opn(int op) { int k = op / 256, a = (op % 256) / 2; return k == K_NONE ? 0 : k == K_X ? 4 : k == K_WA ? (a < 14 ? 3 : 4) : k == K_WCP ? 2 : 1; }
#define CAP_1 8
#define P_NPRO 8
#define P_DA 12
#define P_TB 16
#define P_TO 44
#define P_FQ 18
#define PRIO_C 0
#define ROTA 0
#define WS_OFF 0
#define CODE_KB 40
#define PF_DELAY 8
#define P_TFP 66
#define SLP 2
#define LSLP 1
#define GATE_K 0
#define OWN1 4
#define CLR0 4
#define TOUCH 1
#define HNOW 2
#define CLRQ 0
#define HOLD 4
#define IDLE_SLP 9
#define T_PP 62
#define P_PF 4
#define WCP_LATE 0
#define TLBW 0
#define WCP_EARLY 0
#define HA_EARLY 19
#define PRIO_L 0
#define PRIO_A 0
#define PFD 10
#define PF1 0
#define VLATE 1
#define QNOW 6
#define P_NPRE 6
constexpr int NPRE = P_NPRE;
constexpr int MAXO = 24, NPRO = P_NPRO, DA = P_DA, TB = P_TB, S_FQ = P_FQ, T_WH = 20, T_WO = 33, T_O = P_TO;
struct Sched {
  int pro[NPRO], opA[15][MAXO];
  int wA[16];
  int wL[2];
  int wC[56], a0[56], a1[56];
  int ok;
};
constexpr int cl(int n) { return n > 63 ? 63 : n; }
constexpr Sched make_sched() {
  Sched r{};
  r.ok = 1;
  int n = 0, iW[26] = {}, iX[7] = {};
  int lo[128] = {}, le[128] = {}, nl = 0;
  for (int s = 0; s < 26; ++s) {
    if (s < 14 && !(s & 1)) { lo[nl] = OP(K_X, s >> 1, 0); le[nl++] = -1; lo[nl] = OP(K_X, s >> 1, 1); le[nl++] = -1; }
    lo[nl] = OP(K_WA, s); le[nl++] = s < 14 ? s - 5 : (s < 19 ? 5 : (s == 21 || s == 22 ? 13 : 14));
  }
  for (int k = 0; k < NWP / 2; ++k) { lo[nl] = OP(K_WCP, k); le[nl++] = k < WCP_EARLY ? 7 + 2 * (k >> 2) : 14; }
  bool done[128] = {};
  auto note = [&](int op) { n += opn(op); int k = op / 256, a = (op % 256) / 2; if (k == K_X) iX[a] = (op & 1) ? n : 0; if (k == K_WA) iW[a] = n; };
  for (int k = 0; k < NPRO; ++k) { if (k == NPRE) r.wA[0] = cl(n - iW[0]); r.pro[k] = lo[k]; done[k] = true; note(lo[k]); }
  for (int s = 0; s < 15; ++s) {
    int need = iW[s + 1]; if (!need) r.ok = 0;
    if (s + 1 <= 13) { int v = iX[(s + 1) >> 1]; if (!v) r.ok = 0; if (v > need) need = v; }
    r.wA[s + 1] = cl(n - need);
    int cap = s == 14 ? 64 : CAP_1, c = 0, used = 0;
    for (int k = 0; k < nl; ++k) if (!done[k] && le[k] <= s && used + opn(lo[k]) <= cap && c < MAXO) { done[k] = true; r.opA[s][c++] = lo[k]; used += opn(lo[k]); note(lo[k]); if (s == 14 && c == 2) r.wL[0] = cl(n - iW[18]); }
  }
  r.wL[1] = cl(n - iW[25]);
  if (r.opA[14][0] != OP(K_WA, 19)) r.ok = 0;
  for (int k = 0; k < nl; ++k) if (!done[k]) r.ok = 0;
  n = 0;
  int iA[56] = {}, iP = 0, iH = 0, iO = 0, nA = 8 + DA;
  for (int m = 8; m < 8 + DA; ++m) { n += 1; iA[m] = n; }
  for (int s = 8; s < 56; ++s) {
    int need = iA[s]; if (!need) r.ok = 0;
    if (s == TB && iP > need) need = iP;
    if (s == T_WH && iH > need) need = iH;
    if (s == T_WO + 12 && iO > need) need = iO;
    r.wC[s] = cl(n - need);
    if (s == TB - 3) { n += 1; iP = n; }
    r.a0[s] = nA;
    for (int c = 0; c < 2 && nA < 56 && nA <= s + DA && (nA < 32 || s >= TB); ++c) { n += 1; iA[nA++] = n; }
    r.a1[s] = nA;
    if (s < 15) { n += 2; iH = n; }
    if (s >= T_WO && s < T_WO + 7) { n += 2; iO = n; }
    if (s >= T_O && s < T_O + 8) n += 2;
    if (s == T_O + 8) n += 3;
  }
  return r;
}
constexpr Sched SC = make_sched();
static_assert(SC.ok, "schedule");

DI rsrc_t mkrs(const void* p, u32 bytes) { return __builtin_amdgcn_make_buffer_rsrc((void*)p, 0, bytes, 0x00020000); }
template <int N> DI void dman(u32 lds, const u32* v, rsrc_t rs, u32 soff) {
  if constexpr (N == 4) asm volatile("s_mov_b32 m0, %0\n\ts_nop 0\n\tbuffer_load_dwordx4 %1, %5, %6 offen lds\n\tbuffer_load_dwordx4 %2, %5, %6 offen offset:1024 lds\n\t"
    "buffer_load_dwordx4 %3, %5, %6 offen offset:2048 lds\n\tbuffer_load_dwordx4 %4, %5, %6 offen offset:3072 lds" ::"s"(lds), "v"(v[0]), "v"(v[1]), "v"(v[2]), "v"(v[3]), "s"(rs), "s"(soff) : "memory");
  if constexpr (N == 3) asm volatile("s_mov_b32 m0, %0\n\ts_nop 0\n\tbuffer_load_dwordx4 %1, %4, %5 offen lds\n\tbuffer_load_dwordx4 %2, %4, %5 offen offset:1024 lds\n\t"
    "buffer_load_dwordx4 %3, %4, %5 offen offset:2048 lds" ::"s"(lds), "v"(v[0]), "v"(v[1]), "v"(v[2]), "s"(rs), "s"(soff) : "memory");
  if constexpr (N == 2) asm volatile("s_mov_b32 m0, %0\n\ts_nop 0\n\tbuffer_load_dwordx4 %1, %3, %4 offen lds\n\tbuffer_load_dwordx4 %2, %3, %4 offen offset:1024 lds" ::"s"(lds), "v"(v[0]), "v"(v[1]), "s"(rs), "s"(soff) : "memory");
  if constexpr (N == 1) asm volatile("s_mov_b32 m0, %0\n\ts_nop 0\n\tbuffer_load_dwordx4 %1, %2, %3 offen lds" ::"s"(lds), "v"(v[0]), "s"(rs), "s"(soff) : "memory");
}
DI void dma1792c(u32 lds, u32 voff, rsrc_t rs, u32 soff) {
  asm volatile("s_mov_b32 m0, %0\n\ts_nop 0\n\tbuffer_load_dwordx4 %1, %2, %3 offen sc1 lds\n\ts_mov_b32 exec_hi, 0xffff\n\t"
               "buffer_load_dwordx4 %1, %2, %3 offen offset:1024 sc1 lds\n\ts_mov_b32 exec_hi, -1" ::"s"(lds), "v"(voff), "s"(rs), "s"(soff) : "memory");
}
DI void dma4(u32 lds, u32 voff, rsrc_t rs, u32 soff) {
  asm volatile("s_mov_b32 m0, %0\n\ts_nop 0\n\tbuffer_load_dword %1, %2, %3 offen lds" ::"s"(lds), "v"(voff), "s"(rs), "s"(soff) : "memory");
}
template <bool C, int IMM = 0> DI u32x4 ld16_async(u32 voff, rsrc_t rs, u32 soff) {
  u32x4 r;
  if constexpr (C) asm volatile("buffer_load_dwordx4 %0, %1, %2, %3 offen offset:%4 sc1" : "=v"(r) : "v"(voff), "s"(rs), "s"(soff), "n"(IMM) : "memory");
  else asm volatile("buffer_load_dwordx4 %0, %1, %2, %3 offen" : "=v"(r) : "v"(voff), "s"(rs), "s"(soff) : "memory");
  return r;
}
DI u32x2 ld8c_async(u32 voff, rsrc_t rs) {
  u32x2 r; asm volatile("buffer_load_dwordx2 %0, %1, %2, 0 offen sc1" : "=v"(r) : "v"(voff), "s"(rs) : "memory"); return r;
}
template <int N> DI void wait_bar() { asm volatile("s_waitcnt vmcnt(%0) lgkmcnt(0)\n\ts_barrier" ::"n"(N) : "memory"); }
template <int N> DI void vm_bar() { asm volatile("s_waitcnt vmcnt(%0)\n\ts_barrier" ::"n"(N) : "memory"); }
template <int N> DI void vm() { asm volatile("s_waitcnt vmcnt(%0)" ::"n"(N) : "memory"); }
DI void bar() { asm volatile("s_barrier" ::: "memory"); }
#define PIN(x) asm volatile("" : "+v"(x))
#define SB() __builtin_amdgcn_sched_barrier(0)
DI f32x4 mfma(bf16x8 a, bf16x8 b, f32x4 c) { return __builtin_amdgcn_mfma_f32_16x16x32_bf16(a, b, c, 0, 0, 0); }
DI f32x16 mfma32(bf16x8 a, bf16x8 b, f32x16 c) { return __builtin_amdgcn_mfma_f32_32x32x16_bf16(a, b, c, 0, 0, 0); }
DI u32 pk(float a, float b) { bf16x2 v = {(__bf16)a, (__bf16)b}; return __builtin_bit_cast(u32, v); }
DI float fmx(float a, float b) { return __builtin_elementwise_maximum(a, b); }
DI float rbf(float a) { return (float)(__bf16)a; }
DI float bflo(u32 v) { return __uint_as_float(v << 16); }
DI float bfhi(u32 v) { return __uint_as_float(v & 0xffff0000u); }
DI float dot2(u32 a, u32 b, float c) { return __builtin_amdgcn_fdot2_f32_bf16(__builtin_bit_cast(bf16x2, a), __builtin_bit_cast(bf16x2, b), c, false); }
DI float sum8(float v) {
  v += __builtin_bit_cast(float, __builtin_amdgcn_update_dpp(0, __builtin_bit_cast(int, v), 0xb1, 0xf, 0xf, true));
  v += __builtin_bit_cast(float, __builtin_amdgcn_update_dpp(0, __builtin_bit_cast(int, v), 0x4e, 0xf, 0xf, true));
  v += __builtin_bit_cast(float, __builtin_amdgcn_update_dpp(0, __builtin_bit_cast(int, v), 0x141, 0xf, 0xf, true));
  return v;
}
template <class T> DI T lds_ld(const LAS char* base, u32 off) { return *(const LAS T*)(base + off); }
template <class T> DI void lds_st(LAS char* base, u32 off, T v) { *(LAS T*)(base + off) = v; }
DI bf16x8 trfrag(LAS char* base, u32 off0, u32 off1) {
  s16x4 lo = __builtin_amdgcn_ds_read_tr16_b64_v4i16((LAS s16x4*)(base + off0));
  s16x4 hi = __builtin_amdgcn_ds_read_tr16_b64_v4i16((LAS s16x4*)(base + off1));
  return __builtin_bit_cast(bf16x8, __builtin_shufflevector(lo, hi, 0, 1, 2, 3, 4, 5, 6, 7));
}
#define ST_AUX 16
template <int AUX> DI void st16(u32x4 v, rsrc_t rs, u32 voff) { __builtin_amdgcn_raw_buffer_store_b128(v, rs, voff, 0, AUX ? ST_AUX : 0); }
DI void st_flag(rsrc_t rc, u32 off, u64 v) { __builtin_amdgcn_raw_buffer_store_b64(u32x2{(u32)v, (u32)(v >> 32)}, rc, off, 0, 16); }
DI bool tagged(u32x2 v, u64 tag) { return __all(v[0] == (u32)tag && v[1] == (u32)(tag >> 32)); }
template <int SLEEP> DI void spin(rsrc_t rc, u32 off, u64 tag) {
#pragma unroll 1
  for (u32 n = 0; n < (1u << 17); ++n) {
    asm volatile("" ::: "memory");
    if (tagged(__builtin_amdgcn_raw_buffer_load_b64(rc, off, 0, 16), tag)) break;
    __builtin_amdgcn_s_sleep(SLEEP);
  }
  asm volatile("" ::: "memory");
}
DI u32 cnt_ld(LAS char* L, int k) { return __hip_atomic_load((LAS u32*)(L + L_CNT + 4 * k), __ATOMIC_RELAXED, __HIP_MEMORY_SCOPE_WORKGROUP); }
DI void cnt_wait(LAS char* L, int k, u32 v) {
#pragma unroll 1
  for (u32 n = 0; n < (1u << 20); ++n) {
    if ((u32)__builtin_amdgcn_readfirstlane(cnt_ld(L, k)) >= v) break;
    __builtin_amdgcn_s_sleep(LSLP);
  }
  asm volatile("" ::: "memory");
}
DI void prog_st(LAS char* L, int k, u32 v) { asm volatile("" ::: "memory"); __hip_atomic_store((LAS u32*)(L + L_CNT + 4 * k), v, __ATOMIC_RELAXED, __HIP_MEMORY_SCOPE_WORKGROUP); asm volatile("" ::: "memory"); }
DI u32 land_ld(LAS char* L) { const u64 v = __hip_atomic_load((LAS u64*)(L + L_CNT + 4 * C_LAND), __ATOMIC_RELAXED, __HIP_MEMORY_SCOPE_WORKGROUP); return min((u32)v, (u32)(v >> 32)); }
DI u32 done_ld(LAS char* L) {
  const u64 a = __hip_atomic_load((LAS u64*)(L + L_CNT), __ATOMIC_RELAXED, __HIP_MEMORY_SCOPE_WORKGROUP), b = __hip_atomic_load((LAS u64*)(L + L_CNT + 8), __ATOMIC_RELAXED, __HIP_MEMORY_SCOPE_WORKGROUP);
  return min(min((u32)a, (u32)(a >> 32)), min((u32)b, (u32)(b >> 32)));
}
template <bool DONE> DI void prog_wait(LAS char* L, u32 v) {
#pragma unroll 1
  for (u32 n = 0; n < (1u << 20); ++n) {
    if ((u32)__builtin_amdgcn_readfirstlane(DONE ? done_ld(L) : land_ld(L)) >= v) break;
    __builtin_amdgcn_s_sleep(LSLP);
  }
  asm volatile("" ::: "memory");
}
DI void cnt_add(u32 lb, int k) {
  asm volatile("s_mov_b64 exec, 1\n\tds_add_u32 %0, %1\n\ts_mov_b64 exec, -1" ::"v"(lb + L_CNT + 4 * k), "v"(1u) : "memory");
}
template <int N> DI bool vm_le() {
  u32 r; asm volatile("s_getreg_b32 %0, hwreg(HW_REG_IB_STS)" : "=s"(r)::"memory");
  return (r & 0xc0000fu) <= (u32)N;
}
DI bool last_of4(LAS char* L, int k) {
  u32 old = 0;
  asm volatile("" ::: "memory");
  if (__builtin_amdgcn_mbcnt_hi(~0u, __builtin_amdgcn_mbcnt_lo(~0u, 0u)) == 0) old = __hip_atomic_fetch_add((LAS u32*)(L + L_CNT + 4 * k), 1u, __ATOMIC_RELAXED, __HIP_MEMORY_SCOPE_WORKGROUP);
  asm volatile("" ::: "memory");
  return __builtin_amdgcn_readfirstlane(old) == 3;
}
#define STAMP(k)
#define FLUSH()

__global__ __launch_bounds__(512, 2) void fused_block(const char* __restrict__ x, const char* __restrict__ wqkv, const char* __restrict__ wo,
                                                      const char* __restrict__ w1, const char* __restrict__ b1, const char* __restrict__ w2,
                                                      const char* __restrict__ b2, char* __restrict__ out, char* ws) {
  __shared__ __attribute__((aligned(1024))) char smem[163840];
  asm volatile("" : "+s"(x), "+s"(wqkv), "+s"(wo), "+s"(w1), "+s"(b1), "+s"(w2), "+s"(b2), "+s"(out), "+s"(ws));
  u64 pc; asm volatile("s_getpc_b64 %0" : "=s"(pc));
  LAS char* const L = (LAS char*)smem;
  const u32 lb = (u32)(uintptr_t)smem;
  int l = threadIdx.x & 63, g, q4, p4, l15;
  auto lanes = [&] { PIN(l); g = l >> 4; q4 = (l >> 2) & 3; p4 = l & 3; l15 = l & 15; };
  lanes();
  int w = __builtin_amdgcn_readfirstlane(threadIdx.x >> 6);
  asm volatile("s_nop 4" : "+s"(w));
  const int x8 = blockIdx.x & 7, rk = blockIdx.x >> 3;
  const bool busy = rk < 28;
  if (w == 0) STAMP(0);
  const rsrc_t rQKV = mkrs(ws + WS_QKV, QKV_BYTES), rAC = mkrs(ws + WS_AC, AC_BYTES), rCTL = mkrs(ws, CTL_BYTES);
  const rsrc_t rX = mkrs(x, 2048 * 896), rW2 = mkrs(w2, 1792 * 896);
  const int kh = x8 >> 2, cc = rk % 7, rb = (x8 & 3) * 4 + rk / 7, cb = kh * 7 + cc;
  const int ai = rk & 15, ah = 2 * x8 + (rk >> 4);
  const int ro6 = ROTA ? (cc + ((3 * (rk / 7)) >> 1)) % 6 : 0;
  auto rotl = [&](int j) { const int t = j + ro6; return j == 0 ? 0 : (t > 6 ? t - 6 : t); };
  auto kstep = [&](int p) {
    if (p < 8) return 8 * cb + p;
    const int t = cc + 1 + ((p - 8) % 24) / 4;
    return 8 * (7 * kh + (t >= 7 ? t - 7 : t)) + (p & 3) + (p >= 32 ? 4 : 0);
  };
  if (busy && (w == 4 || w == 5)) {
    u32 v[4];
#pragma unroll
    for (int j = 0; j < 4; ++j) v[j] = (8 * j + (l >> 3)) * 896 + (((l & 7) ^ ((j & 1) * 4 + g)) << 4) - 1024 * j;
    dman<4>(lb + (w & 1) * 8192, v, rX, (rb * 128 + 64 * (w & 1)) * 896);
    dman<4>(lb + (w & 1) * 8192 + 4096, v, rX, (rb * 128 + 64 * (w & 1) + 32) * 896);
    u32 vq[3];
#pragma unroll
    for (int j = 0; j < 3; ++j) { const u32 n = 64 * j + l; vq[j] = (n / 12) * 2688 + (n % 12) * 16 - 1024 * j; }
#pragma unroll
    for (int a = 0; a < 2; ++a) {
      dman<3>(lb + stage_addr(a) + (w & 1) * 3072, vq, mkrs(wqkv, 448 * 2688), cb * 192 + (16 * (w & 1) + 32 * a) * 2688);
    }
  }
  if (w == 6) dma4(lb + L_Q + 512, (rk * 32 + (l & 31)) * 64, mkrs((const void*)(pc & ~255ull), CODE_KB * 1024), 0);
  u32x4 d[14];
  const bool stat = busy && (w == 6 || (w == 7 && cb < 2));
  const int srow = rb * 128 + (w == 6 ? cb : 14 + cb) * 8 + (l >> 3);
  if (stat) {
#pragma unroll
    for (int k = 0; k < 7; ++k) d[k] = ld16_async<false>(srow * 896 + (l & 7) * 16, rX, k * 128);
  }
  if (busy && w == 7) {
    const int r = rb * 28 + (l >> 1);
    if (l < 56) dma4(lb + L_Q, r * 2688 + cb * 192 + (l & 1) * 188, mkrs(wqkv, 448 * 2688), 0);
  }
  SB();
  u64 tag;
  auto epoch = [&] { asm volatile("s_load_dwordx2 %0, %1, 0x0\n\ts_waitcnt lgkmcnt(0)" : "=s"(tag) : "s"(ws) : "memory"); tag += 1; };
  auto vW2 = [&](u32* v) { v[0] = (l >> 3) * 896 + (((l & 7) ^ ((g & 1) << 2)) << 4); v[1] = v[0] + 7168 - 1024; };

  if (w < 4) {
    if (!busy) {
      const int q = (rk - 28) * 4 + w;
      bar();
      for (int k = 0; k < PF_DELAY; ++k) __builtin_amdgcn_s_sleep(2);
      const rsrc_t rW1 = mkrs(w1, 448 * 3584), rWo = mkrs(wo, 448 * 896);
      const u32 v = l * 16;
      auto pf1 = [&] {
        const u32 v1 = l < 56 ? (l >> 3) * 256 + 128 + (l & 7) * 16 : 0x7fffffffu;
#pragma unroll 1
        for (int k = 0; k < 28; ++k) { dman<1>(lb + L_WC + w * 16384 + (k & 15) * 1024, &v1, rW1, (k * 16 + q) * 3584 + kh * 1792); vm<PFD>(); }
      };
      if (PF1 == 1) pf1();
#pragma unroll 1
      for (int k = 0; k < 49; ++k) {
        int r = k * 16 + q + (x8 & 3) * 196; r = r >= 784 ? r - 784 : r;
        dman<1>(lb + L_WC + w * 16384 + (k & 15) * 1024, &v, rW2, kh * 802816 + r * 1024);
        vm<PFD>();
      }
      STAMP(38);
      if (PF1 == 2) pf1();
#pragma unroll 1
      for (int k = 0; k < 25; ++k) { dman<1>(lb + L_WC + w * 16384 + (k & 15) * 1024, &v, rWo, (k * 16 + q) * 1024); vm<PFD>(); }
      vm<0>();
      STAMP(39);
      FLUSH();
      return;
    }
    __builtin_amdgcn_s_setprio(PRIO_A);
    constexpr int PF = P_PF;
    u32 oX[4], oH[2], oQ[3];
#pragma unroll
    for (int p = 0; p < 4; ++p) oX[p] = (4 * w + ((l & 31) >> 3)) * 1024 + (l & 7) * 128 + (((2 * p + (g >> 1)) ^ (l15 >> 1)) << 4);
    const int rq = w == 3 ? 0 : w;
#pragma unroll
    for (int i = 0; i < 3; ++i) oQ[i] = RING + (8 * (g >> 1) + q4) * 192 + ((i + rq) % 3) * 64 + (g & 1) * 32 + p4 * 8;
#pragma unroll
    for (int i = 0; i < 2; ++i) oH[i] = RING + (g >> 1) * 1024 + q4 * 128 + ((i ^ (q4 >> 1)) << 6) + (g & 1) * 32 + p4 * 8;
    const u32 selv = l15 == (g & 1) ? 0x3f803f80u : 0u;
    const bf16x8 sel = __builtin_bit_cast(bf16x8, u32x4{selv, selv, selv, selv});
    bf16x8 xf[28], wf[8];
    f32x16 aH[2], aQ[3] = {};
    f32x4 cs = {};
    u32x2 ch[2];
    float rs;
    bf16x8 lnb;
    u32x4 hA[72], qp[6];
    auto rdW = [&](auto SS, auto II) {
      constexpr int s = decltype(SS)::v, i = decltype(II)::v;
      constexpr int slot = (s < 14 ? s * 6 + i : 84 + i) % 8;
      if constexpr (s < 14) { const u32 o = oQ[i % 3] + (stage_addr(s) - RING) + (i / 3) * 3072; wf[slot] = trfrag(L, o, o + 768); }
      else if constexpr (s < 28) { const u32 o = oH[i & 1] + (stage_addr(s) - RING) + (i >> 1) * 2048; wf[slot] = trfrag(L, o, o + 512); }
    };
    auto rdX = [&](auto SS, auto EE) { constexpr int k = 2 * decltype(SS)::v + decltype(EE)::v; if constexpr (k < 28) xf[k] = lds_ld<bf16x8>(L, oX[k & 3] + (k >> 2) * 16384); };
    auto rdcs = [&](int half) {
#pragma unroll
      for (int i = 0; i < 2; ++i) ch[i] = lds_ld<u32x2>(L, L_CSH + (half * 64 + i * 32 + (l & 31)) * 8);
    };
    auto hpack = [&](int half, int c) {
      u32x4 hp;
#pragma unroll
      for (int j = 0; j < 4; ++j) {
        const float t0 = aH[c >> 1][8 * (c & 1) + 2 * j], t1 = aH[c >> 1][8 * (c & 1) + 2 * j + 1];
        hp[j] = pk(t0 * fmx(t0, 0.f), t1 * fmx(t1, 0.f));
      }
      hA[half * 4 + c] = hp;
    };
    auto hstore = [&](int m) { st16<16>(hA[m], rAC, l * 16 + ((rb * 4 + w) * 144 + cb * 8 + m) * 1024); };
    auto qpack = [&](int c) {
      const int i = c >> 1, e = c & 1, tq = 6 * cb + 2 * ((i + rq) % 3) + e;
      const float m = tq < 28 ? rs * (0.18898223650461361f * 1.4426950408889634f) : rs;
      const auto s0 = __builtin_amdgcn_permlane16_swap(pk(aQ[i][8 * e] * m, aQ[i][8 * e + 1] * m), pk(aQ[i][8 * e + 4] * m, aQ[i][8 * e + 5] * m), false, false);
      const auto s1 = __builtin_amdgcn_permlane16_swap(pk(aQ[i][8 * e + 2] * m, aQ[i][8 * e + 3] * m), pk(aQ[i][8 * e + 6] * m, aQ[i][8 * e + 7] * m), false, false);
      qp[c] = u32x4{s0[0], s1[0], s0[1], s1[1]};
    };
    auto qstore = [&](int c) {
      const int fl = ((g & 1) << 1) | (g >> 1), tq = 6 * cb + 2 * (((c >> 1) + rq) % 3) + (c & 1);
      u32x4 v = qp[c];
      u32 vo = fl * 256 + l15 * 16;
      if (tq >= 56) {
        asm volatile("" ::: "memory");
        if (fl & 1) v = u32x4{v[2], v[3], v[0], v[1]};
        vo ^= (2 * (tq & 1) + (fl >> 1)) << 6;
      }
      st16<16>(v, rQKV, vo + ((rb * 4 + w) * 336 + 4 * tq) * 256);
    };
    bool sigq = false, siga = false;
    const bool giver = (w >> 1) != kh;
    bar();
    STAMP(1);
    rdX(IC<0>{}, IC<0>{}); rdX(IC<0>{}, IC<1>{});
    sfor<0, PF>([&](auto I) { rdW(IC<0>{}, I); });
    sfor<0, 28>([&](auto SS) {
      constexpr int s = decltype(SS)::v, NT = s < 14 ? 6 : 8;
      if constexpr (s == 14) wait_bar<63>();
      else if constexpr (s < 14) bar();
      if constexpr (s == 13) lanes();
      if constexpr (s == 14) {
        STAMP(2);
        u32 cq[3];
#pragma unroll
        for (int i = 0; i < 3; ++i) cq[i] = lds_ld<u32>(L, L_CSQ + (((i + rq) % 3) * 32 + (l & 31)) * 4);
        const u32 rdy = cnt_ld(L, C_ST);
        u32 st = lds_ld<u32>(L, L_ST + (w * 32 + (l & 31)) * 16);
        if (!__builtin_amdgcn_readfirstlane(rdy)) { cnt_wait(L, C_ST, 1); st = lds_ld<u32>(L, L_ST + (w * 32 + (l & 31)) * 16); }
        SB();
        lnb = __builtin_bit_cast(bf16x8, u32x4{l < 32 ? (st & 0xffffu) * 0x10001u : 0u, l < 32 ? st >> 16 : 0u, 0u, 0u});
        rs = __builtin_amdgcn_rcpf(bfhi(st));
#pragma unroll
        for (int i = 0; i < 3; ++i) aQ[i] = mfma32(__builtin_bit_cast(bf16x8, u32x4{cq[i], 0u, 0u, 0u}), lnb, aQ[i]);
        SB();
#pragma unroll
        for (int c = 0; c < 6; ++c) { qpack(c); if (c % (6 / QNOW) == 6 / QNOW - 1) qstore(c / (6 / QNOW)); SB(); }
        STAMP(3);
      }
      if constexpr (s == 21) {
        rdcs(1);
        SB();
#pragma unroll
        for (int c = 0; c < 4; ++c) { hpack(0, c); if (c < HNOW) hstore(c); SB(); }
      }
      u32 land;
      sfor<0, NT>([&](auto II) {
        constexpr int i = decltype(II)::v, slot = (s < 14 ? s * 6 + i : 84 + i) % 8;
        constexpr u32 need = s == 15 ? 1 : s == 18 ? 2 : s == 22 ? 3 : s == 25 ? 4 : 0;
        if constexpr (need && i == 0) land = land_ld(L);
        if constexpr (need && i == NT - PF) {
          if ((u32)__builtin_amdgcn_readfirstlane(land) < need) prog_wait<false>(L, need);
          asm volatile("" ::: "memory");
        }
        if constexpr (i + PF < NT) rdW(SS, IC<i + PF>{});
        else rdW(IC<s + 1>{}, IC<i + PF - NT>{});
        if constexpr (i < 2) rdX(IC<s + 1>{}, II);
        if constexpr (s < 14) {
          aQ[i % 3] = mfma32(wf[slot], xf[2 * s + i / 3], aQ[i % 3]);
          if constexpr (i % 3 == 0) cs = mfma(wf[slot], sel, cs);
          if constexpr (s == 13 && i == 4) {
            u32x4 v;
#pragma unroll
            for (int j = 0; j < 4; ++j) { const float hi = rbf(cs[j]); v[j] = pk(hi, cs[j] - hi); }
            if (l15 < 2) lds_st<u32x4>(L, L_CSQ + w * 128 + (l15 * 16 + 4 * g) * 4, v);
          }
        } else {
          constexpr int k = 4 * ((s - 14) % 7) + (i >> 1), m = (s < 21 ? QNOW : HNOW) + 2 * ((s - 14) % 7) + (i >> 2);
          if constexpr (k == 0 && s == 21) aH[i] = mfma32(__builtin_bit_cast(bf16x8, u32x4{ch[i][0], ch[i][1], 0u, 0u}), lnb, f32x16{});
          aH[i & 1] = mfma32(wf[slot], xf[k], s == 14 && k == 0 ? f32x16{} : aH[i & 1]);
          if constexpr (s == 19 && i == 7) { cnt_wait(L, C_CSH, 1); rdcs(0); }
          if constexpr (s == 20 && (i == 2 || i == 3)) aH[i & 1] = mfma32(__builtin_bit_cast(bf16x8, u32x4{ch[i & 1][0], ch[i & 1][1], 0u, 0u}), lnb, aH[i & 1]);
          if constexpr ((i & 3) == 1 && s < 21 && m < 6) qstore(m);
          if constexpr ((i & 3) == 1 && s >= 21 && m < 4) hstore(m);
        }
        SB();
      });
      if constexpr (s == 18 || s == 20 || s == 27) prog_st(L, C_DONE + w, s);
      if constexpr (s >= 14 && s <= S_FQ && QNOW + 2 * (s - 14) >= 6) { if (!sigq && (s == S_FQ || vm_le<0>())) { vm<0>(); cnt_add(lb, C_FQ); sigq = true; } }
      if constexpr (s >= 21 && HNOW + 2 * (s - 21) >= 4) { if (!siga && vm_le<0>()) { vm<0>(); cnt_add(lb, C_FA); siga = true; } }
    });
    STAMP(4);
    lanes();
    const u32 vFG = WS_FG + (rb * 16 + 7 * kh + (l < 7 ? l : 0)) * FS;
#pragma unroll
    for (int c = 0; c < 4; ++c) hpack(1, c);
    SB();
    if (!siga) { vm<0>(); cnt_add(lb, C_FA); }
#pragma unroll
    for (int m = 4; m < 8; ++m) hstore(m);
    STAMP(5);
    { const u32x2 t = lds_ld<u32x2>(L, L_CNT + 112); tag = (u64)(u32)__builtin_amdgcn_readfirstlane(t[0]) | (u64)(u32)__builtin_amdgcn_readfirstlane(t[1]) << 32; }
    u32x2 polled;
    u32 vW[2];
    vW2(vW);
    const u32 vA = l * 16, sA = (rb * 4 + w) * 144, sO = ((rb * 4 + (giver ? w ^ 2 : w)) * 144 + (giver ? 128 : 112)) * 1024;
    u32 sN[6];
#pragma unroll
    for (int j = 0; j < 6; ++j) { sN[j] = (sA + kstep(8 + 4 * j)) * 1024; asm volatile("" : "+s"(sN[j])); }
    const u32 vB = vA + 4096;
    auto ldA = [&](auto M) { constexpr int m = decltype(M)::v; hA[m] = ld16_async<true, (m & 3) * 1024>(m >= 32 ? vB : vA, rAC, sN[((m - 8) % 24) / 4]); };
    auto ldO = [&](auto M) { constexpr int m = decltype(M)::v; hA[m] = ld16_async<true, (m & 3) * 1024>(vA, rAC, sO + ((m - 56) & ~3) * 1024); };
    const rsrc_t rWo = mkrs(wo, 448 * 896);
    u32 oW[2], oO[2];
#pragma unroll
    for (int b = 0; b < 2; ++b) { oW[b] = (4 * (g >> 1) + q4) * 128 + ((b ^ (q4 >> 1)) << 6) + (g & 1) * 32 + p4 * 8; oO[b] = oW[b] + L_WC + (giver ? 8 * 3584 : 0); }
    bf16x8 wc[2][2];
    auto rdC = [&](int m) {
#pragma unroll
      for (int b = 0; b < 2; ++b) {
        const u32 o = m < 56 ? oW[b] + L_WC + m * 2048 : oO[b] + ((m - 56) >> 1) * 3584 + (m & 1) * 2048;
        wc[m & 1][b] = trfrag(L, o, o + 1024);
      }
    };
    f32x16 acc[2];
    f32x16 aco[2];
    u32 top, pre;
    u32x4 xres[2], bres;
    u32x2 pp[4];
    const int tile = giver ? w ^ 2 : w, r0 = (giver ? 16 : 0) + (l >> 3);
    auto step = [&](auto N) {
      constexpr int n = decltype(N)::v;
      if constexpr (n + 1 < 72) rdC(n + 1);
      SB();
      f32x16* a = n < 56 ? acc : aco;
      a[0] = mfma32(wc[n & 1][0], __builtin_bit_cast(bf16x8, hA[n]), n == 0 || n == 56 ? f32x16{} : a[0]);
      SB();
      if constexpr (n >= 8 && n < 56) sfor<SC.a0[n], SC.a1[n]>(ldA);
      if constexpr (n >= 8 && n < 15) dman<2>(lb + L_WC + (NWP + 4 * (n - 8) + w) * 2048, vW, rW2, (kstep(NWP + 4 * (n - 8)) + w) * (16 * 896) + cc * 128);
      if constexpr (n >= T_WO && n < T_WO + 7) dman<2>(lb + L_WC + (4 * (n - T_WO) + w) * 2048, vW, rWo, (4 * (n - T_WO) + w) * (16 * 896) + cc * 128);
      if constexpr (n == T_WO) { if (w == 0) dman<2>(lb + L_WC + 28 * 2048, vW, rWo, 28 * (16 * 896) + cc * 128); }
      if constexpr (n >= 56) { top = max(max(n == 56 ? 0u : top, hA[n][0]), hA[n][1]); top = max(max(top, hA[n][2]), hA[n][3]); PIN(top); }
      a[1] = mfma32(wc[n & 1][1], __builtin_bit_cast(bf16x8, hA[n]), n == 0 || n == 56 ? f32x16{} : a[1]);
      SB();
    };
    cnt_wait(L, C_WLO, 2);
    rdC(0);
    sfor<0, OWN1>(step);
    bool sigg = false;
    auto pubg = [&] { if (last_of4(L, C_FG) && l == 0) st_flag(rCTL, WS_FG + (rb * 16 + cb) * FS, tag); sigg = true; };
#pragma unroll 1
    for (u32 n = 0; n < (1u << 20); ++n) {
      if (!sigg && vm_le<0>()) { vm<0>(); pubg(); }
      if (__builtin_amdgcn_readfirstlane(cnt_ld(L, C_FH))) break;
      __builtin_amdgcn_s_sleep(LSLP);
    }
    asm volatile("" ::: "memory");
    STAMP(6);
    __builtin_amdgcn_s_setprio(PRIO_C);
    sfor<8, 8 + DA>(ldA);
    prog_wait<true>(L, 27);
    sfor<OWN1, 8>(step);
    sfor<8, 56>([&](auto N) {
      constexpr int n = decltype(N)::v;
      asm volatile("s_waitcnt vmcnt(%1)" : "+v"(hA[n]) : "n"(SC.wC[n]) : "memory");
      if constexpr (n == 30) cnt_add(lb, C_P30);
      constexpr auto gate = [](int m) { return m == T_WO ? C_P30 : m == 55 ? C_WO : m == NWP - 1 ? C_WHI : m == 51 ? C_LATE : -1; };
      if constexpr (gate(n) >= 0) { if ((u32)__builtin_amdgcn_readfirstlane(pre) < 4u) cnt_wait(L, gate(n), 4); asm volatile("" ::: "memory"); }
      if constexpr (gate(n + 2) >= 0) pre = cnt_ld(L, gate(n + 2));
      if constexpr (n == T_WO + 12) cnt_add(lb, C_WO);
      if constexpr (n == 8) { if (!sigg) pubg(); }
      if constexpr (n == T_WH) cnt_add(lb, C_WHI);
      if constexpr (n == TB - 3) polled = ld8c_async(vFG, rCTL);
      if constexpr (n == TB) { PIN(polled); if (!tagged(polled, tag)) spin<SLP>(rCTL, vFG, tag); }
      step(N);
      if constexpr (n >= T_O && n < T_O + 8) { ldO(IC<56 + 2 * (n - T_O)>{}); ldO(IC<57 + 2 * (n - T_O)>{}); }
      if constexpr (n == T_O + 8) {
#pragma unroll
        for (int k = 0; k < 2; ++k) xres[k] = ld16_async<false>((r0 + 8 * k) * 896 + (l & 7) * 16, rX, (rb * 128 + tile * 32) * 896 + cc * 128);
        bres = ld16_async<false>((l & 7) * 16, mkrs(b2, 896), cc * 128);
      }
    });
    asm volatile("s_waitcnt vmcnt(0)" : "+v"(hA[56]), "+v"(hA[57]), "+v"(hA[58]), "+v"(hA[59]), "+v"(hA[60]), "+v"(hA[61]), "+v"(hA[62]), "+v"(hA[63]), "+v"(xres[0]), "+v"(xres[1]),
                 "+v"(hA[64]), "+v"(hA[65]), "+v"(hA[66]), "+v"(hA[67]), "+v"(hA[68]), "+v"(hA[69]), "+v"(hA[70]), "+v"(hA[71]), "+v"(bres)::"memory");
    const float rs2 = rs * rs;
    if (giver) {
#pragma unroll
      for (int k = 0; k < 4; ++k) {
        const f32x16& a = acc[k >> 1];
        const int o = 8 * (k & 1);
        st16<16>(u32x4{pk(a[o] * rs2, a[o + 1] * rs2), pk(a[o + 2] * rs2, a[o + 3] * rs2), pk(a[o + 4] * rs2, a[o + 5] * rs2), pk(a[o + 6] * rs2, a[o + 7] * rs2)}, rAC,
                 PX + ((rb * 7 + cc) * 4 + w) * 4096 + k * 1024 + l * 16);
      }
    }
    const u32 vP = PX + ((rb * 7 + cc) * 4 + tile) * 4096 + ((l & 7) >> 1) * 1024 + r0 * 16 + (l & 1) * 8;
    sfor<56, T_PP>(step);
#pragma unroll
    for (int k = 0; k < 4; ++k) pp[k] = ld8c_async(vP + (k >> 1) * 128 + (k & 1) * 512, rAC);
    pre = cnt_ld(L, C_ATT);
    sfor<T_PP, 72>(step);
    asm volatile("s_waitcnt vmcnt(0)" : "+v"(pp[0]), "+v"(pp[1]), "+v"(pp[2]), "+v"(pp[3])::"memory");
#pragma unroll 1
    for (u32 n = 0; n < (1u << 17) && !__all(top != ~0u); ++n) {
#pragma unroll 1
      for (; n < (1u << 17); ++n) {
        asm volatile("" ::: "memory");
        if (__all(__builtin_amdgcn_raw_buffer_load_b32(rAC, sO + (l & 7) * 2048 + 1024, 0, 16) != ~0u)) break;
        __builtin_amdgcn_s_sleep(SLP);
      }
#pragma unroll
      for (int k = 0; k < 16; ++k) hA[56 + k] = __builtin_amdgcn_raw_buffer_load_b128(rAC, vA + sO + k * 1024, 0, 16);
      rdC(56);
      sfor<56, 72>(step);
    }
    STAMP(10);
    if (!giver) { aco[0] += acc[0] * rs2; aco[1] += acc[1] * rs2; }
    if ((u32)__builtin_amdgcn_readfirstlane(pre) < 4u) cnt_wait(L, C_ATT, 4);
    asm volatile("" ::: "memory");
#pragma unroll
    for (int b = 0; b < 2; ++b)
#pragma unroll
      for (int k = 0; k < 4; ++k)
        lds_st<f32x4>(L, L_RED + w * 8192 + (l & 31) * 256 + (((b * 8 + k * 2 + (g >> 1)) ^ (l & 7)) << 4), f32x4{aco[b][4 * k], aco[b][4 * k + 1], aco[b][4 * k + 2], aco[b][4 * k + 3]});
    cnt_add(lb, C_PAIR0 + (w & 1));
#pragma unroll 1
    for (u32 n = 0; n < (1u << 17) && !__all(max(max(max(pp[0][0], pp[0][1]), max(pp[1][0], pp[1][1])), max(max(pp[2][0], pp[2][1]), max(pp[3][0], pp[3][1]))) != ~0u); ++n) {
      asm volatile("" ::: "memory");
#pragma unroll
      for (int k = 0; k < 4; ++k) pp[k] = __builtin_amdgcn_raw_buffer_load_b64(rAC, vP + (k >> 1) * 128 + (k & 1) * 512, 0, 16);
    }
    f32x4 ra[2], rc[2];
#pragma unroll
    for (int k = 0; k < 2; ++k) {
      ra[k] = f32x4{bflo(xres[k][0]) + bflo(bres[0]), bfhi(xres[k][0]) + bfhi(bres[0]), bflo(xres[k][1]) + bflo(bres[1]), bfhi(xres[k][1]) + bfhi(bres[1])} +
              f32x4{bflo(pp[2 * k][0]), bfhi(pp[2 * k][0]), bflo(pp[2 * k][1]), bfhi(pp[2 * k][1])};
      rc[k] = f32x4{bflo(xres[k][2]) + bflo(bres[2]), bfhi(xres[k][2]) + bfhi(bres[2]), bflo(xres[k][3]) + bflo(bres[3]), bfhi(xres[k][3]) + bfhi(bres[3])} +
              f32x4{bflo(pp[2 * k + 1][0]), bfhi(pp[2 * k + 1][0]), bflo(pp[2 * k + 1][1]), bfhi(pp[2 * k + 1][1])};
      PIN(ra[k]); PIN(rc[k]);
    }
    cnt_wait(L, C_PAIR0 + (w & 1), 2);
    const rsrc_t rO = mkrs(out, 2048 * 896);
#pragma unroll
    for (int k = 0; k < 2; ++k) {
      const int row = r0 + 8 * k;
      f32x4 a = ra[k], b = rc[k];
#pragma unroll
      for (int j = 0; j < 2; ++j) {
        a += lds_ld<f32x4>(L, L_RED + ((w & 1) + 2 * j) * 8192 + row * 256 + (((2 * (l & 7)) ^ (row & 7)) << 4));
        b += lds_ld<f32x4>(L, L_RED + ((w & 1) + 2 * j) * 8192 + row * 256 + (((2 * (l & 7) + 1) ^ (row & 7)) << 4));
      }
      st16<16>(u32x4{pk(a[0], a[1]), pk(a[2], a[3]), pk(b[0], b[1]), pk(b[2], b[3])}, rO, (rb * 128 + tile * 32 + row) * 896 + cc * 128 + (l & 7) * 16);
    }
    if (w == 0) STAMP(11);
    FLUSH();
    return;
  }

  const int li = w & 1;
  auto clear_o = [&] {
#pragma unroll
    for (int k = 0; k < 8; ++k) st16<16>(u32x4{~0u, ~0u, ~0u, ~0u}, rAC, ((ai * 4 + (k >> 1)) * 144 + 112 + 2 * ah + (k & 1)) * 1024 + l * 16);
  };
  if (!busy) {
    const int blk = (rk - 28) * 8 + x8;
#pragma unroll
    for (int k = 0; k < 14; ++k) d[k] = __builtin_amdgcn_raw_buffer_load_b128(mkrs(w1, 448 * 3584), (112 * (w - 4) + (l >> 3)) * 3584 + (l & 7) * 16 + (blk < 28 ? blk * 128 : 0x7fffffff), k * 8 * 3584, 0);
    const u32 bias = __builtin_amdgcn_raw_buffer_load_b16(mkrs(b1, 3584), (blk * 64 + l) * 2, 0, 0);
    SB();
    epoch();
    if (w == 6 && l < C_N) lds_st<u32>(L, L_CNT + l * 4, l < 4 ? 64u : 0u);
    wait_bar<63>();
    float a[8] = {};
#pragma unroll
    for (int k = 0; k < 14; ++k)
#pragma unroll
      for (int j = 0; j < 4; ++j) { a[2 * j] += bflo(d[k][j]); a[2 * j + 1] += bfhi(d[k][j]); }
    lds_st<f32x4>(L, RING + ((w - 4) * 64 + l) * 32, f32x4{a[0], a[1], a[2], a[3]});
    lds_st<f32x4>(L, RING + ((w - 4) * 64 + l) * 32 + 16, f32x4{a[4], a[5], a[6], a[7]});
    if (last_of4(L, C_CS0) && blk < 28) {
      float t = 0.f;
#pragma unroll
      for (int k = 0; k < 32; ++k) t += lds_ld<float>(L, RING + k * 256 + l * 4);
      const float hi = rbf(t);
      __builtin_amdgcn_raw_buffer_store_b64(u32x2{pk(hi, t - hi), bias}, rCTL, WS_CS + (blk * 64 + l) * 8, 0, 16);
      vm<0>();
      if (l == 0) st_flag(rCTL, WS_FC + blk * FS, tag);
    }
    if (w == 7) { clear_o(); vm<0>(); if (l == 0) st_flag(rCTL, WS_FZ + (ai * 16 + ah) * FS, tag); }
  } else if (w < 6) {
    __builtin_amdgcn_s_setprio(PRIO_L);
    const rsrc_t rW1 = mkrs(w1, 448 * 3584), rWq = mkrs(wqkv, 448 * 2688);
    u32 vX[4], vH[4], vQ[3], vC[2], so7[7];
#pragma unroll
    for (int j = 0; j < 4; ++j) vX[j] = (8 * j + (l >> 3)) * 896 + (((l & 7) ^ ((j & 1) * 4 + g)) << 4) - 1024 * j;
    so7[0] = 0;
    const u32 sX = (rb * 128 + 64 * li) * 896, sH = cb * 256 + 32 * li * 3584, sQ = cb * 192 + 16 * li * 2688;
    auto issue = [&](auto O) {
      constexpr int op = decltype(O)::v, k = op / 256, a = (op % 256) / 2, e = op & 1;
      if constexpr (k == K_X) dman<4>(lb + (a * 16 + 8 * li + 4 * e) * 1024, vX, rX, sX + so7[a] * 128 + e * 28672);
      if constexpr (k == K_WA && a < 14) dman<3>(lb + stage_addr(a) + li * 3072, vQ, rWq, sQ + so7[a >> 1] * (64 * 2688) + (a & 1) * (32 * 2688));
      if constexpr (k == K_WA && a >= 14) dman<4>(lb + stage_addr(a) + li * 4096, vH, rW1, sH + so7[(a - 14) % 7] * (64 * 3584) + (a >= 21 ? 128 : 0));
      if constexpr (k == K_WCP) dman<2>(lb + L_WC + (2 * a + li) * 2048, vC, rW2, (kstep(2 * a) + li) * (16 * 896) + cc * 128);
    };
    lanes();
#pragma unroll
    for (int j = 0; j < 3; ++j) { const u32 n = 64 * j + l; vQ[j] = (n / 12) * 2688 + (n % 12) * 16 - 1024 * j; }
#pragma unroll
    for (int j = 0; j < 4; ++j) vH[j] = (8 * j + (l >> 3)) * 3584 + (((l & 7) ^ ((g & 1) << 2)) << 4) - 1024 * j;
#pragma unroll
    for (int j = 1; j < 7; ++j) so7[j] = rotl(j);
    sfor<4, NPRE>([&](auto K) { issue(IC<SC.pro[decltype(K)::v]>{}); });
    vW2(vC);
    vm_bar<SC.wA[0]>();
    sfor<NPRE, NPRO>([&](auto K) { issue(IC<SC.pro[decltype(K)::v]>{}); });
    sfor<0, 15>([&](auto SS) {
      constexpr int s = decltype(SS)::v;
      vm_bar<SC.wA[s + 1]>();
      sfor<0, (s == 14 ? 2 : MAXO)>([&](auto K) { issue(IC<SC.opA[s][decltype(K)::v]>{}); });
    });
    vm<SC.wL[0]>();
    prog_st(L, C_LAND + li, 1);
    vm<0>();
    prog_st(L, C_LAND + li, 2);
    cnt_wait(L, C_FQ, HOLD);
    sfor<2, MAXO>([&](auto K) { issue(IC<SC.opA[14][decltype(K)::v]>{}); });
    vm<SC.wL[1]>();
    prog_st(L, C_LAND + li, 3);
    epoch();
    prog_wait<true>(L, 20);
    issue(IC<OP(K_WA, 26)>{}); issue(IC<OP(K_WA, 27)>{});
    vm<0>();
    prog_st(L, C_LAND + li, 4);
    cnt_add(lb, C_WLO);
    if (w == 4) { STAMP(13); cnt_wait(L, C_FA, 5); STAMP(14); if (l == 0) st_flag(rCTL, WS_FH + (rb * 16 + cb) * FS, tag); }
    else { cnt_wait(L, C_FA, 5); spin<SLP>(rCTL, l >= 16 && l < 32 ? WS_FZ + (rb * 16 + l - 16) * FS : WS_FH + (rb * 16 + (l < 7 ? 7 * kh + l : 7 * (1 - kh) + cc)) * FS, tag); STAMP(15); cnt_add(lb, C_FH); }
  } else {
    if (stat) {
      epoch();
      bar();
      asm volatile("s_waitcnt vmcnt(0)" : "+v"(d[0]), "+v"(d[1]), "+v"(d[2]), "+v"(d[3]), "+v"(d[4]), "+v"(d[5]), "+v"(d[6])::"memory");
      float s1 = 0.f, s2 = 0.f;
#pragma unroll
      for (int r = 0; r < 2; ++r) {
        bar();
#pragma unroll
        for (int k = 14 * r; k < 14 * r + 14; ++k) { const u32 v = d[k >> 2][k & 3]; s1 = dot2(v, 0x3f803f80u, s1); s2 = dot2(v, v, s2); }
        PIN(s1); PIN(s2);
        SB();
      }
      bar();
      const float mean = sum8(s1) * (1.f / 448.f);
      const float var = fmaxf(sum8(s2) * (1.f / 448.f) - mean * mean, 0.f);
      const float sd = rbf(__builtin_amdgcn_sqrtf(rbf(rbf(var) + 1e-5f)));
      if ((l & 7) == 0) st16<16>(u32x4{pk(-mean, sd), (u32)tag, pk(-mean, sd), (u32)(tag >> 32)}, rCTL, WS_ST + srow * 16);
      bar();
    } else { bar(); bar(); epoch(); bar(); bar(); bar(); }
    if (w == 6) {
      const u32 fp = WS_FC + (2 * cb + ((l >> 1) & 1)) * FS + (l & 1) * 4;
      int ss = 0, cs = 0;
      auto look = [&] {
        vm<0>();
        if (ss == 1) {
          const u32x4 a = lds_ld<u32x4>(L, L_ST + l * 16), b = lds_ld<u32x4>(L, L_ST + 1024 + l * 16);
          if (__all(a[1] == (u32)tag && a[3] == (u32)(tag >> 32) && b[1] == (u32)tag && b[3] == (u32)(tag >> 32))) { cnt_add(lb, C_ST); ss = 3; STAMP(33); }
        }
        if (ss < 3) {
          asm volatile("s_mov_b32 m0, %0\n\ts_nop 0\n\tbuffer_load_dwordx4 %1, %2, 0 offen sc1 lds\n\tbuffer_load_dwordx4 %1, %2, 0 offen offset:1024 sc1 lds" ::"s"(lb + L_ST), "v"(l * 16 + WS_ST + rb * 2048), "s"(rCTL) : "memory");
          ss = 1;
        }
        if (cs == 2) { cnt_add(lb, C_CSH); cs = 3; STAMP(37); }
        if (cs == 1 && __all(lds_ld<u32>(L, L_POLL + l * 4) == (u32)(l & 1 ? tag >> 32 : tag))) {
          asm volatile("s_mov_b32 m0, %0\n\ts_nop 0\n\tbuffer_load_dwordx4 %1, %2, 0 offen sc1 lds" ::"s"(lb + L_CSH), "v"(l * 16 + WS_CS + cb * 1024), "s"(rCTL) : "memory");
          cs = 2;
        }
        if (cs < 2) { asm volatile("s_mov_b32 m0, %0\n\ts_nop 0\n\tbuffer_load_dword %1, %2, 0 offen sc1 lds" ::"s"(lb + L_POLL), "v"(fp), "s"(rCTL) : "memory"); cs = 1; }
      };
#pragma unroll 1
      for (int s = 4; s <= 14; ++s) {
        wait_bar<63>();
        if (s == 6 && l < 30) lds_st<u32>(L, L_CNT + l * 4, l < 28 ? 0u : (u32)(l & 1 ? tag >> 32 : tag));
        if (s >= 7 && ss + cs != 6 && vm_le<0>()) look();
      }
#pragma unroll 1
      for (u32 n = 0; n < (1u << 17) && ss + cs != 6; ++n) look();
    } else {
      vm<0>();
      int k = 0;
      auto clear = [&] {
        st16<16>(u32x4{~0u, ~0u, ~0u, ~0u}, rAC, ((ai * 4 + (k >> 1)) * 144 + 112 + 2 * ah + (k & 1)) * 1024 + l * 16);
        st16<16>(u32x4{~0u, ~0u, ~0u, ~0u}, rAC, PX + ((rb * 7 + cc) * 4 + 2 * kh) * 4096 + k * 1024 + l * 16);
        ++k;
      };
#pragma unroll 1
      for (int s = 4; s <= 14; ++s) {
        bar();
        if (s >= CLR0 && k < 8 && vm_le<CLRQ>()) clear();
      }
      if (k < 8) cnt_wait(L, C_FQ, 4);
#pragma unroll 1
      while (k < 8) clear();
      vm<0>();
      cnt_add(lb, C_FA);
      if (l == 0) st_flag(rCTL, WS_FZ + (ai * 16 + ah) * FS, tag);
    }
  }
  lanes();
  const int aw = w - 4, hh = g >> 1;
  if (w >= 6) {
    const int e = l & 1, k = l < 14 ? l >> 1 : 0;
    const int ty = k == 0 ? 0 : (k < 4 ? 1 : 2), ro = k == 0 ? 0 : (k < 4 ? k - 1 : k - 4);
    const int rr = ai - ro < 0 ? 0 : ai - ro;
    if (!busy) { for (int k = 0; k < IDLE_SLP; ++k) __builtin_amdgcn_s_sleep(15); }
    if (busy) { cnt_wait(L, C_FQ, 4); if (w == 6) STAMP(12); if (w == 6 && l == 0) st_flag(rCTL, WS_FQ + (rb * 16 + cb) * FS, tag); }
    prog_wait<true>(L, 18);
#pragma unroll
    for (int k = 0; k < 3; ++k) lds_st<u32x2>(L, L_V + (6 * li + 2 * k + (l >> 5)) * 2048 + 1792 + (l & 31) * 8, u32x2{0x3f80u, 0u});
    if (w == 6) { spin<SLP>(rCTL, WS_FQ + (rr * 16 + (ty * 448 + 28 * ah + 27 * e) / 96) * FS, tag); cnt_add(lb, C_SQ); }
    else cnt_wait(L, C_SQ, 1);
    if (w == 6) STAMP(7);
#pragma unroll
    for (int e = 0; e < 2; ++e) {
#pragma unroll
      for (int j = 0; j < 6; ++j) {
        const int b = 2 * j + li, tb = 4 * ai - 8 + b < 0 ? 0 : 4 * ai - 8 + b;
        dma1792c(lb + (e ? L_V + b * 2048 : L_K + b * 1792), l * 16, rQKV, (tb * 336 + 112 + 112 * e + 7 * ah) * 256);
      }
      if (e == 0) {
#pragma unroll
        for (int j = 0; j < 2; ++j) dma1792c(lb + L_Q + (2 * li + j) * 2048, l * 16, rQKV, ((4 * ai + 2 * li + j) * 336 + 7 * ah) * 256);
        if (VLATE == 1) { vm<0>(); cnt_add(lb, C_KV); }
        if (VLATE > 1) vm<VLATE>();
      }
    }
    if (VLATE != 1) { vm<12>(); cnt_add(lb, C_KV); }
  }
  u32 oK = L_K + aw * 1792 + hh * 256 + l15 * 16 + (g & 1) * 8, oK3 = hh ? oK + 1280 : oK + 1536;
  u32 oQv = L_Q + aw * 2048 + hh * 256 + l15 * 16 + (g & 1) * 8;
  const int F = 7 * ah + 4 * (g & 1) + p4;
  u32 oV[2][2];
#pragma unroll
  for (int t = 0; t < 2; ++t)
#pragma unroll
    for (int e = 0; e < 2; ++e) { oV[t][e] = L_V + aw * 2048 + (4 * (g & 1) + p4) * 256 + (((8 * e + 4 * hh + q4) ^ ((F & 6) << 1)) << 4) + ((t ^ (F & 1)) << 3); PIN(oV[t][e]); }
  const int dq = (l & 31) - 4 * hh;
  u32 mk[8], vW[2];
#pragma unroll
  for (int r = 0; r < 16; r += 2) { mk[r >> 1] = (r & 3) + 1 + 8 * (r >> 2) <= dq ? 0xffffffffu : ((r & 3) + 8 * (r >> 2) <= dq ? 0xffffu : 0u); PIN(mk[r >> 1]); }
  vW2(vW);
  PIN(oK); PIN(oK3); PIN(oQv); PIN(vW[0]); PIN(vW[1]);
  cnt_wait(L, C_KV, 2);
  __builtin_amdgcn_s_setprio(PRIO_B);
  if (w == 4) STAMP(8);
  u32x2 qv[4];
#pragma unroll
  for (int k = 0; k < 4; ++k) qv[k] = lds_ld<u32x2>(L, oQv + k * 512);
  f32x16 sc[9];
  bf16x8 kf[9][2], vf[9][2];
  auto rdK = [&](int m) {
    const u32x2 a = lds_ld<u32x2>(L, oK + m * 1792), b = lds_ld<u32x2>(L, oK + m * 1792 + 512), c = lds_ld<u32x2>(L, oK + m * 1792 + 1024), d = lds_ld<u32x2>(L, oK3 + m * 1792);
    kf[m][0] = __builtin_bit_cast(bf16x8, u32x4{a[0], a[1], b[0], b[1]}); kf[m][1] = __builtin_bit_cast(bf16x8, u32x4{c[0], c[1], d[0], d[1]});
  };
  auto rdV = [&](int m) { vf[m][0] = trfrag(L, oV[0][0] + m * 2048, oV[0][1] + m * 2048); vf[m][1] = trfrag(L, oV[1][0] + m * 2048, oV[1][1] + m * 2048); };
  SB(); rdK(0); SB(); rdK(8); SB(); rdK(1); SB();
  if (hh) qv[3] = u32x2{0u, 0u};
  const bf16x8 qf[2] = {__builtin_bit_cast(bf16x8, u32x4{qv[0][0], qv[0][1], qv[1][0], qv[1][1]}), __builtin_bit_cast(bf16x8, u32x4{qv[2][0], qv[2][1], qv[3][0], qv[3][1]})};
  constexpr float NEG = -1e30f;
  const int m0 = 8 - 4 * ai - aw;
  float mx4[4];
  auto rowmax = [&](int m, bool first) {
#pragma unroll
    for (int k = 0; k < 4; ++k) {
      mx4[k] = first ? fmx(sc[m][4 * k], sc[m][4 * k + 1]) : fmx(fmx(mx4[k], sc[m][4 * k]), sc[m][4 * k + 1]);
      mx4[k] = fmx(fmx(mx4[k], sc[m][4 * k + 2]), sc[m][4 * k + 3]);
    }
    PIN(mx4[0]); PIN(mx4[1]); PIN(mx4[2]); PIN(mx4[3]);
  };
#pragma unroll
  for (int i = 0; i < 9; ++i) {
    const int m = i == 0 ? 0 : (i == 1 ? 8 : i - 1);
    if (i + 2 < 8) rdK(i + 2);
    sc[m] = mfma32(kf[m][0], qf[0], f32x16{});
    if (i == 3) {
      if (m0 > 0) {
        asm volatile("" ::: "memory");
#pragma unroll
        for (int r = 0; r < 16; ++r) sc[0][r] = NEG;
      }
#pragma unroll
      for (int r = 0; r < 16; ++r) sc[0][r] = (r & 3) + 8 * (r >> 2) <= dq ? sc[8][r] : sc[0][r];
    }
    if (i == 4) rowmax(0, true);
    sc[m] = mfma32(kf[m][1], qf[1], sc[m]);
    if (i >= 4 && i - 3 >= m0) rowmax(i - 3, false);
    SB();
  }
  if (w >= 6) { vm<0>(); cnt_add(lb, C_V); }
  if (busy) dman<2>(lb + L_Q + aw * 2048, vW, rW2, (kstep(52) + aw) * (16 * 896) + cc * 128);
  if (6 >= m0) rowmax(6, false);
  cnt_wait(L, C_V, 2);
  rdV(1); rdV(2);
  SB();
  if (7 >= m0) rowmax(7, false);
  const float mx0 = fmx(fmx(mx4[0], mx4[1]), fmx(mx4[2], mx4[3]));
  const auto ms = __builtin_amdgcn_permlane32_swap(__float_as_uint(mx0), __float_as_uint(mx0), false, false);
  const float mx = fmx(__uint_as_float(ms[0]), __uint_as_float(ms[1]));
  SB();
  f32x16 o;
  u32x4 pq[2];
#pragma unroll
  for (int i = 0; i < 16; ++i) {
    const int m = ((i >> 1) + 1) & 7, s = i & 1;
    if (!s) { if (i < 10) rdV((i >> 1) + 3); else if (i == 10) rdV(0); else if (i == 12) rdV(8); }
    if (i == 6) { vm<0>(); cnt_add(lb, C_LATE); }
    const float mm = m != 0 && m < m0 ? -NEG : mx;
    f32x2 d[4];
#pragma unroll
    for (int j = 0; j < 4; ++j) { d[j] = f32x2{sc[m][8 * s + 2 * j], sc[m][8 * s + 2 * j + 1]} - f32x2{mm, mm}; PIN(d[j]); }
#pragma unroll
    for (int j = 0; j < 2; ++j) { d[j][0] = __builtin_amdgcn_exp2f(d[j][0]); d[j][1] = __builtin_amdgcn_exp2f(d[j][1]); PIN(d[j]); }
    SB();
    if (i >= 1 && i < 15) o = mfma32(vf[((i - 1) >> 1) + 1][s ^ 1], __builtin_bit_cast(bf16x8, pq[s ^ 1]), i == 1 ? f32x16{} : o);
    if (i == 15) {
      const u32x4 k4 = {mk[0], mk[1], mk[2], mk[3]};
      o = mfma32(vf[0][0], __builtin_bit_cast(bf16x8, pq[0] & ~k4), o);
      o = mfma32(vf[8][0], __builtin_bit_cast(bf16x8, pq[0] & k4), o);
    }
    SB();
#pragma unroll
    for (int j = 2; j < 4; ++j) { d[j][0] = __builtin_amdgcn_exp2f(d[j][0]); d[j][1] = __builtin_amdgcn_exp2f(d[j][1]); }
#pragma unroll
    for (int j = 0; j < 4; ++j) pq[s][j] = pk(d[j][0], d[j][1]);
    PIN(pq[s]);
    SB();
  }
  {
    const u32x4 k4 = {mk[4], mk[5], mk[6], mk[7]};
    o = mfma32(vf[0][1], __builtin_bit_cast(bf16x8, pq[1] & ~k4), o);
    o = mfma32(vf[8][1], __builtin_bit_cast(bf16x8, pq[1] & k4), o);
  }
  SB();
  cnt_add(lb, C_ATT);
  const auto dn = __builtin_amdgcn_permlane32_swap(__float_as_uint(o[12]), __float_as_uint(o[12]), false, false);
  const float inv = __builtin_amdgcn_rcpf(__uint_as_float(dn[1]));
  const float in2 = hh ? 0.f : inv;
  const u32 so = ((ai * 4 + aw) * 144 + 112 + 2 * ah) * 1024;
  st16<16>(u32x4{pk(o[0] * inv, o[1] * inv), pk(o[2] * inv, o[3] * inv), pk(o[4] * inv, o[5] * inv), pk(o[6] * inv, o[7] * inv)}, rAC, l * 16 + so);
  st16<16>(u32x4{pk(o[8] * inv, o[9] * inv), pk(o[10] * inv, o[11] * inv), pk(o[12] * in2, o[13] * in2), pk(o[14] * in2, o[15] * in2)}, rAC, l * 16 + so + 1024);
  if (w == 4) STAMP(9);
  vm<0>();
  if (last_of4(L, C_FB)) {
    if (l == 0) st_flag(rCTL, WS_FB + (ai * 16 + ah) * FS, tag);
    if (blockIdx.x == 255) {
#pragma unroll 1
      for (int k = 0; k < 4; ++k) spin<32>(rCTL, WS_FB + (k * 64 + l) * FS, tag);
      if (l == 0) st_flag(rCTL, 0, tag);
    }
  }
  FLUSH();
}

extern "C" void kernel_launch(void* const* d_in, const int* in_sizes, int n_in, void* d_out, int out_size, void* d_ws, size_t ws_size, hipStream_t stream) {
  fused_block<<<256, 512, 0, stream>>>((const char*)d_in[0], (const char*)d_in[1], (const char*)d_in[2], (const char*)d_in[4], (const char*)d_in[5],
                                       (const char*)d_in[6], (const char*)d_in[7], (char*)d_out, (char*)d_ws + WS_OFF);
}
```

```cpp
#define OWN1 0
#include <hip/hip_runtime.h>
#include <hip/hip_bf16.h>
#include <stdint.h>

#define DI __device__ __forceinline__
#define LAS __attribute__((address_space(3)))
typedef unsigned u32;
typedef unsigned long long u64;
typedef __bf16 bf16x8 __attribute__((ext_vector_type(8)));
typedef __bf16 bf16x2 __attribute__((ext_vector_type(2)));
typedef short s16x4 __attribute__((ext_vector_type(4)));
typedef float f32x4 __attribute__((ext_vector_type(4)));
typedef float f32x2 __attribute__((ext_vector_type(2)));
typedef float f32x16 __attribute__((ext_vector_type(16)));
typedef unsigned u32x4 __attribute__((ext_vector_type(4)));
typedef unsigned u32x2 __attribute__((ext_vector_type(2)));
typedef __amdgpu_buffer_rsrc_t rsrc_t;

template <int N> struct IC { static constexpr int v = N; };
template <int B, int E, class F> DI void sfor(F&& f) { if constexpr (B < E) { f(IC<B>{}); sfor<B + 1, E>(f); } }

constexpr u32 FS = 128;
constexpr u32 WS_FH = FS, WS_FQ = WS_FH + 256 * FS, WS_FB = WS_FQ + 256 * FS, WS_FG = WS_FB + 256 * FS, WS_FP = WS_FG + 256 * FS, WS_FC = WS_FP + 448 * FS;
constexpr u32 WS_FS = WS_FC + 32 * FS, WS_FZ = WS_FS + 128 * FS, WS_CS = WS_FZ + 287 * FS, WS_ST = WS_CS + 16384, CTL_BYTES = WS_ST + 32768;
constexpr u32 WS_QKV = CTL_BYTES, QKV_BYTES = 64u * 336u * 256u;
constexpr u32 WS_AC = WS_QKV + QKV_BYTES, AC_BYTES = 64u * 144u * 1024u + 448u * 4096u;
constexpr u32 PX = 64u * 144u * 1024u;
constexpr u32 RING = 7 * 16384;
constexpr u32 L_ST = 40960, L_POLL = 45056, L_CSQ = 46080, L_CSH = 46848, L_CNT = 48896;
constexpr u32 L_K = 0, L_V = 21504, L_Q = RING + 5 * 8192;
constexpr u32 L_WC = 49152, L_RED = 0;
enum { C_DONE, C_LAND = 4, C_FQ = 6, C_FA, C_FG, C_KV, C_FB, C_WLO, C_WHI, C_P30, C_WO, C_ATT, C_LATE, C_PAIR0, C_PAIR1, C_CSH, C_FH, C_SQ, C_V, C_ST, C_CS0, C_N };
constexpr u32 stage_addr(int s) {
  return s < 14 ? RING + (s % 6) * 6144u : s < 19 ? (s - 14) * 8192u : s < 21 ? RING + (s - 19) * 8192u : s < 23 ? 6 * 16384u + (s - 21) * 8192u : s < 26 ? RING + (s - 21) * 8192u : RING + (s - 26) * 8192u;
}
constexpr int NWP = 24;

enum { K_NONE = 0, K_X, K_WA, K_BIAS, K_WCP };
constexpr int OP(int k, int a, int e = 0) { return k * 256 + 2 * a + e; }
constexpr int opn(int op) { int k = op / 256, a = (op % 256) / 2; return k == K_NONE ? 0 : k == K_X ? 4 : k == K_WA ? (a < 14 ? 3 : 4) : k == K_WCP ? 2 : 1; }
#define CAP_1 8
#define P_NPRO 8
#define P_DA 12
#define P_TB 16
#define P_TO 44
#define P_FQ 18
#define PRIO_C 0
#define PRIO_B 3
#define ROTA 0
#define WS_OFF 0
#define CODE_KB 40
#define PF_DELAY 8
#define P_TFP 66
#define SLP 2
#define LSLP 1
#define GATE_K 0
#define CLR0 4
#define TOUCH 1
#define HNOW 2
#define CLRQ 0
#define HOLD 4
#define IDLE_SLP 9
#define T_PP 62
#define P_PF 4
#define WCP_LATE 0
#define TLBW 0
#define WCP_EARLY 0
#define HA_EARLY 19
#define PRIO_L 0
#define PRIO_A 0
#define PFD 10
#define PF1 0
#define VLATE 1
#define QNOW 6
#define P_NPRE 6
constexpr int NPRE = P_NPRE;
constexpr int MAXO = 24, NPRO = P_NPRO, DA = P_DA, TB = P_TB, S_FQ = P_FQ, T_WH = 20, T_WO = 33, T_O = P_TO;
struct Sched {
  int pro[NPRO], opA[15][MAXO];
  int wA[16];
  int wL[2];
  int wC[56], a0[56], a1[56];
  int ok;
};
constexpr int cl(int n) { return n > 63 ? 63 : n; }
constexpr Sched make_sched() {
  Sched r{};
  r.ok = 1;
  int n = 0, iW[26] = {}, iX[7] = {};
  int lo[128] = {}, le[128] = {}, nl = 0;
  for (int s = 0; s < 26; ++s) {
    if (s < 14 && !(s & 1)) { lo[nl] = OP(K_X, s >> 1, 0); le[nl++] = -1; lo[nl] = OP(K_X, s >> 1, 1); le[nl++] = -1; }
    lo[nl] = OP(K_WA, s); le[nl++] = s < 14 ? s - 5 : (s < 19 ? 5 : (s == 21 || s == 22 ? 13 : 14));
  }
  for (int k = 0; k < NWP / 2; ++k) { lo[nl] = OP(K_WCP, k); le[nl++] = k < WCP_EARLY ? 7 + 2 * (k >> 2) : 14; }
  bool done[128] = {};
  auto note = [&](int op) { n += opn(op); int k = op / 256, a = (op % 256) / 2; if (k == K_X) iX[a] = (op & 1) ? n : 0; if (k == K_WA) iW[a] = n; };
  for (int k = 0; k < NPRO; ++k) { if (k == NPRE) r.wA[0] = cl(n - iW[0]); r.pro[k] = lo[k]; done[k] = true; note(lo[k]); }
  for (int s = 0; s < 15; ++s) {
    int need = iW[s + 1]; if (!need) r.ok = 0;
    if (s + 1 <= 13) { int v = iX[(s + 1) >> 1]; if (!v) r.ok = 0; if (v > need) need = v; }
    r.wA[s + 1] = cl(n - need);
    int cap = s == 14 ? 64 : CAP_1, c = 0, used = 0;
    for (int k = 0; k < nl; ++k) if (!done[k] && le[k] <= s && used + opn(lo[k]) <= cap && c < MAXO) { done[k] = true; r.opA[s][c++] = lo[k]; used += opn(lo[k]); note(lo[k]); if (s == 14 && c == 2) r.wL[0] = cl(n - iW[18]); }
  }
  r.wL[1] = cl(n - iW[25]);
  if (r.opA[14][0] != OP(K_WA, 19)) r.ok = 0;
  for (int k = 0; k < nl; ++k) if (!done[k]) r.ok = 0;
  n = 0;
  int iA[56] = {}, iP = 0, iH = 0, iO = 0, nA = 8 + DA;
  for (int m = 8; m < 8 + DA; ++m) { n += 1; iA[m] = n; }
  for (int s = 8; s < 56; ++s) {
    int need = iA[s]; if (!need) r.ok = 0;
    if (s == TB && iP > need) need = iP;
    if (s == T_WH && iH > need) need = iH;
    if (s == T_WO + 12 && iO > need) need = iO;
    r.wC[s] = cl(n - need);
    if (s == TB - 3) { n += 1; iP = n; }
    r.a0[s] = nA;
    for (int c = 0; c < 2 && nA < 56 && nA <= s + DA && (nA < 32 || s >= TB); ++c) { n += 1; iA[nA++] = n; }
    r.a1[s] = nA;
    if (s < 15) { n += 2; iH = n; }
    if (s >= T_WO && s < T_WO + 7) { n += 2; iO = n; }
    if (s >= T_O && s < T_O + 8) n += 2;
    if (s == T_O + 8) n += 3;
  }
  return r;
}
constexpr Sched SC = make_sched();
static_assert(SC.ok, "schedule");

DI rsrc_t mkrs(const void* p, u32 bytes) { return __builtin_amdgcn_make_buffer_rsrc((void*)p, 0, bytes, 0x00020000); }
template <int N> DI void dman(u32 lds, const u32* v, rsrc_t rs, u32 soff) {
  if constexpr (N == 4) asm volatile("s_mov_b32 m0, %0\n\ts_nop 0\n\tbuffer_load_dwordx4 %1, %5, %6 offen lds\n\tbuffer_load_dwordx4 %2, %5, %6 offen offset:1024 lds\n\t"
    "buffer_load_dwordx4 %3, %5, %6 offen offset:2048 lds\n\tbuffer_load_dwordx4 %4, %5, %6 offen offset:3072 lds" ::"s"(lds), "v"(v[0]), "v"(v[1]), "v"(v[2]), "v"(v[3]), "s"(rs), "s"(soff) : "memory");
  if constexpr (N == 3) asm volatile("s_mov_b32 m0, %0\n\ts_nop 0\n\tbuffer_load_dwordx4 %1, %4, %5 offen lds\n\tbuffer_load_dwordx4 %2, %4, %5 offen offset:1024 lds\n\t"
    "buffer_load_dwordx4 %3, %4, %5 offen offset:2048 lds" ::"s"(lds), "v"(v[0]), "v"(v[1]), "v"(v[2]), "s"(rs), "s"(soff) : "memory");
  if constexpr (N == 2) asm volatile("s_mov_b32 m0, %0\n\ts_nop 0\n\tbuffer_load_dwordx4 %1, %3, %4 offen lds\n\tbuffer_load_dwordx4 %2, %3, %4 offen offset:1024 lds" ::"s"(lds), "v"(v[0]), "v"(v[1]), "s"(rs), "s"(soff) : "memory");
  if constexpr (N == 1) asm volatile("s_mov_b32 m0, %0\n\ts_nop 0\n\tbuffer_load_dwordx4 %1, %2, %3 offen lds" ::"s"(lds), "v"(v[0]), "s"(rs), "s"(soff) : "memory");
}
DI void dma1792c(u32 lds, u32 voff, rsrc_t rs, u32 soff) {
  asm volatile("s_mov_b32 m0, %0\n\ts_nop 0\n\tbuffer_load_dwordx4 %1, %2, %3 offen sc1 lds\n\ts_mov_b32 exec_hi, 0xffff\n\t"
               "buffer_load_dwordx4 %1, %2, %3 offen offset:1024 sc1 lds\n\ts_mov_b32 exec_hi, -1" ::"s"(lds), "v"(voff), "s"(rs), "s"(soff) : "memory");
}
DI void dma4(u32 lds, u32 voff, rsrc_t rs, u32 soff) {
  asm volatile("s_mov_b32 m0, %0\n\ts_nop 0\n\tbuffer_load_dword %1, %2, %3 offen lds" ::"s"(lds), "v"(voff), "s"(rs), "s"(soff) : "memory");
}
template <bool C, int IMM = 0> DI u32x4 ld16_async(u32 voff, rsrc_t rs, u32 soff) {
  u32x4 r;
  if constexpr (C) asm volatile("buffer_load_dwordx4 %0, %1, %2, %3 offen offset:%4 sc1" : "=v"(r) : "v"(voff), "s"(rs), "s"(soff), "n"(IMM) : "memory");
  else asm volatile("buffer_load_dwordx4 %0, %1, %2, %3 offen" : "=v"(r) : "v"(voff), "s"(rs), "s"(soff) : "memory");
  return r;
}
DI u32x2 ld8c_async(u32 voff, rsrc_t rs) {
  u32x2 r; asm volatile("buffer_load_dwordx2 %0, %1, %2, 0 offen sc1" : "=v"(r) : "v"(voff), "s"(rs) : "memory"); return r;
}
template <int N> DI void wait_bar() { asm volatile("s_waitcnt vmcnt(%0) lgkmcnt(0)\n\ts_barrier" ::"n"(N) : "memory"); }
template <int N> DI void vm_bar() { asm volatile("s_waitcnt vmcnt(%0)\n\ts_barrier" ::"n"(N) : "memory"); }
template <int N> DI void vm() { asm volatile("s_waitcnt vmcnt(%0)" ::"n"(N) : "memory"); }
DI void bar() { asm volatile("s_barrier" ::: "memory"); }
#define PIN(x) asm volatile("" : "+v"(x))
#define SB() __builtin_amdgcn_sched_barrier(0)
DI f32x4 mfma(bf16x8 a, bf16x8 b, f32x4 c) { return __builtin_amdgcn_mfma_f32_16x16x32_bf16(a, b, c, 0, 0, 0); }
DI f32x16 mfma32(bf16x8 a, bf16x8 b, f32x16 c) { return __builtin_amdgcn_mfma_f32_32x32x16_bf16(a, b, c, 0, 0, 0); }
DI u32 pk(float a, float b) { bf16x2 v = {(__bf16)a, (__bf16)b}; return __builtin_bit_cast(u32, v); }
DI float fmx(float a, float b) { return __builtin_elementwise_maximum(a, b); }
DI float rbf(float a) { return (float)(__bf16)a; }
DI float bflo(u32 v) { return __uint_as_float(v << 16); }
DI float bfhi(u32 v) { return __uint_as_float(v & 0xffff0000u); }
DI float dot2(u32 a, u32 b, float c) { return __builtin_amdgcn_fdot2_f32_bf16(__builtin_bit_cast(bf16x2, a), __builtin_bit_cast(bf16x2, b), c, false); }
DI float sum8(float v) {
  v += __builtin_bit_cast(float, __builtin_amdgcn_update_dpp(0, __builtin_bit_cast(int, v), 0xb1, 0xf, 0xf, true));
  v += __builtin_bit_cast(float, __builtin_amdgcn_update_dpp(0, __builtin_bit_cast(int, v), 0x4e, 0xf, 0xf, true));
  v += __builtin_bit_cast(float, __builtin_amdgcn_update_dpp(0, __builtin_bit_cast(int, v), 0x141, 0xf, 0xf, true));
  return v;
}
template <class T> DI T lds_ld(const LAS char* base, u32 off) { return *(const LAS T*)(base + off); }
template <class T> DI void lds_st(LAS char* base, u32 off, T v) { *(LAS T*)(base + off) = v; }
DI bf16x8 trfrag(LAS char* base, u32 off0, u32 off1) {
  s16x4 lo = __builtin_amdgcn_ds_read_tr16_b64_v4i16((LAS s16x4*)(base + off0));
  s16x4 hi = __builtin_amdgcn_ds_read_tr16_b64_v4i16((LAS s16x4*)(base + off1));
  return __builtin_bit_cast(bf16x8, __builtin_shufflevector(lo, hi, 0, 1, 2, 3, 4, 5, 6, 7));
}
#define ST_AUX 16
template <int AUX> DI void st16(u32x4 v, rsrc_t rs, u32 voff) { __builtin_amdgcn_raw_buffer_store_b128(v, rs, voff, 0, AUX ? ST_AUX : 0); }
DI void st_flag(rsrc_t rc, u32 off, u64 v) { __builtin_amdgcn_raw_buffer_store_b64(u32x2{(u32)v, (u32)(v >> 32)}, rc, off, 0, 16); }
DI bool tagged(u32x2 v, u64 tag) { return __all(v[0] == (u32)tag && v[1] == (u32)(tag >> 32)); }
template <int SLEEP> DI void spin(rsrc_t rc, u32 off, u64 tag) {
#pragma unroll 1
  for (u32 n = 0; n < (1u << 17); ++n) {
    asm volatile("" ::: "memory");
    if (tagged(__builtin_amdgcn_raw_buffer_load_b64(rc, off, 0, 16), tag)) break;
    __builtin_amdgcn_s_sleep(SLEEP);
  }
  asm volatile("" ::: "memory");
}
DI u32 cnt_ld(LAS char* L, int k) { return __hip_atomic_load((LAS u32*)(L + L_CNT + 4 * k), __ATOMIC_RELAXED, __HIP_MEMORY_SCOPE_WORKGROUP); }
DI void cnt_wait(LAS char* L, int k, u32 v) {
#pragma unroll 1
  for (u32 n = 0; n < (1u << 20); ++n) {
    if ((u32)__builtin_amdgcn_readfirstlane(cnt_ld(L, k)) >= v) break;
    __builtin_amdgcn_s_sleep(LSLP);
  }
  asm volatile("" ::: "memory");
}
DI void prog_st(LAS char* L, int k, u32 v) { asm volatile("" ::: "memory"); __hip_atomic_store((LAS u32*)(L + L_CNT + 4 * k), v, __ATOMIC_RELAXED, __HIP_MEMORY_SCOPE_WORKGROUP); asm volatile("" ::: "memory"); }
DI u32 land_ld(LAS char* L) { const u64 v = __hip_atomic_load((LAS u64*)(L + L_CNT + 4 * C_LAND), __ATOMIC_RELAXED, __HIP_MEMORY_SCOPE_WORKGROUP); return min((u32)v, (u32)(v >> 32)); }
DI u32 done_ld(LAS char* L) {
  const u64 a = __hip_atomic_load((LAS u64*)(L + L_CNT), __ATOMIC_RELAXED, __HIP_MEMORY_SCOPE_WORKGROUP), b = __hip_atomic_load((LAS u64*)(L + L_CNT + 8), __ATOMIC_RELAXED, __HIP_MEMORY_SCOPE_WORKGROUP);
  return min(min((u32)a, (u32)(a >> 32)), min((u32)b, (u32)(b >> 32)));
}
template <bool DONE> DI void prog_wait(LAS char* L, u32 v) {
#pragma unroll 1
  for (u32 n = 0; n < (1u << 20); ++n) {
    if ((u32)__builtin_amdgcn_readfirstlane(DONE ? done_ld(L) : land_ld(L)) >= v) break;
    __builtin_amdgcn_s_sleep(LSLP);
  }
  asm volatile("" ::: "memory");
}
DI void cnt_add(u32 lb, int k) {
  asm volatile("s_mov_b64 exec, 1\n\tds_add_u32 %0, %1\n\ts_mov_b64 exec, -1" ::"v"(lb + L_CNT + 4 * k), "v"(1u) : "memory");
}
template <int N> DI bool vm_le() {
  u32 r; asm volatile("s_getreg_b32 %0, hwreg(HW_REG_IB_STS)" : "=s"(r)::"memory");
  return (r & 0xc0000fu) <= (u32)N;
}
DI bool last_of4(LAS char* L, int k) {
  u32 old = 0;
  asm volatile("" ::: "memory");
  if (__builtin_amdgcn_mbcnt_hi(~0u, __builtin_amdgcn_mbcnt_lo(~0u, 0u)) == 0) old = __hip_atomic_fetch_add((LAS u32*)(L + L_CNT + 4 * k), 1u, __ATOMIC_RELAXED, __HIP_MEMORY_SCOPE_WORKGROUP);
  asm volatile("" ::: "memory");
  return __builtin_amdgcn_readfirstlane(old) == 3;
}
#define STAMP(k)
#define FLUSH()

__global__ __launch_bounds__(512, 2) void fused_block(const char* __restrict__ x, const char* __restrict__ wqkv, const char* __restrict__ wo,
                                                      const char* __restrict__ w1, const char* __restrict__ b1, const char* __restrict__ w2,
                                                      const char* __restrict__ b2, char* __restrict__ out, char* ws) {
  __shared__ __attribute__((aligned(1024))) char smem[163840];
  asm volatile("" : "+s"(x), "+s"(wqkv), "+s"(wo), "+s"(w1), "+s"(b1), "+s"(w2), "+s"(b2), "+s"(out), "+s"(ws));
  u64 pc; asm volatile("s_getpc_b64 %0" : "=s"(pc));
  LAS char* const L = (LAS char*)smem;
  const u32 lb = (u32)(uintptr_t)smem;
  int l = threadIdx.x & 63, g, q4, p4, l15;
  auto lanes = [&] { PIN(l); g = l >> 4; q4 = (l >> 2) & 3; p4 = l & 3; l15 = l & 15; };
  lanes();
  int w = __builtin_amdgcn_readfirstlane(threadIdx.x >> 6);
  asm volatile("s_nop 4" : "+s"(w));
  const int x8 = blockIdx.x & 7, rk = blockIdx.x >> 3;
  const bool busy = rk < 28;
  if (w == 0) STAMP(0);
  const rsrc_t rQKV = mkrs(ws + WS_QKV, QKV_BYTES), rAC = mkrs(ws + WS_AC, AC_BYTES), rCTL = mkrs(ws, CTL_BYTES);
  const rsrc_t rX = mkrs(x, 2048 * 896), rW2 = mkrs(w2, 1792 * 896);
  const int kh = x8 >> 2, cc = rk % 7, rb = (x8 & 3) * 4 + rk / 7, cb = kh * 7 + cc;
  const int ai = rk & 15, ah = 2 * x8 + (rk >> 4);
  const int ro6 = ROTA ? (cc + ((3 * (rk / 7)) >> 1)) % 6 : 0;
  auto rotl = [&](int j) { const int t = j + ro6; return j == 0 ? 0 : (t > 6 ? t - 6 : t); };
  auto kstep = [&](int p) {
    if (p < 8) return 8 * cb + p;
    const int t = cc + 1 + ((p - 8) % 24) / 4;
    return 8 * (7 * kh + (t >= 7 ? t - 7 : t)) + (p & 3) + (p >= 32 ? 4 : 0);
  };
  if (busy && (w == 4 || w == 5)) {
    u32 v[4];
#pragma unroll
    for (int j = 0; j < 4; ++j) v[j] = (8 * j + (l >> 3)) * 896 + (((l & 7) ^ ((j & 1) * 4 + g)) << 4) - 1024 * j;
    dman<4>(lb + (w & 1) * 8192, v, rX, (rb * 128 + 64 * (w & 1)) * 896);
    dman<4>(lb + (w & 1) * 8192 + 4096, v, rX, (rb * 128 + 64 * (w & 1) + 32) * 896);
    u32 vq[3];
#pragma unroll
    for (int j = 0; j < 3; ++j) { const u32 n = 64 * j + l; vq[j] = (n / 12) * 2688 + (n % 12) * 16 - 1024 * j; }
#pragma unroll
    for (int a = 0; a < 2; ++a) {
      dman<3>(lb + stage_addr(a) + (w & 1) * 3072, vq, mkrs(wqkv, 448 * 2688), cb * 192 + (16 * (w & 1) + 32 * a) * 2688);
    }
  }
  if (w == 6) dma4(lb + L_Q + 512, (rk * 32 + (l & 31)) * 64, mkrs((const void*)(pc & ~255ull), CODE_KB * 1024), 0);
  u32x4 d[14];
  const bool stat = busy && (w == 6 || (w == 7 && cb < 2));
  const int srow = rb * 128 + (w == 6 ? cb : 14 + cb) * 8 + (l >> 3);
  if (stat) {
#pragma unroll
    for (int k = 0; k < 7; ++k) d[k] = ld16_async<false>(srow * 896 + (l & 7) * 16, rX, k * 128);
  }
  if (busy && w == 7) {
    const int r = rb * 28 + (l >> 1);
    if (l < 56) dma4(lb + L_Q, r * 2688 + cb * 192 + (l & 1) * 188, mkrs(wqkv, 448 * 2688), 0);
  }
  SB();
  u64 tag;
  auto join = [](u32x2 t) { return (u64)(u32)__builtin_amdgcn_readfirstlane(t[0]) | (u64)(u32)__builtin_amdgcn_readfirstlane(t[1]) << 32; };
  auto epoch = [&] { tag = join(__builtin_amdgcn_raw_buffer_load_b64(rCTL, 0, 0, 16)) + 1; };
  auto lds_tag = [&] { tag = join(lds_ld<u32x2>(L, L_CNT + 112)); };
  auto vW2 = [&](u32* v) { v[0] = (l >> 3) * 896 + (((l & 7) ^ ((g & 1) << 2)) << 4); v[1] = v[0] + 7168 - 1024; };

  if (w < 4) {
    if (!busy) {
      const int q = (rk - 28) * 4 + w;
      bar();
      for (int k = 0; k < PF_DELAY; ++k) __builtin_amdgcn_s_sleep(2);
      const rsrc_t rW1 = mkrs(w1, 448 * 3584), rWo = mkrs(wo, 448 * 896);
      const u32 v = l * 16;
      auto pf1 = [&] {
        const u32 v1 = l < 56 ? (l >> 3) * 256 + 128 + (l & 7) * 16 : 0x7fffffffu;
#pragma unroll 1
        for (int k = 0; k < 28; ++k) { dman<1>(lb + L_WC + w * 16384 + (k & 15) * 1024, &v1, rW1, (k * 16 + q) * 3584 + kh * 1792); vm<PFD>(); }
      };
      if (PF1 == 1) pf1();
#pragma unroll 1
      for (int k = 0; k < 49; ++k) {
        int r = k * 16 + q + (x8 & 3) * 196; r = r >= 784 ? r - 784 : r;
        dman<1>(lb + L_WC + w * 16384 + (k & 15) * 1024, &v, rW2, kh * 802816 + r * 1024);
        vm<PFD>();
      }
      STAMP(38);
      if (PF1 == 2) pf1();
#pragma unroll 1
      for (int k = 0; k < 25; ++k) { dman<1>(lb + L_WC + w * 16384 + (k & 15) * 1024, &v, rWo, (k * 16 + q) * 1024); vm<PFD>(); }
      vm<0>();
      STAMP(39);
      FLUSH();
      return;
    }
    __builtin_amdgcn_s_setprio(PRIO_A);
    constexpr int PF = P_PF;
    u32 oX[4], oH[2], oQ[3];
#pragma unroll
    for (int p = 0; p < 4; ++p) oX[p] = (4 * w + ((l & 31) >> 3)) * 1024 + (l & 7) * 128 + (((2 * p + (g >> 1)) ^ (l15 >> 1)) << 4);
    const int rq = w == 3 ? 0 : w;
#pragma unroll
    for (int i = 0; i < 3; ++i) oQ[i] = RING + (8 * (g >> 1) + q4) * 192 + ((i + rq) % 3) * 64 + (g & 1) * 32 + p4 * 8;
#pragma unroll
    for (int i = 0; i < 2; ++i) oH[i] = RING + (g >> 1) * 1024 + q4 * 128 + ((i ^ (q4 >> 1)) << 6) + (g & 1) * 32 + p4 * 8;
    const u32 selv = l15 == (g & 1) ? 0x3f803f80u : 0u;
    const bf16x8 sel = __builtin_bit_cast(bf16x8, u32x4{selv, selv, selv, selv});
    bf16x8 xf[28], wf[8];
    f32x16 aH[2], aQ[3] = {};
    f32x4 cs = {};
    u32x2 ch[2];
    float rs;
    bf16x8 lnb;
    u32x4 hA[72], qp[6];
    auto rdW = [&](auto SS, auto II) {
      constexpr int s = decltype(SS)::v, i = decltype(II)::v;
      constexpr int slot = (s < 14 ? s * 6 + i : 84 + i) % 8;
      if constexpr (s < 14) { const u32 o = oQ[i % 3] + (stage_addr(s) - RING) + (i / 3) * 3072; wf[slot] = trfrag(L, o, o + 768); }
      else if constexpr (s < 28) { const u32 o = oH[i & 1] + (stage_addr(s) - RING) + (i >> 1) * 2048; wf[slot] = trfrag(L, o, o + 512); }
    };
    auto rdX = [&](auto SS, auto EE) { constexpr int k = 2 * decltype(SS)::v + decltype(EE)::v; if constexpr (k < 28) xf[k] = lds_ld<bf16x8>(L, oX[k & 3] + (k >> 2) * 16384); };
    auto rdcs = [&](int half) {
#pragma unroll
      for (int i = 0; i < 2; ++i) ch[i] = lds_ld<u32x2>(L, L_CSH + (half * 64 + i * 32 + (l & 31)) * 8);
    };
    auto hpack = [&](int half, int c) {
      u32x4 hp;
#pragma unroll
      for (int j = 0; j < 4; ++j) {
        const float t0 = aH[c >> 1][8 * (c & 1) + 2 * j], t1 = aH[c >> 1][8 * (c & 1) + 2 * j + 1];
        hp[j] = pk(t0 * fmx(t0, 0.f), t1 * fmx(t1, 0.f));
      }
      hA[half * 4 + c] = hp;
    };
    auto hstore = [&](int m) { st16<16>(hA[m], rAC, l * 16 + ((rb * 4 + w) * 144 + cb * 8 + m) * 1024); };
    auto qpack = [&](int c) {
      const int i = c >> 1, e = c & 1, tq = 6 * cb + 2 * ((i + rq) % 3) + e;
      const float m = tq < 28 ? rs * (0.18898223650461361f * 1.4426950408889634f) : rs;
      const auto s0 = __builtin_amdgcn_permlane16_swap(pk(aQ[i][8 * e] * m, aQ[i][8 * e + 1] * m), pk(aQ[i][8 * e + 4] * m, aQ[i][8 * e + 5] * m), false, false);
      const auto s1 = __builtin_amdgcn_permlane16_swap(pk(aQ[i][8 * e + 2] * m, aQ[i][8 * e + 3] * m), pk(aQ[i][8 * e + 6] * m, aQ[i][8 * e + 7] * m), false, false);
      qp[c] = u32x4{s0[0], s1[0], s0[1], s1[1]};
    };
    auto qstore = [&](int c) {
      const int fl = ((g & 1) << 1) | (g >> 1), tq = 6 * cb + 2 * (((c >> 1) + rq) % 3) + (c & 1);
      u32x4 v = qp[c];
      u32 vo = fl * 256 + l15 * 16;
      if (tq >= 56) {
        asm volatile("" ::: "memory");
        if (fl & 1) v = u32x4{v[2], v[3], v[0], v[1]};
        vo ^= (2 * (tq & 1) + (fl >> 1)) << 6;
      }
      st16<16>(v, rQKV, vo + ((rb * 4 + w) * 336 + 4 * tq) * 256);
    };
    bool sigq = false, siga = false;
    const bool giver = (w >> 1) != kh;
    bar();
    STAMP(1);
    rdX(IC<0>{}, IC<0>{}); rdX(IC<0>{}, IC<1>{});
    sfor<0, PF>([&](auto I) { rdW(IC<0>{}, I); });
    sfor<0, 28>([&](auto SS) {
      constexpr int s = decltype(SS)::v, NT = s < 14 ? 6 : 8;
      if constexpr (s == 14) wait_bar<63>();
      else if constexpr (s < 14) bar();
      if constexpr (s == 13) lanes();
      if constexpr (s == 14) {
        STAMP(2);
        u32 cq[3];
#pragma unroll
        for (int i = 0; i < 3; ++i) cq[i] = lds_ld<u32>(L, L_CSQ + (((i + rq) % 3) * 32 + (l & 31)) * 4);
        const u32 rdy = cnt_ld(L, C_ST);
        u32 st = lds_ld<u32>(L, L_ST + (w * 32 + (l & 31)) * 16);
        if (!__builtin_amdgcn_readfirstlane(rdy)) { cnt_wait(L, C_ST, 1); st = lds_ld<u32>(L, L_ST + (w * 32 + (l & 31)) * 16); }
        SB();
        lnb = __builtin_bit_cast(bf16x8, u32x4{l < 32 ? (st & 0xffffu) * 0x10001u : 0u, l < 32 ? st >> 16 : 0u, 0u, 0u});
        rs = __builtin_amdgcn_rcpf(bfhi(st));
#pragma unroll
        for (int i = 0; i < 3; ++i) aQ[i] = mfma32(__builtin_bit_cast(bf16x8, u32x4{cq[i], 0u, 0u, 0u}), lnb, aQ[i]);
        SB();
#pragma unroll
        for (int c = 0; c < 6; ++c) { qpack(c); if (c % (6 / QNOW) == 6 / QNOW - 1) qstore(c / (6 / QNOW)); SB(); }
        STAMP(3);
      }
      if constexpr (s == 21) {
        rdcs(1);
        SB();
#pragma unroll
        for (int c = 0; c < 4; ++c) { hpack(0, c); if (c < HNOW) hstore(c); SB(); }
      }
      u32 land;
      sfor<0, NT>([&](auto II) {
        constexpr int i = decltype(II)::v, slot = (s < 14 ? s * 6 + i : 84 + i) % 8;
        constexpr u32 need = s == 15 ? 1 : s == 18 ? 2 : s == 22 ? 3 : s == 25 ? 4 : 0;
        if constexpr (need && i == 0) land = land_ld(L);
        if constexpr (need && i == NT - PF) {
          if ((u32)__builtin_amdgcn_readfirstlane(land) < need) prog_wait<false>(L, need);
          asm volatile("" ::: "memory");
        }
        if constexpr (i + PF < NT) rdW(SS, IC<i + PF>{});
        else rdW(IC<s + 1>{}, IC<i + PF - NT>{});
        if constexpr (i < 2) rdX(IC<s + 1>{}, II);
        if constexpr (s < 14) {
          aQ[i % 3] = mfma32(wf[slot], xf[2 * s + i / 3], aQ[i % 3]);
          if constexpr (i % 3 == 0) cs = mfma(wf[slot], sel, cs);
          if constexpr (s == 13 && i == 4) {
            u32x4 v;
#pragma unroll
            for (int j = 0; j < 4; ++j) { const float hi = rbf(cs[j]); v[j] = pk(hi, cs[j] - hi); }
            if (l15 < 2) lds_st<u32x4>(L, L_CSQ + w * 128 + (l15 * 16 + 4 * g) * 4, v);
          }
        } else {
          constexpr int k = 4 * ((s - 14) % 7) + (i >> 1), m = (s < 21 ? QNOW : HNOW) + 2 * ((s - 14) % 7) + (i >> 2);
          if constexpr (k == 0 && s == 21) aH[i] = mfma32(__builtin_bit_cast(bf16x8, u32x4{ch[i][0], ch[i][1], 0u, 0u}), lnb, f32x16{});
          aH[i & 1] = mfma32(wf[slot], xf[k], s == 14 && k == 0 ? f32x16{} : aH[i & 1]);
          if constexpr (s == 19 && i == 7) { cnt_wait(L, C_CSH, 1); rdcs(0); }
          if constexpr (s == 20 && (i == 2 || i == 3)) aH[i & 1] = mfma32(__builtin_bit_cast(bf16x8, u32x4{ch[i & 1][0], ch[i & 1][1], 0u, 0u}), lnb, aH[i & 1]);
          if constexpr ((i & 3) == 1 && s < 21 && m < 6) qstore(m);
          if constexpr ((i & 3) == 1 && s >= 21 && m < 4) hstore(m);
        }
        SB();
      });
      if constexpr (s == 18 || s == 20 || s == 27) prog_st(L, C_DONE + w, s);
      if constexpr (s >= 14 && s <= S_FQ && QNOW + 2 * (s - 14) >= 6) { if (!sigq && (s == S_FQ || vm_le<0>())) { vm<0>(); cnt_add(lb, C_FQ); sigq = true; } }
      if constexpr (s >= 21 && HNOW + 2 * (s - 21) >= 4) { if (!siga && vm_le<0>()) { vm<0>(); cnt_add(lb, C_FA); siga = true; } }
    });
    STAMP(4);
    lanes();
    const u32 vFG = WS_FG + (rb * 16 + 7 * kh + (l < 7 ? l : 0)) * FS;
#pragma unroll
    for (int c = 0; c < 4; ++c) hpack(1, c);
    SB();
    if (!siga) { vm<0>(); cnt_add(lb, C_FA); }
#pragma unroll
    for (int m = 4; m < 8; ++m) hstore(m);
    STAMP(5);
    lds_tag();
    u32x2 polled;
    u32 vW[2];
    vW2(vW);
    const u32 vA = l * 16, sA = (rb * 4 + w) * 144, sO = ((rb * 4 + (giver ? w ^ 2 : w)) * 144 + (giver ? 128 : 112)) * 1024;
    u32 sN[6];
#pragma unroll
    for (int j = 0; j < 6; ++j) { sN[j] = (sA + kstep(8 + 4 * j)) * 1024; asm volatile("" : "+s"(sN[j])); }
    const u32 vB = vA + 4096;
    auto ldA = [&](auto M) { constexpr int m = decltype(M)::v; hA[m] = ld16_async<true, (m & 3) * 1024>(m >= 32 ? vB : vA, rAC, sN[((m - 8) % 24) / 4]); };
    auto ldO = [&](auto M) { constexpr int m = decltype(M)::v; hA[m] = ld16_async<true, (m & 3) * 1024>(vA, rAC, sO + ((m - 56) & ~3) * 1024); };
    const rsrc_t rWo = mkrs(wo, 448 * 896);
    u32 oW[2], oO[2];
#pragma unroll
    for (int b = 0; b < 2; ++b) { oW[b] = (4 * (g >> 1) + q4) * 128 + ((b ^ (q4 >> 1)) << 6) + (g & 1) * 32 + p4 * 8; oO[b] = oW[b] + L_WC + (giver ? 8 * 3584 : 0); }
    bf16x8 wc[2][2];
    auto rdC = [&](int m) {
#pragma unroll
      for (int b = 0; b < 2; ++b) {
        const u32 o = m < 56 ? oW[b] + L_WC + m * 2048 : oO[b] + ((m - 56) >> 1) * 3584 + (m & 1) * 2048;
        wc[m & 1][b] = trfrag(L, o, o + 1024);
      }
    };
    f32x16 acc[2];
    f32x16 aco[2];
    u32 top, pre;
    u32x4 xres[2], bres;
    u32x2 pp[4];
    const int tile = giver ? w ^ 2 : w, r0 = (giver ? 16 : 0) + (l >> 3);
    auto step = [&](auto N) {
      constexpr int n = decltype(N)::v;
      if constexpr (n + 1 < 72) rdC(n + 1);
      SB();
      f32x16* a = n < 56 ? acc : aco;
      a[0] = mfma32(wc[n & 1][0], __builtin_bit_cast(bf16x8, hA[n]), n == 0 || n == 56 ? f32x16{} : a[0]);
      SB();
      if constexpr (n >= 8 && n < 56) sfor<SC.a0[n], SC.a1[n]>(ldA);
      if constexpr (n >= 8 && n < 15) dman<2>(lb + L_WC + (NWP + 4 * (n - 8) + w) * 2048, vW, rW2, (kstep(NWP + 4 * (n - 8)) + w) * (16 * 896) + cc * 128);
      if constexpr (n >= T_WO && n < T_WO + 7) dman<2>(lb + L_WC + (4 * (n - T_WO) + w) * 2048, vW, rWo, (4 * (n - T_WO) + w) * (16 * 896) + cc * 128);
      if constexpr (n == T_WO) { if (w == 0) dman<2>(lb + L_WC + 28 * 2048, vW, rWo, 28 * (16 * 896) + cc * 128); }
      if constexpr (n >= 56) { top = max(max(n == 56 ? 0u : top, hA[n][0]), hA[n][1]); top = max(max(top, hA[n][2]), hA[n][3]); PIN(top); }
      a[1] = mfma32(wc[n & 1][1], __builtin_bit_cast(bf16x8, hA[n]), n == 0 || n == 56 ? f32x16{} : a[1]);
      SB();
    };
    cnt_wait(L, C_WLO, 2);
    rdC(0);
    sfor<0, OWN1>(step);
    const u32 vFH = l >= 16 && l < 32 ? WS_FZ + (rb * 16 + l - 16) * FS : WS_FH + (rb * 16 + (l < 7 ? 7 * kh + l : 7 * (1 - kh) + cc)) * FS;
    const int POLLW = 2 * kh;
    bool sigg = false;
    auto pubg = [&] { if (last_of4(L, C_FG) && l == 0) st_flag(rCTL, WS_FG + (rb * 16 + cb) * FS, tag); sigg = true; };
#pragma unroll 1
    for (u32 n = 0; n < (1u << 18); ++n) {
      asm volatile("" ::: "memory");
      if (w == POLLW) {
        const bool ok = tagged(__builtin_amdgcn_raw_buffer_load_b64(rCTL, vFH, 0, 16), tag);
        if (ok) cnt_add(lb, C_FH);
        if (!sigg) { vm<0>(); pubg(); }
        if (ok) break;
      } else {
        if (!sigg && vm_le<0>()) { vm<0>(); pubg(); }
        if (__builtin_amdgcn_readfirstlane(cnt_ld(L, C_FH))) break;
      }
      __builtin_amdgcn_s_sleep(LSLP);
    }
    asm volatile("" ::: "memory");
    STAMP(6);
    __builtin_amdgcn_s_setprio(PRIO_C);
    sfor<8, 8 + DA>(ldA);
    prog_wait<true>(L, 27);
    sfor<OWN1, 8>(step);
    sfor<8, 56>([&](auto N) {
      constexpr int n = decltype(N)::v;
      asm volatile("s_waitcnt vmcnt(%1)" : "+v"(hA[n]) : "n"(SC.wC[n]) : "memory");
      if constexpr (n == 30) cnt_add(lb, C_P30);
      constexpr auto gate = [](int m) { return m == T_WO ? C_P30 : m == 55 ? C_WO : m == NWP - 1 ? C_WHI : m == 51 ? C_LATE : -1; };
      if constexpr (gate(n) >= 0) { if ((u32)__builtin_amdgcn_readfirstlane(pre) < 4u) cnt_wait(L, gate(n), 4); asm volatile("" ::: "memory"); }
      if constexpr (gate(n + 2) >= 0) pre = cnt_ld(L, gate(n + 2));
      if constexpr (n == T_WO + 12) cnt_add(lb, C_WO);
      if constexpr (n == 8) { if (!sigg) pubg(); }
      if constexpr (n == T_WH) cnt_add(lb, C_WHI);
      if constexpr (n == TB - 3) polled = ld8c_async(vFG, rCTL);
      if constexpr (n == TB) { PIN(polled); if (!tagged(polled, tag)) spin<SLP>(rCTL, vFG, tag); }
      step(N);
      if constexpr (n >= T_O && n < T_O + 8) { ldO(IC<56 + 2 * (n - T_O)>{}); ldO(IC<57 + 2 * (n - T_O)>{}); }
      if constexpr (n == T_O + 8) {
#pragma unroll
        for (int k = 0; k < 2; ++k) xres[k] = ld16_async<false>((r0 + 8 * k) * 896 + (l & 7) * 16, rX, (rb * 128 + tile * 32) * 896 + cc * 128);
        bres = ld16_async<false>((l & 7) * 16, mkrs(b2, 896), cc * 128);
      }
    });
    asm volatile("s_waitcnt vmcnt(0)" : "+v"(hA[56]), "+v"(hA[57]), "+v"(hA[58]), "+v"(hA[59]), "+v"(hA[60]), "+v"(hA[61]), "+v"(hA[62]), "+v"(hA[63]), "+v"(xres[0]), "+v"(xres[1]),
                 "+v"(hA[64]), "+v"(hA[65]), "+v"(hA[66]), "+v"(hA[67]), "+v"(hA[68]), "+v"(hA[69]), "+v"(hA[70]), "+v"(hA[71]), "+v"(bres)::"memory");
    const float rs2 = rs * rs;
    if (giver) {
#pragma unroll
      for (int k = 0; k < 4; ++k) {
        const f32x16& a = acc[k >> 1];
        const int o = 8 * (k & 1);
        st16<16>(u32x4{pk(a[o] * rs2, a[o + 1] * rs2), pk(a[o + 2] * rs2, a[o + 3] * rs2), pk(a[o + 4] * rs2, a[o + 5] * rs2), pk(a[o + 6] * rs2, a[o + 7] * rs2)}, rAC,
                 PX + ((rb * 7 + cc) * 4 + w) * 4096 + k * 1024 + l * 16);
      }
    }
    const u32 vP = PX + ((rb * 7 + cc) * 4 + tile) * 4096 + ((l & 7) >> 1) * 1024 + r0 * 16 + (l & 1) * 8;
    sfor<56, T_PP>(step);
#pragma unroll
    for (int k = 0; k < 4; ++k) pp[k] = ld8c_async(vP + (k >> 1) * 128 + (k & 1) * 512, rAC);
    pre = cnt_ld(L, C_ATT);
    sfor<T_PP, 72>(step);
    asm volatile("s_waitcnt vmcnt(0)" : "+v"(pp[0]), "+v"(pp[1]), "+v"(pp[2]), "+v"(pp[3])::"memory");
#pragma unroll 1
    for (u32 n = 0; n < (1u << 17) && !__all(top != ~0u); ++n) {
#pragma unroll 1
      for (; n < (1u << 17); ++n) {
        asm volatile("" ::: "memory");
        if (__all(__builtin_amdgcn_raw_buffer_load_b32(rAC, sO + (l & 7) * 2048 + 1024, 0, 16) != ~0u)) break;
        __builtin_amdgcn_s_sleep(SLP);
      }
#pragma unroll
      for (int k = 0; k < 16; ++k) hA[56 + k] = __builtin_amdgcn_raw_buffer_load_b128(rAC, vA + sO + k * 1024, 0, 16);
      rdC(56);
      sfor<56, 72>(step);
    }
    STAMP(10);
    if (!giver) { aco[0] += acc[0] * rs2; aco[1] += acc[1] * rs2; }
    if ((u32)__builtin_amdgcn_readfirstlane(pre) < 4u) cnt_wait(L, C_ATT, 4);
    asm volatile("" ::: "memory");
#pragma unroll
    for (int b = 0; b < 2; ++b)
#pragma unroll
      for (int k = 0; k < 4; ++k)
        lds_st<f32x4>(L, L_RED + w * 8192 + (l & 31) * 256 + (((b * 8 + k * 2 + (g >> 1)) ^ (l & 7)) << 4), f32x4{aco[b][4 * k], aco[b][4 * k + 1], aco[b][4 * k + 2], aco[b][4 * k + 3]});
    cnt_add(lb, C_PAIR0 + (w & 1));
#pragma unroll 1
    for (u32 n = 0; n < (1u << 17) && !__all(max(max(max(pp[0][0], pp[0][1]), max(pp[1][0], pp[1][1])), max(max(pp[2][0], pp[2][1]), max(pp[3][0], pp[3][1]))) != ~0u); ++n) {
      asm volatile("" ::: "memory");
#pragma unroll
      for (int k = 0; k < 4; ++k) pp[k] = __builtin_amdgcn_raw_buffer_load_b64(rAC, vP + (k >> 1) * 128 + (k & 1) * 512, 0, 16);
    }
    f32x4 ra[2], rc[2];
#pragma unroll
    for (int k = 0; k < 2; ++k) {
      ra[k] = f32x4{bflo(xres[k][0]) + bflo(bres[0]), bfhi(xres[k][0]) + bfhi(bres[0]), bflo(xres[k][1]) + bflo(bres[1]), bfhi(xres[k][1]) + bfhi(bres[1])} +
              f32x4{bflo(pp[2 * k][0]), bfhi(pp[2 * k][0]), bflo(pp[2 * k][1]), bfhi(pp[2 * k][1])};
      rc[k] = f32x4{bflo(xres[k][2]) + bflo(bres[2]), bfhi(xres[k][2]) + bfhi(bres[2]), bflo(xres[k][3]) + bflo(bres[3]), bfhi(xres[k][3]) + bfhi(bres[3])} +
              f32x4{bflo(pp[2 * k + 1][0]), bfhi(pp[2 * k + 1][0]), bflo(pp[2 * k + 1][1]), bfhi(pp[2 * k + 1][1])};
      PIN(ra[k]); PIN(rc[k]);
    }
    cnt_wait(L, C_PAIR0 + (w & 1), 2);
    const rsrc_t rO = mkrs(out, 2048 * 896);
#pragma unroll
    for (int k = 0; k < 2; ++k) {
      const int row = r0 + 8 * k;
      f32x4 a = ra[k], b = rc[k];
#pragma unroll
      for (int j = 0; j < 2; ++j) {
        a += lds_ld<f32x4>(L, L_RED + ((w & 1) + 2 * j) * 8192 + row * 256 + (((2 * (l & 7)) ^ (row & 7)) << 4));
        b += lds_ld<f32x4>(L, L_RED + ((w & 1) + 2 * j) * 8192 + row * 256 + (((2 * (l & 7) + 1) ^ (row & 7)) << 4));
      }
      st16<16>(u32x4{pk(a[0], a[1]), pk(a[2], a[3]), pk(b[0], b[1]), pk(b[2], b[3])}, rO, (rb * 128 + tile * 32 + row) * 896 + cc * 128 + (l & 7) * 16);
    }
    if (w == 0) STAMP(11);
    FLUSH();
    return;
  }

  const int li = w & 1;
  auto clear_o = [&] {
#pragma unroll
    for (int k = 0; k < 8; ++k) st16<16>(u32x4{~0u, ~0u, ~0u, ~0u}, rAC, ((ai * 4 + (k >> 1)) * 144 + 112 + 2 * ah + (k & 1)) * 1024 + l * 16);
  };
  if (!busy) {
    const int blk = (rk - 28) * 8 + x8;
#pragma unroll
    for (int k = 0; k < 14; ++k) d[k] = __builtin_amdgcn_raw_buffer_load_b128(mkrs(w1, 448 * 3584), (112 * (w - 4) + (l >> 3)) * 3584 + (l & 7) * 16 + (blk < 28 ? blk * 128 : 0x7fffffff), k * 8 * 3584, 0);
    const u32 bias = __builtin_amdgcn_raw_buffer_load_b16(mkrs(b1, 3584), (blk * 64 + l) * 2, 0, 0);
    SB();
    epoch();
    if (w == 6 && l < C_N) lds_st<u32>(L, L_CNT + l * 4, l < 4 ? 64u : 0u);
    wait_bar<63>();
    float a[8] = {};
#pragma unroll
    for (int k = 0; k < 14; ++k)
#pragma unroll
      for (int j = 0; j < 4; ++j) { a[2 * j] += bflo(d[k][j]); a[2 * j + 1] += bfhi(d[k][j]); }
    lds_st<f32x4>(L, RING + ((w - 4) * 64 + l) * 32, f32x4{a[0], a[1], a[2], a[3]});
    lds_st<f32x4>(L, RING + ((w - 4) * 64 + l) * 32 + 16, f32x4{a[4], a[5], a[6], a[7]});
    if (last_of4(L, C_CS0) && blk < 28) {
      float t = 0.f;
#pragma unroll
      for (int k = 0; k < 32; ++k) t += lds_ld<float>(L, RING + k * 256 + l * 4);
      const float hi = rbf(t);
      __builtin_amdgcn_raw_buffer_store_b64(u32x2{pk(hi, t - hi), bias}, rCTL, WS_CS + (blk * 64 + l) * 8, 0, 16);
      vm<0>();
      if (l == 0) st_flag(rCTL, WS_FC + blk * FS, tag);
    }
    if (w == 7) { clear_o(); vm<0>(); if (l == 0) st_flag(rCTL, WS_FZ + (ai * 16 + ah) * FS, tag); }
  } else if (w < 6) {
    __builtin_amdgcn_s_setprio(PRIO_L);
    const rsrc_t rW1 = mkrs(w1, 448 * 3584), rWq = mkrs(wqkv, 448 * 2688);
    u32 vX[4], vH[4], vQ[3], vC[2], so7[7];
#pragma unroll
    for (int j = 0; j < 4; ++j) vX[j] = (8 * j + (l >> 3)) * 896 + (((l & 7) ^ ((j & 1) * 4 + g)) << 4) - 1024 * j;
    so7[0] = 0;
    const u32 sX = (rb * 128 + 64 * li) * 896, sH = cb * 256 + 32 * li * 3584, sQ = cb * 192 + 16 * li * 2688;
    auto issue = [&](auto O) {
      constexpr int op = decltype(O)::v, k = op / 256, a = (op % 256) / 2, e = op & 1;
      if constexpr (k == K_X) dman<4>(lb + (a * 16 + 8 * li + 4 * e) * 1024, vX, rX, sX + so7[a] * 128 + e * 28672);
      if constexpr (k == K_WA && a < 14) dman<3>(lb + stage_addr(a) + li * 3072, vQ, rWq, sQ + so7[a >> 1] * (64 * 2688) + (a & 1) * (32 * 2688));
      if constexpr (k == K_WA && a >= 14) dman<4>(lb + stage_addr(a) + li * 4096, vH, rW1, sH + so7[(a - 14) % 7] * (64 * 3584) + (a >= 21 ? 128 : 0));
      if constexpr (k == K_WCP) dman<2>(lb + L_WC + (2 * a + li) * 2048, vC, rW2, (kstep(2 * a) + li) * (16 * 896) + cc * 128);
    };
    lanes();
#pragma unroll
    for (int j = 0; j < 3; ++j) { const u32 n = 64 * j + l; vQ[j] = (n / 12) * 2688 + (n % 12) * 16 - 1024 * j; }
#pragma unroll
    for (int j = 0; j < 4; ++j) vH[j] = (8 * j + (l >> 3)) * 3584 + (((l & 7) ^ ((g & 1) << 2)) << 4) - 1024 * j;
#pragma unroll
    for (int j = 1; j < 7; ++j) so7[j] = rotl(j);
    sfor<4, NPRE>([&](auto K) { issue(IC<SC.pro[decltype(K)::v]>{}); });
    vW2(vC);
    vm_bar<SC.wA[0]>();
    sfor<NPRE, NPRO>([&](auto K) { issue(IC<SC.pro[decltype(K)::v]>{}); });
    sfor<0, 15>([&](auto SS) {
      constexpr int s = decltype(SS)::v;
      vm_bar<SC.wA[s + 1]>();
      sfor<0, (s == 14 ? 2 : MAXO)>([&](auto K) { issue(IC<SC.opA[s][decltype(K)::v]>{}); });
    });
    vm<SC.wL[0]>();
    prog_st(L, C_LAND + li, 1);
    vm<0>();
    prog_st(L, C_LAND + li, 2);
    cnt_wait(L, C_FQ, HOLD);
    sfor<2, MAXO>([&](auto K) { issue(IC<SC.opA[14][decltype(K)::v]>{}); });
    vm<SC.wL[1]>();
    prog_st(L, C_LAND + li, 3);
    lds_tag();
    prog_wait<true>(L, 20);
    issue(IC<OP(K_WA, 26)>{}); issue(IC<OP(K_WA, 27)>{});
    vm<0>();
    prog_st(L, C_LAND + li, 4);
    cnt_add(lb, C_WLO);
    if (w == 4) { STAMP(13); cnt_wait(L, C_FA, 5); STAMP(14); if (l == 0) st_flag(rCTL, WS_FH + (rb * 16 + cb) * FS, tag); }
  } else {
    if (stat) {
      bar();
      asm volatile("s_waitcnt vmcnt(0)" : "+v"(d[0]), "+v"(d[1]), "+v"(d[2]), "+v"(d[3]), "+v"(d[4]), "+v"(d[5]), "+v"(d[6])::"memory");
      const u32x2 ep = __builtin_amdgcn_raw_buffer_load_b64(rCTL, 0, 0, 16);
      float s1 = 0.f, s2 = 0.f;
#pragma unroll
      for (int r = 0; r < 2; ++r) {
        bar();
#pragma unroll
        for (int k = 14 * r; k < 14 * r + 14; ++k) { const u32 v = d[k >> 2][k & 3]; s1 = dot2(v, 0x3f803f80u, s1); s2 = dot2(v, v, s2); }
        PIN(s1); PIN(s2);
        SB();
      }
      tag = join(ep) + 1;
      bar();
      const float mean = sum8(s1) * (1.f / 448.f);
      const float var = fmaxf(sum8(s2) * (1.f / 448.f) - mean * mean, 0.f);
      const float sd = rbf(__builtin_amdgcn_sqrtf(rbf(rbf(var) + 1e-5f)));
      if ((l & 7) == 0) st16<16>(u32x4{pk(-mean, sd), (u32)tag, pk(-mean, sd), (u32)(tag >> 32)}, rCTL, WS_ST + srow * 16);
      bar();
    } else { const u32x2 ep = __builtin_amdgcn_raw_buffer_load_b64(rCTL, 0, 0, 16); bar(); bar(); bar(); bar(); tag = join(ep) + 1; bar(); }
    if (w == 6) {
      const u32 fp = WS_FC + (2 * cb + ((l >> 1) & 1)) * FS + (l & 1) * 4;
      int ss = 0, cs = 0;
      auto look = [&] {
        vm<0>();
        if (ss == 1) {
          const u32x4 a = lds_ld<u32x4>(L, L_ST + l * 16), b = lds_ld<u32x4>(L, L_ST + 1024 + l * 16);
          if (__all(a[1] == (u32)tag && a[3] == (u32)(tag >> 32) && b[1] == (u32)tag && b[3] == (u32)(tag >> 32))) { cnt_add(lb, C_ST); ss = 3; STAMP(33); }
        }
        if (ss < 3) {
          asm volatile("s_mov_b32 m0, %0\n\ts_nop 0\n\tbuffer_load_dwordx4 %1, %2, 0 offen sc1 lds\n\tbuffer_load_dwordx4 %1, %2, 0 offen offset:1024 sc1 lds" ::"s"(lb + L_ST), "v"(l * 16 + WS_ST + rb * 2048), "s"(rCTL) : "memory");
          ss = 1;
        }
        if (cs == 2) { cnt_add(lb, C_CSH); cs = 3; STAMP(37); }
        if (cs == 1 && __all(lds_ld<u32>(L, L_POLL + l * 4) == (u32)(l & 1 ? tag >> 32 : tag))) {
          asm volatile("s_mov_b32 m0, %0\n\ts_nop 0\n\tbuffer_load_dwordx4 %1, %2, 0 offen sc1 lds" ::"s"(lb + L_CSH), "v"(l * 16 + WS_CS + cb * 1024), "s"(rCTL) : "memory");
          cs = 2;
        }
        if (cs < 2) { asm volatile("s_mov_b32 m0, %0\n\ts_nop 0\n\tbuffer_load_dword %1, %2, 0 offen sc1 lds" ::"s"(lb + L_POLL), "v"(fp), "s"(rCTL) : "memory"); cs = 1; }
      };
#pragma unroll 1
      for (int s = 4; s <= 14; ++s) {
        wait_bar<63>();
        if (s == 6 && l < 30) lds_st<u32>(L, L_CNT + l * 4, l < 28 ? 0u : (u32)(l & 1 ? tag >> 32 : tag));
        if (s >= 7 && ss + cs != 6 && vm_le<0>()) look();
      }
#pragma unroll 1
      for (u32 n = 0; n < (1u << 17) && ss + cs != 6; ++n) look();
    } else {
      vm<0>();
      int k = 0;
      auto clear = [&] {
        st16<16>(u32x4{~0u, ~0u, ~0u, ~0u}, rAC, ((ai * 4 + (k >> 1)) * 144 + 112 + 2 * ah + (k & 1)) * 1024 + l * 16);
        st16<16>(u32x4{~0u, ~0u, ~0u, ~0u}, rAC, PX + ((rb * 7 + cc) * 4 + 2 * kh) * 4096 + k * 1024 + l * 16);
        ++k;
      };
#pragma unroll 1
      for (int s = 4; s <= 14; ++s) {
        bar();
        if (s >= CLR0 && k < 8 && vm_le<CLRQ>()) clear();
      }
      if (k < 8) cnt_wait(L, C_FQ, 4);
#pragma unroll 1
      while (k < 8) clear();
      vm<0>();
      cnt_add(lb, C_FA);
      if (l == 0) st_flag(rCTL, WS_FZ + (ai * 16 + ah) * FS, tag);
    }
  }
  lanes();
  const int aw = w - 4, hh = g >> 1;
  if (w >= 6) {
    const int e = l & 1, k = l < 14 ? l >> 1 : 0;
    const int ty = k == 0 ? 0 : (k < 4 ? 1 : 2), ro = k == 0 ? 0 : (k < 4 ? k - 1 : k - 4);
    const int rr = ai - ro < 0 ? 0 : ai - ro;
    if (!busy) { for (int k = 0; k < IDLE_SLP; ++k) __builtin_amdgcn_s_sleep(15); }
    if (busy) { cnt_wait(L, C_FQ, 4); if (w == 6) STAMP(12); if (w == 6 && l == 0) st_flag(rCTL, WS_FQ + (rb * 16 + cb) * FS, tag); }
    prog_wait<true>(L, 18);
#pragma unroll
    for (int k = 0; k < 3; ++k) lds_st<u32x2>(L, L_V + (6 * li + 2 * k + (l >> 5)) * 2048 + 1792 + (l & 31) * 8, u32x2{0x3f80u, 0u});
    if (w == 6) { spin<SLP>(rCTL, WS_FQ + (rr * 16 + (ty * 448 + 28 * ah + 27 * e) / 96) * FS, tag); cnt_add(lb, C_SQ); }
    else cnt_wait(L, C_SQ, 1);
    if (w == 6) STAMP(7);
#pragma unroll
    for (int e = 0; e < 2; ++e) {
#pragma unroll
      for (int j = 0; j < 6; ++j) {
        const int b = 2 * j + li, tb = 4 * ai - 8 + b < 0 ? 0 : 4 * ai - 8 + b;
        dma1792c(lb + (e ? L_V + b * 2048 : L_K + b * 1792), l * 16, rQKV, (tb * 336 + 112 + 112 * e + 7 * ah) * 256);
      }
      if (e == 0) {
#pragma unroll
        for (int j = 0; j < 2; ++j) dma1792c(lb + L_Q + (2 * li + j) * 2048, l * 16, rQKV, ((4 * ai + 2 * li + j) * 336 + 7 * ah) * 256);
        if (VLATE == 1) { vm<0>(); cnt_add(lb, C_KV); }
        if (VLATE > 1) vm<VLATE>();
      }
    }
    if (VLATE != 1) { vm<12>(); cnt_add(lb, C_KV); }
  }
  u32 oK = L_K + aw * 1792 + hh * 256 + l15 * 16 + (g & 1) * 8, oK3 = hh ? oK + 1280 : oK + 1536;
  u32 oQv = L_Q + aw * 2048 + hh * 256 + l15 * 16 + (g & 1) * 8;
  const int F = 7 * ah + 4 * (g & 1) + p4;
  u32 oV[2][2];
#pragma unroll
  for (int t = 0; t < 2; ++t)
#pragma unroll
    for (int e = 0; e < 2; ++e) { oV[t][e] = L_V + aw * 2048 + (4 * (g & 1) + p4) * 256 + (((8 * e + 4 * hh + q4) ^ ((F & 6) << 1)) << 4) + ((t ^ (F & 1)) << 3); PIN(oV[t][e]); }
  const int dq = (l & 31) - 4 * hh;
  u32 mk[8], vW[2];
#pragma unroll
  for (int r = 0; r < 16; r += 2) { mk[r >> 1] = (r & 3) + 1 + 8 * (r >> 2) <= dq ? 0xffffffffu : ((r & 3) + 8 * (r >> 2) <= dq ? 0xffffu : 0u); PIN(mk[r >> 1]); }
  vW2(vW);
  PIN(oK); PIN(oK3); PIN(oQv); PIN(vW[0]); PIN(vW[1]);
  cnt_wait(L, C_KV, 2);
  __builtin_amdgcn_s_setprio(PRIO_B);
  if (w == 4) STAMP(8);
  u32x2 qv[4];
#pragma unroll
  for (int k = 0; k < 4; ++k) qv[k] = lds_ld<u32x2>(L, oQv + k * 512);
  f32x16 sc[9];
  bf16x8 kf[9][2], vf[9][2];
  auto rdK = [&](int m) {
    const u32x2 a = lds_ld<u32x2>(L, oK + m * 1792), b = lds_ld<u32x2>(L, oK + m * 1792 + 512), c = lds_ld<u32x2>(L, oK + m * 1792 + 1024), d = lds_ld<u32x2>(L, oK3 + m * 1792);
    kf[m][0] = __builtin_bit_cast(bf16x8, u32x4{a[0], a[1], b[0], b[1]}); kf[m][1] = __builtin_bit_cast(bf16x8, u32x4{c[0], c[1], d[0], d[1]});
  };
  auto rdV = [&](int m) { vf[m][0] = trfrag(L, oV[0][0] + m * 2048, oV[0][1] + m * 2048); vf[m][1] = trfrag(L, oV[1][0] + m * 2048, oV[1][1] + m * 2048); };
  SB(); rdK(0); SB(); rdK(8); SB(); rdK(1); SB();
  if (hh) qv[3] = u32x2{0u, 0u};
  const bf16x8 qf[2] = {__builtin_bit_cast(bf16x8, u32x4{qv[0][0], qv[0][1], qv[1][0], qv[1][1]}), __builtin_bit_cast(bf16x8, u32x4{qv[2][0], qv[2][1], qv[3][0], qv[3][1]})};
  constexpr float NEG = -1e30f;
  const int m0 = 8 - 4 * ai - aw;
  float mx4[4];
  auto rowmax = [&](int m, bool first) {
#pragma unroll
    for (int k = 0; k < 4; ++k) {
      mx4[k] = first ? fmx(sc[m][4 * k], sc[m][4 * k + 1]) : fmx(fmx(mx4[k], sc[m][4 * k]), sc[m][4 * k + 1]);
      mx4[k] = fmx(fmx(mx4[k], sc[m][4 * k + 2]), sc[m][4 * k + 3]);
    }
    PIN(mx4[0]); PIN(mx4[1]); PIN(mx4[2]); PIN(mx4[3]);
  };
#pragma unroll
  for (int i = 0; i < 9; ++i) {
    const int m = i == 0 ? 0 : (i == 1 ? 8 : i - 1);
    if (i + 2 < 8) rdK(i + 2);
    sc[m] = mfma32(kf[m][0], qf[0], f32x16{});
    if (i == 3) {
      if (m0 > 0) {
        asm volatile("" ::: "memory");
#pragma unroll
        for (int r = 0; r < 16; ++r) sc[0][r] = NEG;
      }
#pragma unroll
      for (int r = 0; r < 16; ++r) sc[0][r] = (r & 3) + 8 * (r >> 2) <= dq ? sc[8][r] : sc[0][r];
    }
    if (i == 4) rowmax(0, true);
    sc[m] = mfma32(kf[m][1], qf[1], sc[m]);
    if (i >= 4 && i - 3 >= m0) rowmax(i - 3, false);
    SB();
  }
  if (w >= 6) { vm<0>(); cnt_add(lb, C_V); }
  if (busy) dman<2>(lb + L_Q + aw * 2048, vW, rW2, (kstep(52) + aw) * (16 * 896) + cc * 128);
  if (6 >= m0) rowmax(6, false);
  cnt_wait(L, C_V, 2);
  rdV(1); rdV(2);
  SB();
  if (7 >= m0) rowmax(7, false);
  const float mx0 = fmx(fmx(mx4[0], mx4[1]), fmx(mx4[2], mx4[3]));
  const auto ms = __builtin_amdgcn_permlane32_swap(__float_as_uint(mx0), __float_as_uint(mx0), false, false);
  const float mx = fmx(__uint_as_float(ms[0]), __uint_as_float(ms[1]));
  SB();
  f32x16 o;
  u32x4 pq[2];
#pragma unroll
  for (int i = 0; i < 16; ++i) {
    const int m = ((i >> 1) + 1) & 7, s = i & 1;
    if (!s) { if (i < 10) rdV((i >> 1) + 3); else if (i == 10) rdV(0); else if (i == 12) rdV(8); }
    if (i == 6) { vm<0>(); cnt_add(lb, C_LATE); }
    const float mm = m != 0 && m < m0 ? -NEG : mx;
    f32x2 d[4];
#pragma unroll
    for (int j = 0; j < 4; ++j) { d[j] = f32x2{sc[m][8 * s + 2 * j], sc[m][8 * s + 2 * j + 1]} - f32x2{mm, mm}; PIN(d[j]); }
#pragma unroll
    for (int j = 0; j < 2; ++j) { d[j][0] = __builtin_amdgcn_exp2f(d[j][0]); d[j][1] = __builtin_amdgcn_exp2f(d[j][1]); PIN(d[j]); }
    SB();
    if (i >= 1 && i < 15) o = mfma32(vf[((i - 1) >> 1) + 1][s ^ 1], __builtin_bit_cast(bf16x8, pq[s ^ 1]), i == 1 ? f32x16{} : o);
    if (i == 15) {
      const u32x4 k4 = {mk[0], mk[1], mk[2], mk[3]};
      o = mfma32(vf[0][0], __builtin_bit_cast(bf16x8, pq[0] & ~k4), o);
      o = mfma32(vf[8][0], __builtin_bit_cast(bf16x8, pq[0] & k4), o);
    }
    SB();
#pragma unroll
    for (int j = 2; j < 4; ++j) { d[j][0] = __builtin_amdgcn_exp2f(d[j][0]); d[j][1] = __builtin_amdgcn_exp2f(d[j][1]); }
#pragma unroll
    for (int j = 0; j < 4; ++j) pq[s][j] = pk(d[j][0], d[j][1]);
    PIN(pq[s]);
    SB();
  }
  {
    const u32x4 k4 = {mk[4], mk[5], mk[6], mk[7]};
    o = mfma32(vf[0][1], __builtin_bit_cast(bf16x8, pq[1] & ~k4), o);
    o = mfma32(vf[8][1], __builtin_bit_cast(bf16x8, pq[1] & k4), o);
  }
  SB();
  cnt_add(lb, C_ATT);
  const auto dn = __builtin_amdgcn_permlane32_swap(__float_as_uint(o[12]), __float_as_uint(o[12]), false, false);
  const float inv = __builtin_amdgcn_rcpf(__uint_as_float(dn[1]));
  const float in2 = hh ? 0.f : inv;
  const u32 so = ((ai * 4 + aw) * 144 + 112 + 2 * ah) * 1024;
  st16<16>(u32x4{pk(o[0] * inv, o[1] * inv), pk(o[2] * inv, o[3] * inv), pk(o[4] * inv, o[5] * inv), pk(o[6] * inv, o[7] * inv)}, rAC, l * 16 + so);
  st16<16>(u32x4{pk(o[8] * inv, o[9] * inv), pk(o[10] * inv, o[11] * inv), pk(o[12] * in2, o[13] * in2), pk(o[14] * in2, o[15] * in2)}, rAC, l * 16 + so + 1024);
  if (w == 4) STAMP(9);
  vm<0>();
  if (last_of4(L, C_FB)) {
    if (l == 0) st_flag(rCTL, WS_FB + (ai * 16 + ah) * FS, tag);
    if (blockIdx.x == 255) {
#pragma unroll 1
      for (int k = 0; k < 4; ++k) spin<32>(rCTL, WS_FB + (k * 64 + l) * FS, tag);
      if (l == 0) st_flag(rCTL, 0, tag);
    }
  }
  FLUSH();
}

extern "C" void kernel_launch(void* const* d_in, const int* in_sizes, int n_in, void* d_out, int out_size, void* d_ws, size_t ws_size, hipStream_t stream) {
  fused_block<<<256, 512, 0, stream>>>((const char*)d_in[0], (const char*)d_in[1], (const char*)d_in[2], (const char*)d_in[4], (const char*)d_in[5],
                                       (const char*)d_in[6], (const char*)d_in[7], (char*)d_out, (char*)d_ws + WS_OFF);
}
```

```cpp
#include <hip/hip_runtime.h>
#include <hip/hip_bf16.h>
#include <stdint.h>

#define DI __device__ __forceinline__
#define LAS __attribute__((address_space(3)))
typedef unsigned u32;
typedef unsigned long long u64;
typedef __bf16 bf16x8 __attribute__((ext_vector_type(8)));
typedef __bf16 bf16x2 __attribute__((ext_vector_type(2)));
typedef short s16x4 __attribute__((ext_vector_type(4)));
typedef float f32x4 __attribute__((ext_vector_type(4)));
typedef float f32x2 __attribute__((ext_vector_type(2)));
typedef float f32x16 __attribute__((ext_vector_type(16)));
typedef unsigned u32x4 __attribute__((ext_vector_type(4)));
typedef unsigned u32x2 __attribute__((ext_vector_type(2)));
typedef __amdgpu_buffer_rsrc_t rsrc_t;

template <int N> struct IC { static constexpr int v = N; };
template <int B, int E, class F> DI void sfor(F&& f) { if constexpr (B < E) { f(IC<B>{}); sfor<B + 1, E>(f); } }

constexpr int CAP_1 = 8;
constexpr int NPRE = 6, NPRO = 8;
constexpr int DA = 12;
constexpr int TB = 16, T_O = 44, T_PP = 62, T_WH = 20, T_WO = 33, OWN1 = 4;
constexpr int S_FQ = 18, HNOW = 2, CLR0 = 4;
constexpr int CODE_KB = 39, PF_DELAY = 8, IDLE_SLP = 9, SLP = 2, LSLP = 1;

constexpr u32 FS = 128;
constexpr u32 WS_FH = FS, WS_FQ = WS_FH + 256 * FS, WS_FB = WS_FQ + 256 * FS, WS_FG = WS_FB + 256 * FS, WS_FC = WS_FG + 704 * FS;
constexpr u32 WS_FZ = WS_FC + 160 * FS, WS_CS = WS_FZ + 287 * FS, WS_ST = WS_CS + 16384, CTL_BYTES = WS_ST + 32768;
constexpr u32 WS_QKV = CTL_BYTES, QKV_BYTES = 64u * 336u * 256u;
constexpr u32 WS_AC = WS_QKV + QKV_BYTES, AC_BYTES = 64u * 144u * 1024u + 448u * 4096u;
constexpr u32 PX = 64u * 144u * 1024u;
constexpr u32 RING = 7 * 16384;
constexpr u32 L_ST = 40960, L_POLL = 45056, L_CSQ = 46080, L_CSH = 46848, L_CNT = 48896;
constexpr u32 L_K = 0, L_V = 21504, L_Q = RING + 5 * 8192;
constexpr u32 L_WC = 49152, L_RED = 0;
enum { C_DONE, C_LAND = 4, C_FQ = 6, C_FA, C_FG, C_KV, C_FB, C_WLO, C_WHI, C_P30, C_WO, C_ATT, C_LATE, C_PAIR0, C_PAIR1, C_CSH, C_FH, C_SQ, C_V, C_ST, C_CS0, C_N };
constexpr u32 stage_addr(int s) {
  return s < 14 ? RING + (s % 6) * 6144u : s < 19 ? (s - 14) * 8192u : s < 21 ? RING + (s - 19) * 8192u : s < 23 ? 6 * 16384u + (s - 21) * 8192u : s < 26 ? RING + (s - 21) * 8192u : RING + (s - 26) * 8192u;
}
constexpr int NWP = 24;

enum { K_NONE = 0, K_X, K_WA, K_WCP };
constexpr int OP(int k, int a, int e = 0) { return k * 256 + 2 * a + e; }
constexpr int opn(int op) { int k = op / 256, a = (op % 256) / 2; return k == K_NONE ? 0 : k == K_X ? 4 : k == K_WA ? (a < 14 ? 3 : 4) : 2; }
constexpr int MAXO = 24;
struct Sched {
  int pro[NPRO], opA[15][MAXO];
  int wA[16];
  int wL[2];
  int wC[56], a0[56], a1[56];
  int ok;
};
constexpr int cl(int n) { return n > 63 ? 63 : n; }
constexpr Sched make_sched() {
  Sched r{};
  r.ok = 1;
  int n = 0, iW[26] = {}, iX[7] = {};
  int lo[128] = {}, le[128] = {}, nl = 0;
  for (int s = 0; s < 26; ++s) {
    if (s < 14 && !(s & 1)) { lo[nl] = OP(K_X, s >> 1, 0); le[nl++] = -1; lo[nl] = OP(K_X, s >> 1, 1); le[nl++] = -1; }
    lo[nl] = OP(K_WA, s); le[nl++] = s < 14 ? s - 5 : (s < 19 ? 5 : (s == 21 || s == 22 ? 13 : 14));
  }
  for (int k = 0; k < NWP / 2; ++k) { lo[nl] = OP(K_WCP, k); le[nl++] = 14; }
  bool done[128] = {};
  auto note = [&](int op) { n += opn(op); int k = op / 256, a = (op % 256) / 2; if (k == K_X) iX[a] = (op & 1) ? n : 0; if (k == K_WA) iW[a] = n; };
  for (int k = 0; k < NPRO; ++k) { if (k == NPRE) r.wA[0] = cl(n - iW[0]); r.pro[k] = lo[k]; done[k] = true; note(lo[k]); }
  for (int s = 0; s < 15; ++s) {
    int need = iW[s + 1]; if (!need) r.ok = 0;
    if (s + 1 <= 13) { int v = iX[(s + 1) >> 1]; if (!v) r.ok = 0; if (v > need) need = v; }
    r.wA[s + 1] = cl(n - need);
    int cap = s == 14 ? 64 : CAP_1, c = 0, used = 0;
    for (int k = 0; k < nl; ++k) if (!done[k] && le[k] <= s && used + opn(lo[k]) <= cap && c < MAXO) { done[k] = true; r.opA[s][c++] = lo[k]; used += opn(lo[k]); note(lo[k]); if (s == 14 && c == 2) r.wL[0] = cl(n - iW[18]); }
  }
  r.wL[1] = cl(n - iW[25]);
  if (r.opA[14][0] != OP(K_WA, 19)) r.ok = 0;
  for (int k = 0; k < nl; ++k) if (!done[k]) r.ok = 0;
  n = 0;
  int iA[56] = {}, iP = 0, iH = 0, iO = 0, nA = 8 + DA;
  for (int m = 8; m < 8 + DA; ++m) { n += 1; iA[m] = n; }
  for (int s = 8; s < 56; ++s) {
    int need = iA[s]; if (!need) r.ok = 0;
    if (s == TB && iP > need) need = iP;
    if (s == T_WH && iH > need) need = iH;
    if (s == T_WO + 12 && iO > need) need = iO;
    r.wC[s] = cl(n - need);
    if (s == TB - 3) { n += 1; iP = n; }
    r.a0[s] = nA;
    for (int c = 0; c < 2 && nA < 56 && nA <= s + DA && (nA < 32 || s >= TB); ++c) { n += 1; iA[nA++] = n; }
    r.a1[s] = nA;
    if (s < 15) { n += 2; iH = n; }
    if (s >= T_WO && s < T_WO + 7) { n += 2; iO = n; }
    if (s >= T_O && s < T_O + 8) n += 2;
    if (s == T_O + 8) n += 3;
  }
  return r;
}
constexpr Sched SC = make_sched();
static_assert(SC.ok, "schedule");

DI rsrc_t mkrs(const void* p, u32 bytes) { return __builtin_amdgcn_make_buffer_rsrc((void*)p, 0, bytes, 0x00020000); }
template <int N> DI void dman(u32 lds, const u32* v, rsrc_t rs, u32 soff) {
  if constexpr (N == 4) asm volatile("s_mov_b32 m0, %0\n\ts_nop 0\n\tbuffer_load_dwordx4 %1, %5, %6 offen lds\n\tbuffer_load_dwordx4 %2, %5, %6 offen offset:1024 lds\n\t"
    "buffer_load_dwordx4 %3, %5, %6 offen offset:2048 lds\n\tbuffer_load_dwordx4 %4, %5, %6 offen offset:3072 lds" ::"s"(lds), "v"(v[0]), "v"(v[1]), "v"(v[2]), "v"(v[3]), "s"(rs), "s"(soff) : "memory");
  if constexpr (N == 3) asm volatile("s_mov_b32 m0, %0\n\ts_nop 0\n\tbuffer_load_dwordx4 %1, %4, %5 offen lds\n\tbuffer_load_dwordx4 %2, %4, %5 offen offset:1024 lds\n\t"
    "buffer_load_dwordx4 %3, %4, %5 offen offset:2048 lds" ::"s"(lds), "v"(v[0]), "v"(v[1]), "v"(v[2]), "s"(rs), "s"(soff) : "memory");
  if constexpr (N == 2) asm volatile("s_mov_b32 m0, %0\n\ts_nop 0\n\tbuffer_load_dwordx4 %1, %3, %4 offen lds\n\tbuffer_load_dwordx4 %2, %3, %4 offen offset:1024 lds" ::"s"(lds), "v"(v[0]), "v"(v[1]), "s"(rs), "s"(soff) : "memory");
  if constexpr (N == 1) asm volatile("s_mov_b32 m0, %0\n\ts_nop 0\n\tbuffer_load_dwordx4 %1, %2, %3 offen lds" ::"s"(lds), "v"(v[0]), "s"(rs), "s"(soff) : "memory");
}
DI void dma1792c(u32 lds, u32 voff, rsrc_t rs, u32 soff) {
  asm volatile("s_mov_b32 m0, %0\n\ts_nop 0\n\tbuffer_load_dwordx4 %1, %2, %3 offen sc1 lds\n\ts_mov_b32 exec_hi, 0xffff\n\t"
               "buffer_load_dwordx4 %1, %2, %3 offen offset:1024 sc1 lds\n\ts_mov_b32 exec_hi, -1" ::"s"(lds), "v"(voff), "s"(rs), "s"(soff) : "memory");
}
DI void dma4(u32 lds, u32 voff, rsrc_t rs, u32 soff) {
  asm volatile("s_mov_b32 m0, %0\n\ts_nop 0\n\tbuffer_load_dword %1, %2, %3 offen lds" ::"s"(lds), "v"(voff), "s"(rs), "s"(soff) : "memory");
}
template <bool C, int IMM = 0> DI u32x4 ld16_async(u32 voff, rsrc_t rs, u32 soff) {
  u32x4 r;
  if constexpr (C) asm volatile("buffer_load_dwordx4 %0, %1, %2, %3 offen offset:%4 sc1" : "=v"(r) : "v"(voff), "s"(rs), "s"(soff), "n"(IMM) : "memory");
  else asm volatile("buffer_load_dwordx4 %0, %1, %2, %3 offen" : "=v"(r) : "v"(voff), "s"(rs), "s"(soff) : "memory");
  return r;
}
DI u32x2 ld8c_async(u32 voff, rsrc_t rs) {
  u32x2 r; asm volatile("buffer_load_dwordx2 %0, %1, %2, 0 offen sc1" : "=v"(r) : "v"(voff), "s"(rs) : "memory"); return r;
}
template <int N> DI void wait_bar() { asm volatile("s_waitcnt vmcnt(%0) lgkmcnt(0)\n\ts_barrier" ::"n"(N) : "memory"); }
template <int N> DI void vm_bar() { asm volatile("s_waitcnt vmcnt(%0)\n\ts_barrier" ::"n"(N) : "memory"); }
template <int N> DI void vm() { asm volatile("s_waitcnt vmcnt(%0)" ::"n"(N) : "memory"); }
DI void bar() { asm volatile("s_barrier" ::: "memory"); }
#define PIN(x) asm volatile("" : "+v"(x))
#define SB() __builtin_amdgcn_sched_barrier(0)
DI f32x4 mfma(bf16x8 a, bf16x8 b, f32x4 c) { return __builtin_amdgcn_mfma_f32_16x16x32_bf16(a, b, c, 0, 0, 0); }
DI f32x16 mfma32(bf16x8 a, bf16x8 b, f32x16 c) { return __builtin_amdgcn_mfma_f32_32x32x16_bf16(a, b, c, 0, 0, 0); }
DI u32 pk(float a, float b) { bf16x2 v = {(__bf16)a, (__bf16)b}; return __builtin_bit_cast(u32, v); }
DI float fmx(float a, float b) { return __builtin_elementwise_maximum(a, b); }
DI float rbf(float a) { return (float)(__bf16)a; }
DI float bflo(u32 v) { return __uint_as_float(v << 16); }
DI float bfhi(u32 v) { return __uint_as_float(v & 0xffff0000u); }
DI float dot2(u32 a, u32 b, float c) { return __builtin_amdgcn_fdot2_f32_bf16(__builtin_bit_cast(bf16x2, a), __builtin_bit_cast(bf16x2, b), c, false); }
DI float sum8(float v) {
  v += __builtin_bit_cast(float, __builtin_amdgcn_update_dpp(0, __builtin_bit_cast(int, v), 0xb1, 0xf, 0xf, true));
  v += __builtin_bit_cast(float, __builtin_amdgcn_update_dpp(0, __builtin_bit_cast(int, v), 0x4e, 0xf, 0xf, true));
  v += __builtin_bit_cast(float, __builtin_amdgcn_update_dpp(0, __builtin_bit_cast(int, v), 0x141, 0xf, 0xf, true));
  return v;
}
template <class T> DI T lds_ld(const LAS char* base, u32 off) { return *(const LAS T*)(base + off); }
template <class T> DI void lds_st(LAS char* base, u32 off, T v) { *(LAS T*)(base + off) = v; }
DI bf16x8 trfrag(LAS char* base, u32 off0, u32 off1) {
  s16x4 lo = __builtin_amdgcn_ds_read_tr16_b64_v4i16((LAS s16x4*)(base + off0));
  s16x4 hi = __builtin_amdgcn_ds_read_tr16_b64_v4i16((LAS s16x4*)(base + off1));
  return __builtin_bit_cast(bf16x8, __builtin_shufflevector(lo, hi, 0, 1, 2, 3, 4, 5, 6, 7));
}
template <int AUX> DI void st16(u32x4 v, rsrc_t rs, u32 voff) { __builtin_amdgcn_raw_buffer_store_b128(v, rs, voff, 0, AUX); }
DI void st_flag(rsrc_t rc, u32 off, u64 v) { __builtin_amdgcn_raw_buffer_store_b64(u32x2{(u32)v, (u32)(v >> 32)}, rc, off, 0, 16); }
DI bool tagged(u32x2 v, u64 tag) { return __all(v[0] == (u32)tag && v[1] == (u32)(tag >> 32)); }
template <int SLEEP> DI void spin(rsrc_t rc, u32 off, u64 tag) {
#pragma unroll 1
  for (u32 n = 0; n < (1u << 17); ++n) {
    asm volatile("" ::: "memory");
    if (tagged(__builtin_amdgcn_raw_buffer_load_b64(rc, off, 0, 16), tag)) break;
    __builtin_amdgcn_s_sleep(SLEEP);
  }
  asm volatile("" ::: "memory");
}
DI u32 cnt_ld(LAS char* L, int k) { return __hip_atomic_load((LAS u32*)(L + L_CNT + 4 * k), __ATOMIC_RELAXED, __HIP_MEMORY_SCOPE_WORKGROUP); }
DI void cnt_wait(LAS char* L, int k, u32 v) {
#pragma unroll 1
  for (u32 n = 0; n < (1u << 20); ++n) {
    if ((u32)__builtin_amdgcn_readfirstlane(cnt_ld(L, k)) >= v) break;
    __builtin_amdgcn_s_sleep(LSLP);
  }
  asm volatile("" ::: "memory");
}
DI void prog_st(LAS char* L, int k, u32 v) { asm volatile("" ::: "memory"); __hip_atomic_store((LAS u32*)(L + L_CNT + 4 * k), v, __ATOMIC_RELAXED, __HIP_MEMORY_SCOPE_WORKGROUP); asm volatile("" ::: "memory"); }
DI u32 land_ld(LAS char* L) { const u64 v = __hip_atomic_load((LAS u64*)(L + L_CNT + 4 * C_LAND), __ATOMIC_RELAXED, __HIP_MEMORY_SCOPE_WORKGROUP); return min((u32)v, (u32)(v >> 32)); }
DI u32 done_ld(LAS char* L) {
  const u64 a = __hip_atomic_load((LAS u64*)(L + L_CNT), __ATOMIC_RELAXED, __HIP_MEMORY_SCOPE_WORKGROUP), b = __hip_atomic_load((LAS u64*)(L + L_CNT + 8), __ATOMIC_RELAXED, __HIP_MEMORY_SCOPE_WORKGROUP);
  return min(min((u32)a, (u32)(a >> 32)), min((u32)b, (u32)(b >> 32)));
}
template <bool DONE> DI void prog_wait(LAS char* L, u32 v) {
#pragma unroll 1
  for (u32 n = 0; n < (1u << 20); ++n) {
    if ((u32)__builtin_amdgcn_readfirstlane(DONE ? done_ld(L) : land_ld(L)) >= v) break;
    __builtin_amdgcn_s_sleep(LSLP);
  }
  asm volatile("" ::: "memory");
}
DI void cnt_add(u32 lb, int k) {
  asm volatile("s_mov_b64 exec, 1\n\tds_add_u32 %0, %1\n\ts_mov_b64 exec, -1" ::"v"(lb + L_CNT + 4 * k), "v"(1u) : "memory");
}
template <int N> DI bool vm_le() {
  u32 r; asm volatile("s_getreg_b32 %0, hwreg(HW_REG_IB_STS)" : "=s"(r)::"memory");
  return (r & 0xc0000fu) <= (u32)N;
}
DI bool last_of4(LAS char* L, int k) {
  u32 old = 0;
  asm volatile("" ::: "memory");
  if (__builtin_amdgcn_mbcnt_hi(~0u, __builtin_amdgcn_mbcnt_lo(~0u, 0u)) == 0) old = __hip_atomic_fetch_add((LAS u32*)(L + L_CNT + 4 * k), 1u, __ATOMIC_RELAXED, __HIP_MEMORY_SCOPE_WORKGROUP);
  asm volatile("" ::: "memory");
  return __builtin_amdgcn_readfirstlane(old) == 3;
}

__global__ __launch_bounds__(512, 2) void fused_block(const char* __restrict__ x, const char* __restrict__ wqkv, const char* __restrict__ wo,
                                                      const char* __restrict__ w1, const char* __restrict__ b1, const char* __restrict__ w2,
                                                      const char* __restrict__ b2, char* __restrict__ out, char* ws) {
  __shared__ __attribute__((aligned(1024))) char smem[163840];
  asm volatile("" : "+s"(x), "+s"(wqkv), "+s"(wo), "+s"(w1), "+s"(b1), "+s"(w2), "+s"(b2), "+s"(out), "+s"(ws));
  u64 pc; asm volatile("s_getpc_b64 %0" : "=s"(pc));
  LAS char* const L = (LAS char*)smem;
  const u32 lb = (u32)(uintptr_t)smem;
  int l = threadIdx.x & 63, g, q4, p4, l15;
  auto lanes = [&] { PIN(l); g = l >> 4; q4 = (l >> 2) & 3; p4 = l & 3; l15 = l & 15; };
  lanes();
  int w = __builtin_amdgcn_readfirstlane(threadIdx.x >> 6);
  asm volatile("s_nop 4" : "+s"(w));
  const int x8 = blockIdx.x & 7, rk = blockIdx.x >> 3;
  const bool busy = rk < 28;
  const rsrc_t rQKV = mkrs(ws + WS_QKV, QKV_BYTES), rAC = mkrs(ws + WS_AC, AC_BYTES), rCTL = mkrs(ws, CTL_BYTES);
  const rsrc_t rX = mkrs(x, 2048 * 896), rW2 = mkrs(w2, 1792 * 896);
  const int kh = x8 >> 2, cc = rk % 7, rb = (x8 & 3) * 4 + rk / 7, cb = kh * 7 + cc;
  const int ai = rk & 15, ah = 2 * x8 + (rk >> 4);
  auto kstep = [&](int p) {
    if (p < 8) return 8 * cb + p;
    const int t = cc + 1 + ((p - 8) % 24) / 4;
    return 8 * (7 * kh + (t >= 7 ? t - 7 : t)) + (p & 3) + (p >= 32 ? 4 : 0);
  };
  if (busy && (w == 4 || w == 5)) {
    u32 v[4];
#pragma unroll
    for (int j = 0; j < 4; ++j) v[j] = (8 * j + (l >> 3)) * 896 + (((l & 7) ^ ((j & 1) * 4 + g)) << 4) - 1024 * j;
    dman<4>(lb + (w & 1) * 8192, v, rX, (rb * 128 + 64 * (w & 1)) * 896);
    dman<4>(lb + (w & 1) * 8192 + 4096, v, rX, (rb * 128 + 64 * (w & 1) + 32) * 896);
    u32 vq[3];
#pragma unroll
    for (int j = 0; j < 3; ++j) { const u32 n = 64 * j + l; vq[j] = (n / 12) * 2688 + (n % 12) * 16 - 1024 * j; }
#pragma unroll
    for (int a = 0; a < 2; ++a) {
      dman<3>(lb + stage_addr(a) + (w & 1) * 3072, vq, mkrs(wqkv, 448 * 2688), cb * 192 + (16 * (w & 1) + 32 * a) * 2688);
    }
  }
  if (w == 6) dma4(lb + L_Q + 512, (rk * 32 + (l & 31)) * 64, mkrs((const void*)(pc & ~255ull), CODE_KB * 1024), 0);
  u32x4 d[14];
  const bool stat = busy && (w == 6 || (w == 7 && cb < 2));
  const int srow = rb * 128 + (w == 6 ? cb : 14 + cb) * 8 + (l >> 3);
  if (stat) {
#pragma unroll
    for (int k = 0; k < 7; ++k) d[k] = ld16_async<false>(srow * 896 + (l & 7) * 16, rX, k * 128);
  }
  if (busy && w == 7) {
    const int r = rb * 28 + (l >> 1);
    if (l < 56) dma4(lb + L_Q, r * 2688 + cb * 192 + (l & 1) * 188, mkrs(wqkv, 448 * 2688), 0);
  }
  SB();
  u64 tag;
  auto join = [](u32x2 t) { return (u64)(u32)__builtin_amdgcn_readfirstlane(t[0]) | (u64)(u32)__builtin_amdgcn_readfirstlane(t[1]) << 32; };
  auto epoch = [&] { tag = join(__builtin_amdgcn_raw_buffer_load_b64(rCTL, 0, 0, 16)) + 1; };
  auto lds_tag = [&] { tag = join(lds_ld<u32x2>(L, L_CNT + 112)); };
  auto vW2 = [&](u32* v) { v[0] = (l >> 3) * 896 + (((l & 7) ^ ((g & 1) << 2)) << 4); v[1] = v[0] + 7168 - 1024; };

  if (w < 4) {
    if (!busy) {
      const int q = (rk - 28) * 4 + w;
      bar();
      for (int k = 0; k < PF_DELAY; ++k) __builtin_amdgcn_s_sleep(2);
      const rsrc_t rWo = mkrs(wo, 448 * 896);
      const u32 v = l * 16;
#pragma unroll 1
      for (int k = 0; k < 49; ++k) {
        int r = k * 16 + q + (x8 & 3) * 196; r = r >= 784 ? r - 784 : r;
        dman<1>(lb + L_WC + w * 16384 + (k & 15) * 1024, &v, rW2, kh * 802816 + r * 1024);
        vm<10>();
      }
#pragma unroll 1
      for (int k = 0; k < 25; ++k) { dman<1>(lb + L_WC + w * 16384 + (k & 15) * 1024, &v, rWo, (k * 16 + q) * 1024); vm<10>(); }
      vm<0>();
      return;
    }
    constexpr int PF = 4;
    u32 oX[4], oH[2], oQ[3];
#pragma unroll
    for (int p = 0; p < 4; ++p) oX[p] = (4 * w + ((l & 31) >> 3)) * 1024 + (l & 7) * 128 + (((2 * p + (g >> 1)) ^ (l15 >> 1)) << 4);
    const int rq = w == 3 ? 0 : w;
#pragma unroll
    for (int i = 0; i < 3; ++i) oQ[i] = RING + (8 * (g >> 1) + q4) * 192 + ((i + rq) % 3) * 64 + (g & 1) * 32 + p4 * 8;
#pragma unroll
    for (int i = 0; i < 2; ++i) oH[i] = RING + (g >> 1) * 1024 + q4 * 128 + ((i ^ (q4 >> 1)) << 6) + (g & 1) * 32 + p4 * 8;
    const u32 selv = l15 == (g & 1) ? 0x3f803f80u : 0u;
    const bf16x8 sel = __builtin_bit_cast(bf16x8, u32x4{selv, selv, selv, selv});
    bf16x8 xf[28], wf[8];
    f32x16 aH[2], aQ[3] = {};
    f32x4 cs = {};
    u32x2 ch[2];
    float rs;
    bf16x8 lnb;
    u32x4 hA[72], qp[6];
    auto rdW = [&](auto SS, auto II) {
      constexpr int s = decltype(SS)::v, i = decltype(II)::v;
      constexpr int slot = (s < 14 ? s * 6 + i : 84 + i) % 8;
      if constexpr (s < 14) { const u32 o = oQ[i % 3] + (stage_addr(s) - RING) + (i / 3) * 3072; wf[slot] = trfrag(L, o, o + 768); }
      else if constexpr (s < 28) { const u32 o = oH[i & 1] + (stage_addr(s) - RING) + (i >> 1) * 2048; wf[slot] = trfrag(L, o, o + 512); }
    };
    auto rdX = [&](auto SS, auto EE) { constexpr int k = 2 * decltype(SS)::v + decltype(EE)::v; if constexpr (k < 28) xf[k] = lds_ld<bf16x8>(L, oX[k & 3] + (k >> 2) * 16384); };
    auto rdcs = [&](int half) {
#pragma unroll
      for (int i = 0; i < 2; ++i) ch[i] = lds_ld<u32x2>(L, L_CSH + (half * 64 + i * 32 + (l & 31)) * 8);
    };
    auto hpack = [&](int half, int c) {
      u32x4 hp;
#pragma unroll
      for (int j = 0; j < 4; ++j) {
        const float t0 = aH[c >> 1][8 * (c & 1) + 2 * j], t1 = aH[c >> 1][8 * (c & 1) + 2 * j + 1];
        hp[j] = pk(t0 * fmx(t0, 0.f), t1 * fmx(t1, 0.f));
      }
      hA[half * 4 + c] = hp;
    };
    auto hstore = [&](int m) { st16<16>(hA[m], rAC, l * 16 + ((rb * 4 + w) * 144 + cb * 8 + m) * 1024); };
    auto qpack = [&](int c) {
      const int i = c >> 1, e = c & 1, tq = 6 * cb + 2 * ((i + rq) % 3) + e;
      const float m = tq < 28 ? rs * (0.18898223650461361f * 1.4426950408889634f) : rs;
      const auto s0 = __builtin_amdgcn_permlane16_swap(pk(aQ[i][8 * e] * m, aQ[i][8 * e + 1] * m), pk(aQ[i][8 * e + 4] * m, aQ[i][8 * e + 5] * m), false, false);
      const auto s1 = __builtin_amdgcn_permlane16_swap(pk(aQ[i][8 * e + 2] * m, aQ[i][8 * e + 3] * m), pk(aQ[i][8 * e + 6] * m, aQ[i][8 * e + 7] * m), false, false);
      qp[c] = u32x4{s0[0], s1[0], s0[1], s1[1]};
    };
    auto qstore = [&](int c) {
      const int fl = ((g & 1) << 1) | (g >> 1), tq = 6 * cb + 2 * (((c >> 1) + rq) % 3) + (c & 1);
      u32x4 v = qp[c];
      u32 vo = fl * 256 + l15 * 16;
      if (tq >= 56) {
        asm volatile("" ::: "memory");
        if (fl & 1) v = u32x4{v[2], v[3], v[0], v[1]};
        vo ^= (2 * (tq & 1) + (fl >> 1)) << 6;
      }
      st16<16>(v, rQKV, vo + ((rb * 4 + w) * 336 + 4 * tq) * 256);
    };
    bool sigq = false, siga = false;
    const bool giver = (w >> 1) != kh;
    bar();
    rdX(IC<0>{}, IC<0>{}); rdX(IC<0>{}, IC<1>{});
    sfor<0, PF>([&](auto I) { rdW(IC<0>{}, I); });
    sfor<0, 28>([&](auto SS) {
      constexpr int s = decltype(SS)::v, NT = s < 14 ? 6 : 8;
      if constexpr (s == 14) wait_bar<63>();
      else if constexpr (s < 14) bar();
      if constexpr (s == 13) lanes();
      if constexpr (s == 14) {
        u32 cq[3];
#pragma unroll
        for (int i = 0; i < 3; ++i) cq[i] = lds_ld<u32>(L, L_CSQ + (((i + rq) % 3) * 32 + (l & 31)) * 4);
        const u32 rdy = cnt_ld(L, C_ST);
        asm volatile("" ::: "memory");
        u32 st = lds_ld<u32>(L, L_ST + (w * 32 + (l & 31)) * 16);
        if (!__builtin_amdgcn_readfirstlane(rdy)) { cnt_wait(L, C_ST, 1); st = lds_ld<u32>(L, L_ST + (w * 32 + (l & 31)) * 16); }
        SB();
        lnb = __builtin_bit_cast(bf16x8, u32x4{l < 32 ? (st & 0xffffu) * 0x10001u : 0u, l < 32 ? st >> 16 : 0u, 0u, 0u});
        rs = __builtin_amdgcn_rcpf(bfhi(st));
#pragma unroll
        for (int i = 0; i < 3; ++i) aQ[i] = mfma32(__builtin_bit_cast(bf16x8, u32x4{cq[i], 0u, 0u, 0u}), lnb, aQ[i]);
        SB();
#pragma unroll
        for (int c = 0; c < 6; ++c) { qpack(c); qstore(c); SB(); }
      }
      if constexpr (s == 21) {
        rdcs(1);
        SB();
#pragma unroll
        for (int c = 0; c < 4; ++c) { hpack(0, c); if (c < HNOW) hstore(c); SB(); }
      }
      u32 land;
      sfor<0, NT>([&](auto II) {
        constexpr int i = decltype(II)::v, slot = (s < 14 ? s * 6 + i : 84 + i) % 8;
        constexpr u32 need = s == 15 ? 1 : s == 18 ? 2 : s == 22 ? 3 : s == 25 ? 4 : 0;
        if constexpr (need && i == 0) land = land_ld(L);
        if constexpr (need && i == NT - PF) {
          if ((u32)__builtin_amdgcn_readfirstlane(land) < need) prog_wait<false>(L, need);
          asm volatile("" ::: "memory");
        }
        if constexpr (i + PF < NT) rdW(SS, IC<i + PF>{});
        else rdW(IC<s + 1>{}, IC<i + PF - NT>{});
        if constexpr (i < 2) rdX(IC<s + 1>{}, II);
        if constexpr (s < 14) {
          aQ[i % 3] = mfma32(wf[slot], xf[2 * s + i / 3], aQ[i % 3]);
          if constexpr (i % 3 == 0) cs = mfma(wf[slot], sel, cs);
          if constexpr (s == 13 && i == 4) {
            u32x4 v;
#pragma unroll
            for (int j = 0; j < 4; ++j) { const float hi = rbf(cs[j]); v[j] = pk(hi, cs[j] - hi); }
            if (l15 < 2) lds_st<u32x4>(L, L_CSQ + w * 128 + (l15 * 16 + 4 * g) * 4, v);
          }
        } else {
          constexpr int k = 4 * ((s - 14) % 7) + (i >> 1), m = HNOW + 2 * (s - 21) + (i >> 2);
          if constexpr (k == 0 && s == 21) aH[i] = mfma32(__builtin_bit_cast(bf16x8, u32x4{ch[i][0], ch[i][1], 0u, 0u}), lnb, f32x16{});
          aH[i & 1] = mfma32(wf[slot], xf[k], s == 14 && k == 0 ? f32x16{} : aH[i & 1]);
          if constexpr (s == 19 && i == 7) { cnt_wait(L, C_CSH, 1); rdcs(0); }
          if constexpr (s == 20 && (i == 2 || i == 3)) aH[i & 1] = mfma32(__builtin_bit_cast(bf16x8, u32x4{ch[i & 1][0], ch[i & 1][1], 0u, 0u}), lnb, aH[i & 1]);
          if constexpr ((i & 3) == 1 && s >= 21 && m < 4) hstore(m);
        }
        SB();
      });
      if constexpr (s == 18 || s == 20 || s == 27) prog_st(L, C_DONE + w, s);
      if constexpr (s >= 14 && s <= S_FQ) { if (!sigq && (s == S_FQ || vm_le<0>())) { vm<0>(); cnt_add(lb, C_FQ); sigq = true; } }
      if constexpr (s >= 21 && HNOW + 2 * (s - 21) >= 4) { if (!siga && vm_le<0>()) { vm<0>(); cnt_add(lb, C_FA); siga = true; } }
    });
    lanes();
    const u32 vFG = WS_FG + (rb * 16 + 7 * kh + (l < 7 ? l : 0)) * FS;
#pragma unroll
    for (int c = 0; c < 4; ++c) hpack(1, c);
    SB();
    if (!siga) { vm<0>(); cnt_add(lb, C_FA); }
#pragma unroll
    for (int m = 4; m < 8; ++m) hstore(m);
    lds_tag();
    u32x2 polled;
    u32 vW[2];
    vW2(vW);
    const u32 vA = l * 16, sA = (rb * 4 + w) * 144, sO = ((rb * 4 + (giver ? w ^ 2 : w)) * 144 + (giver ? 128 : 112)) * 1024;
    u32 sN[6];
#pragma unroll
    for (int j = 0; j < 6; ++j) { sN[j] = (sA + kstep(8 + 4 * j)) * 1024; asm volatile("" : "+s"(sN[j])); }
    const u32 vB = vA + 4096;
    auto ldA = [&](auto M) { constexpr int m = decltype(M)::v; hA[m] = ld16_async<true, (m & 3) * 1024>(m >= 32 ? vB : vA, rAC, sN[((m - 8) % 24) / 4]); };
    auto ldO = [&](auto M) { constexpr int m = decltype(M)::v; hA[m] = ld16_async<true, (m & 3) * 1024>(vA, rAC, sO + ((m - 56) & ~3) * 1024); };
    const rsrc_t rWo = mkrs(wo, 448 * 896);
    u32 oW[2], oO[2];
#pragma unroll
    for (int b = 0; b < 2; ++b) { oW[b] = (4 * (g >> 1) + q4) * 128 + ((b ^ (q4 >> 1)) << 6) + (g & 1) * 32 + p4 * 8; oO[b] = oW[b] + L_WC + (giver ? 8 * 3584 : 0); }
    bf16x8 wc[2][2];
    auto rdC = [&](int m) {
#pragma unroll
      for (int b = 0; b < 2; ++b) {
        const u32 o = m < 56 ? oW[b] + L_WC + m * 2048 : oO[b] + ((m - 56) >> 1) * 3584 + (m & 1) * 2048;
        wc[m & 1][b] = trfrag(L, o, o + 1024);
      }
    };
    f32x16 acc[2];
    f32x16 aco[2];
    u32 top, pre;
    u32x4 xres[2], bres;
    u32x2 pp[4];
    const int tile = giver ? w ^ 2 : w, r0 = (giver ? 16 : 0) + (l >> 3);
    auto step = [&](auto N) {
      constexpr int n = decltype(N)::v;
      if constexpr (n + 1 < 72) rdC(n + 1);
      SB();
      f32x16* a = n < 56 ? acc : aco;
      a[0] = mfma32(wc[n & 1][0], __builtin_bit_cast(bf16x8, hA[n]), n == 0 || n == 56 ? f32x16{} : a[0]);
      SB();
      if constexpr (n >= 8 && n < 56) sfor<SC.a0[n], SC.a1[n]>(ldA);
      if constexpr (n >= 8 && n < 15) dman<2>(lb + L_WC + (NWP + 4 * (n - 8) + w) * 2048, vW, rW2, (kstep(NWP + 4 * (n - 8)) + w) * (16 * 896) + cc * 128);
      if constexpr (n >= T_WO && n < T_WO + 7) dman<2>(lb + L_WC + (4 * (n - T_WO) + w) * 2048, vW, rWo, (4 * (n - T_WO) + w) * (16 * 896) + cc * 128);
      if constexpr (n == T_WO) { if (w == 0) dman<2>(lb + L_WC + 28 * 2048, vW, rWo, 28 * (16 * 896) + cc * 128); }
      if constexpr (n >= 56) { top = max(max(n == 56 ? 0u : top, hA[n][0]), hA[n][1]); top = max(max(top, hA[n][2]), hA[n][3]); PIN(top); }
      a[1] = mfma32(wc[n & 1][1], __builtin_bit_cast(bf16x8, hA[n]), n == 0 || n == 56 ? f32x16{} : a[1]);
      SB();
    };
    cnt_wait(L, C_WLO, 2);
    rdC(0);
    sfor<0, OWN1>(step);
    bool sigg = false;
    auto pubg = [&] { if (last_of4(L, C_FG) && l == 0) st_flag(rCTL, WS_FG + (rb * 16 + cb) * FS, tag); sigg = true; };
#pragma unroll 1
    for (u32 n = 0; n < (1u << 20); ++n) {
      if (!sigg && vm_le<0>()) { vm<0>(); pubg(); }
      if (__builtin_amdgcn_readfirstlane(cnt_ld(L, C_FH))) break;
      __builtin_amdgcn_s_sleep(LSLP);
    }
    asm volatile("" ::: "memory");
    sfor<8, 8 + DA>(ldA);
    prog_wait<true>(L, 27);
    sfor<OWN1, 8>(step);
    sfor<8, 56>([&](auto N) {
      constexpr int n = decltype(N)::v;
      asm volatile("s_waitcnt vmcnt(%1)" : "+v"(hA[n]) : "n"(SC.wC[n]) : "memory");
      if constexpr (n == 30) cnt_add(lb, C_P30);
      constexpr auto gate = [](int m) { return m == T_WO ? C_P30 : m == 55 ? C_WO : m == NWP - 1 ? C_WHI : m == 51 ? C_LATE : -1; };
      if constexpr (gate(n) >= 0) { if ((u32)__builtin_amdgcn_readfirstlane(pre) < 4u) cnt_wait(L, gate(n), 4); asm volatile("" ::: "memory"); }
      if constexpr (gate(n + 2) >= 0) pre = cnt_ld(L, gate(n + 2));
      if constexpr (n == T_WO + 12) cnt_add(lb, C_WO);
      if constexpr (n == 8) { if (!sigg) pubg(); }
      if constexpr (n == T_WH) cnt_add(lb, C_WHI);
      if constexpr (n == TB - 3) polled = ld8c_async(vFG, rCTL);
      if constexpr (n == TB) { PIN(polled); if (!tagged(polled, tag)) spin<SLP>(rCTL, vFG, tag); }
      step(N);
      if constexpr (n >= T_O && n < T_O + 8) { ldO(IC<56 + 2 * (n - T_O)>{}); ldO(IC<57 + 2 * (n - T_O)>{}); }
      if constexpr (n == T_O + 8) {
#pragma unroll
        for (int k = 0; k < 2; ++k) xres[k] = ld16_async<false>((r0 + 8 * k) * 896 + (l & 7) * 16, rX, (rb * 128 + tile * 32) * 896 + cc * 128);
        bres = ld16_async<false>((l & 7) * 16, mkrs(b2, 896), cc * 128);
      }
    });
    asm volatile("s_waitcnt vmcnt(0)" : "+v"(hA[56]), "+v"(hA[57]), "+v"(hA[58]), "+v"(hA[59]), "+v"(hA[60]), "+v"(hA[61]), "+v"(hA[62]), "+v"(hA[63]), "+v"(xres[0]), "+v"(xres[1]),
                 "+v"(hA[64]), "+v"(hA[65]), "+v"(hA[66]), "+v"(hA[67]), "+v"(hA[68]), "+v"(hA[69]), "+v"(hA[70]), "+v"(hA[71]), "+v"(bres)::"memory");
    const float rs2 = rs * rs;
    if (giver) {
#pragma unroll
      for (int k = 0; k < 4; ++k) {
        const f32x16& a = acc[k >> 1];
        const int o = 8 * (k & 1);
        st16<16>(u32x4{pk(a[o] * rs2, a[o + 1] * rs2), pk(a[o + 2] * rs2, a[o + 3] * rs2), pk(a[o + 4] * rs2, a[o + 5] * rs2), pk(a[o + 6] * rs2, a[o + 7] * rs2)}, rAC,
                 PX + ((rb * 7 + cc) * 4 + w) * 4096 + k * 1024 + l * 16);
      }
    }
    const u32 vP = PX + ((rb * 7 + cc) * 4 + tile) * 4096 + ((l & 7) >> 1) * 1024 + r0 * 16 + (l & 1) * 8;
    sfor<56, T_PP>(step);
#pragma unroll
    for (int k = 0; k < 4; ++k) pp[k] = ld8c_async(vP + (k >> 1) * 128 + (k & 1) * 512, rAC);
    pre = cnt_ld(L, C_ATT);
    sfor<T_PP, 72>(step);
    asm volatile("s_waitcnt vmcnt(0)" : "+v"(pp[0]), "+v"(pp[1]), "+v"(pp[2]), "+v"(pp[3])::"memory");
#pragma unroll 1
    for (u32 n = 0; n < (1u << 17) && !__all(top != ~0u); ++n) {
#pragma unroll 1
      for (; n < (1u << 17); ++n) {
        asm volatile("" ::: "memory");
        if (__all(__builtin_amdgcn_raw_buffer_load_b32(rAC, sO + (l & 7) * 2048 + 1024, 0, 16) != ~0u)) break;
        __builtin_amdgcn_s_sleep(SLP);
      }
#pragma unroll
      for (int k = 0; k < 16; ++k) hA[56 + k] = __builtin_amdgcn_raw_buffer_load_b128(rAC, vA + sO + k * 1024, 0, 16);
      rdC(56);
      sfor<56, 72>(step);
    }
    if (!giver) { aco[0] += acc[0] * rs2; aco[1] += acc[1] * rs2; }
    if ((u32)__builtin_amdgcn_readfirstlane(pre) < 4u) cnt_wait(L, C_ATT, 4);
    asm volatile("" ::: "memory");
#pragma unroll
    for (int b = 0; b < 2; ++b)
#pragma unroll
      for (int k = 0; k < 4; ++k)
        lds_st<f32x4>(L, L_RED + w * 8192 + (l & 31) * 256 + (((b * 8 + k * 2 + (g >> 1)) ^ (l & 7)) << 4), f32x4{aco[b][4 * k], aco[b][4 * k + 1], aco[b][4 * k + 2], aco[b][4 * k + 3]});
    cnt_add(lb, C_PAIR0 + (w & 1));
#pragma unroll 1
    for (u32 n = 0; n < (1u << 17) && !__all(max(max(max(pp[0][0], pp[0][1]), max(pp[1][0], pp[1][1])), max(max(pp[2][0], pp[2][1]), max(pp[3][0], pp[3][1]))) != ~0u); ++n) {
      asm volatile("" ::: "memory");
#pragma unroll
      for (int k = 0; k < 4; ++k) pp[k] = __builtin_amdgcn_raw_buffer_load_b64(rAC, vP + (k >> 1) * 128 + (k & 1) * 512, 0, 16);
    }
    f32x4 ra[2], rc[2];
#pragma unroll
    for (int k = 0; k < 2; ++k) {
      ra[k] = f32x4{bflo(xres[k][0]) + bflo(bres[0]), bfhi(xres[k][0]) + bfhi(bres[0]), bflo(xres[k][1]) + bflo(bres[1]), bfhi(xres[k][1]) + bfhi(bres[1])} +
              f32x4{bflo(pp[2 * k][0]), bfhi(pp[2 * k][0]), bflo(pp[2 * k][1]), bfhi(pp[2 * k][1])};
      rc[k] = f32x4{bflo(xres[k][2]) + bflo(bres[2]), bfhi(xres[k][2]) + bfhi(bres[2]), bflo(xres[k][3]) + bflo(bres[3]), bfhi(xres[k][3]) + bfhi(bres[3])} +
              f32x4{bflo(pp[2 * k + 1][0]), bfhi(pp[2 * k + 1][0]), bflo(pp[2 * k + 1][1]), bfhi(pp[2 * k + 1][1])};
      PIN(ra[k]); PIN(rc[k]);
    }
    cnt_wait(L, C_PAIR0 + (w & 1), 2);
    const rsrc_t rO = mkrs(out, 2048 * 896);
#pragma unroll
    for (int k = 0; k < 2; ++k) {
      const int row = r0 + 8 * k;
      f32x4 a = ra[k], b = rc[k];
#pragma unroll
      for (int j = 0; j < 2; ++j) {
        a += lds_ld<f32x4>(L, L_RED + ((w & 1) + 2 * j) * 8192 + row * 256 + (((2 * (l & 7)) ^ (row & 7)) << 4));
        b += lds_ld<f32x4>(L, L_RED + ((w & 1) + 2 * j) * 8192 + row * 256 + (((2 * (l & 7) + 1) ^ (row & 7)) << 4));
      }
      st16<16>(u32x4{pk(a[0], a[1]), pk(a[2], a[3]), pk(b[0], b[1]), pk(b[2], b[3])}, rO, (rb * 128 + tile * 32 + row) * 896 + cc * 128 + (l & 7) * 16);
    }
    return;
  }

  const int li = w & 1;
  auto clear_o = [&] {
#pragma unroll
    for (int k = 0; k < 8; ++k) st16<16>(u32x4{~0u, ~0u, ~0u, ~0u}, rAC, ((ai * 4 + (k >> 1)) * 144 + 112 + 2 * ah + (k & 1)) * 1024 + l * 16);
  };
  if (!busy) {
    const int blk = (rk - 28) * 8 + x8;
#pragma unroll
    for (int k = 0; k < 14; ++k) d[k] = __builtin_amdgcn_raw_buffer_load_b128(mkrs(w1, 448 * 3584), (112 * (w - 4) + (l >> 3)) * 3584 + (l & 7) * 16 + (blk < 28 ? blk * 128 : 0x7fffffff), k * 8 * 3584, 0);
    const u32 bias = __builtin_amdgcn_raw_buffer_load_b16(mkrs(b1, 3584), (blk * 64 + l) * 2, 0, 0);
    SB();
    epoch();
    if (w == 6 && l < C_N) lds_st<u32>(L, L_CNT + l * 4, l < 4 ? 64u : 0u);
    wait_bar<63>();
    float a[8] = {};
#pragma unroll
    for (int k = 0; k < 14; ++k)
#pragma unroll
      for (int j = 0; j < 4; ++j) { a[2 * j] += bflo(d[k][j]); a[2 * j + 1] += bfhi(d[k][j]); }
    lds_st<f32x4>(L, RING + ((w - 4) * 64 + l) * 32, f32x4{a[0], a[1], a[2], a[3]});
    lds_st<f32x4>(L, RING + ((w - 4) * 64 + l) * 32 + 16, f32x4{a[4], a[5], a[6], a[7]});
    if (last_of4(L, C_CS0) && blk < 28) {
      float t = 0.f;
#pragma unroll
      for (int k = 0; k < 32; ++k) t += lds_ld<float>(L, RING + k * 256 + l * 4);
      const float hi = rbf(t);
      __builtin_amdgcn_raw_buffer_store_b64(u32x2{pk(hi, t - hi), bias}, rCTL, WS_CS + (blk * 64 + l) * 8, 0, 16);
      vm<0>();
      if (l == 0) st_flag(rCTL, WS_FC + blk * FS, tag);
    }
    if (w == 7) { clear_o(); vm<0>(); if (l == 0) st_flag(rCTL, WS_FZ + (ai * 16 + ah) * FS, tag); }
  } else if (w < 6) {
    const rsrc_t rW1 = mkrs(w1, 448 * 3584), rWq = mkrs(wqkv, 448 * 2688);
    u32 vX[4], vH[4], vQ[3], vC[2];
#pragma unroll
    for (int j = 0; j < 4; ++j) vX[j] = (8 * j + (l >> 3)) * 896 + (((l & 7) ^ ((j & 1) * 4 + g)) << 4) - 1024 * j;
    const u32 sX = (rb * 128 + 64 * li) * 896, sH = cb * 256 + 32 * li * 3584, sQ = cb * 192 + 16 * li * 2688;
    auto issue = [&](auto O) {
      constexpr int op = decltype(O)::v, k = op / 256, a = (op % 256) / 2, e = op & 1;
      if constexpr (k == K_X) dman<4>(lb + (a * 16 + 8 * li + 4 * e) * 1024, vX, rX, sX + a * 128 + e * 28672);
      if constexpr (k == K_WA && a < 14) dman<3>(lb + stage_addr(a) + li * 3072, vQ, rWq, sQ + a * (32 * 2688));
      if constexpr (k == K_WA && a >= 14) dman<4>(lb + stage_addr(a) + li * 4096, vH, rW1, sH + (a - 14) % 7 * (64 * 3584) + (a >= 21 ? 128 : 0));
      if constexpr (k == K_WCP) dman<2>(lb + L_WC + (2 * a + li) * 2048, vC, rW2, (kstep(2 * a) + li) * (16 * 896) + cc * 128);
    };
    lanes();
#pragma unroll
    for (int j = 0; j < 3; ++j) { const u32 n = 64 * j + l; vQ[j] = (n / 12) * 2688 + (n % 12) * 16 - 1024 * j; }
#pragma unroll
    for (int j = 0; j < 4; ++j) vH[j] = (8 * j + (l >> 3)) * 3584 + (((l & 7) ^ ((g & 1) << 2)) << 4) - 1024 * j;
    sfor<4, NPRE>([&](auto K) { issue(IC<SC.pro[decltype(K)::v]>{}); });
    vW2(vC);
    vm_bar<SC.wA[0]>();
    sfor<NPRE, NPRO>([&](auto K) { issue(IC<SC.pro[decltype(K)::v]>{}); });
    sfor<0, 15>([&](auto SS) {
      constexpr int s = decltype(SS)::v;
      vm_bar<SC.wA[s + 1]>();
      sfor<0, (s == 14 ? 2 : MAXO)>([&](auto K) { issue(IC<SC.opA[s][decltype(K)::v]>{}); });
    });
    vm<SC.wL[0]>();
    prog_st(L, C_LAND + li, 1);
    vm<0>();
    prog_st(L, C_LAND + li, 2);
    cnt_wait(L, C_FQ, 4);
    sfor<2, MAXO>([&](auto K) { issue(IC<SC.opA[14][decltype(K)::v]>{}); });
    vm<SC.wL[1]>();
    prog_st(L, C_LAND + li, 3);
    lds_tag();
    prog_wait<true>(L, 20);
    issue(IC<OP(K_WA, 26)>{}); issue(IC<OP(K_WA, 27)>{});
    vm<0>();
    prog_st(L, C_LAND + li, 4);
    cnt_add(lb, C_WLO);
    if (w == 4) { cnt_wait(L, C_FA, 5); if (l == 0) st_flag(rCTL, WS_FH + (rb * 16 + cb) * FS, tag); }
    else { cnt_wait(L, C_FA, 5); spin<SLP>(rCTL, l >= 16 && l < 32 ? WS_FZ + (rb * 16 + l - 16) * FS : WS_FH + (rb * 16 + (l < 7 ? 7 * kh + l : 7 * (1 - kh) + cc)) * FS, tag); cnt_add(lb, C_FH); }
  } else {
    if (stat) {
      bar();
      asm volatile("s_waitcnt vmcnt(0)" : "+v"(d[0]), "+v"(d[1]), "+v"(d[2]), "+v"(d[3]), "+v"(d[4]), "+v"(d[5]), "+v"(d[6])::"memory");
      const u32x2 ep = __builtin_amdgcn_raw_buffer_load_b64(rCTL, 0, 0, 16);
      float s1 = 0.f, s2 = 0.f;
#pragma unroll
      for (int r = 0; r < 2; ++r) {
        bar();
#pragma unroll
        for (int k = 14 * r; k < 14 * r + 14; ++k) { const u32 v = d[k >> 2][k & 3]; s1 = dot2(v, 0x3f803f80u, s1); s2 = dot2(v, v, s2); }
        PIN(s1); PIN(s2);
        SB();
      }
      tag = join(ep) + 1;
      bar();
      const float mean = sum8(s1) * (1.f / 448.f);
      const float var = fmaxf(sum8(s2) * (1.f / 448.f) - mean * mean, 0.f);
      const float sd = rbf(__builtin_amdgcn_sqrtf(rbf(rbf(var) + 1e-5f)));
      if ((l & 7) == 0) st16<16>(u32x4{pk(-mean, sd), (u32)tag, pk(-mean, sd), (u32)(tag >> 32)}, rCTL, WS_ST + srow * 16);
      bar();
    } else { const u32x2 ep = __builtin_amdgcn_raw_buffer_load_b64(rCTL, 0, 0, 16); bar(); bar(); bar(); bar(); tag = join(ep) + 1; bar(); }
    if (w == 6) {
      const u32 fp = WS_FC + (2 * cb + ((l >> 1) & 1)) * FS + (l & 1) * 4;
      int ss = 0, cs = 0;
      auto look = [&] {
        vm<0>();
        if (ss == 1) {
          const u32x4 a = lds_ld<u32x4>(L, L_ST + l * 16), b = lds_ld<u32x4>(L, L_ST + 1024 + l * 16);
          if (__all(a[1] == (u32)tag && a[3] == (u32)(tag >> 32) && b[1] == (u32)tag && b[3] == (u32)(tag >> 32))) { cnt_add(lb, C_ST); ss = 3; }
        }
        if (ss < 3) {
          asm volatile("s_mov_b32 m0, %0\n\ts_nop 0\n\tbuffer_load_dwordx4 %1, %2, 0 offen sc1 lds\n\tbuffer_load_dwordx4 %1, %2, 0 offen offset:1024 sc1 lds" ::"s"(lb + L_ST), "v"(l * 16 + WS_ST + rb * 2048), "s"(rCTL) : "memory");
          ss = 1;
        }
        if (cs == 2) { cnt_add(lb, C_CSH); cs = 3; }
        if (cs == 1 && __all(lds_ld<u32>(L, L_POLL + l * 4) == (u32)(l & 1 ? tag >> 32 : tag))) {
          asm volatile("s_mov_b32 m0, %0\n\ts_nop 0\n\tbuffer_load_dwordx4 %1, %2, 0 offen sc1 lds" ::"s"(lb + L_CSH), "v"(l * 16 + WS_CS + cb * 1024), "s"(rCTL) : "memory");
          cs = 2;
        }
        if (cs < 2) { asm volatile("s_mov_b32 m0, %0\n\ts_nop 0\n\tbuffer_load_dword %1, %2, 0 offen sc1 lds" ::"s"(lb + L_POLL), "v"(fp), "s"(rCTL) : "memory"); cs = 1; }
      };
#pragma unroll 1
      for (int s = 4; s <= 14; ++s) {
        wait_bar<63>();
        if (s == 6 && l < 30) lds_st<u32>(L, L_CNT + l * 4, l < 28 ? 0u : (u32)(l & 1 ? tag >> 32 : tag));
        if (s >= 7 && ss + cs != 6 && vm_le<0>()) look();
      }
#pragma unroll 1
      for (u32 n = 0; n < (1u << 17) && ss + cs != 6; ++n) look();
    } else {
      vm<0>();
      int k = 0;
      auto clear = [&] {
        st16<16>(u32x4{~0u, ~0u, ~0u, ~0u}, rAC, ((ai * 4 + (k >> 1)) * 144 + 112 + 2 * ah + (k & 1)) * 1024 + l * 16);
        st16<16>(u32x4{~0u, ~0u, ~0u, ~0u}, rAC, PX + ((rb * 7 + cc) * 4 + 2 * kh) * 4096 + k * 1024 + l * 16);
        ++k;
      };
#pragma unroll 1
      for (int s = 4; s <= 14; ++s) {
        bar();
        if (s >= CLR0 && k < 8 && vm_le<0>()) clear();
      }
      if (k < 8) cnt_wait(L, C_FQ, 4);
#pragma unroll 1
      while (k < 8) clear();
      vm<0>();
      cnt_add(lb, C_FA);
      if (l == 0) st_flag(rCTL, WS_FZ + (ai * 16 + ah) * FS, tag);
    }
  }
  lanes();
  const int aw = w - 4, hh = g >> 1;
  if (w >= 6) {
    const int e = l & 1, k = l < 14 ? l >> 1 : 0;
    const int ty = k == 0 ? 0 : (k < 4 ? 1 : 2), ro = k == 0 ? 0 : (k < 4 ? k - 1 : k - 4);
    const int rr = ai - ro < 0 ? 0 : ai - ro;
    if (!busy) { for (int k = 0; k < IDLE_SLP; ++k) __builtin_amdgcn_s_sleep(15); }
    if (busy) { cnt_wait(L, C_FQ, 4); if (w == 6 && l == 0) st_flag(rCTL, WS_FQ + (rb * 16 + cb) * FS, tag); }
    prog_wait<true>(L, 18);
#pragma unroll
    for (int k = 0; k < 3; ++k) lds_st<u32x2>(L, L_V + (6 * li + 2 * k + (l >> 5)) * 2048 + 1792 + (l & 31) * 8, u32x2{0x3f80u, 0u});
    if (w == 6) { spin<SLP>(rCTL, WS_FQ + (rr * 16 + (ty * 448 + 28 * ah + 27 * e) / 96) * FS, tag); cnt_add(lb, C_SQ); }
    else cnt_wait(L, C_SQ, 1);
#pragma unroll
    for (int e = 0; e < 2; ++e) {
#pragma unroll
      for (int j = 0; j < 6; ++j) {
        const int b = 2 * j + li, tb = 4 * ai - 8 + b < 0 ? 0 : 4 * ai - 8 + b;
        dma1792c(lb + (e ? L_V + b * 2048 : L_K + b * 1792), l * 16, rQKV, (tb * 336 + 112 + 112 * e + 7 * ah) * 256);
      }
      if (e == 0) {
#pragma unroll
        for (int j = 0; j < 2; ++j) dma1792c(lb + L_Q + (2 * li + j) * 2048, l * 16, rQKV, ((4 * ai + 2 * li + j) * 336 + 7 * ah) * 256);
        vm<0>();
        cnt_add(lb, C_KV);
      }
    }
  }
  u32 oK = L_K + aw * 1792 + hh * 256 + l15 * 16 + (g & 1) * 8, oK3 = hh ? oK + 1280 : oK + 1536;
  u32 oQv = L_Q + aw * 2048 + hh * 256 + l15 * 16 + (g & 1) * 8;
  const int F = 7 * ah + 4 * (g & 1) + p4;
  u32 oV[2][2];
#pragma unroll
  for (int t = 0; t < 2; ++t)
#pragma unroll
    for (int e = 0; e < 2; ++e) { oV[t][e] = L_V + aw * 2048 + (4 * (g & 1) + p4) * 256 + (((8 * e + 4 * hh + q4) ^ ((F & 6) << 1)) << 4) + ((t ^ (F & 1)) << 3); PIN(oV[t][e]); }
  const int dq = (l & 31) - 4 * hh;
  u32 mk[8], vW[2];
#pragma unroll
  for (int r = 0; r < 16; r += 2) { mk[r >> 1] = (r & 3) + 1 + 8 * (r >> 2) <= dq ? 0xffffffffu : ((r & 3) + 8 * (r >> 2) <= dq ? 0xffffu : 0u); PIN(mk[r >> 1]); }
  vW2(vW);
  PIN(oK); PIN(oK3); PIN(oQv); PIN(vW[0]); PIN(vW[1]);
  cnt_wait(L, C_KV, 2);
  __builtin_amdgcn_s_setprio(3);
  u32x2 qv[4];
#pragma unroll
  for (int k = 0; k < 4; ++k) qv[k] = lds_ld<u32x2>(L, oQv + k * 512);
  f32x16 sc[9];
  bf16x8 kf[9][2], vf[9][2];
  auto rdK = [&](int m) {
    const u32x2 a = lds_ld<u32x2>(L, oK + m * 1792), b = lds_ld<u32x2>(L, oK + m * 1792 + 512), c = lds_ld<u32x2>(L, oK + m * 1792 + 1024), d = lds_ld<u32x2>(L, oK3 + m * 1792);
    kf[m][0] = __builtin_bit_cast(bf16x8, u32x4{a[0], a[1], b[0], b[1]}); kf[m][1] = __builtin_bit_cast(bf16x8, u32x4{c[0], c[1], d[0], d[1]});
  };
  auto rdV = [&](int m) { vf[m][0] = trfrag(L, oV[0][0] + m * 2048, oV[0][1] + m * 2048); vf[m][1] = trfrag(L, oV[1][0] + m * 2048, oV[1][1] + m * 2048); };
  SB(); rdK(0); SB(); rdK(8); SB(); rdK(1); SB();
  if (hh) qv[3] = u32x2{0u, 0u};
  const bf16x8 qf[2] = {__builtin_bit_cast(bf16x8, u32x4{qv[0][0], qv[0][1], qv[1][0], qv[1][1]}), __builtin_bit_cast(bf16x8, u32x4{qv[2][0], qv[2][1], qv[3][0], qv[3][1]})};
  constexpr float NEG = -1e30f;
  const int m0 = 8 - 4 * ai - aw;
  float mx4[4];
  auto rowmax = [&](int m, bool first) {
#pragma unroll
    for (int k = 0; k < 4; ++k) {
      mx4[k] = first ? fmx(sc[m][4 * k], sc[m][4 * k + 1]) : fmx(fmx(mx4[k], sc[m][4 * k]), sc[m][4 * k + 1]);
      mx4[k] = fmx(fmx(mx4[k], sc[m][4 * k + 2]), sc[m][4 * k + 3]);
    }
    PIN(mx4[0]); PIN(mx4[1]); PIN(mx4[2]); PIN(mx4[3]);
  };
#pragma unroll
  for (int i = 0; i < 9; ++i) {
    const int m = i == 0 ? 0 : (i == 1 ? 8 : i - 1);
    if (i + 2 < 8) rdK(i + 2);
    sc[m] = mfma32(kf[m][0], qf[0], f32x16{});
    if (i == 3) {
      if (m0 > 0) {
        asm volatile("" ::: "memory");
#pragma unroll
        for (int r = 0; r < 16; ++r) sc[0][r] = NEG;
      }
#pragma unroll
      for (int r = 0; r < 16; ++r) sc[0][r] = (r & 3) + 8 * (r >> 2) <= dq ? sc[8][r] : sc[0][r];
    }
    if (i == 4) rowmax(0, true);
    sc[m] = mfma32(kf[m][1], qf[1], sc[m]);
    if (i >= 4 && i - 3 >= m0) rowmax(i - 3, false);
    SB();
  }
  if (w >= 6) { vm<0>(); cnt_add(lb, C_V); }
  if (busy) dman<2>(lb + L_Q + aw * 2048, vW, rW2, (kstep(52) + aw) * (16 * 896) + cc * 128);
  if (6 >= m0) rowmax(6, false);
  cnt_wait(L, C_V, 2);
  rdV(1); rdV(2);
  SB();
  if (7 >= m0) rowmax(7, false);
  const float mx0 = fmx(fmx(mx4[0], mx4[1]), fmx(mx4[2], mx4[3]));
  const auto ms = __builtin_amdgcn_permlane32_swap(__float_as_uint(mx0), __float_as_uint(mx0), false, false);
  const float mx = fmx(__uint_as_float(ms[0]), __uint_as_float(ms[1]));
  SB();
  f32x16 o;
  u32x4 pq[2];
#pragma unroll
  for (int i = 0; i < 16; ++i) {
    const int m = ((i >> 1) + 1) & 7, s = i & 1;
    if (!s) { if (i < 10) rdV((i >> 1) + 3); else if (i == 10) rdV(0); else if (i == 12) rdV(8); }
    if (i == 6) { vm<0>(); cnt_add(lb, C_LATE); }
    const float mm = m != 0 && m < m0 ? -NEG : mx;
    f32x2 d[4];
#pragma unroll
    for (int j = 0; j < 4; ++j) { d[j] = f32x2{sc[m][8 * s + 2 * j], sc[m][8 * s + 2 * j + 1]} - f32x2{mm, mm}; PIN(d[j]); }
#pragma unroll
    for (int j = 0; j < 2; ++j) { d[j][0] = __builtin_amdgcn_exp2f(d[j][0]); d[j][1] = __builtin_amdgcn_exp2f(d[j][1]); PIN(d[j]); }
    SB();
    if (i >= 1 && i < 15) o = mfma32(vf[((i - 1) >> 1) + 1][s ^ 1], __builtin_bit_cast(bf16x8, pq[s ^ 1]), i == 1 ? f32x16{} : o);
    if (i == 15) {
      const u32x4 k4 = {mk[0], mk[1], mk[2], mk[3]};
      o = mfma32(vf[0][0], __builtin_bit_cast(bf16x8, pq[0] & ~k4), o);
      o = mfma32(vf[8][0], __builtin_bit_cast(bf16x8, pq[0] & k4), o);
    }
    SB();
#pragma unroll
    for (int j = 2; j < 4; ++j) { d[j][0] = __builtin_amdgcn_exp2f(d[j][0]); d[j][1] = __builtin_amdgcn_exp2f(d[j][1]); }
#pragma unroll
    for (int j = 0; j < 4; ++j) pq[s][j] = pk(d[j][0], d[j][1]);
    PIN(pq[s]);
    SB();
  }
  {
    const u32x4 k4 = {mk[4], mk[5], mk[6], mk[7]};
    o = mfma32(vf[0][1], __builtin_bit_cast(bf16x8, pq[1] & ~k4), o);
    o = mfma32(vf[8][1], __builtin_bit_cast(bf16x8, pq[1] & k4), o);
  }
  SB();
  cnt_add(lb, C_ATT);
  const auto dn = __builtin_amdgcn_permlane32_swap(__float_as_uint(o[12]), __float_as_uint(o[12]), false, false);
  const float inv = __builtin_amdgcn_rcpf(__uint_as_float(dn[1]));
  const float in2 = hh ? 0.f : inv;
  const u32 so = ((ai * 4 + aw) * 144 + 112 + 2 * ah) * 1024;
  st16<16>(u32x4{pk(o[0] * inv, o[1] * inv), pk(o[2] * inv, o[3] * inv), pk(o[4] * inv, o[5] * inv), pk(o[6] * inv, o[7] * inv)}, rAC, l * 16 + so);
  st16<16>(u32x4{pk(o[8] * inv, o[9] * inv), pk(o[10] * inv, o[11] * inv), pk(o[12] * in2, o[13] * in2), pk(o[14] * in2, o[15] * in2)}, rAC, l * 16 + so + 1024);
  vm<0>();
  if (last_of4(L, C_FB)) {
    if (l == 0) st_flag(rCTL, WS_FB + (ai * 16 + ah) * FS, tag);
    if (blockIdx.x == 255) {
#pragma unroll 1
      for (int k = 0; k < 4; ++k) spin<32>(rCTL, WS_FB + (k * 64 + l) * FS, tag);
      if (l == 0) st_flag(rCTL, 0, tag);
    }
  }
}

extern "C" void kernel_launch(void* const* d_in, const int* in_sizes, int n_in, void* d_out, int out_size, void* d_ws, size_t ws_size, hipStream_t stream) {
  fused_block<<<256, 512, 0, stream>>>((const char*)d_in[0], (const char*)d_in[1], (const char*)d_in[2], (const char*)d_in[4], (const char*)d_in[5],
                                       (const char*)d_in[6], (const char*)d_in[7], (char*)d_out, (char*)d_ws);
}
```
